```python
import torch

def _softmax(x, axis=-1):
    return torch.softmax(x, dim=axis)
def _gelu(x):
    return torch.nn.functional.gelu(x, approximate="tanh")
def _silu(x):
    return torch.nn.functional.silu(x)
def _sigmoid(x):
    return torch.sigmoid(x)
def _layernorm(x, axis=-1, eps=1e-5):
    m = x.mean(dim=axis, keepdim=True)
    v = x.var(dim=axis, keepdim=True, unbiased=False)
    return (x - m) / torch.sqrt(v + eps)
def _scatter_add(tgt, idx, src, axis=0):
    return tgt.clone().index_add_(axis, idx, src)
def _segment_sum(src, seg, n):
    out = torch.zeros((n,) + src.shape[1:], dtype=src.dtype)
    return out.index_add_(0, seg, src)
def _pad(x, pw):
    flat = tuple(v for pair in reversed(pw) for v in pair)
    return torch.nn.functional.pad(x, flat)

kernel_name = 'interleave_layers__sliding_local_dilated__moe_arch_mod__prompt'

def setup_inputs():

    _w = (lambda a: (lambda t: t.to(torch.bfloat16) if t.is_floating_point() else t)(torch.as_tensor(a)))
    def _A_setup():
        B, S, D = 1, 2048, 576
        return {
            "x": _w((lambda *s: torch.randn(*s, dtype=torch.bfloat16) * 0.05)(B, S, D)),
            "wqkv": _w((lambda *s: torch.randn(*s, dtype=torch.bfloat16) * 0.05)(D, 3 * D)),
            "wo": _w((lambda *s: torch.randn(*s, dtype=torch.bfloat16) * 0.05)(D, D)),
        }
    _A_in = _A_setup()

    def _B_setup():
        B, S, D, F, C = 1, 2048, 576, 4608, 1
        return {
            "x": _w((lambda *s: torch.randn(*s, dtype=torch.bfloat16) * 0.05)(B, S, D)),
            "w_r": _w((lambda *s: torch.randn(*s, dtype=torch.bfloat16) * 0.05)(D)),
            "w_gate": _w((lambda *s: torch.randn(*s, dtype=torch.bfloat16) * 0.05)(D, F)),
            "w_up": _w((lambda *s: torch.randn(*s, dtype=torch.bfloat16) * 0.05)(D, F)),
            "w_down": _w((lambda *s: torch.randn(*s, dtype=torch.bfloat16) * 0.05)(F, D)),
        }
    _B_in = _B_setup()

    return {
        "A_x": _A_in["x"],
        "A_wqkv": _A_in["wqkv"],
        "A_wo": _A_in["wo"],
        "B_x": _B_in["x"],
        "B_w_r": _B_in["w_r"],
        "B_w_gate": _B_in["w_gate"],
        "B_w_up": _B_in["w_up"],
        "B_w_down": _B_in["w_down"],
    }

def reference(A_x, A_wqkv, A_wo, B_x, B_w_r, B_w_gate, B_w_up, B_w_down):
    def _A_ref(x, wqkv, wo):
        B, S, D = x.shape
        H = 32; Dh = D // H
        W, dil = 8, 2
        qkv = (lambda x, *d: x.permute(*d))((x @ wqkv).reshape(B, S, 3, H, Dh), 2, 0, 3, 1, 4)
        q, k, v = qkv[0], qkv[1], qkv[2]
        scale = Dh ** -0.5
        pos = torch.arange(S)
        off = torch.arange(W) * dil
        idx = pos[:, None] - off[None, :]
        mask = idx >= 0
        idx_c = torch.where(mask, idx, 0).reshape(-1)
        kg = k[:, :, idx_c, :].reshape(B, H, S, W, Dh)
        vg = v[:, :, idx_c, :].reshape(B, H, S, W, Dh)
        logits = torch.einsum("bhsd,bhswd->bhsw", q, kg) * scale
        logits = torch.where(mask[None, None, :, :], logits, -1e9)
        attn = _softmax(logits, axis=-1)
        o = torch.einsum("bhsw,bhswd->bhsd", attn, vg)
        return (lambda x, *d: x.permute(*d))(o, 0, 2, 1, 3).reshape(B, S, D) @ wo

    def _B_ref(x, w_r, w_gate, w_up, w_down):
        B, S, D = x.shape
        C = 1
        r = x @ w_r
        idx = torch.argsort(-r, axis=-1)[:, :C]
        idx_e = (lambda x, n, axis: torch.repeat_interleave(x, n, dim=axis))(idx.reshape(B, C, 1), D, axis=2)
        x_sel = (lambda x, i, axis: torch.take_along_dim(x, i, dim=axis))(x, idx_e, axis=1)
        g = _silu(x_sel @ w_gate)
        u = x_sel @ w_up
        y = (g * u) @ w_down
        r_sel = _sigmoid((lambda x, i, axis: torch.take_along_dim(x, i, dim=axis))(r, idx, axis=1))
        y = y * r_sel.reshape(B, C, 1)
        b_off = (torch.arange(B) * S).reshape(B, 1)
        flat_idx = (idx + b_off).reshape(B * C)
        zero = (x * 0.0).reshape(B * S, D)
        upd = _scatter_add(zero, flat_idx, y.reshape(B * C, D), axis=0)
        return x + upd.reshape(B, S, D)

    x = A_x
    x = x + _A_ref(_layernorm(x, axis=-1), A_wqkv, A_wo)
    x = x + _B_ref(_layernorm(x, axis=-1), B_w_r, B_w_gate, B_w_up, B_w_down)
    x = x + _A_ref(_layernorm(x, axis=-1), A_wqkv, A_wo)
    x = x + _B_ref(_layernorm(x, axis=-1), B_w_r, B_w_gate, B_w_up, B_w_down)
    return x
```

```cpp
#include <hip/hip_runtime.h>
#include <stdint.h>

#define WREP 8
#define FST 32
#define NTD " nt"
#define XFS 32
#define GN1 14
#define XCI 0x04208760u
#define XCN 0x02221110u
#define PFA 3

#if defined(__HIP_DEVICE_COMPILE__)
#define NOPK __attribute__((target("no-packed-fp32-ops")))
#else
#define NOPK
#endif
#define DEV __device__ __forceinline__ NOPK
typedef unsigned short u16;
typedef unsigned int u32;
typedef unsigned long long u64;
#define __uint_as_float(x) __builtin_bit_cast(float, (u32)(x))
#define __float_as_uint(x) __builtin_bit_cast(u32, (float)(x))
#define fmaxf(a, b) __builtin_fmaxf((a), (b))
typedef __attribute__((ext_vector_type(2))) float f32x2;
typedef __attribute__((ext_vector_type(2))) __bf16 bf16x2;
typedef __attribute__((ext_vector_type(2))) u32 u32x2;
typedef __attribute__((ext_vector_type(4))) u32 u32x4;

extern __shared__ __attribute__((aligned(16))) char smem[];

#define SPIN_MAX 20000
#define LSPIN_MAX 400000

#define WS_BYTES 0x1000000
#define OFF_EPOCH 0
#define OFF_TMO 256
#define OFF_FLAG 0xF0000
#define OFF_GFLAG 4096
#define OFF_XFLAG 0x18000
#define OFF_KEY 16384
#define OFF_X2R 81920
#define OFF_STATS 0x20000
#define STATS_B 0x40000
#define OFF_CROW 0xA0000
#define OFF_YP 0x100000
#define YP_B 589824
#define OFF_OT 0x240000
#define OFF_X1G 0x6C0000
#define OFF_WI 0x900000
#define OFF_WOI 0xB00000
#define OFF_YP2 0xD00000
#define OOB 0x7FFFFF00

#define LDS_BYTES 163840
#define XCH 10240
#define L_XH 73728
#define L_KV 0
#define KVS 304
#define L_Q 24320
#define L_WOE 33536
#define L_SCR 18432
#define L_G 98048
#define L_U 125696
#define L_MISC 153344
#define L_MR (L_MISC)
#define L_BC (L_MISC + 640)
#define BC_BEST 0
#define BC_WRS 4
#define BC_GC 16
#define BC_C1 20
#define BC_GO 28
#define BC_PST 32
#define BC_FIN 36
#define BC_BEST1 40
#define BC_XC 44
#define BC_ZERO 48
#define BC_G0 96
#define BC_TB 168
#define BC_KC 64
#define BC_MC 160
#define BC_FC 76
#define BC_CR 56
#define BC_PK 84
#define BC_PC 152
#define BC_WC 144
#define BC_HF 128
#define L_FP (L_SCR3 + 2304)
#define L_RS 92160
#define L_PK(t) ((t) < 2 ? 92672 + (t) * 2304 : ((t) == 2 ? L_SCR3 : L_HROW))
#define L_WR (L_MISC + 9888)
#define L_CS (L_MISC + 928)
#define L_HROW (L_MISC + 1824)
#define L_XR (L_MISC + 4128)
#define L_SCR3 (L_MISC + 5280)

#define SENT 0xFFFFFFFFu

DEV void bar() {
    asm volatile("s_waitcnt lgkmcnt(0)" ::: "memory");
    __builtin_amdgcn_s_barrier();
    asm volatile("" ::: "memory");
}
#define VMWAIT(n) asm volatile("s_waitcnt vmcnt(" #n ")" ::: "memory")
DEV void vmwait8n(u32 (&d)[8], const int n) {
    asm volatile("s_waitcnt vmcnt(%8)" : "+v"(d[0]), "+v"(d[1]), "+v"(d[2]), "+v"(d[3]), "+v"(d[4]), "+v"(d[5]), "+v"(d[6]), "+v"(d[7]) : "n"(n) : "memory");
}
DEV u32x4 mk_srd(const void* p, u32 bytes) {
    u64 a = (u64)p;
    asm volatile("" : "+s"(a));
    return (u32x4){(u32)a, (u32)(a >> 32) & 0xffffu, bytes, 0x00020000u};
}
DEV void dma16g(u32x4 srd, u32 voff, u32 soff, u32 lds_dst) {
    asm volatile("s_mov_b32 m0, %0\n\ts_nop 3\n\tbuffer_load_dwordx4 %1, %2, %3 offen lds" :: "s"(lds_dst), "v"(voff), "s"(srd), "s"(soff) : "memory");
}
DEV void dma16x4_sc1(u32x4 srd, u32 voff, u32 soff, u32 lds_dst) {
    asm volatile("s_mov_b32 m0, %0\n\ts_nop 3\n\tbuffer_load_dwordx4 %1, %2, %3 offen sc1 lds\n\tbuffer_load_dwordx4 %1, %2, %3 offen offset:1024 sc1 lds\n\t"
                 "buffer_load_dwordx4 %1, %2, %3 offen offset:2048 sc1 lds\n\tbuffer_load_dwordx4 %1, %2, %3 offen offset:3072 sc1 lds"
                 :: "s"(lds_dst), "v"(voff), "s"(srd), "s"(soff) : "memory");
}
DEV void bld32(u32& d, u32x4 srd, u32 voff, u32 soff, const int imm) {
    asm volatile("s_nop 4\n\tbuffer_load_dword %0, %1, %2, %3 offen offset:%4" : "=v"(d) : "v"(voff), "s"(srd), "s"(soff), "n"(imm));
}
DEV void old8(u32 (&d)[8], u32x4 srd, u32 voff, u32 s2) {
    asm volatile("s_nop 4\n\tbuffer_load_dword %0, %8, %9, 0 offen nt\n\tbuffer_load_dword %1, %8, %9, 0 offen offset:1152 nt\n\t"
                 "buffer_load_dword %2, %8, %9, 0 offen offset:2304 nt\n\tbuffer_load_dword %3, %8, %9, 0 offen offset:3456 nt\n\t"
                 "buffer_load_dword %4, %8, %9, %10 offen nt\n\tbuffer_load_dword %5, %8, %9, %10 offen offset:1152 nt\n\t"
                 "buffer_load_dword %6, %8, %9, %10 offen offset:2304 nt\n\tbuffer_load_dword %7, %8, %9, %10 offen offset:3456 nt"
                 : "=&v"(d[0]), "=&v"(d[1]), "=&v"(d[2]), "=&v"(d[3]), "=&v"(d[4]), "=&v"(d[5]), "=&v"(d[6]), "=&v"(d[7])
                 : "v"(voff), "s"(srd), "s"(s2));
}
DEV void wld8p(u32x4 srd, u32 voff, u32 soff, int w) {
    u32 t;
    asm volatile("s_cmp_gt_u32 %4, 3\n\ts_cbranch_scc1 Lw8_%=\n\ts_setprio 3\n\ts_nop 1\n\t"
                 "buffer_load_dword v48, %1, %2, %3 offen nt\n\tbuffer_load_dword v49, %1, %2, %3 offen offset:3456 nt\n\ts_add_u32 %0, %3, 6912\n\t"
                 "buffer_load_dword v50, %1, %2, %0 offen nt\n\tbuffer_load_dword v51, %1, %2, %0 offen offset:3456 nt\n\ts_add_u32 %0, %3, 13824\n\t"
                 "buffer_load_dword v52, %1, %2, %0 offen nt\n\tbuffer_load_dword v53, %1, %2, %0 offen offset:3456 nt\n\ts_add_u32 %0, %3, 20736\n\t"
                 "buffer_load_dword v54, %1, %2, %0 offen nt\n\tbuffer_load_dword v55, %1, %2, %0 offen offset:3456 nt\n"
                 "Lw8_%=:"
                 : "=&s"(t) : "v"(voff), "s"(srd), "s"(soff), "s"(w) : "scc", "memory");
}
DEV void x_dma(u32x4 srd, u32 xv, u32 orm, u32 c, u32 n) {
    u32 z1, z2, z3, y0, y1, so, t;
    asm volatile("s_cmp_eq_u32 %8, 0\n\ts_cbranch_scc1 Lx1_%=\n\t"
                 "v_xor_b32 %2, 64, %9\n\tv_or_b32 %3, %11, %9\n\tv_add_u32 %0, 0x2000, %2\n\tv_add_u32 %1, 0x4000, %9\n\tv_or_b32 %4, %11, %0\n\tv_add_u32 %2, 0x6000, %2\n"
                 "Lx0_%=:\n\t"
                 "s_cmp_gt_u32 %7, 8\n\ts_cselect_b32 %6, 9, 0\n\ts_sub_u32 %7, %7, %6\n\ts_mul_i32 m0, %7, 10240\n\ts_lshl_b32 %5, %7, 7\n\t"
                 "s_mov_b32 exec_lo, 0xffff0000\n\tbuffer_load_dwordx4 %3, %10, %5 offen lds\n\ts_mov_b32 exec_lo, -1\n\t"
                 "buffer_load_dwordx4 %4, %10, %5 offen offset:1024 lds\n\tbuffer_load_dwordx4 %1, %10, %5 offen offset:2048 lds\n\tbuffer_load_dwordx4 %2, %10, %5 offen offset:3072 lds\n\t"
                 "s_add_u32 m0, m0, 4096\n\ts_add_u32 %5, %5, 36864\n\t"
                 "buffer_load_dwordx4 %9, %10, %5 offen lds\n\tbuffer_load_dwordx4 %0, %10, %5 offen offset:1024 lds\n\t"
                 "buffer_load_dwordx4 %1, %10, %5 offen offset:2048 lds\n\tbuffer_load_dwordx4 %2, %10, %5 offen offset:3072 lds\n\t"
                 "s_add_u32 m0, m0, 4096\n\ts_add_u32 %5, %5, 36864\n\t"
                 "buffer_load_dwordx4 %9, %10, %5 offen lds\n\tbuffer_load_dwordx4 %0, %10, %5 offen offset:1024 lds\n\t"
                 "s_add_u32 %7, %7, 1\n\ts_sub_u32 %8, %8, 1\n\ts_cmp_lg_u32 %8, 0\n\ts_cbranch_scc1 Lx0_%=\n"
                 "Lx1_%=:"
                 : "=&v"(z1), "=&v"(z2), "=&v"(z3), "=&v"(y0), "=&v"(y1), "=&s"(so), "=&s"(t), "+s"(c), "+s"(n) : "v"(xv), "s"(srd), "s"(orm) : "scc", "memory");
}
DEV void wunit_st(u32x4 srd, u32 ib, int w) {
    asm volatile("s_cmp_gt_u32 %2, 3\n\ts_cbranch_scc1 Lu_%=\n\ts_waitcnt vmcnt(0)\n\t"
                 "v_perm_b32 v56, v49, v48, %3\n\tv_perm_b32 v57, v51, v50, %3\n\tv_perm_b32 v58, v53, v52, %3\n\tv_perm_b32 v59, v55, v54, %3\n\t"
                 "buffer_store_dwordx4 v[56:59], %0, %1, 0 offen sc1\n\t"
                 "v_perm_b32 v60, v49, v48, %4\n\tv_perm_b32 v61, v51, v50, %4\n\tv_perm_b32 v62, v53, v52, %4\n\tv_perm_b32 v63, v55, v54, %4\n\t"
                 "buffer_store_dwordx4 v[60:63], %0, %1, 0 offen offset:1024 sc1\n"
                 "Lu_%=:" :: "v"(ib), "s"(srd), "s"(w), "s"(0x05040100u), "s"(0x07060302u) : "scc", "memory");
}
DEV u32 plo(u32 hi, u32 lo) { return __builtin_amdgcn_perm(hi, lo, 0x05040100u); }
DEV u32 phi(u32 hi, u32 lo) { return __builtin_amdgcn_perm(hi, lo, 0x07060302u); }
DEV void wfrag(const u32 (&d)[8], u32x4& f0, u32x4& f1) {
    f0 = (u32x4){plo(d[1], d[0]), plo(d[3], d[2]), plo(d[5], d[4]), plo(d[7], d[6])};
    f1 = (u32x4){phi(d[1], d[0]), phi(d[3], d[2]), phi(d[5], d[4]), phi(d[7], d[6])};
}
DEV u32 pk2(float a, float b) { f32x2 v = {a, b}; return __builtin_bit_cast(u32, __builtin_convertvector(v, bf16x2)); }
DEV float rbf(float a) { return (float)(__bf16)a; }
DEV float ln_rs(float var) { return __builtin_amdgcn_rcpf(rbf(__builtin_amdgcn_sqrtf(rbf(rbf(var) + 1e-5f)))); }
DEV float ln_m(float m) { return rbf(m); }
DEV u32x2 ln_next(float m, float var, float mb, float rs) {
    const float c = 1.f + rs;
    const float mb2 = ln_m(c * m - mb * rs);
    return (u32x2){__float_as_uint((mb * rs + mb2) * __builtin_amdgcn_rcpf(c)), __float_as_uint(c * ln_rs(c * c * var))};
}
DEV float lo16(u32 v) { return __uint_as_float(v << 16); }
DEV float hi16(u32 v) { return __uint_as_float(v & 0xffff0000u); }
DEV u32 san(u32 v) { return v < 0xFFFFFFFEu ? v : 0xFFFFFFFEu; }
DEV u32 umax2(u32 a, u32 b) { return a > b ? a : b; }
DEV float dot2(u32 a, u32 b, float c) {
    return __builtin_amdgcn_fdot2_f32_bf16(__builtin_bit_cast(bf16x2, a), __builtin_bit_cast(bf16x2, b), c, false);
}
DEV float gsum(float v) {
    u32 u = __float_as_uint(v);
    auto a = __builtin_amdgcn_permlane16_swap(u, u, false, false);
    v = __uint_as_float(a[0]) + __uint_as_float(a[1]);
    u = __float_as_uint(v);
    auto c = __builtin_amdgcn_permlane32_swap(u, u, false, false);
    return __uint_as_float(c[0]) + __uint_as_float(c[1]);
}
#define DPP(v, ctrl) ((u32)__builtin_amdgcn_update_dpp(0, (int)(v), (ctrl), 0xf, 0xf, false))
DEV u32 rmax(u32 v) {
    v = umax2(v, DPP(v, 0x128)); v = umax2(v, DPP(v, 0x124)); v = umax2(v, DPP(v, 0x122)); v = umax2(v, DPP(v, 0x121));
    return v;
}
DEV float rsum(float v) {
    v += __uint_as_float(DPP(__float_as_uint(v), 0x128)); v += __uint_as_float(DPP(__float_as_uint(v), 0x124));
    v += __uint_as_float(DPP(__float_as_uint(v), 0x122)); v += __uint_as_float(DPP(__float_as_uint(v), 0x121));
    return v;
}
DEV u32 lds32(int off) { return *(const u32*)(smem + off); }
typedef __attribute__((address_space(3))) volatile u32* lds_vu32;
DEV u32 lds32v(int off) { return *(lds_vu32)(smem + off); }
DEV u32x2 lds64(int off) { return *(const u32x2*)(smem + off); }
DEV u32x4 lds128(int off) { return *(const u32x4*)(smem + off); }
DEV void sts32(int off, u32 v) { *(u32*)(smem + off) = v; }
DEV void sts32v(int off, u32 v) { *(lds_vu32)(smem + off) = v; }
DEV void sts64(int off, u32x2 v) { *(u32x2*)(smem + off) = v; }
DEV void sts128(int off, u32x4 v) { *(u32x4*)(smem + off) = v; }
DEV void lds_inc(int off, int lane) {
    (void)lane;
    asm volatile("s_mov_b64 exec, 1\n\tds_add_u32 %0, %1\n\ts_mov_b64 exec, -1" :: "v"(off), "v"(1u) : "memory");
}
DEV u32 lds_inc_rtn(int off) {
    u32 old;
    asm volatile("s_mov_b64 exec, 1\n\tds_add_rtn_u32 %0, %1, %2\n\ts_mov_b64 exec, -1\n\ts_waitcnt lgkmcnt(0)" : "=&v"(old) : "v"(off), "v"(1u) : "memory");
    return __builtin_amdgcn_readfirstlane(old);
}
#define BST128(v, off, soff) __builtin_amdgcn_raw_buffer_store_b128((v), rs, (off) + (soff), 0, 16)
#define BST64(v, off, soff) __builtin_amdgcn_raw_buffer_store_b64((v), rs, (off), (soff), 16)
#define BST32(v, off, soff) __builtin_amdgcn_raw_buffer_store_b32((v), rs, (off), (soff), 16)
#define CFENCE() asm volatile("" ::: "memory")
#define SB() __builtin_amdgcn_sched_barrier(0)
#define TMO(code) (tmo = (code))
#define LWAIT(off, target, code) do { int it_ = 0; _Pragma("nounroll") for (; it_ < LSPIN_MAX; ++it_) { if (lds32v(off) >= (u32)(target)) break; __builtin_amdgcn_s_sleep(1); } \
    if (it_ == LSPIN_MAX) TMO(code); CFENCE(); } while (0)

#define LWAIT0(off, target, code) do { int it_ = 0; _Pragma("nounroll") for (; it_ < LSPIN_MAX; ++it_) { if (lds32v(off) >= (u32)(target)) break; } \
    if (it_ == LSPIN_MAX) TMO(code); CFENCE(); } while (0)

#define ONES2 0x3F803F80u
#define LATE_MASK(r) ((((r) & 63) >= 50 ? 15ull : 3ull) << (((r) >> 6) * 2))
#define RG_OF(b) (((b) & 7) * 4 + (((b) >> 3) & 3))
#define CB_OF(b) ((b) >> 5)
#define IDS \
    int lane; asm volatile("v_mbcnt_lo_u32_b32 %0, -1, 0\n\tv_mbcnt_hi_u32_b32 %0, -1, %0" : "=v"(lane)); \
    int w = w_; asm volatile("" : "+s"(w)); \
    int b = b_; asm volatile("" : "+s"(b)); \
    const int rg = RG_OF(b), cb = CB_OF(b); \
    const int r16 = lane & 15, g = lane >> 4; \
    (void)rg; (void)cb; (void)r16; (void)g; (void)w;

#define CSL(w) ((w) == 3 ? 6 : ((w) == 6 ? 3 : (w)))
struct LaneC { int ka, kt, qa, qt, va, ta, l0; u32 av, tv0, ot; int sd, sstr; };
DEV LaneC lane_consts(int w_, int b_) {
    IDS
    LaneC c;
    const int rt4 = w & 3, pp = w >> 2;
    const int kvb = L_KV + 16 * rt4 * KVS + 72 * pp;
    const int qb = L_Q + (16 * rt4 + r16) * 144 + 72 * pp;
    const int trr = kvb + (4 * g + (r16 >> 2)) * KVS + 144;
    c.kt = kvb + r16 * KVS + 32; c.ka = kvb + r16 * KVS + g * 8;
    c.qa = qb + g * 8; c.qt = g == 2 ? qb + 32 : L_BC + BC_ZERO;
    c.va = trr + (r16 & 3) * 8; c.ta = trr + 32;
    c.l0 = 4096 + (8 * g + (r16 & 7)) * 16;
    c.av = lane * 16;
    c.tv0 = pp ? 7168 + g * 256 + r16 * 8 : 7168 + lane * 16;
    c.ot = 0;
    const int P = 4 * CSL(w) + g;
    c.sstr = P >= 18 ? 144 : KVS;
    c.sd = (P >= 18 ? (L_Q - 16 * 144 + (P - 18) * 16) : (L_KV + P * 16)) + r16 * c.sstr;
    c.sstr *= 16;
    return c;
}

struct MidS { u32x4 srd; u32 ob, st, tt, c0, l1d, tvd, wi, ld, wx, ldx, msk, scr, stat, x1, layer, pk, n7, skip, wc, lb; };

DEV void lc_set(const LaneC& c) {
    asm volatile("v_mov_b32 v100, %0\n\t" "v_mov_b32 v101, %1\n\t" "v_mov_b32 v102, %2\n\t" "v_mov_b32 v103, %3\n\t" "v_mov_b32 v104, %4\n\t" "v_mov_b32 v105, %5\n\t" "v_mov_b32 v106, %6\n\t" "v_mov_b32 v107, %7\n\t" "v_mov_b32 v108, %8\n\t" "v_mov_b32 v110, %10\n\t" "v_mov_b32 v111, %11\n\t"
        :: "v"(c.ka), "v"(c.kt), "v"(c.qa), "v"(c.qt), "v"(c.va), "v"(c.ta), "v"(c.l0), "v"(c.av), "v"(c.tv0), "v"(c.ot), "v"(c.sd), "v"(c.sstr));
}
DEV void xt_get(u32 (&x)[10]) {
    asm volatile("v_mov_b32 %0, v88\n\t" "v_mov_b32 %1, v89\n\t" "v_mov_b32 %2, v90\n\t" "v_mov_b32 %3, v91\n\t" "v_mov_b32 %4, v92\n\t" "v_mov_b32 %5, v93\n\t" "v_mov_b32 %6, v94\n\t" "v_mov_b32 %7, v95\n\t" "v_mov_b32 %8, v96\n\t" "v_mov_b32 %9, v97\n\t"
        : "=v"(x[0]), "=v"(x[1]), "=v"(x[2]), "=v"(x[3]), "=v"(x[4]), "=v"(x[5]), "=v"(x[6]), "=v"(x[7]), "=v"(x[8]), "=v"(x[9]));
}
DEV void xt_set(const u32 (&x)[10]) {
    asm volatile("v_mov_b32 v88, %0\n\t" "v_mov_b32 v89, %1\n\t" "v_mov_b32 v90, %2\n\t" "v_mov_b32 v91, %3\n\t" "v_mov_b32 v92, %4\n\t" "v_mov_b32 v93, %5\n\t" "v_mov_b32 v94, %6\n\t" "v_mov_b32 v95, %7\n\t" "v_mov_b32 v96, %8\n\t" "v_mov_b32 v97, %9\n\t"
        :: "v"(x[0]), "v"(x[1]), "v"(x[2]), "v"(x[3]), "v"(x[4]), "v"(x[5]), "v"(x[6]), "v"(x[7]), "v"(x[8]), "v"(x[9]));
}

DEV void gemm0(u32x4 srd, int xa, u32 iv, u32 si, int (&xt)[3], u32 fo, u32 fexp, u32 cs, u64 va, u64 v0, u32& cn) {
    int xb; u32 fl, sf;
    asm volatile(
        "s_nop 4\n\t"
        "s_setprio 2\n\t"
        "buffer_load_dwordx4 v[112:115], %[iv], %[ssrd], %[si] offen offset:0 sc1\n\t"
        "buffer_load_dwordx4 v[116:119], %[iv], %[ssrd], %[si] offen offset:1024 sc1\n\t"
        "buffer_load_dwordx4 v[120:123], %[iv], %[ssrd], %[si] offen offset:2048 sc1\n\t"
        "buffer_load_dwordx4 v[124:127], %[iv], %[ssrd], %[si] offen offset:3072 sc1\n\t"
        "s_add_u32 %[si], %[si], 4096\n\t"
        "buffer_load_dwordx4 v[128:131], %[iv], %[ssrd], %[si] offen offset:0 sc1\n\t"
        "buffer_load_dwordx4 v[132:135], %[iv], %[ssrd], %[si] offen offset:1024 sc1\n\t"
        "buffer_load_dwordx4 v[136:139], %[iv], %[ssrd], %[si] offen offset:2048 sc1\n\t"
        "buffer_load_dwordx4 v[140:143], %[iv], %[ssrd], %[si] offen offset:3072 sc1\n\t"
        "s_add_u32 %[si], %[si], 4096\n\t"
        "buffer_load_dwordx4 v[144:147], %[iv], %[ssrd], %[si] offen offset:0 sc1\n\t"
        "buffer_load_dwordx4 v[148:151], %[iv], %[ssrd], %[si] offen offset:1024 sc1\n\t"
        "ds_read_b128 v[88:91], %[x0]\n\t"
        "ds_read_b128 v[92:95], %[x1]\n\t"
        "ds_read_b64 v[96:97], %[x2]\n\t"
        "v_xor_b32 %[xb], 64, %[xa]\n\t"
        "ds_read_b128 v[24:27], %[xa] offset:0\n\t"
        "ds_read_b128 v[28:31], %[xa] offset:2048\n\t"
        "ds_read_b128 v[32:35], %[xa] offset:4096\n\t"
        "ds_read_b128 v[36:39], %[xa] offset:6144\n\t"
        "ds_read_b128 v[40:43], %[xa] offset:8192\n\t"
        "ds_read_b128 v[44:47], %[xb] offset:0\n\t"
        "s_waitcnt vmcnt(8) lgkmcnt(5)\n\t"
        "v_mfma_f32_16x16x32_bf16 v[48:51], v[112:115], v[24:27], 0\n\t"
        "v_mfma_f32_16x16x32_bf16 v[68:71], v[116:119], v[24:27], 0\n\t"
        "ds_read_b128 v[24:27], %[xb] offset:2048\n\t"
        "s_waitcnt lgkmcnt(5)\n\t"
        "v_mfma_f32_16x16x32_bf16 v[52:55], v[112:115], v[28:31], 0\n\t"
        "v_mfma_f32_16x16x32_bf16 v[72:75], v[116:119], v[28:31], 0\n\t"
        "buffer_load_dwordx4 v[152:155], %[iv], %[ssrd], %[si] offen offset:2048 sc1\n\t"
        "buffer_load_dwordx4 v[156:159], %[iv], %[ssrd], %[si] offen offset:3072 sc1\n\t"
        "ds_read_b128 v[28:31], %[xb] offset:4096\n\t"
        "s_waitcnt lgkmcnt(5)\n\t"
        "v_mfma_f32_16x16x32_bf16 v[56:59], v[112:115], v[32:35], 0\n\t"
        "v_mfma_f32_16x16x32_bf16 v[76:79], v[116:119], v[32:35], 0\n\t"
        "ds_read_b128 v[32:35], %[xb] offset:6144\n\t"
        "s_waitcnt lgkmcnt(5)\n\t"
        "v_mfma_f32_16x16x32_bf16 v[60:63], v[112:115], v[36:39], 0\n\t"
        "v_mfma_f32_16x16x32_bf16 v[80:83], v[116:119], v[36:39], 0\n\t"
        "ds_read_b128 v[36:39], %[xb] offset:8192\n\t"
        "s_waitcnt lgkmcnt(5)\n\t"
        "v_mfma_f32_16x16x32_bf16 v[64:67], v[112:115], v[40:43], 0\n\t"
        "v_mfma_f32_16x16x32_bf16 v[84:87], v[116:119], v[40:43], 0\n\t"
        "ds_read_b128 v[40:43], %[xa] offset:10240\n\t"
        "s_waitcnt vmcnt(8) lgkmcnt(5)\n\t"
        "v_mfma_f32_16x16x32_bf16 v[48:51], v[120:123], v[44:47], v[48:51]\n\t"
        "v_mfma_f32_16x16x32_bf16 v[68:71], v[124:127], v[44:47], v[68:71]\n\t"
        "ds_read_b128 v[44:47], %[xa] offset:12288\n\t"
        "s_waitcnt lgkmcnt(5)\n\t"
        "v_mfma_f32_16x16x32_bf16 v[52:55], v[120:123], v[24:27], v[52:55]\n\t"
        "v_mfma_f32_16x16x32_bf16 v[72:75], v[124:127], v[24:27], v[72:75]\n\t"
        "s_add_u32 %[si], %[si], 4096\n\t"
        "buffer_load_dwordx4 v[160:163], %[iv], %[ssrd], %[si] offen offset:0 sc1\n\t"
        "buffer_load_dwordx4 v[164:167], %[iv], %[ssrd], %[si] offen offset:1024 sc1\n\t"
        "ds_read_b128 v[24:27], %[xa] offset:14336\n\t"
        "s_waitcnt lgkmcnt(5)\n\t"
        "v_mfma_f32_16x16x32_bf16 v[56:59], v[120:123], v[28:31], v[56:59]\n\t"
        "v_mfma_f32_16x16x32_bf16 v[76:79], v[124:127], v[28:31], v[76:79]\n\t"
        "ds_read_b128 v[28:31], %[xa] offset:16384\n\t"
        "s_waitcnt lgkmcnt(5)\n\t"
        "v_mfma_f32_16x16x32_bf16 v[60:63], v[120:123], v[32:35], v[60:63]\n\t"
        "v_mfma_f32_16x16x32_bf16 v[80:83], v[124:127], v[32:35], v[80:83]\n\t"
        "ds_read_b128 v[32:35], %[xa] offset:18432\n\t"
        "s_waitcnt lgkmcnt(5)\n\t"
        "v_mfma_f32_16x16x32_bf16 v[64:67], v[120:123], v[36:39], v[64:67]\n\t"
        "v_mfma_f32_16x16x32_bf16 v[84:87], v[124:127], v[36:39], v[84:87]\n\t"
        "ds_read_b128 v[36:39], %[xb] offset:10240\n\t"
        "s_waitcnt vmcnt(8) lgkmcnt(5)\n\t"
        "v_mfma_f32_16x16x32_bf16 v[48:51], v[128:131], v[40:43], v[48:51]\n\t"
        "v_mfma_f32_16x16x32_bf16 v[68:71], v[132:135], v[40:43], v[68:71]\n\t"
        "ds_read_b128 v[40:43], %[xb] offset:12288\n\t"
        "s_waitcnt lgkmcnt(5)\n\t"
        "v_mfma_f32_16x16x32_bf16 v[52:55], v[128:131], v[44:47], v[52:55]\n\t"
        "v_mfma_f32_16x16x32_bf16 v[72:75], v[132:135], v[44:47], v[72:75]\n\t"
        "buffer_load_dwordx4 v[168:171], %[iv], %[ssrd], %[si] offen offset:2048 sc1\n\t"
        "buffer_load_dwordx4 v[172:175], %[iv], %[ssrd], %[si] offen offset:3072 sc1\n\t"
        "ds_read_b128 v[44:47], %[xb] offset:14336\n\t"
        "s_waitcnt lgkmcnt(5)\n\t"
        "v_mfma_f32_16x16x32_bf16 v[56:59], v[128:131], v[24:27], v[56:59]\n\t"
        "v_mfma_f32_16x16x32_bf16 v[76:79], v[132:135], v[24:27], v[76:79]\n\t"
        "ds_read_b128 v[24:27], %[xb] offset:16384\n\t"
        "s_waitcnt lgkmcnt(5)\n\t"
        "v_mfma_f32_16x16x32_bf16 v[60:63], v[128:131], v[28:31], v[60:63]\n\t"
        "v_mfma_f32_16x16x32_bf16 v[80:83], v[132:135], v[28:31], v[80:83]\n\t"
        "ds_read_b128 v[28:31], %[xb] offset:18432\n\t"
        "s_waitcnt lgkmcnt(5)\n\t"
        "v_mfma_f32_16x16x32_bf16 v[64:67], v[128:131], v[32:35], v[64:67]\n\t"
        "v_mfma_f32_16x16x32_bf16 v[84:87], v[132:135], v[32:35], v[84:87]\n\t"
        "ds_read_b128 v[32:35], %[xa] offset:20480\n\t"
        "s_waitcnt vmcnt(8) lgkmcnt(5)\n\t"
        "v_mfma_f32_16x16x32_bf16 v[48:51], v[136:139], v[36:39], v[48:51]\n\t"
        "v_mfma_f32_16x16x32_bf16 v[68:71], v[140:143], v[36:39], v[68:71]\n\t"
        "ds_read_b128 v[36:39], %[xa] offset:22528\n\t"
        "s_waitcnt lgkmcnt(5)\n\t"
        "v_mfma_f32_16x16x32_bf16 v[52:55], v[136:139], v[40:43], v[52:55]\n\t"
        "v_mfma_f32_16x16x32_bf16 v[72:75], v[140:143], v[40:43], v[72:75]\n\t"
        "s_add_u32 %[si], %[si], 4096\n\t"
        "buffer_load_dwordx4 v[176:179], %[iv], %[ssrd], %[si] offen offset:0 sc1\n\t"
        "buffer_load_dwordx4 v[180:183], %[iv], %[ssrd], %[si] offen offset:1024 sc1\n\t"
        "ds_read_b128 v[40:43], %[xa] offset:24576\n\t"
        "s_waitcnt lgkmcnt(5)\n\t"
        "v_mfma_f32_16x16x32_bf16 v[56:59], v[136:139], v[44:47], v[56:59]\n\t"
        "v_mfma_f32_16x16x32_bf16 v[76:79], v[140:143], v[44:47], v[76:79]\n\t"
        "ds_read_b128 v[44:47], %[xa] offset:26624\n\t"
        "s_waitcnt lgkmcnt(5)\n\t"
        "v_mfma_f32_16x16x32_bf16 v[60:63], v[136:139], v[24:27], v[60:63]\n\t"
        "v_mfma_f32_16x16x32_bf16 v[80:83], v[140:143], v[24:27], v[80:83]\n\t"
        "ds_read_b128 v[24:27], %[xa] offset:28672\n\t"
        "s_waitcnt lgkmcnt(5)\n\t"
        "v_mfma_f32_16x16x32_bf16 v[64:67], v[136:139], v[28:31], v[64:67]\n\t"
        "v_mfma_f32_16x16x32_bf16 v[84:87], v[140:143], v[28:31], v[84:87]\n\t"
        "ds_read_b128 v[28:31], %[xb] offset:20480\n\t"
        "s_waitcnt vmcnt(8) lgkmcnt(5)\n\t"
        "v_mfma_f32_16x16x32_bf16 v[48:51], v[144:147], v[32:35], v[48:51]\n\t"
        "v_mfma_f32_16x16x32_bf16 v[68:71], v[148:151], v[32:35], v[68:71]\n\t"
        "ds_read_b128 v[32:35], %[xb] offset:22528\n\t"
        "s_waitcnt lgkmcnt(5)\n\t"
        "v_mfma_f32_16x16x32_bf16 v[52:55], v[144:147], v[36:39], v[52:55]\n\t"
        "v_mfma_f32_16x16x32_bf16 v[72:75], v[148:151], v[36:39], v[72:75]\n\t"
        "buffer_load_dwordx4 v[184:187], %[iv], %[ssrd], %[si] offen offset:2048 sc1\n\t"
        "buffer_load_dwordx4 v[188:191], %[iv], %[ssrd], %[si] offen offset:3072 sc1\n\t"
        "ds_read_b128 v[36:39], %[xb] offset:24576\n\t"
        "s_waitcnt lgkmcnt(5)\n\t"
        "v_mfma_f32_16x16x32_bf16 v[56:59], v[144:147], v[40:43], v[56:59]\n\t"
        "v_mfma_f32_16x16x32_bf16 v[76:79], v[148:151], v[40:43], v[76:79]\n\t"
        "ds_read_b128 v[40:43], %[xb] offset:26624\n\t"
        "s_waitcnt lgkmcnt(5)\n\t"
        "v_mfma_f32_16x16x32_bf16 v[60:63], v[144:147], v[44:47], v[60:63]\n\t"
        "v_mfma_f32_16x16x32_bf16 v[80:83], v[148:151], v[44:47], v[80:83]\n\t"
        "ds_read_b128 v[44:47], %[xb] offset:28672\n\t"
        "s_waitcnt lgkmcnt(5)\n\t"
        "v_mfma_f32_16x16x32_bf16 v[64:67], v[144:147], v[24:27], v[64:67]\n\t"
        "v_mfma_f32_16x16x32_bf16 v[84:87], v[148:151], v[24:27], v[84:87]\n\t"
        "ds_read_b128 v[24:27], %[xa] offset:30720\n\t"
        "s_waitcnt vmcnt(8) lgkmcnt(5)\n\t"
        "v_mfma_f32_16x16x32_bf16 v[48:51], v[152:155], v[28:31], v[48:51]\n\t"
        "v_mfma_f32_16x16x32_bf16 v[68:71], v[156:159], v[28:31], v[68:71]\n\t"
        "ds_read_b128 v[28:31], %[xa] offset:32768\n\t"
        "s_waitcnt lgkmcnt(5)\n\t"
        "v_mfma_f32_16x16x32_bf16 v[52:55], v[152:155], v[32:35], v[52:55]\n\t"
        "v_mfma_f32_16x16x32_bf16 v[72:75], v[156:159], v[32:35], v[72:75]\n\t"
        "s_add_u32 %[si], %[si], 4096\n\t"
        "buffer_load_dwordx4 v[192:195], %[iv], %[ssrd], %[si] offen offset:0 sc1\n\t"
        "buffer_load_dwordx4 v[196:199], %[iv], %[ssrd], %[si] offen offset:1024 sc1\n\t"
        "ds_read_b128 v[32:35], %[xa] offset:34816\n\t"
        "s_waitcnt lgkmcnt(5)\n\t"
        "v_mfma_f32_16x16x32_bf16 v[56:59], v[152:155], v[36:39], v[56:59]\n\t"
        "v_mfma_f32_16x16x32_bf16 v[76:79], v[156:159], v[36:39], v[76:79]\n\t"
        "ds_read_b128 v[36:39], %[xa] offset:36864\n\t"
        "s_waitcnt lgkmcnt(5)\n\t"
        "v_mfma_f32_16x16x32_bf16 v[60:63], v[152:155], v[40:43], v[60:63]\n\t"
        "v_mfma_f32_16x16x32_bf16 v[80:83], v[156:159], v[40:43], v[80:83]\n\t"
        "ds_read_b128 v[40:43], %[xa] offset:38912\n\t"
        "s_waitcnt lgkmcnt(5)\n\t"
        "v_mfma_f32_16x16x32_bf16 v[64:67], v[152:155], v[44:47], v[64:67]\n\t"
        "v_mfma_f32_16x16x32_bf16 v[84:87], v[156:159], v[44:47], v[84:87]\n\t"
        "ds_read_b128 v[44:47], %[xb] offset:30720\n\t"
        "s_setprio 1\n\t"
        "s_waitcnt vmcnt(8) lgkmcnt(5)\n\t"
        "v_mfma_f32_16x16x32_bf16 v[48:51], v[160:163], v[24:27], v[48:51]\n\t"
        "v_mfma_f32_16x16x32_bf16 v[68:71], v[164:167], v[24:27], v[68:71]\n\t"
        "ds_read_b128 v[24:27], %[xb] offset:32768\n\t"
        "s_waitcnt lgkmcnt(5)\n\t"
        "v_mfma_f32_16x16x32_bf16 v[52:55], v[160:163], v[28:31], v[52:55]\n\t"
        "v_mfma_f32_16x16x32_bf16 v[72:75], v[164:167], v[28:31], v[72:75]\n\t"
        "buffer_load_dwordx4 v[200:203], %[iv], %[ssrd], %[si] offen offset:2048 sc1\n\t"
        "buffer_load_dwordx4 v[204:207], %[iv], %[ssrd], %[si] offen offset:3072 sc1\n\t"
        "ds_read_b128 v[28:31], %[xb] offset:34816\n\t"
        "s_waitcnt lgkmcnt(5)\n\t"
        "v_mfma_f32_16x16x32_bf16 v[56:59], v[160:163], v[32:35], v[56:59]\n\t"
        "v_mfma_f32_16x16x32_bf16 v[76:79], v[164:167], v[32:35], v[76:79]\n\t"
        "ds_read_b128 v[32:35], %[xb] offset:36864\n\t"
        "s_waitcnt lgkmcnt(5)\n\t"
        "v_mfma_f32_16x16x32_bf16 v[60:63], v[160:163], v[36:39], v[60:63]\n\t"
        "v_mfma_f32_16x16x32_bf16 v[80:83], v[164:167], v[36:39], v[80:83]\n\t"
        "ds_read_b128 v[36:39], %[xb] offset:38912\n\t"
        "s_waitcnt lgkmcnt(5)\n\t"
        "v_mfma_f32_16x16x32_bf16 v[64:67], v[160:163], v[40:43], v[64:67]\n\t"
        "v_mfma_f32_16x16x32_bf16 v[84:87], v[164:167], v[40:43], v[84:87]\n\t"
        "ds_read_b128 v[40:43], %[xa] offset:40960\n\t"
        "v_mov_b32 %[x2], 153984\n\t"
        "v_mov_b32 %[x0], 1\n\t"
        "s_mov_b64 exec, 1\n\t"
        "ds_add_u32 %[x2], %[x0] offset:%[omc]+4\n\t"
        "s_mov_b64 exec, -1\n\t"
        "s_waitcnt vmcnt(8) lgkmcnt(6)\n\t"
        "v_mfma_f32_16x16x32_bf16 v[48:51], v[168:171], v[44:47], v[48:51]\n\t"
        "v_mfma_f32_16x16x32_bf16 v[68:71], v[172:175], v[44:47], v[68:71]\n\t"
        "ds_read_b128 v[44:47], %[xa] offset:43008\n\t"
        "s_waitcnt lgkmcnt(6)\n\t"
        "v_mfma_f32_16x16x32_bf16 v[52:55], v[168:171], v[24:27], v[52:55]\n\t"
        "v_mfma_f32_16x16x32_bf16 v[72:75], v[172:175], v[24:27], v[72:75]\n\t"
        "s_add_u32 %[si], %[si], 4096\n\t"
        "buffer_load_dwordx4 v[208:211], %[iv], %[ssrd], %[si] offen offset:0 sc1\n\t"
        "buffer_load_dwordx4 v[212:215], %[iv], %[ssrd], %[si] offen offset:1024 sc1\n\t"
        "ds_read_b128 v[24:27], %[xa] offset:45056\n\t"
        "s_waitcnt lgkmcnt(6)\n\t"
        "v_mfma_f32_16x16x32_bf16 v[56:59], v[168:171], v[28:31], v[56:59]\n\t"
        "v_mfma_f32_16x16x32_bf16 v[76:79], v[172:175], v[28:31], v[76:79]\n\t"
        "ds_read_b128 v[28:31], %[xa] offset:47104\n\t"
        "s_waitcnt lgkmcnt(6)\n\t"
        "v_mfma_f32_16x16x32_bf16 v[60:63], v[168:171], v[32:35], v[60:63]\n\t"
        "v_mfma_f32_16x16x32_bf16 v[80:83], v[172:175], v[32:35], v[80:83]\n\t"
        "ds_read_b128 v[32:35], %[xa] offset:49152\n\t"
        "s_waitcnt lgkmcnt(6)\n\t"
        "v_mfma_f32_16x16x32_bf16 v[64:67], v[168:171], v[36:39], v[64:67]\n\t"
        "v_mfma_f32_16x16x32_bf16 v[84:87], v[172:175], v[36:39], v[84:87]\n\t"
        "ds_read_b128 v[36:39], %[xb] offset:40960\n\t"
        "s_waitcnt vmcnt(8) lgkmcnt(6)\n\t"
        "v_mfma_f32_16x16x32_bf16 v[48:51], v[176:179], v[40:43], v[48:51]\n\t"
        "v_mfma_f32_16x16x32_bf16 v[68:71], v[180:183], v[40:43], v[68:71]\n\t"
        "ds_read_b128 v[40:43], %[xb] offset:43008\n\t"
        "s_waitcnt lgkmcnt(5)\n\t"
        "v_mfma_f32_16x16x32_bf16 v[52:55], v[176:179], v[44:47], v[52:55]\n\t"
        "v_mfma_f32_16x16x32_bf16 v[72:75], v[180:183], v[44:47], v[72:75]\n\t"
        "buffer_load_dwordx4 v[216:219], %[iv], %[ssrd], %[si] offen offset:2048 sc1\n\t"
        "buffer_load_dwordx4 v[220:223], %[iv], %[ssrd], %[si] offen offset:3072 sc1\n\t"
        "ds_read_b128 v[44:47], %[xb] offset:45056\n\t"
        "s_waitcnt lgkmcnt(5)\n\t"
        "v_mfma_f32_16x16x32_bf16 v[56:59], v[176:179], v[24:27], v[56:59]\n\t"
        "v_mfma_f32_16x16x32_bf16 v[76:79], v[180:183], v[24:27], v[76:79]\n\t"
        "ds_read_b128 v[24:27], %[xb] offset:47104\n\t"
        "s_waitcnt lgkmcnt(5)\n\t"
        "v_mfma_f32_16x16x32_bf16 v[60:63], v[176:179], v[28:31], v[60:63]\n\t"
        "v_mfma_f32_16x16x32_bf16 v[80:83], v[180:183], v[28:31], v[80:83]\n\t"
        "ds_read_b128 v[28:31], %[xb] offset:49152\n\t"
        "s_waitcnt lgkmcnt(5)\n\t"
        "v_mfma_f32_16x16x32_bf16 v[64:67], v[176:179], v[32:35], v[64:67]\n\t"
        "v_mfma_f32_16x16x32_bf16 v[84:87], v[180:183], v[32:35], v[84:87]\n\t"
        "v_add_u32 %[xa], 51200, %[xa]\n\t"
        "ds_read_b128 v[32:35], %[xa] offset:0\n\t"
        "s_waitcnt vmcnt(8) lgkmcnt(5)\n\t"
        "v_mfma_f32_16x16x32_bf16 v[48:51], v[184:187], v[36:39], v[48:51]\n\t"
        "v_mfma_f32_16x16x32_bf16 v[68:71], v[188:191], v[36:39], v[68:71]\n\t"
        "ds_read_b128 v[36:39], %[xa] offset:2048\n\t"
        "s_waitcnt lgkmcnt(5)\n\t"
        "v_mfma_f32_16x16x32_bf16 v[52:55], v[184:187], v[40:43], v[52:55]\n\t"
        "v_mfma_f32_16x16x32_bf16 v[72:75], v[188:191], v[40:43], v[72:75]\n\t"
        "s_add_u32 %[si], %[si], 4096\n\t"
        "buffer_load_dwordx4 v[224:227], %[iv], %[ssrd], %[si] offen offset:0 sc1\n\t"
        "buffer_load_dwordx4 v[228:231], %[iv], %[ssrd], %[si] offen offset:1024 sc1\n\t"
        "ds_read_b128 v[40:43], %[xa] offset:4096\n\t"
        "s_waitcnt lgkmcnt(5)\n\t"
        "v_mfma_f32_16x16x32_bf16 v[56:59], v[184:187], v[44:47], v[56:59]\n\t"
        "v_mfma_f32_16x16x32_bf16 v[76:79], v[188:191], v[44:47], v[76:79]\n\t"
        "ds_read_b128 v[44:47], %[xa] offset:6144\n\t"
        "s_waitcnt lgkmcnt(5)\n\t"
        "v_mfma_f32_16x16x32_bf16 v[60:63], v[184:187], v[24:27], v[60:63]\n\t"
        "v_mfma_f32_16x16x32_bf16 v[80:83], v[188:191], v[24:27], v[80:83]\n\t"
        "ds_read_b128 v[24:27], %[xa] offset:8192\n\t"
        "s_waitcnt lgkmcnt(5)\n\t"
        "v_mfma_f32_16x16x32_bf16 v[64:67], v[184:187], v[28:31], v[64:67]\n\t"
        "v_mfma_f32_16x16x32_bf16 v[84:87], v[188:191], v[28:31], v[84:87]\n\t"
        "v_add_u32 %[xb], 51200, %[xb]\n\t"
        "ds_read_b128 v[28:31], %[xb] offset:0\n\t"
        "s_waitcnt vmcnt(8) lgkmcnt(5)\n\t"
        "v_mfma_f32_16x16x32_bf16 v[48:51], v[192:195], v[32:35], v[48:51]\n\t"
        "v_mfma_f32_16x16x32_bf16 v[68:71], v[196:199], v[32:35], v[68:71]\n\t"
        "ds_read_b128 v[32:35], %[xb] offset:2048\n\t"
        "s_waitcnt lgkmcnt(5)\n\t"
        "v_mfma_f32_16x16x32_bf16 v[52:55], v[192:195], v[36:39], v[52:55]\n\t"
        "v_mfma_f32_16x16x32_bf16 v[72:75], v[196:199], v[36:39], v[72:75]\n\t"
        "buffer_load_dwordx4 v[232:235], %[iv], %[ssrd], %[si] offen offset:2048 sc1\n\t"
        "buffer_load_dwordx4 v[236:239], %[iv], %[ssrd], %[si] offen offset:3072 sc1\n\t"
        "ds_read_b128 v[36:39], %[xb] offset:4096\n\t"
        "s_waitcnt lgkmcnt(5)\n\t"
        "v_mfma_f32_16x16x32_bf16 v[56:59], v[192:195], v[40:43], v[56:59]\n\t"
        "v_mfma_f32_16x16x32_bf16 v[76:79], v[196:199], v[40:43], v[76:79]\n\t"
        "ds_read_b128 v[40:43], %[xb] offset:6144\n\t"
        "s_waitcnt lgkmcnt(5)\n\t"
        "v_mfma_f32_16x16x32_bf16 v[60:63], v[192:195], v[44:47], v[60:63]\n\t"
        "v_mfma_f32_16x16x32_bf16 v[80:83], v[196:199], v[44:47], v[80:83]\n\t"
        "ds_read_b128 v[44:47], %[xb] offset:8192\n\t"
        "s_waitcnt lgkmcnt(5)\n\t"
        "v_mfma_f32_16x16x32_bf16 v[64:67], v[192:195], v[24:27], v[64:67]\n\t"
        "v_mfma_f32_16x16x32_bf16 v[84:87], v[196:199], v[24:27], v[84:87]\n\t"
        "ds_read_b128 v[24:27], %[xa] offset:10240\n\t"
        "s_waitcnt vmcnt(8) lgkmcnt(5)\n\t"
        "v_mfma_f32_16x16x32_bf16 v[48:51], v[200:203], v[28:31], v[48:51]\n\t"
        "v_mfma_f32_16x16x32_bf16 v[68:71], v[204:207], v[28:31], v[68:71]\n\t"
        "ds_read_b128 v[28:31], %[xa] offset:12288\n\t"
        "s_waitcnt lgkmcnt(5)\n\t"
        "v_mfma_f32_16x16x32_bf16 v[52:55], v[200:203], v[32:35], v[52:55]\n\t"
        "v_mfma_f32_16x16x32_bf16 v[72:75], v[204:207], v[32:35], v[72:75]\n\t"
        "s_add_u32 %[si], %[si], 4096\n\t"
        "buffer_load_dwordx4 v[240:243], %[iv], %[ssrd], %[si] offen offset:0 sc1\n\t"
        "buffer_load_dwordx4 v[244:247], %[iv], %[ssrd], %[si] offen offset:1024 sc1\n\t"
        "ds_read_b128 v[32:35], %[xa] offset:14336\n\t"
        "s_waitcnt lgkmcnt(5)\n\t"
        "v_mfma_f32_16x16x32_bf16 v[56:59], v[200:203], v[36:39], v[56:59]\n\t"
        "v_mfma_f32_16x16x32_bf16 v[76:79], v[204:207], v[36:39], v[76:79]\n\t"
        "ds_read_b128 v[36:39], %[xa] offset:16384\n\t"
        "s_waitcnt lgkmcnt(5)\n\t"
        "v_mfma_f32_16x16x32_bf16 v[60:63], v[200:203], v[40:43], v[60:63]\n\t"
        "v_mfma_f32_16x16x32_bf16 v[80:83], v[204:207], v[40:43], v[80:83]\n\t"
        "ds_read_b128 v[40:43], %[xa] offset:18432\n\t"
        "s_waitcnt lgkmcnt(5)\n\t"
        "v_mfma_f32_16x16x32_bf16 v[64:67], v[200:203], v[44:47], v[64:67]\n\t"
        "v_mfma_f32_16x16x32_bf16 v[84:87], v[204:207], v[44:47], v[84:87]\n\t"
        "ds_read_b128 v[44:47], %[xb] offset:10240\n\t"
        "s_setprio 0\n\t"
        "s_waitcnt vmcnt(8) lgkmcnt(5)\n\t"
        "v_mfma_f32_16x16x32_bf16 v[48:51], v[208:211], v[24:27], v[48:51]\n\t"
        "v_mfma_f32_16x16x32_bf16 v[68:71], v[212:215], v[24:27], v[68:71]\n\t"
        "ds_read_b128 v[24:27], %[xb] offset:12288\n\t"
        "s_waitcnt lgkmcnt(5)\n\t"
        "v_mfma_f32_16x16x32_bf16 v[52:55], v[208:211], v[28:31], v[52:55]\n\t"
        "v_mfma_f32_16x16x32_bf16 v[72:75], v[212:215], v[28:31], v[72:75]\n\t"
        "buffer_load_dwordx4 v[248:251], %[iv], %[ssrd], %[si] offen offset:2048 sc1\n\t"
        "buffer_load_dwordx4 v[252:255], %[iv], %[ssrd], %[si] offen offset:3072 sc1\n\t"
        "ds_read_b128 v[28:31], %[xb] offset:14336\n\t"
        "s_waitcnt lgkmcnt(5)\n\t"
        "v_mfma_f32_16x16x32_bf16 v[56:59], v[208:211], v[32:35], v[56:59]\n\t"
        "v_mfma_f32_16x16x32_bf16 v[76:79], v[212:215], v[32:35], v[76:79]\n\t"
        "ds_read_b128 v[32:35], %[xb] offset:16384\n\t"
        "s_waitcnt lgkmcnt(5)\n\t"
        "v_mfma_f32_16x16x32_bf16 v[60:63], v[208:211], v[36:39], v[60:63]\n\t"
        "v_mfma_f32_16x16x32_bf16 v[80:83], v[212:215], v[36:39], v[80:83]\n\t"
        "ds_read_b128 v[36:39], %[xb] offset:18432\n\t"
        "s_waitcnt lgkmcnt(5)\n\t"
        "v_mfma_f32_16x16x32_bf16 v[64:67], v[208:211], v[40:43], v[64:67]\n\t"
        "v_mfma_f32_16x16x32_bf16 v[84:87], v[212:215], v[40:43], v[84:87]\n\t"
        "ds_read_b128 v[40:43], %[xa] offset:20480\n\t"
        "s_waitcnt vmcnt(8) lgkmcnt(5)\n\t"
        "v_mfma_f32_16x16x32_bf16 v[48:51], v[216:219], v[44:47], v[48:51]\n\t"
        "v_mfma_f32_16x16x32_bf16 v[68:71], v[220:223], v[44:47], v[68:71]\n\t"
        "ds_read_b128 v[44:47], %[xa] offset:22528\n\t"
        "s_waitcnt lgkmcnt(5)\n\t"
        "v_mfma_f32_16x16x32_bf16 v[52:55], v[216:219], v[24:27], v[52:55]\n\t"
        "v_mfma_f32_16x16x32_bf16 v[72:75], v[220:223], v[24:27], v[72:75]\n\t"
        "buffer_load_dword %[fl], %[fo], %[ssrd], 0 offen sc1\n\t"
        "ds_read_b128 v[24:27], %[xa] offset:24576\n\t"
        "s_waitcnt lgkmcnt(5)\n\t"
        "v_mfma_f32_16x16x32_bf16 v[56:59], v[216:219], v[28:31], v[56:59]\n\t"
        "v_mfma_f32_16x16x32_bf16 v[76:79], v[220:223], v[28:31], v[76:79]\n\t"
        "ds_read_b128 v[28:31], %[xa] offset:26624\n\t"
        "s_waitcnt lgkmcnt(5)\n\t"
        "v_mfma_f32_16x16x32_bf16 v[60:63], v[216:219], v[32:35], v[60:63]\n\t"
        "v_mfma_f32_16x16x32_bf16 v[80:83], v[220:223], v[32:35], v[80:83]\n\t"
        "ds_read_b128 v[32:35], %[xa] offset:28672\n\t"
        "s_waitcnt lgkmcnt(5)\n\t"
        "v_mfma_f32_16x16x32_bf16 v[64:67], v[216:219], v[36:39], v[64:67]\n\t"
        "v_mfma_f32_16x16x32_bf16 v[84:87], v[220:223], v[36:39], v[84:87]\n\t"
        "ds_read_b128 v[36:39], %[xb] offset:20480\n\t"
        "s_waitcnt vmcnt(7) lgkmcnt(5)\n\t"
        "v_mfma_f32_16x16x32_bf16 v[48:51], v[224:227], v[40:43], v[48:51]\n\t"
        "v_mfma_f32_16x16x32_bf16 v[68:71], v[228:231], v[40:43], v[68:71]\n\t"
        "ds_read_b128 v[40:43], %[xb] offset:22528\n\t"
        "s_waitcnt lgkmcnt(5)\n\t"
        "v_mfma_f32_16x16x32_bf16 v[52:55], v[224:227], v[44:47], v[52:55]\n\t"
        "v_mfma_f32_16x16x32_bf16 v[72:75], v[228:231], v[44:47], v[72:75]\n\t"
        "ds_read_b128 v[44:47], %[xb] offset:24576\n\t"
        "s_waitcnt lgkmcnt(5)\n\t"
        "v_mfma_f32_16x16x32_bf16 v[56:59], v[224:227], v[24:27], v[56:59]\n\t"
        "v_mfma_f32_16x16x32_bf16 v[76:79], v[228:231], v[24:27], v[76:79]\n\t"
        "ds_read_b128 v[24:27], %[xb] offset:26624\n\t"
        "s_waitcnt lgkmcnt(5)\n\t"
        "v_mfma_f32_16x16x32_bf16 v[60:63], v[224:227], v[28:31], v[60:63]\n\t"
        "v_mfma_f32_16x16x32_bf16 v[80:83], v[228:231], v[28:31], v[80:83]\n\t"
        "ds_read_b128 v[28:31], %[xb] offset:28672\n\t"
        "s_waitcnt lgkmcnt(5)\n\t"
        "v_mfma_f32_16x16x32_bf16 v[64:67], v[224:227], v[32:35], v[64:67]\n\t"
        "v_mfma_f32_16x16x32_bf16 v[84:87], v[228:231], v[32:35], v[84:87]\n\t"
        "ds_read_b128 v[32:35], %[xa] offset:30720\n\t"
        "s_waitcnt vmcnt(5) lgkmcnt(5)\n\t"
        "v_mfma_f32_16x16x32_bf16 v[48:51], v[232:235], v[36:39], v[48:51]\n\t"
        "v_mfma_f32_16x16x32_bf16 v[68:71], v[236:239], v[36:39], v[68:71]\n\t"
        "ds_read_b128 v[36:39], %[xa] offset:32768\n\t"
        "s_waitcnt lgkmcnt(5)\n\t"
        "v_mfma_f32_16x16x32_bf16 v[52:55], v[232:235], v[40:43], v[52:55]\n\t"
        "v_mfma_f32_16x16x32_bf16 v[72:75], v[236:239], v[40:43], v[72:75]\n\t"
        "ds_read_b128 v[40:43], %[xa] offset:34816\n\t"
        "s_waitcnt lgkmcnt(5)\n\t"
        "v_mfma_f32_16x16x32_bf16 v[56:59], v[232:235], v[44:47], v[56:59]\n\t"
        "v_mfma_f32_16x16x32_bf16 v[76:79], v[236:239], v[44:47], v[76:79]\n\t"
        "ds_read_b128 v[44:47], %[xa] offset:36864\n\t"
        "s_waitcnt lgkmcnt(5)\n\t"
        "v_mfma_f32_16x16x32_bf16 v[60:63], v[232:235], v[24:27], v[60:63]\n\t"
        "v_mfma_f32_16x16x32_bf16 v[80:83], v[236:239], v[24:27], v[80:83]\n\t"
        "ds_read_b128 v[24:27], %[xa] offset:38912\n\t"
        "s_waitcnt lgkmcnt(5)\n\t"
        "v_mfma_f32_16x16x32_bf16 v[64:67], v[232:235], v[28:31], v[64:67]\n\t"
        "v_mfma_f32_16x16x32_bf16 v[84:87], v[236:239], v[28:31], v[84:87]\n\t"
        "ds_read_b128 v[28:31], %[xb] offset:30720\n\t"
        "s_waitcnt vmcnt(3) lgkmcnt(5)\n\t"
        "v_mfma_f32_16x16x32_bf16 v[48:51], v[240:243], v[32:35], v[48:51]\n\t"
        "v_mfma_f32_16x16x32_bf16 v[68:71], v[244:247], v[32:35], v[68:71]\n\t"
        "ds_read_b128 v[32:35], %[xb] offset:32768\n\t"
        "s_waitcnt lgkmcnt(5)\n\t"
        "v_mfma_f32_16x16x32_bf16 v[52:55], v[240:243], v[36:39], v[52:55]\n\t"
        "v_mfma_f32_16x16x32_bf16 v[72:75], v[244:247], v[36:39], v[72:75]\n\t"
        "ds_read_b128 v[36:39], %[xb] offset:34816\n\t"
        "v_and_b32 %[x0], 0xf0, v107\n\t"
        "v_lshrrev_b32 %[x0], 1, %[x0]\n\t"
        "v_add_u32 %[x0], 153344, %[x0]\n\t"
        "v_lshrrev_b32 %[x1], 8, v107\n\t"
        "v_lshl_add_u32 %[x1], %[x1], 5, %[scs]\n\t"
        "s_waitcnt lgkmcnt(5)\n\t"
        "v_mfma_f32_16x16x32_bf16 v[56:59], v[240:243], v[40:43], v[56:59]\n\t"
        "v_mfma_f32_16x16x32_bf16 v[76:79], v[244:247], v[40:43], v[76:79]\n\t"
        "ds_read_b128 v[40:43], %[xb] offset:36864\n\t"
        "s_waitcnt lgkmcnt(5)\n\t"
        "v_mfma_f32_16x16x32_bf16 v[60:63], v[240:243], v[44:47], v[60:63]\n\t"
        "v_mfma_f32_16x16x32_bf16 v[80:83], v[244:247], v[44:47], v[80:83]\n\t"
        "ds_read_b128 v[44:47], %[xb] offset:38912\n\t"
        "s_waitcnt lgkmcnt(5)\n\t"
        "v_mfma_f32_16x16x32_bf16 v[64:67], v[240:243], v[24:27], v[64:67]\n\t"
        "v_mfma_f32_16x16x32_bf16 v[84:87], v[244:247], v[24:27], v[84:87]\n\t"
        "s_waitcnt vmcnt(1) lgkmcnt(4)\n\t"
        "v_mfma_f32_16x16x32_bf16 v[48:51], v[248:251], v[28:31], v[48:51]\n\t"
        "v_mfma_f32_16x16x32_bf16 v[68:71], v[252:255], v[28:31], v[68:71]\n\t"
        "s_waitcnt lgkmcnt(3)\n\t"
        "v_mfma_f32_16x16x32_bf16 v[52:55], v[248:251], v[32:35], v[52:55]\n\t"
        "v_mfma_f32_16x16x32_bf16 v[72:75], v[252:255], v[32:35], v[72:75]\n\t"
        "ds_read_b64 v[34:35], %[x2] offset:%[omc]\n\t"
        "ds_read_b64 v[24:25], %[x0] offset:0\n\t"
        "ds_read_b64 v[26:27], %[x0] offset:128\n\t"
        "ds_read_b64 v[28:29], %[x0] offset:256\n\t"
        "ds_read_b64 v[30:31], %[x0] offset:384\n\t"
        "ds_read_b64 v[32:33], %[x0] offset:512\n\t"
        "s_waitcnt lgkmcnt(8)\n\t"
        "v_mfma_f32_16x16x32_bf16 v[56:59], v[248:251], v[36:39], v[56:59]\n\t"
        "v_mfma_f32_16x16x32_bf16 v[76:79], v[252:255], v[36:39], v[76:79]\n\t"
        "s_waitcnt lgkmcnt(7)\n\t"
        "v_mfma_f32_16x16x32_bf16 v[60:63], v[248:251], v[40:43], v[60:63]\n\t"
        "v_mfma_f32_16x16x32_bf16 v[80:83], v[252:255], v[40:43], v[80:83]\n\t"
        "s_waitcnt lgkmcnt(6)\n\t"
        "v_mfma_f32_16x16x32_bf16 v[64:67], v[248:251], v[44:47], v[64:67]\n\t"
        "v_mfma_f32_16x16x32_bf16 v[84:87], v[252:255], v[44:47], v[84:87]\n\t"
        "v_mov_b32_dpp v36, v48 row_newbcast:0 row_mask:0xf bank_mask:0xf bound_ctrl:1\n\t"
        "v_mov_b32_dpp v37, v49 row_newbcast:0 row_mask:0xf bank_mask:0xf bound_ctrl:1\n\t"
        "v_mov_b32_dpp v38, v50 row_newbcast:0 row_mask:0xf bank_mask:0xf bound_ctrl:1\n\t"
        "v_mov_b32_dpp v39, v51 row_newbcast:0 row_mask:0xf bank_mask:0xf bound_ctrl:1\n\t"
        "v_mov_b32_dpp v40, v68 row_newbcast:0 row_mask:0xf bank_mask:0xf bound_ctrl:1\n\t"
        "v_mov_b32_dpp v41, v69 row_newbcast:0 row_mask:0xf bank_mask:0xf bound_ctrl:1\n\t"
        "v_mov_b32_dpp v42, v70 row_newbcast:0 row_mask:0xf bank_mask:0xf bound_ctrl:1\n\t"
        "v_mov_b32_dpp v43, v71 row_newbcast:0 row_mask:0xf bank_mask:0xf bound_ctrl:1\n\t"
        "Lm_%=:\n\t"
        "s_waitcnt vmcnt(0) lgkmcnt(0)\n\t"
        "v_readfirstlane_b32 %[sf], v34\n\t"
        "v_cmp_ne_u32 vcc, %[sexp], %[fl]\n\t"
        "s_cmp_lt_u32 %[sf], 3\n\t"
        "s_cbranch_scc1 Ln_%=\n\t"
        "s_cbranch_vccnz Ln_%=\n\t"
        "Lo_%=:\n\t"
        "s_mov_b64 exec, %[sm16]\n\t"
        "ds_write_b128 %[x1], v[36:39]\n\t"
        "ds_write_b128 %[x1], v[40:43] offset:16\n\t"
        "s_mov_b64 exec, -1\n\t"
        "v_fma_f32 v48, -v24, v36, v48\n\t"
        "v_fma_f32 v49, -v24, v37, v49\n\t"
        "v_fma_f32 v50, -v24, v38, v50\n\t"
        "v_fma_f32 v51, -v24, v39, v51\n\t"
        "v_fma_f32 v68, -v24, v40, v68\n\t"
        "v_fma_f32 v69, -v24, v41, v69\n\t"
        "v_fma_f32 v70, -v24, v42, v70\n\t"
        "v_fma_f32 v71, -v24, v43, v71\n\t"
        "v_mul_f32 v48, v25, v48\n\t"
        "v_mul_f32 v49, v25, v49\n\t"
        "v_mul_f32 v50, v25, v50\n\t"
        "v_mul_f32 v51, v25, v51\n\t"
        "v_mul_f32 v68, v25, v68\n\t"
        "v_mul_f32 v69, v25, v69\n\t"
        "v_mul_f32 v70, v25, v70\n\t"
        "v_mul_f32 v71, v25, v71\n\t"
        "v_cvt_pk_bf16_f32 v48, v48, v68\n\t"
        "v_cvt_pk_bf16_f32 v49, v49, v69\n\t"
        "v_cvt_pk_bf16_f32 v50, v50, v70\n\t"
        "v_cvt_pk_bf16_f32 v51, v51, v71\n\t"
        "v_add_u32 v68, v110, v111\n\t"
        "v_add_u32 v69, v68, v111\n\t"
        "v_add_u32 v70, v69, v111\n\t"
        "v_add_u32 v71, v70, v111\n\t"
        "v_fma_f32 v52, -v26, v36, v52\n\t"
        "v_fma_f32 v53, -v26, v37, v53\n\t"
        "v_fma_f32 v54, -v26, v38, v54\n\t"
        "v_fma_f32 v55, -v26, v39, v55\n\t"
        "v_fma_f32 v72, -v26, v40, v72\n\t"
        "v_fma_f32 v73, -v26, v41, v73\n\t"
        "v_fma_f32 v74, -v26, v42, v74\n\t"
        "v_fma_f32 v75, -v26, v43, v75\n\t"
        "v_mul_f32 v52, v27, v52\n\t"
        "v_mul_f32 v53, v27, v53\n\t"
        "v_mul_f32 v54, v27, v54\n\t"
        "v_mul_f32 v55, v27, v55\n\t"
        "v_mul_f32 v72, v27, v72\n\t"
        "v_mul_f32 v73, v27, v73\n\t"
        "v_mul_f32 v74, v27, v74\n\t"
        "v_mul_f32 v75, v27, v75\n\t"
        "v_cvt_pk_bf16_f32 v52, v52, v72\n\t"
        "v_cvt_pk_bf16_f32 v53, v53, v73\n\t"
        "v_cvt_pk_bf16_f32 v54, v54, v74\n\t"
        "v_cvt_pk_bf16_f32 v55, v55, v75\n\t"
        "v_fma_f32 v56, -v28, v36, v56\n\t"
        "v_fma_f32 v57, -v28, v37, v57\n\t"
        "v_fma_f32 v58, -v28, v38, v58\n\t"
        "v_fma_f32 v59, -v28, v39, v59\n\t"
        "v_fma_f32 v76, -v28, v40, v76\n\t"
        "v_fma_f32 v77, -v28, v41, v77\n\t"
        "v_fma_f32 v78, -v28, v42, v78\n\t"
        "v_fma_f32 v79, -v28, v43, v79\n\t"
        "v_mul_f32 v56, v29, v56\n\t"
        "v_mul_f32 v57, v29, v57\n\t"
        "v_mul_f32 v58, v29, v58\n\t"
        "v_mul_f32 v59, v29, v59\n\t"
        "v_mul_f32 v76, v29, v76\n\t"
        "v_mul_f32 v77, v29, v77\n\t"
        "v_mul_f32 v78, v29, v78\n\t"
        "v_mul_f32 v79, v29, v79\n\t"
        "v_cvt_pk_bf16_f32 v56, v56, v76\n\t"
        "v_cvt_pk_bf16_f32 v57, v57, v77\n\t"
        "v_cvt_pk_bf16_f32 v58, v58, v78\n\t"
        "v_cvt_pk_bf16_f32 v59, v59, v79\n\t"
        "v_fma_f32 v60, -v30, v36, v60\n\t"
        "v_fma_f32 v61, -v30, v37, v61\n\t"
        "v_fma_f32 v62, -v30, v38, v62\n\t"
        "v_fma_f32 v63, -v30, v39, v63\n\t"
        "v_fma_f32 v80, -v30, v40, v80\n\t"
        "v_fma_f32 v81, -v30, v41, v81\n\t"
        "v_fma_f32 v82, -v30, v42, v82\n\t"
        "v_fma_f32 v83, -v30, v43, v83\n\t"
        "v_mul_f32 v60, v31, v60\n\t"
        "v_mul_f32 v61, v31, v61\n\t"
        "v_mul_f32 v62, v31, v62\n\t"
        "v_mul_f32 v63, v31, v63\n\t"
        "v_mul_f32 v80, v31, v80\n\t"
        "v_mul_f32 v81, v31, v81\n\t"
        "v_mul_f32 v82, v31, v82\n\t"
        "v_mul_f32 v83, v31, v83\n\t"
        "v_cvt_pk_bf16_f32 v60, v60, v80\n\t"
        "v_cvt_pk_bf16_f32 v61, v61, v81\n\t"
        "v_cvt_pk_bf16_f32 v62, v62, v82\n\t"
        "v_cvt_pk_bf16_f32 v63, v63, v83\n\t"
        "v_fma_f32 v64, -v32, v36, v64\n\t"
        "v_fma_f32 v65, -v32, v37, v65\n\t"
        "v_fma_f32 v66, -v32, v38, v66\n\t"
        "v_fma_f32 v67, -v32, v39, v67\n\t"
        "v_fma_f32 v84, -v32, v40, v84\n\t"
        "v_fma_f32 v85, -v32, v41, v85\n\t"
        "v_fma_f32 v86, -v32, v42, v86\n\t"
        "v_fma_f32 v87, -v32, v43, v87\n\t"
        "v_mul_f32 v64, v33, v64\n\t"
        "v_mul_f32 v65, v33, v65\n\t"
        "v_mul_f32 v66, v33, v66\n\t"
        "v_mul_f32 v67, v33, v67\n\t"
        "v_mul_f32 v84, v33, v84\n\t"
        "v_mul_f32 v85, v33, v85\n\t"
        "v_mul_f32 v86, v33, v86\n\t"
        "v_mul_f32 v87, v33, v87\n\t"
        "v_cvt_pk_bf16_f32 v64, v64, v84\n\t"
        "v_cvt_pk_bf16_f32 v65, v65, v85\n\t"
        "v_cvt_pk_bf16_f32 v66, v66, v86\n\t"
        "v_cvt_pk_bf16_f32 v67, v67, v87\n\t"
        "Lu_%=:\n\t"
        "v_readfirstlane_b32 %[sf], v35\n\t"
        "s_cmp_ge_u32 %[sf], 7\n\t"
        "s_cbranch_scc1 Lv_%=\n\t"
        "s_cmp_lt_i32 %[scn], 1\n\t"
        "s_cbranch_scc1 Lv_%=\n\t"
        "s_sub_u32 %[scn], %[scn], 1\n\t"
        "s_sleep 4\n\t"
        "ds_read_b32 v35, %[x2] offset:%[omc]+4\n\t"
        "s_waitcnt lgkmcnt(0)\n\t"
        "s_branch Lu_%=\n\t"
        "Lv_%=:\n\t"
        "s_mov_b64 exec, %[sva]\n\t"
        "ds_write_b128 v68, v[52:55]\n\t"
        "ds_write_b128 v69, v[56:59]\n\t"
        "ds_write_b128 v70, v[60:63]\n\t"
        "ds_write_b128 v71, v[64:67]\n\t"
        "s_mov_b64 exec, %[sv0]\n\t"
        "ds_write_b128 v110, v[48:51]\n\t"
        "s_mov_b64 exec, -1\n\t"
        "s_branch Le_%=\n\t"
        "Ln_%=:\n\t"
        "s_cmp_lt_i32 %[scn], 1\n\t"
        "s_cbranch_scc1 Lo_%=\n\t"
        "s_sub_u32 %[scn], %[scn], 1\n\t"
        "buffer_load_dword %[fl], %[fo], %[ssrd], 0 offen sc1\n\t"
        "ds_read_b64 v[34:35], %[x2] offset:%[omc]\n\t"
        "ds_read_b64 v[24:25], %[x0] offset:0\n\t"
        "ds_read_b64 v[26:27], %[x0] offset:128\n\t"
        "ds_read_b64 v[28:29], %[x0] offset:256\n\t"
        "ds_read_b64 v[30:31], %[x0] offset:384\n\t"
        "ds_read_b64 v[32:33], %[x0] offset:512\n\t"
        "s_branch Lm_%=\n\t"
        "Le_%=:\n\t"
        : [xa] "+v"(xa), [xb] "=&v"(xb), [si] "+s"(si), [fl] "=&v"(fl), [sf] "=&s"(sf), [scn] "+s"(cn), [x0] "+v"(xt[0]), [x1] "+v"(xt[1]), [x2] "+v"(xt[2])
        : [ssrd] "s"(srd), [iv] "v"(iv), [fo] "v"(fo), [sexp] "s"(fexp), [scs] "s"(cs), [sva] "s"(va), [sv0] "s"(v0), [sm16] "s"(0x0001000100010001ull), [omc] "n"(BC_MC)
        : "memory", "scc", "vcc", "v255", "v24", "v25", "v26", "v27", "v28", "v29", "v30", "v31", "v32", "v33", "v34", "v35", "v36", "v37", "v38", "v39", "v40", "v41", "v42", "v43", "v44", "v45", "v46", "v47");
}

DEV void gemm1a(int xo0, int fb, u32 cs, u64 va, u64 v0, u32& cn) {
    int xa, xh; u32 one, fl, sf;
    asm volatile(
        "v_add_u32 %[xa], 36864, %[xo0]\n\t"
        "v_mov_b32 %[one], 1\n\t"
        "s_setprio 2\n\t"
        "ds_read_b128 v[24:27], %[xo0] offset:0\n\t"
        "ds_read_b128 v[28:31], %[xo0] offset:256\n\t"
        "ds_read_b128 v[32:35], %[xo0] offset:512\n\t"
        "ds_read_b128 v[36:39], %[xo0] offset:768\n\t"
        "ds_read_b128 v[40:43], %[xo0] offset:4096\n\t"
        "ds_read_b128 v[44:47], %[xo0] offset:4352\n\t"
        "ds_read_b128 v[8:11], %[xo0] offset:4608\n\t"
        "ds_read_b128 v[12:15], %[xo0] offset:4864\n\t"
        "ds_read_b128 v[16:19], %[xo0] offset:8192\n\t"
        "ds_read_b128 v[20:23], %[xo0] offset:8448\n\t"
        "ds_read_b128 v[48:51], %[xo0] offset:8704\n\t"
        "ds_read_b128 v[68:71], %[xo0] offset:8960\n\t"
        "s_waitcnt lgkmcnt(11)\n\t"
        "v_mfma_f32_16x16x32_bf16 v[52:55], v[112:115], v[24:27], 0\n\t"
        "v_mfma_f32_16x16x32_bf16 v[72:75], v[116:119], v[24:27], 0\n\t"
        "s_waitcnt lgkmcnt(10)\n\t"
        "v_mfma_f32_16x16x32_bf16 v[56:59], v[112:115], v[28:31], 0\n\t"
        "ds_read_b128 v[24:27], %[xo0] offset:12288\n\t"
        "v_mfma_f32_16x16x32_bf16 v[76:79], v[116:119], v[28:31], 0\n\t"
        "s_waitcnt lgkmcnt(10)\n\t"
        "v_mfma_f32_16x16x32_bf16 v[60:63], v[112:115], v[32:35], 0\n\t"
        "ds_read_b128 v[28:31], %[xo0] offset:12544\n\t"
        "v_mfma_f32_16x16x32_bf16 v[80:83], v[116:119], v[32:35], 0\n\t"
        "s_waitcnt lgkmcnt(10)\n\t"
        "v_mfma_f32_16x16x32_bf16 v[64:67], v[112:115], v[36:39], 0\n\t"
        "ds_read_b128 v[32:35], %[xo0] offset:12800\n\t"
        "v_mfma_f32_16x16x32_bf16 v[84:87], v[116:119], v[36:39], 0\n\t"
        "s_waitcnt lgkmcnt(10)\n\t"
        "v_mfma_f32_16x16x32_bf16 v[52:55], v[120:123], v[40:43], v[52:55]\n\t"
        "ds_read_b128 v[36:39], %[xo0] offset:13056\n\t"
        "v_mfma_f32_16x16x32_bf16 v[72:75], v[124:127], v[40:43], v[72:75]\n\t"
        "s_waitcnt lgkmcnt(10)\n\t"
        "v_mfma_f32_16x16x32_bf16 v[56:59], v[120:123], v[44:47], v[56:59]\n\t"
        "ds_read_b32 %[fl], %[fb] offset:%[og0]+4\n\t"
        "v_mfma_f32_16x16x32_bf16 v[76:79], v[124:127], v[44:47], v[76:79]\n\t"
        "s_waitcnt lgkmcnt(10)\n\t"
        "v_mfma_f32_16x16x32_bf16 v[60:63], v[120:123], v[8:11], v[60:63]\n\t"
        "v_mfma_f32_16x16x32_bf16 v[80:83], v[124:127], v[8:11], v[80:83]\n\t"
        "s_waitcnt lgkmcnt(9)\n\t"
        "v_mfma_f32_16x16x32_bf16 v[64:67], v[120:123], v[12:15], v[64:67]\n\t"
        "v_mfma_f32_16x16x32_bf16 v[84:87], v[124:127], v[12:15], v[84:87]\n\t"
        "s_waitcnt lgkmcnt(8)\n\t"
        "v_mfma_f32_16x16x32_bf16 v[52:55], v[128:131], v[16:19], v[52:55]\n\t"
        "v_mfma_f32_16x16x32_bf16 v[72:75], v[132:135], v[16:19], v[72:75]\n\t"
        "s_waitcnt lgkmcnt(7)\n\t"
        "v_mfma_f32_16x16x32_bf16 v[56:59], v[128:131], v[20:23], v[56:59]\n\t"
        "v_mfma_f32_16x16x32_bf16 v[76:79], v[132:135], v[20:23], v[76:79]\n\t"
        "s_waitcnt lgkmcnt(6)\n\t"
        "v_mfma_f32_16x16x32_bf16 v[60:63], v[128:131], v[48:51], v[60:63]\n\t"
        "s_waitcnt lgkmcnt(0)\n\t"
        "v_cmp_gt_u32 vcc, 2, %[fl]\n\t"
        "v_mfma_f32_16x16x32_bf16 v[80:83], v[132:135], v[48:51], v[80:83]\n\t"
        "v_mfma_f32_16x16x32_bf16 v[64:67], v[128:131], v[68:71], v[64:67]\n\t"
        "s_cbranch_vccnz Ls16_%=\n\t"
        "Lg16_%=:\n\t"
        "ds_read_b128 v[40:43], %[xo0] offset:16384\n\t"
        "v_mfma_f32_16x16x32_bf16 v[84:87], v[132:135], v[68:71], v[84:87]\n\t"
        "ds_read_b128 v[44:47], %[xo0] offset:16640\n\t"
        "v_mfma_f32_16x16x32_bf16 v[52:55], v[136:139], v[24:27], v[52:55]\n\t"
        "ds_read_b128 v[8:11], %[xo0] offset:16896\n\t"
        "v_mfma_f32_16x16x32_bf16 v[72:75], v[140:143], v[24:27], v[72:75]\n\t"
        "ds_read_b128 v[12:15], %[xo0] offset:17152\n\t"
        "v_mfma_f32_16x16x32_bf16 v[56:59], v[136:139], v[28:31], v[56:59]\n\t"
        "ds_read_b128 v[16:19], %[xo0] offset:20480\n\t"
        "v_mfma_f32_16x16x32_bf16 v[76:79], v[140:143], v[28:31], v[76:79]\n\t"
        "ds_read_b128 v[20:23], %[xo0] offset:20736\n\t"
        "v_mfma_f32_16x16x32_bf16 v[60:63], v[136:139], v[32:35], v[60:63]\n\t"
        "ds_read_b128 v[48:51], %[xo0] offset:20992\n\t"
        "v_mfma_f32_16x16x32_bf16 v[80:83], v[140:143], v[32:35], v[80:83]\n\t"
        "ds_read_b128 v[68:71], %[xo0] offset:21248\n\t"
        "v_mfma_f32_16x16x32_bf16 v[64:67], v[136:139], v[36:39], v[64:67]\n\t"
        "ds_read_b128 v[24:27], %[xo0] offset:24576\n\t"
        "v_mfma_f32_16x16x32_bf16 v[84:87], v[140:143], v[36:39], v[84:87]\n\t"
        "ds_read_b128 v[28:31], %[xo0] offset:24832\n\t"
        "s_waitcnt lgkmcnt(9)\n\t"
        "v_mfma_f32_16x16x32_bf16 v[52:55], v[144:147], v[40:43], v[52:55]\n\t"
        "ds_read_b128 v[32:35], %[xo0] offset:25088\n\t"
        "v_mfma_f32_16x16x32_bf16 v[72:75], v[148:151], v[40:43], v[72:75]\n\t"
        "ds_read_b128 v[36:39], %[xo0] offset:25344\n\t"
        "s_waitcnt lgkmcnt(10)\n\t"
        "v_mfma_f32_16x16x32_bf16 v[56:59], v[144:147], v[44:47], v[56:59]\n\t"
        "ds_read_b128 v[40:43], %[xo0] offset:28672\n\t"
        "v_mfma_f32_16x16x32_bf16 v[76:79], v[148:151], v[44:47], v[76:79]\n\t"
        "s_waitcnt lgkmcnt(10)\n\t"
        "v_mfma_f32_16x16x32_bf16 v[60:63], v[144:147], v[8:11], v[60:63]\n\t"
        "ds_read_b128 v[44:47], %[xo0] offset:28928\n\t"
        "v_mfma_f32_16x16x32_bf16 v[80:83], v[148:151], v[8:11], v[80:83]\n\t"
        "s_waitcnt lgkmcnt(10)\n\t"
        "v_mfma_f32_16x16x32_bf16 v[64:67], v[144:147], v[12:15], v[64:67]\n\t"
        "ds_read_b128 v[8:11], %[xo0] offset:29184\n\t"
        "v_mfma_f32_16x16x32_bf16 v[84:87], v[148:151], v[12:15], v[84:87]\n\t"
        "s_waitcnt lgkmcnt(10)\n\t"
        "v_mfma_f32_16x16x32_bf16 v[52:55], v[152:155], v[16:19], v[52:55]\n\t"
        "ds_read_b128 v[12:15], %[xo0] offset:29440\n\t"
        "v_mfma_f32_16x16x32_bf16 v[72:75], v[156:159], v[16:19], v[72:75]\n\t"
        "s_waitcnt lgkmcnt(10)\n\t"
        "v_mfma_f32_16x16x32_bf16 v[56:59], v[152:155], v[20:23], v[56:59]\n\t"
        "ds_read_b32 %[fl], %[fb] offset:%[og0]+8\n\t"
        "v_mfma_f32_16x16x32_bf16 v[76:79], v[156:159], v[20:23], v[76:79]\n\t"
        "s_waitcnt lgkmcnt(10)\n\t"
        "v_mfma_f32_16x16x32_bf16 v[60:63], v[152:155], v[48:51], v[60:63]\n\t"
        "v_mfma_f32_16x16x32_bf16 v[80:83], v[156:159], v[48:51], v[80:83]\n\t"
        "s_waitcnt lgkmcnt(9)\n\t"
        "v_mfma_f32_16x16x32_bf16 v[64:67], v[152:155], v[68:71], v[64:67]\n\t"
        "v_mfma_f32_16x16x32_bf16 v[84:87], v[156:159], v[68:71], v[84:87]\n\t"
        "s_waitcnt lgkmcnt(8)\n\t"
        "v_mfma_f32_16x16x32_bf16 v[52:55], v[160:163], v[24:27], v[52:55]\n\t"
        "v_mfma_f32_16x16x32_bf16 v[72:75], v[164:167], v[24:27], v[72:75]\n\t"
        "s_waitcnt lgkmcnt(7)\n\t"
        "v_mfma_f32_16x16x32_bf16 v[56:59], v[160:163], v[28:31], v[56:59]\n\t"
        "v_mfma_f32_16x16x32_bf16 v[76:79], v[164:167], v[28:31], v[76:79]\n\t"
        "s_waitcnt lgkmcnt(6)\n\t"
        "v_mfma_f32_16x16x32_bf16 v[60:63], v[160:163], v[32:35], v[60:63]\n\t"
        "s_waitcnt lgkmcnt(0)\n\t"
        "v_cmp_gt_u32 vcc, 2, %[fl]\n\t"
        "v_mfma_f32_16x16x32_bf16 v[80:83], v[164:167], v[32:35], v[80:83]\n\t"
        "v_mfma_f32_16x16x32_bf16 v[64:67], v[160:163], v[36:39], v[64:67]\n\t"
        "s_cbranch_vccnz Ls32_%=\n\t"
        "Lg32_%=:\n\t"
        "ds_read_b128 v[16:19], %[xo0] offset:32768\n\t"
        "v_mfma_f32_16x16x32_bf16 v[84:87], v[164:167], v[36:39], v[84:87]\n\t"
        "ds_read_b128 v[20:23], %[xo0] offset:33024\n\t"
        "v_mfma_f32_16x16x32_bf16 v[52:55], v[168:171], v[40:43], v[52:55]\n\t"
        "ds_read_b128 v[48:51], %[xo0] offset:33280\n\t"
        "v_mfma_f32_16x16x32_bf16 v[72:75], v[172:175], v[40:43], v[72:75]\n\t"
        "ds_read_b128 v[68:71], %[xo0] offset:33536\n\t"
        "s_mov_b64 exec, 1\n\t"
        "ds_add_u32 %[fb], %[one] offset:%[omc]+4\n\t"
        "s_mov_b64 exec, -1\n\t"
        "v_and_b32 %[xo0], 0xf0, v107\n\t"
        "v_lshrrev_b32 %[xo0], 1, %[xo0]\n\t"
        "v_mfma_f32_16x16x32_bf16 v[56:59], v[168:171], v[44:47], v[56:59]\n\t"
        "ds_read_b128 v[24:27], %[xa] offset:0\n\t"
        "v_add_u32 %[xo0], 153344, %[xo0]\n\t"
        "v_lshrrev_b32 %[xh], 8, v107\n\t"
        "v_mfma_f32_16x16x32_bf16 v[76:79], v[172:175], v[44:47], v[76:79]\n\t"
        "ds_read_b128 v[28:31], %[xa] offset:256\n\t"
        "v_lshl_add_u32 %[xh], %[xh], 5, %[scs]\n\t"
        "v_mfma_f32_16x16x32_bf16 v[60:63], v[168:171], v[8:11], v[60:63]\n\t"
        "ds_read_b128 v[32:35], %[xa] offset:512\n\t"
        "v_mfma_f32_16x16x32_bf16 v[80:83], v[172:175], v[8:11], v[80:83]\n\t"
        "ds_read_b128 v[36:39], %[xa] offset:768\n\t"
        "v_mfma_f32_16x16x32_bf16 v[64:67], v[168:171], v[12:15], v[64:67]\n\t"
        "ds_read_b128 v[40:43], %[xa] offset:4096\n\t"
        "v_mfma_f32_16x16x32_bf16 v[84:87], v[172:175], v[12:15], v[84:87]\n\t"
        "ds_read_b128 v[44:47], %[xa] offset:4352\n\t"
        "s_waitcnt lgkmcnt(10)\n\t"
        "v_mfma_f32_16x16x32_bf16 v[52:55], v[176:179], v[16:19], v[52:55]\n\t"
        "v_mfma_f32_16x16x32_bf16 v[72:75], v[180:183], v[16:19], v[72:75]\n\t"
        "s_waitcnt lgkmcnt(9)\n\t"
        "v_mfma_f32_16x16x32_bf16 v[56:59], v[176:179], v[20:23], v[56:59]\n\t"
        "v_mfma_f32_16x16x32_bf16 v[76:79], v[180:183], v[20:23], v[76:79]\n\t"
        "s_waitcnt lgkmcnt(8)\n\t"
        "v_mfma_f32_16x16x32_bf16 v[60:63], v[176:179], v[48:51], v[60:63]\n\t"
        "v_mfma_f32_16x16x32_bf16 v[80:83], v[180:183], v[48:51], v[80:83]\n\t"
        "s_waitcnt lgkmcnt(7)\n\t"
        "v_mfma_f32_16x16x32_bf16 v[64:67], v[176:179], v[68:71], v[64:67]\n\t"
        "ds_read_b128 v[48:51], %[xa] offset:4608\n\t"
        "v_mfma_f32_16x16x32_bf16 v[84:87], v[180:183], v[68:71], v[84:87]\n\t"
        "s_waitcnt lgkmcnt(6)\n\t"
        "v_mfma_f32_16x16x32_bf16 v[52:55], v[184:187], v[24:27], v[52:55]\n\t"
        "ds_read_b128 v[68:71], %[xa] offset:4864\n\t"
        "v_mfma_f32_16x16x32_bf16 v[72:75], v[188:191], v[24:27], v[72:75]\n\t"
        "s_waitcnt lgkmcnt(6)\n\t"
        "v_mfma_f32_16x16x32_bf16 v[56:59], v[184:187], v[28:31], v[56:59]\n\t"
        "ds_read_b32 %[fl], %[fb] offset:%[og0]+12\n\t"
        "ds_read_b128 v[24:27], %[xa] offset:8192\n\t"
        "v_mfma_f32_16x16x32_bf16 v[76:79], v[188:191], v[28:31], v[76:79]\n\t"
        "s_waitcnt lgkmcnt(7)\n\t"
        "v_mfma_f32_16x16x32_bf16 v[60:63], v[184:187], v[32:35], v[60:63]\n\t"
        "ds_read_b128 v[28:31], %[xa] offset:8448\n\t"
        "v_mfma_f32_16x16x32_bf16 v[80:83], v[188:191], v[32:35], v[80:83]\n\t"
        "s_waitcnt lgkmcnt(7)\n\t"
        "v_mfma_f32_16x16x32_bf16 v[64:67], v[184:187], v[36:39], v[64:67]\n\t"
        "ds_read_b128 v[32:35], %[xa] offset:8704\n\t"
        "v_mfma_f32_16x16x32_bf16 v[84:87], v[188:191], v[36:39], v[84:87]\n\t"
        "s_waitcnt lgkmcnt(7)\n\t"
        "v_mfma_f32_16x16x32_bf16 v[52:55], v[192:195], v[40:43], v[52:55]\n\t"
        "ds_read_b128 v[36:39], %[xa] offset:8960\n\t"
        "v_mfma_f32_16x16x32_bf16 v[72:75], v[196:199], v[40:43], v[72:75]\n\t"
        "s_waitcnt lgkmcnt(7)\n\t"
        "v_mfma_f32_16x16x32_bf16 v[56:59], v[192:195], v[44:47], v[56:59]\n\t"
        "v_mfma_f32_16x16x32_bf16 v[76:79], v[196:199], v[44:47], v[76:79]\n\t"
        "s_waitcnt lgkmcnt(6)\n\t"
        "v_mfma_f32_16x16x32_bf16 v[60:63], v[192:195], v[48:51], v[60:63]\n\t"
        "s_waitcnt lgkmcnt(4)\n\t"
        "v_cmp_gt_u32 vcc, 2, %[fl]\n\t"
        "ds_read_b128 v[8:11], %[xh] offset:0\n\t"
        "v_mfma_f32_16x16x32_bf16 v[80:83], v[196:199], v[48:51], v[80:83]\n\t"
        "ds_read_b128 v[12:15], %[xh] offset:16\n\t"
        "v_mfma_f32_16x16x32_bf16 v[64:67], v[192:195], v[68:71], v[64:67]\n\t"
        "s_cbranch_vccnz Ls48_%=\n\t"
        "Lg48_%=:\n\t"
        "ds_read_b128 v[40:43], %[xa] offset:12288\n\t"
        "v_mfma_f32_16x16x32_bf16 v[84:87], v[196:199], v[68:71], v[84:87]\n\t"
        "ds_read_b64 v[98:99], %[fb] offset:%[omc]\n\t"
        "s_waitcnt lgkmcnt(7)\n\t"
        "v_mfma_f32_16x16x32_bf16 v[52:55], v[200:203], v[24:27], v[52:55]\n\t"
        "ds_read_b128 v[44:47], %[xa] offset:16384\n\t"
        "v_mfma_f32_16x16x32_bf16 v[72:75], v[204:207], v[24:27], v[72:75]\n\t"
        "ds_read_b64 v[16:17], %[xo0] offset:128\n\t"
        "s_waitcnt lgkmcnt(8)\n\t"
        "v_mfma_f32_16x16x32_bf16 v[56:59], v[200:203], v[28:31], v[56:59]\n\t"
        "ds_read_b128 v[48:51], %[xa] offset:20480\n\t"
        "v_mfma_f32_16x16x32_bf16 v[76:79], v[204:207], v[28:31], v[76:79]\n\t"
        "ds_read_b64 v[18:19], %[xo0] offset:256\n\t"
        "s_waitcnt lgkmcnt(9)\n\t"
        "v_mfma_f32_16x16x32_bf16 v[60:63], v[200:203], v[32:35], v[60:63]\n\t"
        "ds_read_b128 v[68:71], %[xa] offset:24576\n\t"
        "v_mfma_f32_16x16x32_bf16 v[80:83], v[204:207], v[32:35], v[80:83]\n\t"
        "ds_read_b64 v[20:21], %[xo0] offset:384\n\t"
        "s_waitcnt lgkmcnt(10)\n\t"
        "v_mfma_f32_16x16x32_bf16 v[64:67], v[200:203], v[36:39], v[64:67]\n\t"
        "ds_read_b128 v[24:27], %[xa] offset:28672\n\t"
        "v_mfma_f32_16x16x32_bf16 v[84:87], v[204:207], v[36:39], v[84:87]\n\t"
        "ds_read_b64 v[22:23], %[xo0] offset:512\n\t"
        "s_waitcnt lgkmcnt(9)\n\t"
        "v_mfma_f32_16x16x32_bf16 v[52:55], v[208:211], v[40:43], v[52:55]\n\t"
        "ds_read_b128 v[28:31], %[xa] offset:32768\n\t"
        "v_mfma_f32_16x16x32_bf16 v[72:75], v[212:215], v[40:43], v[72:75]\n\t"
        "ds_read_b128 v[32:35], %[xa] offset:12544\n\t"
        "s_waitcnt lgkmcnt(9)\n\t"
        "v_mfma_f32_16x16x32_bf16 v[52:55], v[216:219], v[44:47], v[52:55]\n\t"
        "ds_read_b128 v[36:39], %[xa] offset:16640\n\t"
        "v_mfma_f32_16x16x32_bf16 v[72:75], v[220:223], v[44:47], v[72:75]\n\t"
        "ds_read_b128 v[40:43], %[xa] offset:20736\n\t"
        "s_waitcnt lgkmcnt(9)\n\t"
        "v_mfma_f32_16x16x32_bf16 v[52:55], v[224:227], v[48:51], v[52:55]\n\t"
        "ds_read_b128 v[44:47], %[xa] offset:24832\n\t"
        "v_add_u32 %[xh], 73728, v107\n\t"
        "v_mfma_f32_16x16x32_bf16 v[72:75], v[228:231], v[48:51], v[72:75]\n\t"
        "s_waitcnt lgkmcnt(8)\n\t"
        "v_mfma_f32_16x16x32_bf16 v[52:55], v[232:235], v[68:71], v[52:55]\n\t"
        "ds_read_b128 v[48:51], %[xa] offset:28928\n\t"
        "v_mfma_f32_16x16x32_bf16 v[72:75], v[236:239], v[68:71], v[72:75]\n\t"
        "s_waitcnt lgkmcnt(7)\n\t"
        "v_mfma_f32_16x16x32_bf16 v[52:55], v[240:243], v[24:27], v[52:55]\n\t"
        "ds_read_b128 v[68:71], %[xa] offset:33024\n\t"
        "v_mfma_f32_16x16x32_bf16 v[72:75], v[244:247], v[24:27], v[72:75]\n\t"
        "s_waitcnt lgkmcnt(6)\n\t"
        "v_mfma_f32_16x16x32_bf16 v[52:55], v[248:251], v[28:31], v[52:55]\n\t"
        "v_cmp_gt_u32 vcc, 5, v98\n\t"
        "ds_read_b128 v[24:27], %[xa] offset:12800\n\t"
        "v_mfma_f32_16x16x32_bf16 v[72:75], v[252:255], v[28:31], v[72:75]\n\t"
        "s_waitcnt lgkmcnt(6)\n\t"
        "v_mfma_f32_16x16x32_bf16 v[56:59], v[208:211], v[32:35], v[56:59]\n\t"
        "s_cbranch_vccnz Ln_%=\n\t"
        "Lo_%=:\n\t"
        "ds_read_b128 v[28:31], %[xa] offset:16896\n\t"
        "v_mfma_f32_16x16x32_bf16 v[76:79], v[212:215], v[32:35], v[76:79]\n\t"
        "s_waitcnt lgkmcnt(6)\n\t"
        "v_mfma_f32_16x16x32_bf16 v[56:59], v[216:219], v[36:39], v[56:59]\n\t"
        "ds_read_b128 v[32:35], %[xa] offset:20992\n\t"
        "v_fma_f32 v52, -v16, v8, v52\n\t"
        "v_fma_f32 v53, -v16, v9, v53\n\t"
        "v_mfma_f32_16x16x32_bf16 v[76:79], v[220:223], v[36:39], v[76:79]\n\t"
        "s_waitcnt lgkmcnt(6)\n\t"
        "v_fma_f32 v54, -v16, v10, v54\n\t"
        "v_fma_f32 v55, -v16, v11, v55\n\t"
        "v_mfma_f32_16x16x32_bf16 v[56:59], v[224:227], v[40:43], v[56:59]\n\t"
        "ds_read_b128 v[36:39], %[xa] offset:25088\n\t"
        "v_fma_f32 v72, -v16, v12, v72\n\t"
        "v_fma_f32 v73, -v16, v13, v73\n\t"
        "v_mfma_f32_16x16x32_bf16 v[76:79], v[228:231], v[40:43], v[76:79]\n\t"
        "s_waitcnt lgkmcnt(6)\n\t"
        "v_fma_f32 v74, -v16, v14, v74\n\t"
        "v_fma_f32 v75, -v16, v15, v75\n\t"
        "v_mfma_f32_16x16x32_bf16 v[56:59], v[232:235], v[44:47], v[56:59]\n\t"
        "ds_read_b128 v[40:43], %[xa] offset:29184\n\t"
        "v_mul_f32 v52, v17, v52\n\t"
        "v_mul_f32 v53, v17, v53\n\t"
        "v_mfma_f32_16x16x32_bf16 v[76:79], v[236:239], v[44:47], v[76:79]\n\t"
        "s_waitcnt lgkmcnt(6)\n\t"
        "v_mul_f32 v54, v17, v54\n\t"
        "v_mul_f32 v55, v17, v55\n\t"
        "v_mfma_f32_16x16x32_bf16 v[56:59], v[240:243], v[48:51], v[56:59]\n\t"
        "ds_read_b128 v[44:47], %[xa] offset:33280\n\t"
        "v_mul_f32 v72, v17, v72\n\t"
        "v_mul_f32 v73, v17, v73\n\t"
        "v_mfma_f32_16x16x32_bf16 v[76:79], v[244:247], v[48:51], v[76:79]\n\t"
        "s_waitcnt lgkmcnt(6)\n\t"
        "v_mul_f32 v74, v17, v74\n\t"
        "v_mul_f32 v75, v17, v75\n\t"
        "v_mfma_f32_16x16x32_bf16 v[56:59], v[248:251], v[68:71], v[56:59]\n\t"
        "ds_read_b128 v[48:51], %[xa] offset:13056\n\t"
        "v_cvt_pk_bf16_f32 v52, v52, v72\n\t"
        "v_cvt_pk_bf16_f32 v53, v53, v73\n\t"
        "v_mfma_f32_16x16x32_bf16 v[76:79], v[252:255], v[68:71], v[76:79]\n\t"
        "s_waitcnt lgkmcnt(6)\n\t"
        "v_cvt_pk_bf16_f32 v54, v54, v74\n\t"
        "v_cvt_pk_bf16_f32 v55, v55, v75\n\t"
        "v_mfma_f32_16x16x32_bf16 v[60:63], v[208:211], v[24:27], v[60:63]\n\t"
        "v_add_u32 v16, v110, v111\n\t"
        "v_mfma_f32_16x16x32_bf16 v[80:83], v[212:215], v[24:27], v[80:83]\n\t"
        "ds_read_b128 v[72:75], %[xa] offset:17152\n\t"
        "s_waitcnt lgkmcnt(6)\n\t"
        "s_cmp_eq_u64 %[sv0], 0\n\t"
        "s_cbranch_scc1 Lq_%=\n\t"
        "v_mfma_f32_16x16x32_bf16 v[60:63], v[216:219], v[28:31], v[60:63]\n\t"
        "ds_read_b32 %[fl], %[fb] offset:%[og0]+16\n\t"
        "ds_read_b128 v[24:27], %[xa] offset:21248\n\t"
        "v_fma_f32 v56, -v18, v8, v56\n\t"
        "v_fma_f32 v57, -v18, v9, v57\n\t"
        "v_mfma_f32_16x16x32_bf16 v[80:83], v[220:223], v[28:31], v[80:83]\n\t"
        "s_waitcnt lgkmcnt(7)\n\t"
        "v_fma_f32 v58, -v18, v10, v58\n\t"
        "v_fma_f32 v59, -v18, v11, v59\n\t"
        "v_mfma_f32_16x16x32_bf16 v[60:63], v[224:227], v[32:35], v[60:63]\n\t"
        "ds_read_b128 v[28:31], %[xa] offset:25344\n\t"
        "v_fma_f32 v76, -v18, v12, v76\n\t"
        "v_fma_f32 v77, -v18, v13, v77\n\t"
        "v_mfma_f32_16x16x32_bf16 v[80:83], v[228:231], v[32:35], v[80:83]\n\t"
        "s_waitcnt lgkmcnt(7)\n\t"
        "v_fma_f32 v78, -v18, v14, v78\n\t"
        "v_fma_f32 v79, -v18, v15, v79\n\t"
        "v_mfma_f32_16x16x32_bf16 v[60:63], v[232:235], v[36:39], v[60:63]\n\t"
        "ds_read_b128 v[32:35], %[xa] offset:29440\n\t"
        "v_mul_f32 v56, v19, v56\n\t"
        "v_mul_f32 v57, v19, v57\n\t"
        "v_mfma_f32_16x16x32_bf16 v[80:83], v[236:239], v[36:39], v[80:83]\n\t"
        "s_waitcnt lgkmcnt(7)\n\t"
        "v_mul_f32 v58, v19, v58\n\t"
        "v_mul_f32 v59, v19, v59\n\t"
        "v_mfma_f32_16x16x32_bf16 v[60:63], v[240:243], v[40:43], v[60:63]\n\t"
        "ds_read_b128 v[36:39], %[xa] offset:33536\n\t"
        "s_mov_b64 exec, 1\n\t"
        "ds_add_u32 %[fb], %[one] offset:%[oc1]\n\t"
        "s_mov_b64 exec, -1\n\t"
        "v_mul_f32 v76, v19, v76\n\t"
        "v_mul_f32 v77, v19, v77\n\t"
        "v_mfma_f32_16x16x32_bf16 v[80:83], v[244:247], v[40:43], v[80:83]\n\t"
        "s_waitcnt lgkmcnt(8)\n\t"
        "v_mul_f32 v78, v19, v78\n\t"
        "v_mul_f32 v79, v19, v79\n\t"
        "v_mfma_f32_16x16x32_bf16 v[60:63], v[248:251], v[44:47], v[60:63]\n\t"
        "v_cvt_pk_bf16_f32 v56, v56, v76\n\t"
        "v_cvt_pk_bf16_f32 v57, v57, v77\n\t"
        "v_mfma_f32_16x16x32_bf16 v[80:83], v[252:255], v[44:47], v[80:83]\n\t"
        "s_waitcnt lgkmcnt(7)\n\t"
        "v_cvt_pk_bf16_f32 v58, v58, v78\n\t"
        "v_cvt_pk_bf16_f32 v59, v59, v79\n\t"
        "v_mfma_f32_16x16x32_bf16 v[64:67], v[208:211], v[48:51], v[64:67]\n\t"
        "s_waitcnt lgkmcnt(5)\n\t"
        "v_cmp_gt_u32 vcc, 2, %[fl]\n\t"
        "v_add_u32 v18, v16, v111\n\t"
        "v_mfma_f32_16x16x32_bf16 v[84:87], v[212:215], v[48:51], v[84:87]\n\t"
        "v_mfma_f32_16x16x32_bf16 v[64:67], v[216:219], v[72:75], v[64:67]\n\t"
        "s_cbranch_vccnz Ls72_%=\n\t"
        "Lg72_%=:\n\t"
        "ds_read_b128 v[40:43], %[xh] offset:0\n\t"
        "v_fma_f32 v60, -v20, v8, v60\n\t"
        "v_fma_f32 v61, -v20, v9, v61\n\t"
        "v_mfma_f32_16x16x32_bf16 v[84:87], v[220:223], v[72:75], v[84:87]\n\t"
        "ds_read_b128 v[44:47], %[xh] offset:1024\n\t"
        "s_waitcnt lgkmcnt(6)\n\t"
        "v_fma_f32 v62, -v20, v10, v62\n\t"
        "v_fma_f32 v63, -v20, v11, v63\n\t"
        "v_mfma_f32_16x16x32_bf16 v[64:67], v[224:227], v[24:27], v[64:67]\n\t"
        "ds_read_b128 v[76:79], %[xh] offset:2048\n\t"
        "v_fma_f32 v80, -v20, v12, v80\n\t"
        "v_fma_f32 v81, -v20, v13, v81\n\t"
        "v_mfma_f32_16x16x32_bf16 v[84:87], v[228:231], v[24:27], v[84:87]\n\t"
        "ds_read_b128 v[72:75], %[xh] offset:3072\n\t"
        "s_waitcnt lgkmcnt(7)\n\t"
        "v_fma_f32 v82, -v20, v14, v82\n\t"
        "v_fma_f32 v83, -v20, v15, v83\n\t"
        "v_mfma_f32_16x16x32_bf16 v[64:67], v[232:235], v[28:31], v[64:67]\n\t"
        "ds_read_b128 v[24:27], %[xh] offset:4096\n\t"
        "v_mul_f32 v60, v21, v60\n\t"
        "v_mul_f32 v61, v21, v61\n\t"
        "v_mfma_f32_16x16x32_bf16 v[84:87], v[236:239], v[28:31], v[84:87]\n\t"
        "s_waitcnt lgkmcnt(7)\n\t"
        "v_mul_f32 v62, v21, v62\n\t"
        "v_mul_f32 v63, v21, v63\n\t"
        "v_mfma_f32_16x16x32_bf16 v[64:67], v[240:243], v[32:35], v[64:67]\n\t"
        "ds_read_b128 v[28:31], %[xh] offset:5120\n\t"
        "v_mul_f32 v80, v21, v80\n\t"
        "v_mul_f32 v81, v21, v81\n\t"
        "v_mfma_f32_16x16x32_bf16 v[84:87], v[244:247], v[32:35], v[84:87]\n\t"
        "s_waitcnt lgkmcnt(7)\n\t"
        "v_mul_f32 v82, v21, v82\n\t"
        "v_mul_f32 v83, v21, v83\n\t"
        "v_mfma_f32_16x16x32_bf16 v[64:67], v[248:251], v[36:39], v[64:67]\n\t"
        "ds_read_b128 v[32:35], %[xh] offset:6144\n\t"
        "v_cvt_pk_bf16_f32 v60, v60, v80\n\t"
        "v_cvt_pk_bf16_f32 v61, v61, v81\n\t"
        "v_mfma_f32_16x16x32_bf16 v[84:87], v[252:255], v[36:39], v[84:87]\n\t"
        "s_waitcnt lgkmcnt(6)\n\t"
        "v_cvt_pk_bf16_f32 v62, v62, v82\n\t"
        "v_cvt_pk_bf16_f32 v63, v63, v83\n\t"
        "v_mfma_f32_16x16x32_bf16 v[48:51], v[112:115], v[40:43], 0\n\t"
        "ds_read_b128 v[36:39], %[xh] offset:7168\n\t"
        "v_add_u32 v20, v18, v111\n\t"
        "v_mfma_f32_16x16x32_bf16 v[68:71], v[116:119], v[40:43], 0\n\t"
        "ds_read_b128 v[80:83], %[xh] offset:8192\n\t"
        "s_waitcnt lgkmcnt(7)\n\t"
        "v_mfma_f32_16x16x32_bf16 v[48:51], v[120:123], v[44:47], v[48:51]\n\t"
        "ds_read_b128 v[40:43], %[xh] offset:9216\n\t"
        "v_fma_f32 v64, -v22, v8, v64\n\t"
        "v_fma_f32 v65, -v22, v9, v65\n\t"
        "v_mfma_f32_16x16x32_bf16 v[68:71], v[124:127], v[44:47], v[68:71]\n\t"
        "s_waitcnt lgkmcnt(7)\n\t"
        "v_fma_f32 v66, -v22, v10, v66\n\t"
        "v_fma_f32 v67, -v22, v11, v67\n\t"
        "v_mfma_f32_16x16x32_bf16 v[48:51], v[128:131], v[76:79], v[48:51]\n\t"
        "ds_read_b128 v[44:47], %[xh] offset:10240\n\t"
        "v_fma_f32 v84, -v22, v12, v84\n\t"
        "v_fma_f32 v85, -v22, v13, v85\n\t"
        "v_mfma_f32_16x16x32_bf16 v[68:71], v[132:135], v[76:79], v[68:71]\n\t"
        "s_waitcnt lgkmcnt(7)\n\t"
        "v_fma_f32 v86, -v22, v14, v86\n\t"
        "v_fma_f32 v87, -v22, v15, v87\n\t"
        "v_mfma_f32_16x16x32_bf16 v[48:51], v[136:139], v[72:75], v[48:51]\n\t"
        "ds_read_b128 v[76:79], %[xh] offset:11264\n\t"
        "v_mul_f32 v64, v23, v64\n\t"
        "v_mul_f32 v65, v23, v65\n\t"
        "v_mfma_f32_16x16x32_bf16 v[68:71], v[140:143], v[72:75], v[68:71]\n\t"
        "s_waitcnt lgkmcnt(7)\n\t"
        "v_mul_f32 v66, v23, v66\n\t"
        "v_mul_f32 v67, v23, v67\n\t"
        "v_mfma_f32_16x16x32_bf16 v[48:51], v[144:147], v[24:27], v[48:51]\n\t"
        "ds_read_b128 v[72:75], %[xh] offset:12288\n\t"
        "v_mul_f32 v84, v23, v84\n\t"
        "v_mul_f32 v85, v23, v85\n\t"
        "v_mfma_f32_16x16x32_bf16 v[68:71], v[148:151], v[24:27], v[68:71]\n\t"
        "s_waitcnt lgkmcnt(7)\n\t"
        "v_mul_f32 v86, v23, v86\n\t"
        "v_mul_f32 v87, v23, v87\n\t"
        "v_mfma_f32_16x16x32_bf16 v[48:51], v[152:155], v[28:31], v[48:51]\n\t"
        "ds_read_b128 v[24:27], %[xh] offset:13312\n\t"
        "v_cvt_pk_bf16_f32 v64, v64, v84\n\t"
        "v_cvt_pk_bf16_f32 v65, v65, v85\n\t"
        "v_mfma_f32_16x16x32_bf16 v[68:71], v[156:159], v[28:31], v[68:71]\n\t"
        "s_waitcnt lgkmcnt(7)\n\t"
        "v_cvt_pk_bf16_f32 v66, v66, v86\n\t"
        "v_cvt_pk_bf16_f32 v67, v67, v87\n\t"
        "v_mfma_f32_16x16x32_bf16 v[48:51], v[160:163], v[32:35], v[48:51]\n\t"
        "ds_read_b128 v[28:31], %[xh] offset:14336\n\t"
        "v_add_u32 v22, v20, v111\n\t"
        "v_mfma_f32_16x16x32_bf16 v[68:71], v[164:167], v[32:35], v[68:71]\n\t"
        "s_waitcnt lgkmcnt(7)\n\t"
        "v_mfma_f32_16x16x32_bf16 v[48:51], v[168:171], v[36:39], v[48:51]\n\t"
        "ds_read_b128 v[32:35], %[xh] offset:15360\n\t"
        "v_mfma_f32_16x16x32_bf16 v[68:71], v[172:175], v[36:39], v[68:71]\n\t"
        "s_waitcnt lgkmcnt(7)\n\t"
        "v_mfma_f32_16x16x32_bf16 v[48:51], v[176:179], v[80:83], v[48:51]\n\t"
        "ds_read_b128 v[36:39], %[xh] offset:16384\n\t"
        "v_mfma_f32_16x16x32_bf16 v[68:71], v[180:183], v[80:83], v[68:71]\n\t"
        "s_waitcnt lgkmcnt(7)\n\t"
        "v_mfma_f32_16x16x32_bf16 v[48:51], v[184:187], v[40:43], v[48:51]\n\t"
        "ds_read_b128 v[80:83], %[xh] offset:17408\n\t"
        "v_mfma_f32_16x16x32_bf16 v[68:71], v[188:191], v[40:43], v[68:71]\n\t"
        "ds_read_b64 v[84:85], %[xo0]\n\t"
        "s_waitcnt lgkmcnt(8)\n\t"
        "v_mfma_f32_16x16x32_bf16 v[48:51], v[192:195], v[44:47], v[48:51]\n\t"
        "ds_read_b32 v99, %[fb] offset:%[omc]+4\n\t"
        "v_mfma_f32_16x16x32_bf16 v[68:71], v[196:199], v[44:47], v[68:71]\n\t"
        "s_waitcnt lgkmcnt(8)\n\t"
        "v_mfma_f32_16x16x32_bf16 v[48:51], v[200:203], v[76:79], v[48:51]\n\t"
        "v_mfma_f32_16x16x32_bf16 v[68:71], v[204:207], v[76:79], v[68:71]\n\t"
        "s_waitcnt lgkmcnt(7)\n\t"
        "v_mfma_f32_16x16x32_bf16 v[48:51], v[208:211], v[72:75], v[48:51]\n\t"
        "v_mfma_f32_16x16x32_bf16 v[68:71], v[212:215], v[72:75], v[68:71]\n\t"
        "s_waitcnt lgkmcnt(6)\n\t"
        "v_mfma_f32_16x16x32_bf16 v[48:51], v[216:219], v[24:27], v[48:51]\n\t"
        "v_mfma_f32_16x16x32_bf16 v[68:71], v[220:223], v[24:27], v[68:71]\n\t"
        "s_waitcnt lgkmcnt(5)\n\t"
        "v_mfma_f32_16x16x32_bf16 v[48:51], v[224:227], v[28:31], v[48:51]\n\t"
        "v_mfma_f32_16x16x32_bf16 v[68:71], v[228:231], v[28:31], v[68:71]\n\t"
        "s_waitcnt lgkmcnt(4)\n\t"
        "v_mfma_f32_16x16x32_bf16 v[48:51], v[232:235], v[32:35], v[48:51]\n\t"
        "v_mfma_f32_16x16x32_bf16 v[68:71], v[236:239], v[32:35], v[68:71]\n\t"
        "s_waitcnt lgkmcnt(3)\n\t"
        "v_mfma_f32_16x16x32_bf16 v[48:51], v[240:243], v[36:39], v[48:51]\n\t"
        "v_mfma_f32_16x16x32_bf16 v[68:71], v[244:247], v[36:39], v[68:71]\n\t"
        "s_waitcnt lgkmcnt(2)\n\t"
        "v_mfma_f32_16x16x32_bf16 v[48:51], v[248:251], v[80:83], v[48:51]\n\t"
        "v_mfma_f32_16x16x32_bf16 v[68:71], v[252:255], v[80:83], v[68:71]\n\t"
        "s_setprio 0\n\t"
        "s_waitcnt lgkmcnt(0)\n\t"
        "s_nop 4\n\t"
        "v_fma_f32 v48, -v84, v8, v48\n\t"
        "v_fma_f32 v49, -v84, v9, v49\n\t"
        "v_fma_f32 v50, -v84, v10, v50\n\t"
        "v_fma_f32 v51, -v84, v11, v51\n\t"
        "v_fma_f32 v68, -v84, v12, v68\n\t"
        "v_fma_f32 v69, -v84, v13, v69\n\t"
        "v_fma_f32 v70, -v84, v14, v70\n\t"
        "v_fma_f32 v71, -v84, v15, v71\n\t"
        "v_mul_f32 v48, v85, v48\n\t"
        "v_mul_f32 v49, v85, v49\n\t"
        "v_mul_f32 v50, v85, v50\n\t"
        "v_mul_f32 v51, v85, v51\n\t"
        "v_mul_f32 v68, v85, v68\n\t"
        "v_mul_f32 v69, v85, v69\n\t"
        "v_mul_f32 v70, v85, v70\n\t"
        "v_mul_f32 v71, v85, v71\n\t"
        "v_cvt_pk_bf16_f32 v48, v48, v68\n\t"
        "v_cvt_pk_bf16_f32 v49, v49, v69\n\t"
        "v_cvt_pk_bf16_f32 v50, v50, v70\n\t"
        "v_cvt_pk_bf16_f32 v51, v51, v71\n\t"
        "s_branch Lu_%=\n\t"
        "Lq_%=:\n\t"
        "v_mfma_f32_16x16x32_bf16 v[60:63], v[216:219], v[28:31], v[60:63]\n\t"
        "ds_read_b128 v[24:27], %[xa] offset:21248\n\t"
        "v_fma_f32 v56, -v18, v8, v56\n\t"
        "v_fma_f32 v57, -v18, v9, v57\n\t"
        "v_mfma_f32_16x16x32_bf16 v[80:83], v[220:223], v[28:31], v[80:83]\n\t"
        "s_waitcnt lgkmcnt(6)\n\t"
        "v_fma_f32 v58, -v18, v10, v58\n\t"
        "v_fma_f32 v59, -v18, v11, v59\n\t"
        "v_mfma_f32_16x16x32_bf16 v[60:63], v[224:227], v[32:35], v[60:63]\n\t"
        "ds_read_b128 v[28:31], %[xa] offset:25344\n\t"
        "v_fma_f32 v76, -v18, v12, v76\n\t"
        "v_fma_f32 v77, -v18, v13, v77\n\t"
        "v_mfma_f32_16x16x32_bf16 v[80:83], v[228:231], v[32:35], v[80:83]\n\t"
        "s_waitcnt lgkmcnt(6)\n\t"
        "v_fma_f32 v78, -v18, v14, v78\n\t"
        "v_fma_f32 v79, -v18, v15, v79\n\t"
        "v_mfma_f32_16x16x32_bf16 v[60:63], v[232:235], v[36:39], v[60:63]\n\t"
        "ds_read_b128 v[32:35], %[xa] offset:29440\n\t"
        "v_mul_f32 v56, v19, v56\n\t"
        "v_mul_f32 v57, v19, v57\n\t"
        "v_mfma_f32_16x16x32_bf16 v[80:83], v[236:239], v[36:39], v[80:83]\n\t"
        "s_waitcnt lgkmcnt(6)\n\t"
        "v_mul_f32 v58, v19, v58\n\t"
        "v_mul_f32 v59, v19, v59\n\t"
        "v_mfma_f32_16x16x32_bf16 v[60:63], v[240:243], v[40:43], v[60:63]\n\t"
        "ds_read_b128 v[36:39], %[xa] offset:33536\n\t"
        "s_mov_b64 exec, 1\n\t"
        "ds_add_u32 %[fb], %[one] offset:%[oc1]\n\t"
        "s_mov_b64 exec, -1\n\t"
        "v_mul_f32 v76, v19, v76\n\t"
        "v_mul_f32 v77, v19, v77\n\t"
        "v_mfma_f32_16x16x32_bf16 v[80:83], v[244:247], v[40:43], v[80:83]\n\t"
        "s_waitcnt lgkmcnt(7)\n\t"
        "v_mul_f32 v78, v19, v78\n\t"
        "v_mul_f32 v79, v19, v79\n\t"
        "v_mfma_f32_16x16x32_bf16 v[60:63], v[248:251], v[44:47], v[60:63]\n\t"
        "v_cvt_pk_bf16_f32 v56, v56, v76\n\t"
        "v_cvt_pk_bf16_f32 v57, v57, v77\n\t"
        "v_mfma_f32_16x16x32_bf16 v[80:83], v[252:255], v[44:47], v[80:83]\n\t"
        "s_waitcnt lgkmcnt(6)\n\t"
        "v_cvt_pk_bf16_f32 v58, v58, v78\n\t"
        "v_cvt_pk_bf16_f32 v59, v59, v79\n\t"
        "v_mfma_f32_16x16x32_bf16 v[64:67], v[208:211], v[48:51], v[64:67]\n\t"
        "v_add_u32 v18, v16, v111\n\t"
        "v_mfma_f32_16x16x32_bf16 v[84:87], v[212:215], v[48:51], v[84:87]\n\t"
        "s_waitcnt lgkmcnt(5)\n\t"
        "v_mfma_f32_16x16x32_bf16 v[64:67], v[216:219], v[72:75], v[64:67]\n\t"
        "v_fma_f32 v60, -v20, v8, v60\n\t"
        "v_fma_f32 v61, -v20, v9, v61\n\t"
        "v_mfma_f32_16x16x32_bf16 v[84:87], v[220:223], v[72:75], v[84:87]\n\t"
        "s_waitcnt lgkmcnt(4)\n\t"
        "v_fma_f32 v62, -v20, v10, v62\n\t"
        "v_fma_f32 v63, -v20, v11, v63\n\t"
        "v_mfma_f32_16x16x32_bf16 v[64:67], v[224:227], v[24:27], v[64:67]\n\t"
        "v_fma_f32 v80, -v20, v12, v80\n\t"
        "v_fma_f32 v81, -v20, v13, v81\n\t"
        "v_mfma_f32_16x16x32_bf16 v[84:87], v[228:231], v[24:27], v[84:87]\n\t"
        "s_waitcnt lgkmcnt(3)\n\t"
        "v_fma_f32 v82, -v20, v14, v82\n\t"
        "v_fma_f32 v83, -v20, v15, v83\n\t"
        "v_mfma_f32_16x16x32_bf16 v[64:67], v[232:235], v[28:31], v[64:67]\n\t"
        "v_mul_f32 v60, v21, v60\n\t"
        "v_mul_f32 v61, v21, v61\n\t"
        "v_mfma_f32_16x16x32_bf16 v[84:87], v[236:239], v[28:31], v[84:87]\n\t"
        "s_waitcnt lgkmcnt(2)\n\t"
        "v_mul_f32 v62, v21, v62\n\t"
        "v_mul_f32 v63, v21, v63\n\t"
        "v_mfma_f32_16x16x32_bf16 v[64:67], v[240:243], v[32:35], v[64:67]\n\t"
        "v_mul_f32 v80, v21, v80\n\t"
        "v_mul_f32 v81, v21, v81\n\t"
        "v_mfma_f32_16x16x32_bf16 v[84:87], v[244:247], v[32:35], v[84:87]\n\t"
        "s_waitcnt lgkmcnt(1)\n\t"
        "v_mul_f32 v82, v21, v82\n\t"
        "v_mul_f32 v83, v21, v83\n\t"
        "v_mfma_f32_16x16x32_bf16 v[64:67], v[248:251], v[36:39], v[64:67]\n\t"
        "v_cvt_pk_bf16_f32 v60, v60, v80\n\t"
        "v_cvt_pk_bf16_f32 v61, v61, v81\n\t"
        "v_mfma_f32_16x16x32_bf16 v[84:87], v[252:255], v[36:39], v[84:87]\n\t"
        "v_cvt_pk_bf16_f32 v62, v62, v82\n\t"
        "v_cvt_pk_bf16_f32 v63, v63, v83\n\t"
        "s_setprio 0\n\t"
        "ds_read_b32 v99, %[fb] offset:%[omc]+4\n\t"
        "v_add_u32 v20, v18, v111\n\t"
        "v_fma_f32 v64, -v22, v8, v64\n\t"
        "v_fma_f32 v65, -v22, v9, v65\n\t"
        "v_fma_f32 v66, -v22, v10, v66\n\t"
        "v_fma_f32 v67, -v22, v11, v67\n\t"
        "v_fma_f32 v84, -v22, v12, v84\n\t"
        "v_fma_f32 v85, -v22, v13, v85\n\t"
        "v_fma_f32 v86, -v22, v14, v86\n\t"
        "v_fma_f32 v87, -v22, v15, v87\n\t"
        "v_mul_f32 v64, v23, v64\n\t"
        "v_mul_f32 v65, v23, v65\n\t"
        "v_mul_f32 v66, v23, v66\n\t"
        "v_mul_f32 v67, v23, v67\n\t"
        "v_mul_f32 v84, v23, v84\n\t"
        "v_mul_f32 v85, v23, v85\n\t"
        "v_mul_f32 v86, v23, v86\n\t"
        "v_mul_f32 v87, v23, v87\n\t"
        "v_cvt_pk_bf16_f32 v64, v64, v84\n\t"
        "v_cvt_pk_bf16_f32 v65, v65, v85\n\t"
        "v_cvt_pk_bf16_f32 v66, v66, v86\n\t"
        "v_cvt_pk_bf16_f32 v67, v67, v87\n\t"
        "v_add_u32 v22, v20, v111\n\t"
        "s_waitcnt lgkmcnt(0)\n\t"
        "Lu_%=:\n\t"
        "v_readfirstlane_b32 %[sf], v99\n\t"
        "s_cmp_ge_u32 %[sf], 14\n\t"
        "s_cbranch_scc1 Lv_%=\n\t"
        "s_cmp_lt_i32 %[scn], 1\n\t"
        "s_cbranch_scc1 Lv_%=\n\t"
        "s_sub_u32 %[scn], %[scn], 1\n\t"
        "s_sleep 4\n\t"
        "ds_read_b32 v99, %[fb] offset:%[omc]+4\n\t"
        "s_waitcnt lgkmcnt(0)\n\t"
        "s_branch Lu_%=\n\t"
        "Lv_%=:\n\t"
        "s_mov_b64 exec, %[sva]\n\t"
        "ds_write_b128 v16, v[52:55]\n\t"
        "ds_write_b128 v18, v[56:59]\n\t"
        "ds_write_b128 v20, v[60:63]\n\t"
        "ds_write_b128 v22, v[64:67]\n\t"
        "s_mov_b64 exec, %[sv0]\n\t"
        "ds_write_b128 v110, v[48:51]\n\t"
        "s_mov_b64 exec, -1\n\t"
        "s_branch Le_%=\n\t"
        "Ls16_%=:\n\t"
        "s_cmp_lt_i32 %[scn], 1\n\t"
        "s_cbranch_scc1 Lg16_%=\n\t"
        "s_sub_u32 %[scn], %[scn], 1\n\t"
        "ds_read_b32 %[fl], %[fb] offset:%[og0]+4\n\t"
        "s_waitcnt lgkmcnt(0)\n\t"
        "v_cmp_gt_u32 vcc, 2, %[fl]\n\t"
        "s_cbranch_vccnz Ls16_%=\n\t"
        "s_branch Lg16_%=\n\t"
        "Ls32_%=:\n\t"
        "s_cmp_lt_i32 %[scn], 1\n\t"
        "s_cbranch_scc1 Lg32_%=\n\t"
        "s_sub_u32 %[scn], %[scn], 1\n\t"
        "ds_read_b32 %[fl], %[fb] offset:%[og0]+8\n\t"
        "s_waitcnt lgkmcnt(0)\n\t"
        "v_cmp_gt_u32 vcc, 2, %[fl]\n\t"
        "s_cbranch_vccnz Ls32_%=\n\t"
        "s_branch Lg32_%=\n\t"
        "Ls48_%=:\n\t"
        "s_cmp_lt_i32 %[scn], 1\n\t"
        "s_cbranch_scc1 Lg48_%=\n\t"
        "s_sub_u32 %[scn], %[scn], 1\n\t"
        "ds_read_b32 %[fl], %[fb] offset:%[og0]+12\n\t"
        "s_waitcnt lgkmcnt(0)\n\t"
        "v_cmp_gt_u32 vcc, 2, %[fl]\n\t"
        "s_cbranch_vccnz Ls48_%=\n\t"
        "s_branch Lg48_%=\n\t"
        "Ls72_%=:\n\t"
        "s_cmp_lt_i32 %[scn], 1\n\t"
        "s_cbranch_scc1 Lg72_%=\n\t"
        "s_sub_u32 %[scn], %[scn], 1\n\t"
        "ds_read_b32 %[fl], %[fb] offset:%[og0]+16\n\t"
        "s_waitcnt lgkmcnt(0)\n\t"
        "v_cmp_gt_u32 vcc, 2, %[fl]\n\t"
        "s_cbranch_vccnz Ls72_%=\n\t"
        "s_branch Lg72_%=\n\t"
        "Ln_%=:\n\t"
        "s_cmp_lt_i32 %[scn], 1\n\t"
        "s_cbranch_scc1 Lo_%=\n\t"
        "s_sub_u32 %[scn], %[scn], 1\n\t"
        "s_sleep 1\n\t"
        "ds_read_b64 v[98:99], %[fb] offset:%[omc]\n\t"
        "ds_read_b64 v[16:17], %[xo0] offset:128\n\t"
        "ds_read_b64 v[18:19], %[xo0] offset:256\n\t"
        "ds_read_b64 v[20:21], %[xo0] offset:384\n\t"
        "ds_read_b64 v[22:23], %[xo0] offset:512\n\t"
        "s_waitcnt lgkmcnt(0)\n\t"
        "v_cmp_gt_u32 vcc, 5, v98\n\t"
        "s_cbranch_vccnz Ln_%=\n\t"
        "s_branch Lo_%=\n\t"
        "Le_%=:\n\t"
        : [xa] "=&v"(xa), [xh] "=&v"(xh), [xo0] "+v"(xo0), [one] "=&v"(one), [fl] "=&v"(fl), [sf] "=&s"(sf), [scn] "+s"(cn)
        : [fb] "v"(fb), [scs] "s"(cs), [sva] "s"(va), [sv0] "s"(v0), [omc] "n"(BC_MC), [oc1] "n"(BC_C1), [og0] "n"(BC_G0)
        : "memory", "scc", "vcc", "v8", "v9", "v10", "v11", "v12", "v13", "v14", "v15", "v16", "v17", "v18", "v19", "v20", "v21", "v22", "v23", "v24", "v25", "v26", "v27", "v28", "v29", "v30", "v31", "v32", "v33", "v34", "v35", "v36", "v37", "v38", "v39", "v40", "v41", "v42", "v43", "v44", "v45", "v46", "v47");
}

DEV void gemm1b(int fb, int xr, int xq, u32 w1, u32 cs, u64 va, u64 v0, u32& cn) {
    u32 sf, st; u64 mw, mx;
    asm volatile(
        "v_add_u32 v36, 73728, v107\n\t"
        "v_mov_b32 v35, 153344\n\t"
        "v_lshrrev_b32 v37, 8, v107\n\t"
        "v_lshl_add_u32 v37, v37, 5, %[scs]\n\t"
        "s_waitcnt lgkmcnt(0)\n\t"
        "ds_read_b128 v[48:51], v37 offset:0\n\t"
        "ds_read_b128 v[68:71], v37 offset:16\n\t"
        "v_lshrrev_b32 v37, 4, v111\n\t"
        "Lw_%=:\n\t"
        "ds_read_b32 v34, %[fb] offset:%[ogo]\n\t"
        "ds_read_b128 v[52:55], v36 offset:0\n\t"
        "ds_read_b128 v[56:59], v36 offset:1024\n\t"
        "ds_read_b128 v[60:63], v36 offset:2048\n\t"
        "ds_read_b128 v[64:67], v36 offset:3072\n\t"
        "s_sub_u32 %[scn], %[scn], 1\n\t"
        "s_waitcnt lgkmcnt(4)\n\t"
        "v_readfirstlane_b32 %[sf], v34\n\t"
        "s_cmp_lt_i32 %[scn], 1\n\t"
        "s_cbranch_scc1 Lg_%=\n\t"
        "s_cmp_lg_u32 %[sf], 0\n\t"
        "s_cbranch_scc1 Lg_%=\n\t"
        "s_sleep 1\n\t"
        "s_branch Lw_%=\n\t"
        "Lg_%=:\n\t"
        "ds_read_b128 v[72:75], v36 offset:4096\n\t"
        "ds_read_b128 v[76:79], v36 offset:5120\n\t"
        "ds_read_b128 v[80:83], v36 offset:6144\n\t"
        "ds_read_b128 v[84:87], v36 offset:7168\n\t"
        "s_lshr_b32 %[sf], %[sf], 8\n\t"
        "s_and_b32 %[st], %[sf], 15\n\t"
        "s_lshl_b64 %[smx], %[sm16], %[st]\n\t"
        "s_cmp_ge_u32 %[sf], 16\n\t"
        "s_cselect_b64 %[smw], %[sva], %[sv0]\n\t"
        "s_and_b64 %[smw], %[smw], %[sm16]\n\t"
        "s_lshr_b32 %[st], %[sf], 4\n\t"
        "s_cmp_eq_u32 %[st], %[sw1]\n\t"
        "s_cselect_b64 %[smx], %[smx], 0\n\t"
        "s_cmp_eq_u64 %[smw], 0\n\t"
        "s_cbranch_scc1 Le_%=\n\t"
        "s_waitcnt lgkmcnt(7)\n\t"
        "v_mfma_f32_16x16x32_bf16 v[24:27], v[112:115], v[52:55], 0\n\t"
        "v_mfma_f32_16x16x32_bf16 v[28:31], v[116:119], v[52:55], 0\n\t"
        "ds_read_b128 v[52:55], v36 offset:8192\n\t"
        "s_waitcnt lgkmcnt(7)\n\t"
        "v_mfma_f32_16x16x32_bf16 v[24:27], v[120:123], v[56:59], v[24:27]\n\t"
        "v_mfma_f32_16x16x32_bf16 v[28:31], v[124:127], v[56:59], v[28:31]\n\t"
        "ds_read_b128 v[56:59], v36 offset:9216\n\t"
        "v_mad_u32_u24 v37, v37, %[sf], v110\n\t"
        "s_waitcnt lgkmcnt(7)\n\t"
        "v_mfma_f32_16x16x32_bf16 v[24:27], v[128:131], v[60:63], v[24:27]\n\t"
        "v_mfma_f32_16x16x32_bf16 v[28:31], v[132:135], v[60:63], v[28:31]\n\t"
        "ds_read_b128 v[60:63], v36 offset:10240\n\t"
        "s_waitcnt lgkmcnt(7)\n\t"
        "v_mfma_f32_16x16x32_bf16 v[24:27], v[136:139], v[64:67], v[24:27]\n\t"
        "v_mfma_f32_16x16x32_bf16 v[28:31], v[140:143], v[64:67], v[28:31]\n\t"
        "ds_read_b128 v[64:67], v36 offset:11264\n\t"
        "s_waitcnt lgkmcnt(7)\n\t"
        "v_mfma_f32_16x16x32_bf16 v[24:27], v[144:147], v[72:75], v[24:27]\n\t"
        "v_mfma_f32_16x16x32_bf16 v[28:31], v[148:151], v[72:75], v[28:31]\n\t"
        "ds_read_b128 v[72:75], v36 offset:12288\n\t"
        "s_waitcnt lgkmcnt(7)\n\t"
        "v_mfma_f32_16x16x32_bf16 v[24:27], v[152:155], v[76:79], v[24:27]\n\t"
        "v_mfma_f32_16x16x32_bf16 v[28:31], v[156:159], v[76:79], v[28:31]\n\t"
        "ds_read_b128 v[76:79], v36 offset:13312\n\t"
        "s_waitcnt lgkmcnt(7)\n\t"
        "v_mfma_f32_16x16x32_bf16 v[24:27], v[160:163], v[80:83], v[24:27]\n\t"
        "v_mfma_f32_16x16x32_bf16 v[28:31], v[164:167], v[80:83], v[28:31]\n\t"
        "ds_read_b128 v[80:83], v36 offset:14336\n\t"
        "s_waitcnt lgkmcnt(7)\n\t"
        "v_mfma_f32_16x16x32_bf16 v[24:27], v[168:171], v[84:87], v[24:27]\n\t"
        "v_mfma_f32_16x16x32_bf16 v[28:31], v[172:175], v[84:87], v[28:31]\n\t"
        "ds_read_b128 v[84:87], v36 offset:15360\n\t"
        "s_waitcnt lgkmcnt(7)\n\t"
        "v_mfma_f32_16x16x32_bf16 v[24:27], v[176:179], v[52:55], v[24:27]\n\t"
        "v_mfma_f32_16x16x32_bf16 v[28:31], v[180:183], v[52:55], v[28:31]\n\t"
        "ds_read_b128 v[52:55], v36 offset:16384\n\t"
        "s_waitcnt lgkmcnt(7)\n\t"
        "v_mfma_f32_16x16x32_bf16 v[24:27], v[184:187], v[56:59], v[24:27]\n\t"
        "v_mfma_f32_16x16x32_bf16 v[28:31], v[188:191], v[56:59], v[28:31]\n\t"
        "ds_read_b128 v[56:59], v36 offset:17408\n\t"
        "s_waitcnt lgkmcnt(7)\n\t"
        "v_mfma_f32_16x16x32_bf16 v[24:27], v[192:195], v[60:63], v[24:27]\n\t"
        "v_mfma_f32_16x16x32_bf16 v[28:31], v[196:199], v[60:63], v[28:31]\n\t"
        "s_waitcnt lgkmcnt(6)\n\t"
        "v_mfma_f32_16x16x32_bf16 v[24:27], v[200:203], v[64:67], v[24:27]\n\t"
        "v_mfma_f32_16x16x32_bf16 v[28:31], v[204:207], v[64:67], v[28:31]\n\t"
        "s_waitcnt lgkmcnt(5)\n\t"
        "v_mfma_f32_16x16x32_bf16 v[24:27], v[208:211], v[72:75], v[24:27]\n\t"
        "v_mfma_f32_16x16x32_bf16 v[28:31], v[212:215], v[72:75], v[28:31]\n\t"
        "ds_read_b32 v34, %[fb] offset:%[ops]\n\t"
        "ds_read_b64 v[32:33], v35\n\t"
        "s_waitcnt lgkmcnt(6)\n\t"
        "v_mfma_f32_16x16x32_bf16 v[24:27], v[216:219], v[76:79], v[24:27]\n\t"
        "v_mfma_f32_16x16x32_bf16 v[28:31], v[220:223], v[76:79], v[28:31]\n\t"
        "s_waitcnt lgkmcnt(5)\n\t"
        "v_mfma_f32_16x16x32_bf16 v[24:27], v[224:227], v[80:83], v[24:27]\n\t"
        "v_mfma_f32_16x16x32_bf16 v[28:31], v[228:231], v[80:83], v[28:31]\n\t"
        "s_waitcnt lgkmcnt(4)\n\t"
        "v_mfma_f32_16x16x32_bf16 v[24:27], v[232:235], v[84:87], v[24:27]\n\t"
        "v_mfma_f32_16x16x32_bf16 v[28:31], v[236:239], v[84:87], v[28:31]\n\t"
        "s_waitcnt lgkmcnt(3)\n\t"
        "v_mfma_f32_16x16x32_bf16 v[24:27], v[240:243], v[52:55], v[24:27]\n\t"
        "v_mfma_f32_16x16x32_bf16 v[28:31], v[244:247], v[52:55], v[28:31]\n\t"
        "s_waitcnt lgkmcnt(2)\n\t"
        "v_mfma_f32_16x16x32_bf16 v[24:27], v[248:251], v[56:59], v[24:27]\n\t"
        "v_mfma_f32_16x16x32_bf16 v[28:31], v[252:255], v[56:59], v[28:31]\n\t"
        "Lm_%=:\n\t"
        "s_waitcnt lgkmcnt(0)\n\t"
        "v_readfirstlane_b32 %[st], v34\n\t"
        "s_cmp_ge_u32 %[st], %[sf]\n\t"
        "s_cbranch_scc1 Lo_%=\n\t"
        "s_cmp_lt_i32 %[scn], 1\n\t"
        "s_cbranch_scc1 Lo_%=\n\t"
        "s_sub_u32 %[scn], %[scn], 1\n\t"
        "ds_read_b32 v34, %[fb] offset:%[ops]\n\t"
        "ds_read_b64 v[32:33], v35\n\t"
        "s_branch Lm_%=\n\t"
        "Lo_%=:\n\t"
        "s_mov_b64 exec, %[smx]\n\t"
        "ds_read_b128 v[88:91], %[xr]\n\t"
        "ds_read_b128 v[92:95], %[xr] offset:64\n\t"
        "ds_read_b64 v[96:97], %[xq]\n\t"
        "s_mov_b64 exec, -1\n\t"
        "v_fma_f32 v24, -v32, v48, v24\n\t"
        "v_fma_f32 v25, -v32, v49, v25\n\t"
        "v_fma_f32 v26, -v32, v50, v26\n\t"
        "v_fma_f32 v27, -v32, v51, v27\n\t"
        "v_fma_f32 v28, -v32, v68, v28\n\t"
        "v_fma_f32 v29, -v32, v69, v29\n\t"
        "v_fma_f32 v30, -v32, v70, v30\n\t"
        "v_fma_f32 v31, -v32, v71, v31\n\t"
        "v_mul_f32 v24, v33, v24\n\t"
        "v_mul_f32 v25, v33, v25\n\t"
        "v_mul_f32 v26, v33, v26\n\t"
        "v_mul_f32 v27, v33, v27\n\t"
        "v_mul_f32 v28, v33, v28\n\t"
        "v_mul_f32 v29, v33, v29\n\t"
        "v_mul_f32 v30, v33, v30\n\t"
        "v_mul_f32 v31, v33, v31\n\t"
        "v_cvt_pk_bf16_f32 v24, v24, v28\n\t"
        "v_cvt_pk_bf16_f32 v25, v25, v29\n\t"
        "v_cvt_pk_bf16_f32 v26, v26, v30\n\t"
        "v_cvt_pk_bf16_f32 v27, v27, v31\n\t"
        "s_mov_b64 exec, %[smw]\n\t"
        "ds_write_b128 v37, v[24:27]\n\t"
        "s_mov_b64 exec, -1\n\t"
        "Le_%=:\n\t"
        "s_waitcnt lgkmcnt(0)\n\t"
        : [sf] "=&s"(sf), [st] "=&s"(st), [smw] "=&s"(mw), [smx] "=&s"(mx), [scn] "+s"(cn)
        : [fb] "v"(fb), [xr] "v"(xr), [xq] "v"(xq), [sw1] "s"(w1), [scs] "s"(cs), [sva] "s"(va), [sv0] "s"(v0), [sm16] "s"(0x0001000100010001ull), [ogo] "n"(BC_GO), [ops] "n"(BC_PST)
        : "memory", "scc", "v24", "v25", "v26", "v27", "v28", "v29", "v30", "v31", "v32", "v33", "v34", "v35", "v36", "v37");
}

DEV void att(const MidS& a) {
    u32 tm;
    asm volatile(
        "s_mov_b32 m0, %[sldx]\n\t"
        "s_nop 3\n\t"
        "s_waitcnt lgkmcnt(0)\n\t"
        "s_barrier\n\t"
        "ds_read_b64 v[36:37], v102 offset:0\n\t"
        "ds_read_b32 v38, v103 offset:0\n\t"
        "ds_read_b64 v[20:21], v100 offset:0\n\t"
        "ds_read_b32 v22, v101 offset:0\n\t"
        "ds_read_b64 v[24:25], v100 offset:4864\n\t"
        "ds_read_b32 v26, v101 offset:4864\n\t"
        "ds_read_b64 v[40:41], v102 offset:40\n\t"
        "ds_read_b32 v42, v103 offset:4\n\t"
        "ds_read_b64 v[28:29], v100 offset:40\n\t"
        "ds_read_b32 v30, v101 offset:4\n\t"
        "ds_read_b64 v[32:33], v100 offset:4904\n\t"
        "ds_read_b32 v34, v101 offset:4868\n\t"
        "ds_read_b64_tr_b16 v[52:53], v105\n\t"
        "ds_read_b64_tr_b16 v[54:55], v105 offset:4864\n\t"
        "s_bitcmp1_b32 %[sskip], 0\n\t"
        "s_cbranch_scc1 Le0_%=\n\t"
        "buffer_load_dwordx4 v107, %[ssrd], %[swx] offen sc1 lds\n\t"
        "buffer_load_dwordx4 v107, %[ssrd], %[swx] offen offset:1024 sc1 lds\n\t"
        "Le0_%=:\n\t"
        "v_mov_b32 v23, 0\n\t"
        "v_mov_b32 v27, 0\n\t"
        "v_mov_b32 v31, 0\n\t"
        "v_mov_b32 v35, 0\n\t"
        "v_mov_b32 v39, 0\n\t"
        "v_mov_b32 v43, 0\n\t"
        "v_mov_b32 v60, %[sc0]\n\t"
        "v_mov_b32 v61, %[sc0]\n\t"
        "v_mov_b32 v62, %[sc0]\n\t"
        "v_mov_b32 v63, %[sc0]\n\t"
        "v_mov_b32 v68, %[sc0]\n\t"
        "v_mov_b32 v69, %[sc0]\n\t"
        "v_mov_b32 v70, %[sc0]\n\t"
        "v_mov_b32 v71, %[sc0]\n\t"
        "s_waitcnt lgkmcnt(10)\n\t"
        "v_mfma_f32_16x16x32_bf16 v[60:63], v[20:23], v[36:39], v[60:63]\n\t"
        "ds_read_b64_tr_b16 v[44:45], v104\n\t"
        "ds_read_b64_tr_b16 v[46:47], v104 offset:4864\n\t"
        "s_waitcnt lgkmcnt(10)\n\t"
        "v_mfma_f32_16x16x32_bf16 v[64:67], v[24:27], v[36:39], 0\n\t"
        "ds_read_b64_tr_b16 v[48:49], v104 offset:40\n\t"
        "ds_read_b64_tr_b16 v[50:51], v104 offset:4904\n\t"
        "s_waitcnt lgkmcnt(8)\n\t"
        "v_mfma_f32_16x16x32_bf16 v[68:71], v[28:31], v[40:43], v[68:71]\n\t"
        "s_waitcnt lgkmcnt(6)\n\t"
        "v_mfma_f32_16x16x32_bf16 v[72:75], v[32:35], v[40:43], 0\n\t"
        "s_cbranch_scc1 Le1_%=\n\t"
        "buffer_load_dwordx4 v107, %[ssrd], %[swx] offen offset:2048 sc1 lds\n\t"
        "buffer_load_dwordx4 v107, %[ssrd], %[swx] offen offset:3072 sc1 lds\n\t"
        "Le1_%=:\n\t"
        "v_mov_b32 v56, 0x0c0c0100\n\t"
        "v_mov_b32 v57, 0x01000c0c\n\t"
        "v_mov_b32 v58, 0x0c0c0c0c\n\t"
        "v_cndmask_b32 v56, v56, v57, %[spar]\n\t"
        "v_add_u32 v57, 0x0202, v56\n\t"
        "v_add_u32 v59, 0x02020000, v56\n\t"
        "v_cndmask_b32 v57, v57, v59, %[spar]\n\t"
        "v_cndmask_b32 v76, v56, v58, %[ssa]\n\t"
        "v_cndmask_b32 v78, v58, v56, %[ssa]\n\t"
        "v_cndmask_b32 v77, v57, v58, %[ssb]\n\t"
        "v_cndmask_b32 v79, v58, v57, %[ssb]\n\t"
        "v_mov_b32 v17, 0x3f803f80\n\t"
        "v_cndmask_b32 v28, v60, v61, %[spar]\n\t"
        "v_cndmask_b32 v36, v68, v69, %[spar]\n\t"
        "v_cndmask_b32 v29, v62, v63, %[spar]\n\t"
        "v_cndmask_b32 v37, v70, v71, %[spar]\n\t"
        "v_cndmask_b32 v30, v64, v65, %[spar]\n\t"
        "v_cndmask_b32 v38, v72, v73, %[spar]\n\t"
        "v_cndmask_b32 v31, v66, v67, %[spar]\n\t"
        "v_cndmask_b32 v39, v74, v75, %[spar]\n\t"
        "v_cndmask_b32 v28, v28, v30, %[ssa]\n\t"
        "v_cndmask_b32 v36, v36, v38, %[ssa]\n\t"
        "v_cndmask_b32 v29, v29, v31, %[ssb]\n\t"
        "v_cndmask_b32 v37, v37, v39, %[ssb]\n\t"
        "v_max_f32 v30, v28, v29\n\t"
        "v_max_f32 v38, v36, v37\n\t"
        "v_max_f32 v31, v28, v29\n\t"
        "v_max_f32 v39, v36, v37\n\t"
        "s_waitcnt lgkmcnt(4)\n\t"
        "v_permlane16_swap_b32 v30, v31\n\t"
        "v_permlane16_swap_b32 v38, v39\n\t"
        "v_max_f32 v32, v30, v31\n\t"
        "v_max_f32 v40, v38, v39\n\t"
        "v_max_f32 v33, v30, v31\n\t"
        "v_max_f32 v41, v38, v39\n\t"
        "v_cndmask_b32 v56, v17, v52, %[sl2]\n\t"
        "v_permlane32_swap_b32 v32, v33\n\t"
        "v_permlane32_swap_b32 v40, v41\n\t"
        "v_max_f32 v30, v32, v33\n\t"
        "v_max_f32 v38, v40, v41\n\t"
        "v_sub_f32 v28, v28, v30\n\t"
        "v_sub_f32 v36, v36, v38\n\t"
        "v_sub_f32 v29, v29, v30\n\t"
        "v_sub_f32 v37, v37, v38\n\t"
        "v_mul_f32 v28, 0x3eae1a93, v28\n\t"
        "v_mul_f32 v36, 0x3eae1a93, v36\n\t"
        "v_mul_f32 v29, 0x3eae1a93, v29\n\t"
        "v_mul_f32 v37, 0x3eae1a93, v37\n\t"
        "v_exp_f32 v28, v28\n\t"
        "v_exp_f32 v36, v36\n\t"
        "v_exp_f32 v29, v29\n\t"
        "v_exp_f32 v37, v37\n\t"
        "v_cndmask_b32 v57, v17, v53, %[sl2]\n\t"
        "v_cndmask_b32 v58, v17, v54, %[sl2]\n\t"
        "v_cndmask_b32 v59, v17, v55, %[sl2]\n\t"
        "v_cndmask_b32 v52, v52, v17, %[sl2]\n\t"
        "v_cndmask_b32 v53, v53, v17, %[sl2]\n\t"
        "v_cndmask_b32 v54, v54, v17, %[sl2]\n\t"
        "v_cndmask_b32 v55, v55, v17, %[sl2]\n\t"
        "v_cvt_pk_bf16_f32 v28, v28, v29\n\t"
        "v_cvt_pk_bf16_f32 v36, v36, v37\n\t"
        "v_perm_b32 v20, v28, v28, v76\n\t"
        "v_perm_b32 v21, v28, v28, v77\n\t"
        "v_perm_b32 v22, v28, v28, v78\n\t"
        "v_perm_b32 v23, v28, v28, v79\n\t"
        "v_perm_b32 v24, v36, v36, v76\n\t"
        "v_perm_b32 v25, v36, v36, v77\n\t"
        "v_perm_b32 v26, v36, v36, v78\n\t"
        "v_perm_b32 v27, v36, v36, v79\n\t"
        "s_waitcnt lgkmcnt(0)\n\t"
        "v_mfma_f32_16x16x32_bf16 v[64:67], v[52:55], v[20:23], 0\n\t"
        "v_mfma_f32_16x16x32_bf16 v[60:63], v[44:47], v[20:23], 0\n\t"
        "v_mfma_f32_16x16x32_bf16 v[72:75], v[56:59], v[24:27], 0\n\t"
        "v_mfma_f32_16x16x32_bf16 v[68:71], v[48:51], v[24:27], 0\n\t"
        "v_lshrrev_b32 v17, 1, v107\n\t"
        "s_nop 3\n\t"
        "v_rcp_f32 v36, v66\n\t"
        "s_nop 0\n\t"
        "v_mul_f32 v60, v60, v36\n\t"
        "v_mul_f32 v61, v61, v36\n\t"
        "v_mul_f32 v62, v62, v36\n\t"
        "v_mul_f32 v63, v63, v36\n\t"
        "v_rcp_f32 v37, v72\n\t"
        "v_cvt_pk_bf16_f32 v80, v60, v61\n\t"
        "v_cvt_pk_bf16_f32 v81, v62, v63\n\t"
        "v_mul_f32 v68, v68, v37\n\t"
        "v_mul_f32 v69, v69, v37\n\t"
        "v_mul_f32 v70, v70, v37\n\t"
        "v_mul_f32 v71, v71, v37\n\t"
        "v_bfi_b32 v80, 1, %[slay], v80\n\t"
        "v_cvt_pk_bf16_f32 v82, v68, v69\n\t"
        "v_cvt_pk_bf16_f32 v83, v70, v71\n\t"
        "v_mul_f32 v64, v64, v36\n\t"
        "v_mul_f32 v65, v65, v36\n\t"
        "v_bfi_b32 v82, 1, %[slay], v82\n\t"
        "buffer_store_dwordx4 v[80:83], v107, %[ssrd], %[sst] offen sc1\n\t"
        "v_mul_f32 v74, v74, v37\n\t"
        "v_mul_f32 v75, v75, v37\n\t"
        "v_cvt_pk_bf16_f32 v84, v64, v65\n\t"
        "v_cvt_pk_bf16_f32 v85, v74, v75\n\t"
        "v_bfi_b32 v84, 1, %[slay], v84\n\t"
        "s_mov_b64 exec, 0xffff\n\t"
        "buffer_store_dwordx2 v[84:85], v17, %[ssrd], %[stt] offen sc1\n\t"
        "s_mov_b64 exec, -1\n\t"
        "s_waitcnt vmcnt(2)\n\t"
        "s_barrier\n\t"
        "s_cmp_eq_u32 %[smsk], 0\n\t"
        "s_cbranch_scc0 Lpr_%=\n\t"
        "s_setprio 1\n\t"
        "Lpr_%=:\n\t"
        : [stm] "=&s"(tm)
        : [ssrd] "s"(a.srd), [sst] "s"(a.st), [stt] "s"(a.tt), [sc0] "s"(a.c0), [swx] "s"(a.wx), [sldx] "s"(a.ldx), [sskip] "s"(a.skip), [smsk] "s"(a.msk), [swi] "s"(a.wi), [sld] "s"(a.ld), [sn7] "s"(a.n7), [slay] "s"(a.layer),
          [spar] "s"(0xAAAAAAAAAAAAAAAAull), [ssa] "s"(0xf000ff00fff0ffffull), [ssb] "s"(0xc000fc00ffc0fffcull), [sl2] "s"(0xCCCCCCCCCCCCCCCCull)
        : "memory", "scc", "vcc", "v14", "v15", "v16", "v17", "v18", "v19", "v20", "v21", "v22", "v23", "v24", "v25", "v26", "v27", "v28", "v29", "v30", "v31", "v32", "v33", "v34", "v35", "v36", "v37", "v38", "v39", "v40", "v41", "v42", "v43", "v44", "v45", "v46", "v47");
}
DEV void mid(const MidS& a, u32& cn) {
    u32 ob4, ob4x;
    asm volatile(
        "s_nop 4\n\t"
        "s_add_u32 %[sob4], %[sob], 4096\n\t"
        "v_add_u32 v19, %[stvd], v108\n\t"
        "s_cmp_lg_u32 %[sskip], 0\n\t"
        "s_cbranch_scc1 L6_%=\n\t"
        "s_mov_b32 m0, %[sld]\n\t"
        "s_add_u32 %[sob4x], %[swi], 4096\n\t"
        "buffer_load_dwordx4 v107, %[ssrd], %[swi] offen offset:0 sc1 lds\n\t"
        "buffer_load_dwordx4 v107, %[ssrd], %[swi] offen offset:1024 sc1 lds\n\t"
        "buffer_load_dwordx4 v107, %[ssrd], %[swi] offen offset:2048 sc1 lds\n\t"
        "buffer_load_dwordx4 v107, %[ssrd], %[swi] offen offset:3072 sc1 lds\n\t"
        "s_add_u32 m0, m0, 4096\n\t"
        "s_cmp_eq_u32 %[sn7], 0\n\t"
        "buffer_load_dwordx4 v107, %[ssrd], %[sob4x] offen offset:0 sc1 lds\n\t"
        "buffer_load_dwordx4 v107, %[ssrd], %[sob4x] offen offset:1024 sc1 lds\n\t"
        "s_cbranch_scc1 L6_%=\n\t"
        "buffer_load_dwordx4 v107, %[ssrd], %[sob4x] offen offset:2048 sc1 lds\n\t"
        "L6_%=:\n\t"
        "s_and_b32 %[sob4x], %[smsk], 0xffff0000\n\t"
        "buffer_load_dwordx4 v[40:43], v107, %[ssrd], %[sob] offen offset:0 sc1\n\t"
        "v_lshlrev_b32 v20, 16, v88\n\t"
        "v_and_b32 v24, %[sob4x], v88\n\t"
        "v_and_b32 v20, %[smsk], v20\n\t"
        "buffer_load_dwordx4 v[44:47], v107, %[ssrd], %[sob] offen offset:1024 sc1\n\t"
        "v_lshlrev_b32 v21, 16, v89\n\t"
        "v_and_b32 v25, %[sob4x], v89\n\t"
        "v_and_b32 v21, %[smsk], v21\n\t"
        "buffer_load_dwordx4 v[48:51], v107, %[ssrd], %[sob] offen offset:2048 sc1\n\t"
        "v_lshlrev_b32 v22, 16, v90\n\t"
        "v_and_b32 v26, %[sob4x], v90\n\t"
        "v_and_b32 v22, %[smsk], v22\n\t"
        "buffer_load_dwordx4 v[52:55], v107, %[ssrd], %[sob] offen offset:3072 sc1\n\t"
        "v_lshlrev_b32 v23, 16, v91\n\t"
        "v_and_b32 v27, %[sob4x], v91\n\t"
        "v_and_b32 v23, %[smsk], v23\n\t"
        "buffer_load_dwordx4 v[56:59], v107, %[ssrd], %[sob4] offen offset:0 sc1\n\t"
        "v_lshlrev_b32 v28, 16, v92\n\t"
        "v_and_b32 v32, %[sob4x], v92\n\t"
        "v_and_b32 v28, %[smsk], v28\n\t"
        "buffer_load_dwordx4 v[60:63], v107, %[ssrd], %[sob4] offen offset:1024 sc1\n\t"
        "v_lshlrev_b32 v29, 16, v93\n\t"
        "v_and_b32 v33, %[sob4x], v93\n\t"
        "v_and_b32 v29, %[smsk], v29\n\t"
        "buffer_load_dwordx4 v[64:67], v107, %[ssrd], %[sob4] offen offset:2048 sc1\n\t"
        "v_lshlrev_b32 v30, 16, v94\n\t"
        "v_and_b32 v34, %[sob4x], v94\n\t"
        "v_and_b32 v30, %[smsk], v30\n\t"
        "buffer_load_dwordx2 v[68:69], v108, %[ssrd], %[sob] offen offset:0 sc1\n\t"
        "buffer_load_dwordx2 v[70:71], v19, %[ssrd], %[sob] offen offset:0 sc1\n\t"
        "v_lshlrev_b32 v31, 16, v95\n\t"
        "v_and_b32 v35, %[sob4x], v95\n\t"
        "v_and_b32 v31, %[smsk], v31\n\t"
        "buffer_load_dwordx2 v[72:73], v108, %[ssrd], %[sob] offen offset:1024 sc1\n\t"
        "buffer_load_dwordx2 v[74:75], v19, %[ssrd], %[sob] offen offset:1024 sc1\n\t"
        "v_lshlrev_b32 v36, 16, v96\n\t"
        "v_and_b32 v37, %[sob4x], v96\n\t"
        "v_and_b32 v36, %[smsk], v36\n\t"
        "v_lshlrev_b32 v38, 16, v97\n\t"
        "v_and_b32 v39, %[sob4x], v97\n\t"
        "v_and_b32 v38, %[smsk], v38\n\t"
        "v_mov_b32 v15, %[swc]\n\t"
        "v_mov_b32 v16, 1\n\t"
        "v_add_u32 v18, %[slb], v107\n\t"
        "v_add_u32 v14, %[slb], v106\n\t"
        "ds_read_b128 v[76:79], v18 offset:0\n\t"
        "ds_read_b128 v[80:83], v18 offset:1024\n\t"
        "ds_read_b128 v[84:87], v18 offset:2048\n\t"
        "ds_read_b128 v[88:91], v18 offset:3072\n\t"
        "ds_read_b128 v[92:95], v14 offset:0\n\t"
        "ds_read_b128 v[96:99], v18 offset:4608\n\t"
        "s_waitcnt vmcnt(11)\n\t"
        "s_mov_b64 exec, 1\n\t"
        "ds_add_u32 v15, v16\n\t"
        "s_mov_b64 exec, -1\n\t"
        "Lr0_%=:\n\t"
        "s_waitcnt vmcnt(10)\n\t"
        "v_bitop3_b32 v17, v40, v42, %[slay] bitop3:0x7e\n\t"
        "v_and_b32 v17, 1, v17\n\t"
        "v_cmp_eq_u32 vcc, 1, v17\n\t"
        "s_cbranch_vccnz Lx0_%=\n\t"
        "Ld0_%=:\n\t"
        "s_waitcnt lgkmcnt(6)\n\t"
        "v_mfma_f32_16x16x32_bf16 v[20:23], v[76:79], v[40:43], v[20:23]\n\t"
        "ds_read_b128 v[76:79], v18 offset:5632\n\t"
        "s_waitcnt lgkmcnt(6)\n\t"
        "v_mfma_f32_16x16x32_bf16 v[24:27], v[80:83], v[40:43], v[24:27]\n\t"
        "ds_read_b128 v[80:83], v18 offset:6656\n\t"
        "s_waitcnt lgkmcnt(6)\n\t"
        "v_mfma_f32_16x16x32_bf16 v[28:31], v[84:87], v[40:43], v[28:31]\n\t"
        "s_waitcnt vmcnt(9)\n\t"
        "v_bitop3_b32 v17, v44, v46, %[slay] bitop3:0x7e\n\t"
        "v_and_b32 v17, 1, v17\n\t"
        "v_cmp_eq_u32 vcc, 1, v17\n\t"
        "ds_read_b128 v[84:87], v18 offset:7680\n\t"
        "s_waitcnt lgkmcnt(6)\n\t"
        "v_mfma_f32_16x16x32_bf16 v[32:35], v[88:91], v[40:43], v[32:35]\n\t"
        "ds_read_b128 v[88:91], v14 offset:4608\n\t"
        "s_waitcnt lgkmcnt(6)\n\t"
        "v_mfma_f32_16x16x32_bf16 v[36:39], v[92:95], v[40:43], v[36:39]\n\t"
        "s_cbranch_vccnz Lx1_%=\n\t"
        "Ld1_%=:\n\t"
        "ds_read_b32 v16, v15\n\t"
        "ds_read_b128 v[92:95], v18 offset:9216\n\t"
        "ds_read_b128 v[40:43], v18 offset:10240\n\t"
        "s_waitcnt lgkmcnt(8)\n\t"
        "v_mfma_f32_16x16x32_bf16 v[20:23], v[96:99], v[44:47], v[20:23]\n\t"
        "ds_read_b128 v[96:99], v18 offset:11264\n\t"
        "s_waitcnt lgkmcnt(7)\n\t"
        "v_mfma_f32_16x16x32_bf16 v[24:27], v[76:79], v[44:47], v[24:27]\n\t"
        "ds_read_b128 v[76:79], v18 offset:12288\n\t"
        "s_waitcnt lgkmcnt(7)\n\t"
        "v_mfma_f32_16x16x32_bf16 v[28:31], v[80:83], v[44:47], v[28:31]\n\t"
        "s_waitcnt vmcnt(8)\n\t"
        "v_bitop3_b32 v17, v48, v50, %[slay] bitop3:0x7e\n\t"
        "v_and_b32 v17, 1, v17\n\t"
        "v_cmp_eq_u32 vcc, 1, v17\n\t"
        "ds_read_b128 v[80:83], v14 offset:9216\n\t"
        "s_waitcnt lgkmcnt(7)\n\t"
        "v_mfma_f32_16x16x32_bf16 v[32:35], v[84:87], v[44:47], v[32:35]\n\t"
        "s_waitcnt lgkmcnt(5)\n\t"
        "Lwc_%=:\n\t"
        "v_readfirstlane_b32 %[sob4x], v16\n\t"
        "s_cmp_lt_u32 %[sob4x], %[swt]\n\t"
        "s_cbranch_scc1 Lws_%=\n\t"
        "Lwg_%=:\n\t"
        "ds_read_b128 v[84:87], v18 offset:13824\n\t"
        "v_mfma_f32_16x16x32_bf16 v[36:39], v[88:91], v[44:47], v[36:39]\n\t"
        "s_cbranch_vccnz Lx2_%=\n\t"
        "Ld2_%=:\n\t"
        "ds_read_b128 v[88:91], v18 offset:14848\n\t"
        "ds_read_b128 v[44:47], v18 offset:15872\n\t"
        "s_waitcnt lgkmcnt(7)\n\t"
        "v_mfma_f32_16x16x32_bf16 v[20:23], v[92:95], v[48:51], v[20:23]\n\t"
        "ds_read_b128 v[92:95], v18 offset:16896\n\t"
        "s_waitcnt lgkmcnt(7)\n\t"
        "v_mfma_f32_16x16x32_bf16 v[24:27], v[40:43], v[48:51], v[24:27]\n\t"
        "ds_read_b128 v[40:43], v14 offset:13824\n\t"
        "s_waitcnt lgkmcnt(7)\n\t"
        "v_mfma_f32_16x16x32_bf16 v[28:31], v[96:99], v[48:51], v[28:31]\n\t"
        "s_waitcnt vmcnt(7)\n\t"
        "v_bitop3_b32 v17, v52, v54, %[slay] bitop3:0x7e\n\t"
        "v_and_b32 v17, 1, v17\n\t"
        "v_cmp_eq_u32 vcc, 1, v17\n\t"
        "ds_read_b128 v[96:99], v18 offset:18432\n\t"
        "s_waitcnt lgkmcnt(7)\n\t"
        "v_mfma_f32_16x16x32_bf16 v[32:35], v[76:79], v[48:51], v[32:35]\n\t"
        "ds_read_b128 v[76:79], v18 offset:19456\n\t"
        "s_waitcnt lgkmcnt(7)\n\t"
        "v_mfma_f32_16x16x32_bf16 v[36:39], v[80:83], v[48:51], v[36:39]\n\t"
        "s_cbranch_vccnz Lx3_%=\n\t"
        "Ld3_%=:\n\t"
        "ds_read_b128 v[80:83], v18 offset:20480\n\t"
        "ds_read_b128 v[48:51], v18 offset:21504\n\t"
        "s_waitcnt lgkmcnt(8)\n\t"
        "v_mfma_f32_16x16x32_bf16 v[20:23], v[84:87], v[52:55], v[20:23]\n\t"
        "ds_read_b128 v[84:87], v14 offset:18432\n\t"
        "s_waitcnt lgkmcnt(8)\n\t"
        "v_mfma_f32_16x16x32_bf16 v[24:27], v[88:91], v[52:55], v[24:27]\n\t"
        "v_add_u32 v18, %[sl1d], v18\n\t"
        "v_add_u32 v14, %[sl1d], v14\n\t"
        "ds_read_b128 v[88:91], v18 offset:0\n\t"
        "s_waitcnt lgkmcnt(8)\n\t"
        "v_mfma_f32_16x16x32_bf16 v[28:31], v[44:47], v[52:55], v[28:31]\n\t"
        "s_waitcnt vmcnt(6)\n\t"
        "v_bitop3_b32 v17, v56, v58, %[slay] bitop3:0x7e\n\t"
        "v_and_b32 v17, 1, v17\n\t"
        "v_cmp_eq_u32 vcc, 1, v17\n\t"
        "ds_read_b128 v[44:47], v18 offset:1024\n\t"
        "s_waitcnt lgkmcnt(8)\n\t"
        "v_mfma_f32_16x16x32_bf16 v[32:35], v[92:95], v[52:55], v[32:35]\n\t"
        "ds_read_b128 v[92:95], v18 offset:2048\n\t"
        "s_waitcnt lgkmcnt(8)\n\t"
        "v_mfma_f32_16x16x32_bf16 v[36:39], v[40:43], v[52:55], v[36:39]\n\t"
        "s_cbranch_vccnz Lx4_%=\n\t"
        "Ld4_%=:\n\t"
        "ds_read_b128 v[40:43], v18 offset:3072\n\t"
        "s_waitcnt lgkmcnt(8)\n\t"
        "v_mfma_f32_16x16x32_bf16 v[20:23], v[96:99], v[56:59], v[20:23]\n\t"
        "ds_read_b128 v[52:55], v14 offset:0\n\t"
        "s_waitcnt lgkmcnt(8)\n\t"
        "v_mfma_f32_16x16x32_bf16 v[24:27], v[76:79], v[56:59], v[24:27]\n\t"
        "ds_read_b128 v[96:99], v18 offset:4608\n\t"
        "s_waitcnt lgkmcnt(8)\n\t"
        "v_mfma_f32_16x16x32_bf16 v[28:31], v[80:83], v[56:59], v[28:31]\n\t"
        "s_waitcnt vmcnt(5)\n\t"
        "v_bitop3_b32 v17, v60, v62, %[slay] bitop3:0x7e\n\t"
        "v_and_b32 v17, 1, v17\n\t"
        "v_cmp_eq_u32 vcc, 1, v17\n\t"
        "ds_read_b128 v[76:79], v18 offset:5632\n\t"
        "s_waitcnt lgkmcnt(8)\n\t"
        "v_mfma_f32_16x16x32_bf16 v[32:35], v[48:51], v[56:59], v[32:35]\n\t"
        "ds_read_b128 v[80:83], v18 offset:6656\n\t"
        "s_waitcnt lgkmcnt(8)\n\t"
        "v_mfma_f32_16x16x32_bf16 v[36:39], v[84:87], v[56:59], v[36:39]\n\t"
        "s_cbranch_vccnz Lx5_%=\n\t"
        "Ld5_%=:\n\t"
        "ds_read_b128 v[48:51], v18 offset:7680\n\t"
        "s_waitcnt lgkmcnt(8)\n\t"
        "v_mfma_f32_16x16x32_bf16 v[20:23], v[88:91], v[60:63], v[20:23]\n\t"
        "ds_read_b128 v[84:87], v14 offset:4608\n\t"
        "s_waitcnt lgkmcnt(8)\n\t"
        "v_mfma_f32_16x16x32_bf16 v[24:27], v[44:47], v[60:63], v[24:27]\n\t"
        "ds_read_b128 v[56:59], v18 offset:9216\n\t"
        "s_waitcnt lgkmcnt(8)\n\t"
        "v_mfma_f32_16x16x32_bf16 v[28:31], v[92:95], v[60:63], v[28:31]\n\t"
        "s_waitcnt vmcnt(4)\n\t"
        "v_bitop3_b32 v17, v64, v66, %[slay] bitop3:0x7e\n\t"
        "v_and_b32 v17, 1, v17\n\t"
        "v_cmp_eq_u32 vcc, 1, v17\n\t"
        "ds_read_b128 v[88:91], v18 offset:10240\n\t"
        "s_waitcnt lgkmcnt(8)\n\t"
        "v_mfma_f32_16x16x32_bf16 v[32:35], v[40:43], v[60:63], v[32:35]\n\t"
        "ds_read_b128 v[44:47], v18 offset:11264\n\t"
        "s_waitcnt lgkmcnt(8)\n\t"
        "v_mfma_f32_16x16x32_bf16 v[36:39], v[52:55], v[60:63], v[36:39]\n\t"
        "s_cbranch_vccnz Lx6_%=\n\t"
        "Ld6_%=:\n\t"
        "ds_read_b128 v[92:95], v18 offset:12288\n\t"
        "s_waitcnt lgkmcnt(8)\n\t"
        "v_mfma_f32_16x16x32_bf16 v[20:23], v[96:99], v[64:67], v[20:23]\n\t"
        "ds_read_b128 v[40:43], v14 offset:9216\n\t"
        "s_waitcnt lgkmcnt(8)\n\t"
        "v_mfma_f32_16x16x32_bf16 v[24:27], v[76:79], v[64:67], v[24:27]\n\t"
        "ds_read_b128 v[52:55], v18 offset:13824\n\t"
        "s_waitcnt lgkmcnt(8)\n\t"
        "v_mfma_f32_16x16x32_bf16 v[28:31], v[80:83], v[64:67], v[28:31]\n\t"
        "s_waitcnt vmcnt(2)\n\t"
        "v_bitop3_b32 v17, v68, v70, %[slay] bitop3:0x7e\n\t"
        "v_and_b32 v17, 1, v17\n\t"
        "v_cmp_eq_u32 vcc, 1, v17\n\t"
        "ds_read_b128 v[60:63], v18 offset:14848\n\t"
        "s_waitcnt lgkmcnt(8)\n\t"
        "v_mfma_f32_16x16x32_bf16 v[32:35], v[48:51], v[64:67], v[32:35]\n\t"
        "ds_read_b128 v[96:99], v18 offset:15872\n\t"
        "s_waitcnt lgkmcnt(8)\n\t"
        "v_mfma_f32_16x16x32_bf16 v[36:39], v[84:87], v[64:67], v[36:39]\n\t"
        "s_cbranch_vccnz Lx7_%=\n\t"
        "Ld7_%=:\n\t"
        "ds_read_b128 v[76:79], v18 offset:16896\n\t"
        "s_waitcnt lgkmcnt(8)\n\t"
        "v_mfma_f32_16x16x32_bf16 v[20:23], v[56:59], v[68:71], v[20:23]\n\t"
        "ds_read_b128 v[80:83], v14 offset:13824\n\t"
        "s_waitcnt lgkmcnt(8)\n\t"
        "v_mfma_f32_16x16x32_bf16 v[24:27], v[88:91], v[68:71], v[24:27]\n\t"
        "s_waitcnt lgkmcnt(7)\n\t"
        "v_mfma_f32_16x16x32_bf16 v[28:31], v[44:47], v[68:71], v[28:31]\n\t"
        "s_waitcnt vmcnt(0)\n\t"
        "v_bitop3_b32 v17, v72, v74, %[slay] bitop3:0x7e\n\t"
        "v_and_b32 v17, 1, v17\n\t"
        "v_cmp_eq_u32 vcc, 1, v17\n\t"
        "s_waitcnt lgkmcnt(6)\n\t"
        "v_mfma_f32_16x16x32_bf16 v[32:35], v[92:95], v[68:71], v[32:35]\n\t"
        "s_waitcnt lgkmcnt(5)\n\t"
        "v_mfma_f32_16x16x32_bf16 v[36:39], v[40:43], v[68:71], v[36:39]\n\t"
        "s_cbranch_vccnz Lx8_%=\n\t"
        "Ld8_%=:\n\t"
        "s_waitcnt lgkmcnt(4)\n\t"
        "v_mfma_f32_16x16x32_bf16 v[20:23], v[52:55], v[72:75], v[20:23]\n\t"
        "s_waitcnt lgkmcnt(3)\n\t"
        "v_mfma_f32_16x16x32_bf16 v[24:27], v[60:63], v[72:75], v[24:27]\n\t"
        "v_add_u32 v18, %[sscr], v107\n\t"
        "v_mov_b32 v15, 153984\n\t"
        "s_cmp_eq_u32 %[smsk], 0\n\t"
        "s_waitcnt lgkmcnt(2)\n\t"
        "v_mfma_f32_16x16x32_bf16 v[28:31], v[96:99], v[72:75], v[28:31]\n\t"
        "s_waitcnt lgkmcnt(1)\n\t"
        "v_mfma_f32_16x16x32_bf16 v[32:35], v[76:79], v[72:75], v[32:35]\n\t"
        "s_waitcnt lgkmcnt(0)\n\t"
        "v_mfma_f32_16x16x32_bf16 v[36:39], v[80:83], v[72:75], v[36:39]\n\t"
        "s_cbranch_scc0 Lk0_%=\n\t"
        "v_mov_b32 v16, 1\n\t"
        "ds_write_b128 v18, v[20:23] offset:0\n\t"
        "ds_write_b128 v18, v[24:27] offset:1024\n\t"
        "ds_write_b128 v18, v[28:31] offset:2048\n\t"
        "s_nop 0\n\t"
        "ds_write_b128 v18, v[32:35] offset:3072\n\t"
        "s_mov_b32 exec_hi, 0\n\t"
        "ds_write_b128 v18, v[36:39] offset:4096\n\t"
        "s_mov_b32 exec_lo, 1\n\t"
        "ds_add_u32 v15, v16 offset:%[opc]\n\t"
        "s_mov_b64 exec, -1\n\t"
        "s_branch Lend_%=\n\t"
        "Lx0_%=:\n\t"
        "s_cmp_eq_u32 %[scn], 0\n\t"
        "s_cbranch_scc1 Ld0_%=\n\t"
        "s_sub_u32 %[scn], %[scn], 1\n\t"
        "buffer_load_dwordx4 v[40:43], v107, %[ssrd], %[sob] offen offset:0 sc1\n\t"
        "buffer_load_dwordx4 v[44:47], v107, %[ssrd], %[sob] offen offset:1024 sc1\n\t"
        "buffer_load_dwordx4 v[48:51], v107, %[ssrd], %[sob] offen offset:2048 sc1\n\t"
        "buffer_load_dwordx4 v[52:55], v107, %[ssrd], %[sob] offen offset:3072 sc1\n\t"
        "buffer_load_dwordx4 v[56:59], v107, %[ssrd], %[sob4] offen offset:0 sc1\n\t"
        "buffer_load_dwordx4 v[60:63], v107, %[ssrd], %[sob4] offen offset:1024 sc1\n\t"
        "buffer_load_dwordx4 v[64:67], v107, %[ssrd], %[sob4] offen offset:2048 sc1\n\t"
        "buffer_load_dwordx2 v[68:69], v108, %[ssrd], %[sob] offen offset:0 sc1\n\t"
        "buffer_load_dwordx2 v[70:71], v19, %[ssrd], %[sob] offen offset:0 sc1\n\t"
        "buffer_load_dwordx2 v[72:73], v108, %[ssrd], %[sob] offen offset:1024 sc1\n\t"
        "buffer_load_dwordx2 v[74:75], v19, %[ssrd], %[sob] offen offset:1024 sc1\n\t"
        "s_waitcnt vmcnt(10)\n\t"
        "v_bitop3_b32 v17, v40, v42, %[slay] bitop3:0x7e\n\t"
        "v_and_b32 v17, 1, v17\n\t"
        "v_cmp_eq_u32 vcc, 1, v17\n\t"
        "s_cbranch_vccnz Lx0_%=\n\t"
        "s_branch Ld0_%=\n\t"
        "Lx1_%=:\n\t"
        "s_cmp_eq_u32 %[scn], 0\n\t"
        "s_cbranch_scc1 Ld1_%=\n\t"
        "s_sub_u32 %[scn], %[scn], 1\n\t"
        "buffer_load_dwordx4 v[44:47], v107, %[ssrd], %[sob] offen offset:1024 sc1\n\t"
        "buffer_load_dwordx4 v[48:51], v107, %[ssrd], %[sob] offen offset:2048 sc1\n\t"
        "buffer_load_dwordx4 v[52:55], v107, %[ssrd], %[sob] offen offset:3072 sc1\n\t"
        "buffer_load_dwordx4 v[56:59], v107, %[ssrd], %[sob4] offen offset:0 sc1\n\t"
        "buffer_load_dwordx4 v[60:63], v107, %[ssrd], %[sob4] offen offset:1024 sc1\n\t"
        "buffer_load_dwordx4 v[64:67], v107, %[ssrd], %[sob4] offen offset:2048 sc1\n\t"
        "buffer_load_dwordx2 v[68:69], v108, %[ssrd], %[sob] offen offset:0 sc1\n\t"
        "buffer_load_dwordx2 v[70:71], v19, %[ssrd], %[sob] offen offset:0 sc1\n\t"
        "buffer_load_dwordx2 v[72:73], v108, %[ssrd], %[sob] offen offset:1024 sc1\n\t"
        "buffer_load_dwordx2 v[74:75], v19, %[ssrd], %[sob] offen offset:1024 sc1\n\t"
        "s_waitcnt vmcnt(9)\n\t"
        "v_bitop3_b32 v17, v44, v46, %[slay] bitop3:0x7e\n\t"
        "v_and_b32 v17, 1, v17\n\t"
        "v_cmp_eq_u32 vcc, 1, v17\n\t"
        "s_cbranch_vccnz Lx1_%=\n\t"
        "s_branch Ld1_%=\n\t"
        "Lx2_%=:\n\t"
        "s_cmp_eq_u32 %[scn], 0\n\t"
        "s_cbranch_scc1 Ld2_%=\n\t"
        "s_sub_u32 %[scn], %[scn], 1\n\t"
        "buffer_load_dwordx4 v[48:51], v107, %[ssrd], %[sob] offen offset:2048 sc1\n\t"
        "buffer_load_dwordx4 v[52:55], v107, %[ssrd], %[sob] offen offset:3072 sc1\n\t"
        "buffer_load_dwordx4 v[56:59], v107, %[ssrd], %[sob4] offen offset:0 sc1\n\t"
        "buffer_load_dwordx4 v[60:63], v107, %[ssrd], %[sob4] offen offset:1024 sc1\n\t"
        "buffer_load_dwordx4 v[64:67], v107, %[ssrd], %[sob4] offen offset:2048 sc1\n\t"
        "buffer_load_dwordx2 v[68:69], v108, %[ssrd], %[sob] offen offset:0 sc1\n\t"
        "buffer_load_dwordx2 v[70:71], v19, %[ssrd], %[sob] offen offset:0 sc1\n\t"
        "buffer_load_dwordx2 v[72:73], v108, %[ssrd], %[sob] offen offset:1024 sc1\n\t"
        "buffer_load_dwordx2 v[74:75], v19, %[ssrd], %[sob] offen offset:1024 sc1\n\t"
        "s_waitcnt vmcnt(8)\n\t"
        "v_bitop3_b32 v17, v48, v50, %[slay] bitop3:0x7e\n\t"
        "v_and_b32 v17, 1, v17\n\t"
        "v_cmp_eq_u32 vcc, 1, v17\n\t"
        "s_cbranch_vccnz Lx2_%=\n\t"
        "s_branch Ld2_%=\n\t"
        "Lx3_%=:\n\t"
        "s_cmp_eq_u32 %[scn], 0\n\t"
        "s_cbranch_scc1 Ld3_%=\n\t"
        "s_sub_u32 %[scn], %[scn], 1\n\t"
        "buffer_load_dwordx4 v[52:55], v107, %[ssrd], %[sob] offen offset:3072 sc1\n\t"
        "buffer_load_dwordx4 v[56:59], v107, %[ssrd], %[sob4] offen offset:0 sc1\n\t"
        "buffer_load_dwordx4 v[60:63], v107, %[ssrd], %[sob4] offen offset:1024 sc1\n\t"
        "buffer_load_dwordx4 v[64:67], v107, %[ssrd], %[sob4] offen offset:2048 sc1\n\t"
        "buffer_load_dwordx2 v[68:69], v108, %[ssrd], %[sob] offen offset:0 sc1\n\t"
        "buffer_load_dwordx2 v[70:71], v19, %[ssrd], %[sob] offen offset:0 sc1\n\t"
        "buffer_load_dwordx2 v[72:73], v108, %[ssrd], %[sob] offen offset:1024 sc1\n\t"
        "buffer_load_dwordx2 v[74:75], v19, %[ssrd], %[sob] offen offset:1024 sc1\n\t"
        "s_waitcnt vmcnt(7)\n\t"
        "v_bitop3_b32 v17, v52, v54, %[slay] bitop3:0x7e\n\t"
        "v_and_b32 v17, 1, v17\n\t"
        "v_cmp_eq_u32 vcc, 1, v17\n\t"
        "s_cbranch_vccnz Lx3_%=\n\t"
        "s_branch Ld3_%=\n\t"
        "Lx4_%=:\n\t"
        "s_cmp_eq_u32 %[scn], 0\n\t"
        "s_cbranch_scc1 Ld4_%=\n\t"
        "s_sub_u32 %[scn], %[scn], 1\n\t"
        "buffer_load_dwordx4 v[56:59], v107, %[ssrd], %[sob4] offen offset:0 sc1\n\t"
        "buffer_load_dwordx4 v[60:63], v107, %[ssrd], %[sob4] offen offset:1024 sc1\n\t"
        "buffer_load_dwordx4 v[64:67], v107, %[ssrd], %[sob4] offen offset:2048 sc1\n\t"
        "buffer_load_dwordx2 v[68:69], v108, %[ssrd], %[sob] offen offset:0 sc1\n\t"
        "buffer_load_dwordx2 v[70:71], v19, %[ssrd], %[sob] offen offset:0 sc1\n\t"
        "buffer_load_dwordx2 v[72:73], v108, %[ssrd], %[sob] offen offset:1024 sc1\n\t"
        "buffer_load_dwordx2 v[74:75], v19, %[ssrd], %[sob] offen offset:1024 sc1\n\t"
        "s_waitcnt vmcnt(6)\n\t"
        "v_bitop3_b32 v17, v56, v58, %[slay] bitop3:0x7e\n\t"
        "v_and_b32 v17, 1, v17\n\t"
        "v_cmp_eq_u32 vcc, 1, v17\n\t"
        "s_cbranch_vccnz Lx4_%=\n\t"
        "s_branch Ld4_%=\n\t"
        "Lx5_%=:\n\t"
        "s_cmp_eq_u32 %[scn], 0\n\t"
        "s_cbranch_scc1 Ld5_%=\n\t"
        "s_sub_u32 %[scn], %[scn], 1\n\t"
        "buffer_load_dwordx4 v[60:63], v107, %[ssrd], %[sob4] offen offset:1024 sc1\n\t"
        "buffer_load_dwordx4 v[64:67], v107, %[ssrd], %[sob4] offen offset:2048 sc1\n\t"
        "buffer_load_dwordx2 v[68:69], v108, %[ssrd], %[sob] offen offset:0 sc1\n\t"
        "buffer_load_dwordx2 v[70:71], v19, %[ssrd], %[sob] offen offset:0 sc1\n\t"
        "buffer_load_dwordx2 v[72:73], v108, %[ssrd], %[sob] offen offset:1024 sc1\n\t"
        "buffer_load_dwordx2 v[74:75], v19, %[ssrd], %[sob] offen offset:1024 sc1\n\t"
        "s_waitcnt vmcnt(5)\n\t"
        "v_bitop3_b32 v17, v60, v62, %[slay] bitop3:0x7e\n\t"
        "v_and_b32 v17, 1, v17\n\t"
        "v_cmp_eq_u32 vcc, 1, v17\n\t"
        "s_cbranch_vccnz Lx5_%=\n\t"
        "s_branch Ld5_%=\n\t"
        "Lx6_%=:\n\t"
        "s_cmp_eq_u32 %[scn], 0\n\t"
        "s_cbranch_scc1 Ld6_%=\n\t"
        "s_sub_u32 %[scn], %[scn], 1\n\t"
        "buffer_load_dwordx4 v[64:67], v107, %[ssrd], %[sob4] offen offset:2048 sc1\n\t"
        "buffer_load_dwordx2 v[68:69], v108, %[ssrd], %[sob] offen offset:0 sc1\n\t"
        "buffer_load_dwordx2 v[70:71], v19, %[ssrd], %[sob] offen offset:0 sc1\n\t"
        "buffer_load_dwordx2 v[72:73], v108, %[ssrd], %[sob] offen offset:1024 sc1\n\t"
        "buffer_load_dwordx2 v[74:75], v19, %[ssrd], %[sob] offen offset:1024 sc1\n\t"
        "s_waitcnt vmcnt(4)\n\t"
        "v_bitop3_b32 v17, v64, v66, %[slay] bitop3:0x7e\n\t"
        "v_and_b32 v17, 1, v17\n\t"
        "v_cmp_eq_u32 vcc, 1, v17\n\t"
        "s_cbranch_vccnz Lx6_%=\n\t"
        "s_branch Ld6_%=\n\t"
        "Lx7_%=:\n\t"
        "s_cmp_eq_u32 %[scn], 0\n\t"
        "s_cbranch_scc1 Ld7_%=\n\t"
        "s_sub_u32 %[scn], %[scn], 1\n\t"
        "buffer_load_dwordx2 v[68:69], v108, %[ssrd], %[sob] offen offset:0 sc1\n\t"
        "buffer_load_dwordx2 v[70:71], v19, %[ssrd], %[sob] offen offset:0 sc1\n\t"
        "buffer_load_dwordx2 v[72:73], v108, %[ssrd], %[sob] offen offset:1024 sc1\n\t"
        "buffer_load_dwordx2 v[74:75], v19, %[ssrd], %[sob] offen offset:1024 sc1\n\t"
        "s_waitcnt vmcnt(2)\n\t"
        "v_bitop3_b32 v17, v68, v70, %[slay] bitop3:0x7e\n\t"
        "v_and_b32 v17, 1, v17\n\t"
        "v_cmp_eq_u32 vcc, 1, v17\n\t"
        "s_cbranch_vccnz Lx7_%=\n\t"
        "s_branch Ld7_%=\n\t"
        "Lx8_%=:\n\t"
        "s_cmp_eq_u32 %[scn], 0\n\t"
        "s_cbranch_scc1 Ld8_%=\n\t"
        "s_sub_u32 %[scn], %[scn], 1\n\t"
        "buffer_load_dwordx2 v[72:73], v108, %[ssrd], %[sob] offen offset:1024 sc1\n\t"
        "buffer_load_dwordx2 v[74:75], v19, %[ssrd], %[sob] offen offset:1024 sc1\n\t"
        "s_waitcnt vmcnt(0)\n\t"
        "v_bitop3_b32 v17, v72, v74, %[slay] bitop3:0x7e\n\t"
        "v_and_b32 v17, 1, v17\n\t"
        "v_cmp_eq_u32 vcc, 1, v17\n\t"
        "s_cbranch_vccnz Lx8_%=\n\t"
        "s_branch Ld8_%=\n\t"
        "Lws_%=:\n\t"
        "s_cmp_eq_u32 %[scn], 0\n\t"
        "s_cbranch_scc1 Lwg_%=\n\t"
        "s_sub_u32 %[scn], %[scn], 1\n\t"
        "s_sleep 1\n\t"
        "ds_read_b32 v16, v15\n\t"
        "s_waitcnt lgkmcnt(0)\n\t"
        "s_branch Lwc_%=\n\t"
        "Lk0_%=:\n\t"
        "s_waitcnt vmcnt(0)\n\t"
        "Lp_%=:\n\t"
        "ds_read_b32 v16, v15 offset:%[opc]\n\t"
        "ds_read_b128 v[52:55], v18 offset:0\n\t"
        "ds_read_b128 v[56:59], v18 offset:1024\n\t"
        "ds_read_b128 v[60:63], v18 offset:2048\n\t"
        "ds_read_b128 v[64:67], v18 offset:3072\n\t"
        "ds_read_b128 v[68:71], v18 offset:4096\n\t"
        "v_lshrrev_b32 v19, 8, v107\n\t"
        "v_lshl_add_u32 v19, v19, 4, %[swr]\n\t"
        "v_mov_b32 v17, 163424\n\t"
        "v_cndmask_b32 v19, v17, v19, %[sl1]\n\t"
        "ds_read_b128 v[40:43], v19 offset:0\n\t"
        "ds_read_b128 v[44:47], v19 offset:64\n\t"
        "ds_read_b128 v[48:51], v19 offset:128\n\t"
        "v_mov_b32 v98, 0\n\t"
        "v_mov_b32 v99, 0\n\t"
        "v_and_b32 v84, 0xf0, v107\n\t"
        "v_mov_b32 v83, 0\n\t"
        "s_waitcnt lgkmcnt(8)\n\t"
        "v_readfirstlane_b32 %[sob4x], v16\n\t"
        "s_cmp_ge_u32 %[sob4x], %[swt]\n\t"
        "s_cbranch_scc1 Lq_%=\n\t"
        "s_cmp_eq_u32 %[scn], 0\n\t"
        "s_cbranch_scc1 Lq_%=\n\t"
        "s_sub_u32 %[scn], %[scn], 1\n\t"
        "s_waitcnt lgkmcnt(0)\n\t"
        "s_branch Lp_%=\n\t"
        "Lq_%=:\n\t"
        "s_waitcnt lgkmcnt(6)\n\t"
        "v_add_f32 v20, v20, v52\n\t"
        "v_add_f32 v21, v21, v53\n\t"
        "v_add_f32 v22, v22, v54\n\t"
        "v_add_f32 v23, v23, v55\n\t"
        "v_add_f32 v24, v24, v56\n\t"
        "v_add_f32 v25, v25, v57\n\t"
        "v_add_f32 v26, v26, v58\n\t"
        "v_add_f32 v27, v27, v59\n\t"
        "v_cvt_pk_bf16_f32 v88, v20, v24\n\t"
        "v_cvt_pk_bf16_f32 v89, v21, v25\n\t"
        "v_cvt_pk_bf16_f32 v90, v22, v26\n\t"
        "v_cvt_pk_bf16_f32 v91, v23, v27\n\t"
        "s_waitcnt lgkmcnt(4)\n\t"
        "v_add_f32 v28, v28, v60\n\t"
        "v_add_f32 v29, v29, v61\n\t"
        "v_add_f32 v30, v30, v62\n\t"
        "v_add_f32 v31, v31, v63\n\t"
        "v_add_f32 v32, v32, v64\n\t"
        "v_add_f32 v33, v33, v65\n\t"
        "v_add_f32 v34, v34, v66\n\t"
        "v_add_f32 v35, v35, v67\n\t"
        "v_cvt_pk_bf16_f32 v92, v28, v32\n\t"
        "v_cvt_pk_bf16_f32 v93, v29, v33\n\t"
        "v_cvt_pk_bf16_f32 v94, v30, v34\n\t"
        "v_cvt_pk_bf16_f32 v95, v31, v35\n\t"
        "s_waitcnt lgkmcnt(0)\n\t"
        "v_mfma_f32_16x16x32_bf16 v[72:75], v[40:43], v[88:91], 0\n\t"
        "v_mfma_f32_16x16x32_bf16 v[76:79], v[88:91], v[88:91], 0\n\t"
        "s_waitcnt lgkmcnt(3)\n\t"
        "v_add_f32 v36, v36, v68\n\t"
        "v_add_f32 v37, v37, v69\n\t"
        "v_add_f32 v38, v38, v70\n\t"
        "v_add_f32 v39, v39, v71\n\t"
        "v_cvt_pk_bf16_f32 v96, v36, v37\n\t"
        "v_cvt_pk_bf16_f32 v97, v38, v39\n\t"
        "s_mov_b32 exec_lo, 0\n\t"
        "v_mov_b32 v96, 0\n\t"
        "v_mov_b32 v97, 0\n\t"
        "s_mov_b32 exec_lo, -1\n\t"
        "v_mfma_f32_16x16x32_bf16 v[72:75], v[44:47], v[92:95], v[72:75]\n\t"
        "v_mfma_f32_16x16x32_bf16 v[76:79], v[92:95], v[92:95], v[76:79]\n\t"
        "v_mfma_f32_16x16x32_bf16 v[72:75], v[48:51], v[96:99], v[72:75]\n\t"
        "v_mfma_f32_16x16x32_bf16 v[76:79], v[96:99], v[96:99], v[76:79]\n\t"
        "v_and_b32 v17, 0x300, v107\n\t"
        "v_lshrrev_b32 v19, 5, v17\n\t"
        "v_lshl_add_u32 v17, v17, 2, v84\n\t"
        "v_add_u32 v19, v19, v84\n\t"
        "s_add_u32 %[sob4], %[sx1], 4096\n\t"
        "s_add_u32 %[sob4x], %[sx1], 8192\n\t"
        "s_cmp_lg_u32 %[slay], 0\n\t"
        "v_min_u32 v80, -2, v72\n\t"
        "v_min_u32 v82, -2, v73\n\t"
        "v_cndmask_b32 v76, v76, v77, %[spar]\n\t"
        "v_cndmask_b32 v78, v78, v79, %[spar]\n\t"
        "v_cndmask_b32 v76, v76, v78, %[sl2]\n\t"
        "v_min_u32 v81, -2, v76\n\t"
        "s_mov_b64 exec, %[sdiag]\n\t"
        "buffer_store_dwordx4 v[80:83], v84, %[ssrd], %[sstat] offen sc1\n\t"
        "s_mov_b64 exec, -1\n\t"
        "s_cbranch_scc1 Lpk_%=\n\t"
        "v_min_u32 v88, -2, v88\n\t"
        "v_min_u32 v89, -2, v89\n\t"
        "v_min_u32 v90, -2, v90\n\t"
        "v_min_u32 v91, -2, v91\n\t"
        "buffer_store_dwordx4 v[88:91], v17, %[ssrd], %[sx1] offen sc1\n\t"
        "v_min_u32 v92, -2, v92\n\t"
        "v_min_u32 v93, -2, v93\n\t"
        "v_min_u32 v94, -2, v94\n\t"
        "v_min_u32 v95, -2, v95\n\t"
        "v_min_u32 v96, -2, v96\n\t"
        "v_min_u32 v97, -2, v97\n\t"
        "buffer_store_dwordx4 v[92:95], v17, %[ssrd], %[sob4] offen sc1\n\t"
        "s_mov_b32 exec_hi, 0\n\t"
        "buffer_store_dwordx2 v[96:97], v19, %[ssrd], %[sob4x] offen sc1\n\t"
        "s_mov_b32 exec_hi, -1\n\t"
        "Lpk_%=:\n\t"
        "v_add_u32 v18, %[spk], v107\n\t"
        "v_add_u32 v19, %[spk], v19\n\t"
        "v_mov_b32 v16, 1\n\t"
        "ds_write_b128 v18, v[88:91]\n\t"
        "ds_write_b128 v18, v[92:95] offset:1024\n\t"
        "s_mov_b32 exec_hi, 0\n\t"
        "ds_write_b64 v19, v[96:97] offset:2048\n\t"
        "s_mov_b32 exec_lo, 1\n\t"
        "ds_add_u32 v15, v16 offset:%[opk]\n\t"
        "s_mov_b64 exec, -1\n\t"
        "Lend_%=:\n\t"
        "s_setprio 0\n\t"
        : [sob4] "=&s"(ob4), [sob4x] "=&s"(ob4x), [scn] "+s"(cn)
        : [ssrd] "s"(a.srd), [sob] "s"(a.ob), [sl1d] "s"(a.l1d), [stvd] "s"(a.tvd), [swi] "s"(a.wi), [sld] "s"(a.ld),
          [smsk] "s"(a.msk), [sscr] "s"(a.scr), [sstat] "s"(a.stat), [sx1] "s"(a.x1), [slay] "s"(a.layer), [spk] "s"(a.pk), [sn7] "s"(a.n7), [sskip] "s"(a.skip), [swc] "s"(a.wc), [swt] "s"(4 * a.layer + 4), [slb] "s"(a.lb), [opc] "n"(BC_PC), [opk] "n"(BC_PK), [swr] "s"(163232),
          [spar] "s"(0xAAAAAAAAAAAAAAAAull), [sl2] "s"(0xCCCCCCCCCCCCCCCCull), [sl1] "s"(0x2222222222222222ull), [sdiag] "s"(0xf0000f0000f0000full)
        : "memory", "scc", "vcc", "v14", "v15", "v16", "v17", "v18", "v19", "v20", "v21", "v22", "v23", "v24", "v25", "v26", "v27", "v28", "v29", "v30", "v31", "v32", "v33", "v34", "v35", "v36", "v37", "v38", "v39", "v40", "v41", "v42", "v43", "v44", "v45", "v46", "v47");
}

DEV void dload(u32x4 srd, u32 a2, u32 so) {
    asm volatile(
        "s_nop 4\n\t"
        "buffer_load_dwordx4 v[112:115], v107, %[ssrd], %[so] offen offset:0" NTD "\n\t"
        "buffer_load_dwordx4 v[116:119], v107, %[ssrd], %[so] offen offset:1152" NTD "\n\t"
        "buffer_load_dwordx4 v[120:123], v107, %[ssrd], %[so] offen offset:2304" NTD "\n\t"
        "buffer_load_ushort v220, %[a2], %[ssrd], %[so] offen offset:1024" NTD "\n\t"
        "buffer_load_ushort v221, %[a2], %[ssrd], %[so] offen offset:2176" NTD "\n\t"
        "buffer_load_ushort v222, %[a2], %[ssrd], %[so] offen offset:3328" NTD "\n\t"
        "s_add_u32 %[so], %[so], 3456\n\t"
        "buffer_load_dwordx4 v[124:127], v107, %[ssrd], %[so] offen offset:0" NTD "\n\t"
        "buffer_load_dwordx4 v[128:131], v107, %[ssrd], %[so] offen offset:1152" NTD "\n\t"
        "buffer_load_dwordx4 v[132:135], v107, %[ssrd], %[so] offen offset:2304" NTD "\n\t"
        "buffer_load_ushort v223, %[a2], %[ssrd], %[so] offen offset:1024" NTD "\n\t"
        "buffer_load_ushort v224, %[a2], %[ssrd], %[so] offen offset:2176" NTD "\n\t"
        "buffer_load_ushort v225, %[a2], %[ssrd], %[so] offen offset:3328" NTD "\n\t"
        "s_add_u32 %[so], %[so], 3456\n\t"
        "buffer_load_dwordx4 v[136:139], v107, %[ssrd], %[so] offen offset:0" NTD "\n\t"
        "buffer_load_dwordx4 v[140:143], v107, %[ssrd], %[so] offen offset:1152" NTD "\n\t"
        "buffer_load_dwordx4 v[144:147], v107, %[ssrd], %[so] offen offset:2304" NTD "\n\t"
        "buffer_load_ushort v226, %[a2], %[ssrd], %[so] offen offset:1024" NTD "\n\t"
        "buffer_load_ushort v227, %[a2], %[ssrd], %[so] offen offset:2176" NTD "\n\t"
        "buffer_load_ushort v228, %[a2], %[ssrd], %[so] offen offset:3328" NTD "\n\t"
        "s_add_u32 %[so], %[so], 3456\n\t"
        "buffer_load_dwordx4 v[148:151], v107, %[ssrd], %[so] offen offset:0" NTD "\n\t"
        "buffer_load_dwordx4 v[152:155], v107, %[ssrd], %[so] offen offset:1152" NTD "\n\t"
        "buffer_load_dwordx4 v[156:159], v107, %[ssrd], %[so] offen offset:2304" NTD "\n\t"
        "buffer_load_ushort v229, %[a2], %[ssrd], %[so] offen offset:1024" NTD "\n\t"
        "buffer_load_ushort v230, %[a2], %[ssrd], %[so] offen offset:2176" NTD "\n\t"
        "buffer_load_ushort v231, %[a2], %[ssrd], %[so] offen offset:3328" NTD "\n\t"
        "s_add_u32 %[so], %[so], 3456\n\t"
        "buffer_load_dwordx4 v[160:163], v107, %[ssrd], %[so] offen offset:0" NTD "\n\t"
        "buffer_load_dwordx4 v[164:167], v107, %[ssrd], %[so] offen offset:1152" NTD "\n\t"
        "buffer_load_dwordx4 v[168:171], v107, %[ssrd], %[so] offen offset:2304" NTD "\n\t"
        "buffer_load_ushort v232, %[a2], %[ssrd], %[so] offen offset:1024" NTD "\n\t"
        "buffer_load_ushort v233, %[a2], %[ssrd], %[so] offen offset:2176" NTD "\n\t"
        "buffer_load_ushort v234, %[a2], %[ssrd], %[so] offen offset:3328" NTD "\n\t"
        "s_add_u32 %[so], %[so], 3456\n\t"
        "buffer_load_dwordx4 v[172:175], v107, %[ssrd], %[so] offen offset:0" NTD "\n\t"
        "buffer_load_dwordx4 v[176:179], v107, %[ssrd], %[so] offen offset:1152" NTD "\n\t"
        "buffer_load_dwordx4 v[180:183], v107, %[ssrd], %[so] offen offset:2304" NTD "\n\t"
        "buffer_load_ushort v235, %[a2], %[ssrd], %[so] offen offset:1024" NTD "\n\t"
        "buffer_load_ushort v236, %[a2], %[ssrd], %[so] offen offset:2176" NTD "\n\t"
        "buffer_load_ushort v237, %[a2], %[ssrd], %[so] offen offset:3328" NTD "\n\t"
        "s_add_u32 %[so], %[so], 3456\n\t"
        "buffer_load_dwordx4 v[184:187], v107, %[ssrd], %[so] offen offset:0" NTD "\n\t"
        "buffer_load_dwordx4 v[188:191], v107, %[ssrd], %[so] offen offset:1152" NTD "\n\t"
        "buffer_load_dwordx4 v[192:195], v107, %[ssrd], %[so] offen offset:2304" NTD "\n\t"
        "buffer_load_ushort v238, %[a2], %[ssrd], %[so] offen offset:1024" NTD "\n\t"
        "buffer_load_ushort v239, %[a2], %[ssrd], %[so] offen offset:2176" NTD "\n\t"
        "buffer_load_ushort v240, %[a2], %[ssrd], %[so] offen offset:3328" NTD "\n\t"
        "s_add_u32 %[so], %[so], 3456\n\t"
        "buffer_load_dwordx4 v[196:199], v107, %[ssrd], %[so] offen offset:0" NTD "\n\t"
        "buffer_load_dwordx4 v[200:203], v107, %[ssrd], %[so] offen offset:1152" NTD "\n\t"
        "buffer_load_dwordx4 v[204:207], v107, %[ssrd], %[so] offen offset:2304" NTD "\n\t"
        "buffer_load_ushort v241, %[a2], %[ssrd], %[so] offen offset:1024" NTD "\n\t"
        "buffer_load_ushort v242, %[a2], %[ssrd], %[so] offen offset:2176" NTD "\n\t"
        "buffer_load_ushort v243, %[a2], %[ssrd], %[so] offen offset:3328" NTD "\n\t"
        "s_add_u32 %[so], %[so], 3456\n\t"
        : [so] "+s"(so) : [ssrd] "s"(srd), [a2] "v"(a2) : "memory", "scc");
}

DEV void dperm() {
    u32 t;
    asm volatile(
        "v_perm_b32 %[t], v116, v112, %[slo]\n\t"
        "v_perm_b32 v116, v116, v112, %[shi]\n\t"
        "v_mov_b32 v112, %[t]\n\t"
        "v_perm_b32 %[t], v117, v113, %[slo]\n\t"
        "v_perm_b32 v117, v117, v113, %[shi]\n\t"
        "v_mov_b32 v113, %[t]\n\t"
        "v_perm_b32 %[t], v118, v114, %[slo]\n\t"
        "v_perm_b32 v118, v118, v114, %[shi]\n\t"
        "v_mov_b32 v114, %[t]\n\t"
        "v_perm_b32 %[t], v119, v115, %[slo]\n\t"
        "v_perm_b32 v119, v119, v115, %[shi]\n\t"
        "v_mov_b32 v115, %[t]\n\t"
        "v_perm_b32 v208, v221, v220, %[slo]\n\t"
        "v_perm_b32 %[t], v124, v120, %[slo]\n\t"
        "v_perm_b32 v124, v124, v120, %[shi]\n\t"
        "v_mov_b32 v120, %[t]\n\t"
        "v_perm_b32 %[t], v125, v121, %[slo]\n\t"
        "v_perm_b32 v125, v125, v121, %[shi]\n\t"
        "v_mov_b32 v121, %[t]\n\t"
        "v_perm_b32 %[t], v126, v122, %[slo]\n\t"
        "v_perm_b32 v126, v126, v122, %[shi]\n\t"
        "v_mov_b32 v122, %[t]\n\t"
        "v_perm_b32 %[t], v127, v123, %[slo]\n\t"
        "v_perm_b32 v127, v127, v123, %[shi]\n\t"
        "v_mov_b32 v123, %[t]\n\t"
        "v_perm_b32 v209, v223, v222, %[slo]\n\t"
        "v_perm_b32 %[t], v132, v128, %[slo]\n\t"
        "v_perm_b32 v132, v132, v128, %[shi]\n\t"
        "v_mov_b32 v128, %[t]\n\t"
        "v_perm_b32 %[t], v133, v129, %[slo]\n\t"
        "v_perm_b32 v133, v133, v129, %[shi]\n\t"
        "v_mov_b32 v129, %[t]\n\t"
        "v_perm_b32 %[t], v134, v130, %[slo]\n\t"
        "v_perm_b32 v134, v134, v130, %[shi]\n\t"
        "v_mov_b32 v130, %[t]\n\t"
        "v_perm_b32 %[t], v135, v131, %[slo]\n\t"
        "v_perm_b32 v135, v135, v131, %[shi]\n\t"
        "v_mov_b32 v131, %[t]\n\t"
        "v_perm_b32 v210, v225, v224, %[slo]\n\t"
        "v_perm_b32 %[t], v140, v136, %[slo]\n\t"
        "v_perm_b32 v140, v140, v136, %[shi]\n\t"
        "v_mov_b32 v136, %[t]\n\t"
        "v_perm_b32 %[t], v141, v137, %[slo]\n\t"
        "v_perm_b32 v141, v141, v137, %[shi]\n\t"
        "v_mov_b32 v137, %[t]\n\t"
        "v_perm_b32 %[t], v142, v138, %[slo]\n\t"
        "v_perm_b32 v142, v142, v138, %[shi]\n\t"
        "v_mov_b32 v138, %[t]\n\t"
        "v_perm_b32 %[t], v143, v139, %[slo]\n\t"
        "v_perm_b32 v143, v143, v139, %[shi]\n\t"
        "v_mov_b32 v139, %[t]\n\t"
        "v_perm_b32 v211, v227, v226, %[slo]\n\t"
        "v_perm_b32 %[t], v148, v144, %[slo]\n\t"
        "v_perm_b32 v148, v148, v144, %[shi]\n\t"
        "v_mov_b32 v144, %[t]\n\t"
        "v_perm_b32 %[t], v149, v145, %[slo]\n\t"
        "v_perm_b32 v149, v149, v145, %[shi]\n\t"
        "v_mov_b32 v145, %[t]\n\t"
        "v_perm_b32 %[t], v150, v146, %[slo]\n\t"
        "v_perm_b32 v150, v150, v146, %[shi]\n\t"
        "v_mov_b32 v146, %[t]\n\t"
        "v_perm_b32 %[t], v151, v147, %[slo]\n\t"
        "v_perm_b32 v151, v151, v147, %[shi]\n\t"
        "v_mov_b32 v147, %[t]\n\t"
        "v_perm_b32 v212, v229, v228, %[slo]\n\t"
        "v_perm_b32 %[t], v156, v152, %[slo]\n\t"
        "v_perm_b32 v156, v156, v152, %[shi]\n\t"
        "v_mov_b32 v152, %[t]\n\t"
        "v_perm_b32 %[t], v157, v153, %[slo]\n\t"
        "v_perm_b32 v157, v157, v153, %[shi]\n\t"
        "v_mov_b32 v153, %[t]\n\t"
        "v_perm_b32 %[t], v158, v154, %[slo]\n\t"
        "v_perm_b32 v158, v158, v154, %[shi]\n\t"
        "v_mov_b32 v154, %[t]\n\t"
        "v_perm_b32 %[t], v159, v155, %[slo]\n\t"
        "v_perm_b32 v159, v159, v155, %[shi]\n\t"
        "v_mov_b32 v155, %[t]\n\t"
        "v_perm_b32 v213, v231, v230, %[slo]\n\t"
        "v_perm_b32 %[t], v164, v160, %[slo]\n\t"
        "v_perm_b32 v164, v164, v160, %[shi]\n\t"
        "v_mov_b32 v160, %[t]\n\t"
        "v_perm_b32 %[t], v165, v161, %[slo]\n\t"
        "v_perm_b32 v165, v165, v161, %[shi]\n\t"
        "v_mov_b32 v161, %[t]\n\t"
        "v_perm_b32 %[t], v166, v162, %[slo]\n\t"
        "v_perm_b32 v166, v166, v162, %[shi]\n\t"
        "v_mov_b32 v162, %[t]\n\t"
        "v_perm_b32 %[t], v167, v163, %[slo]\n\t"
        "v_perm_b32 v167, v167, v163, %[shi]\n\t"
        "v_mov_b32 v163, %[t]\n\t"
        "v_perm_b32 v214, v233, v232, %[slo]\n\t"
        "v_perm_b32 %[t], v172, v168, %[slo]\n\t"
        "v_perm_b32 v172, v172, v168, %[shi]\n\t"
        "v_mov_b32 v168, %[t]\n\t"
        "v_perm_b32 %[t], v173, v169, %[slo]\n\t"
        "v_perm_b32 v173, v173, v169, %[shi]\n\t"
        "v_mov_b32 v169, %[t]\n\t"
        "v_perm_b32 %[t], v174, v170, %[slo]\n\t"
        "v_perm_b32 v174, v174, v170, %[shi]\n\t"
        "v_mov_b32 v170, %[t]\n\t"
        "v_perm_b32 %[t], v175, v171, %[slo]\n\t"
        "v_perm_b32 v175, v175, v171, %[shi]\n\t"
        "v_mov_b32 v171, %[t]\n\t"
        "v_perm_b32 v215, v235, v234, %[slo]\n\t"
        "v_perm_b32 %[t], v180, v176, %[slo]\n\t"
        "v_perm_b32 v180, v180, v176, %[shi]\n\t"
        "v_mov_b32 v176, %[t]\n\t"
        "v_perm_b32 %[t], v181, v177, %[slo]\n\t"
        "v_perm_b32 v181, v181, v177, %[shi]\n\t"
        "v_mov_b32 v177, %[t]\n\t"
        "v_perm_b32 %[t], v182, v178, %[slo]\n\t"
        "v_perm_b32 v182, v182, v178, %[shi]\n\t"
        "v_mov_b32 v178, %[t]\n\t"
        "v_perm_b32 %[t], v183, v179, %[slo]\n\t"
        "v_perm_b32 v183, v183, v179, %[shi]\n\t"
        "v_mov_b32 v179, %[t]\n\t"
        "v_perm_b32 v216, v237, v236, %[slo]\n\t"
        "v_perm_b32 %[t], v188, v184, %[slo]\n\t"
        "v_perm_b32 v188, v188, v184, %[shi]\n\t"
        "v_mov_b32 v184, %[t]\n\t"
        "v_perm_b32 %[t], v189, v185, %[slo]\n\t"
        "v_perm_b32 v189, v189, v185, %[shi]\n\t"
        "v_mov_b32 v185, %[t]\n\t"
        "v_perm_b32 %[t], v190, v186, %[slo]\n\t"
        "v_perm_b32 v190, v190, v186, %[shi]\n\t"
        "v_mov_b32 v186, %[t]\n\t"
        "v_perm_b32 %[t], v191, v187, %[slo]\n\t"
        "v_perm_b32 v191, v191, v187, %[shi]\n\t"
        "v_mov_b32 v187, %[t]\n\t"
        "v_perm_b32 v217, v239, v238, %[slo]\n\t"
        "v_perm_b32 %[t], v196, v192, %[slo]\n\t"
        "v_perm_b32 v196, v196, v192, %[shi]\n\t"
        "v_mov_b32 v192, %[t]\n\t"
        "v_perm_b32 %[t], v197, v193, %[slo]\n\t"
        "v_perm_b32 v197, v197, v193, %[shi]\n\t"
        "v_mov_b32 v193, %[t]\n\t"
        "v_perm_b32 %[t], v198, v194, %[slo]\n\t"
        "v_perm_b32 v198, v198, v194, %[shi]\n\t"
        "v_mov_b32 v194, %[t]\n\t"
        "v_perm_b32 %[t], v199, v195, %[slo]\n\t"
        "v_perm_b32 v199, v199, v195, %[shi]\n\t"
        "v_mov_b32 v195, %[t]\n\t"
        "v_perm_b32 v218, v241, v240, %[slo]\n\t"
        "v_perm_b32 %[t], v204, v200, %[slo]\n\t"
        "v_perm_b32 v204, v204, v200, %[shi]\n\t"
        "v_mov_b32 v200, %[t]\n\t"
        "v_perm_b32 %[t], v205, v201, %[slo]\n\t"
        "v_perm_b32 v205, v205, v201, %[shi]\n\t"
        "v_mov_b32 v201, %[t]\n\t"
        "v_perm_b32 %[t], v206, v202, %[slo]\n\t"
        "v_perm_b32 v206, v206, v202, %[shi]\n\t"
        "v_mov_b32 v202, %[t]\n\t"
        "v_perm_b32 %[t], v207, v203, %[slo]\n\t"
        "v_perm_b32 v207, v207, v203, %[shi]\n\t"
        "v_mov_b32 v203, %[t]\n\t"
        "v_perm_b32 v219, v243, v242, %[slo]\n\t"
        : [t] "=&v"(t) : [slo] "s"(0x05040100u), [shi] "s"(0x07060302u) : "memory");
}

DEV void xstats(int xa, float (&sx)[2], float (&sq)[2]) {
    int xb; u32 sc;
    asm volatile(
        "v_mov_b32 v24, 0x3f803f80\n\t"
        "v_mov_b32 v25, 0x3f803f80\n\t"
        "v_mov_b32 v26, 0x3f803f80\n\t"
        "v_mov_b32 v27, 0x3f803f80\n\t"
        "v_mov_b32 v48, 0\n\t"
        "v_mov_b32 v49, 0\n\t"
        "v_mov_b32 v50, 0\n\t"
        "v_mov_b32 v51, 0\n\t"
        "v_mov_b32 v52, 0\n\t"
        "v_mov_b32 v53, 0\n\t"
        "v_mov_b32 v54, 0\n\t"
        "v_mov_b32 v55, 0\n\t"
        "v_mov_b32 v56, 0\n\t"
        "v_mov_b32 v57, 0\n\t"
        "v_mov_b32 v58, 0\n\t"
        "v_mov_b32 v59, 0\n\t"
        "v_mov_b32 v60, 0\n\t"
        "v_mov_b32 v61, 0\n\t"
        "v_mov_b32 v62, 0\n\t"
        "v_mov_b32 v63, 0\n\t"
        "v_xor_b32 %[xb], 64, %[xa]\n\t"
        "s_mov_b32 %[sc], 3\n\t"
        "Ll_%=:\n\t"
        "ds_read_b128 v[208:211], %[xa] offset:0\n\t"
        "ds_read_b128 v[212:215], %[xa] offset:6144\n\t"
        "ds_read_b128 v[216:219], %[xb] offset:0\n\t"
        "ds_read_b128 v[220:223], %[xb] offset:6144\n\t"
        "ds_read_b128 v[224:227], %[xa] offset:10240\n\t"
        "ds_read_b128 v[228:231], %[xa] offset:16384\n\t"
        "ds_read_b128 v[232:235], %[xb] offset:10240\n\t"
        "ds_read_b128 v[236:239], %[xb] offset:16384\n\t"
        "ds_read_b128 v[240:243], %[xa] offset:20480\n\t"
        "ds_read_b128 v[244:247], %[xa] offset:26624\n\t"
        "ds_read_b128 v[248:251], %[xb] offset:20480\n\t"
        "ds_read_b128 v[252:255], %[xb] offset:26624\n\t"
        "v_add_u32 %[xa], 30720, %[xa]\n\t"
        "v_add_u32 %[xb], 30720, %[xb]\n\t"
        "s_waitcnt lgkmcnt(11)\n\t"
        "v_mfma_f32_16x16x32_bf16 v[48:51], v[24:27], v[208:211], v[48:51]\n\t"
        "v_mfma_f32_16x16x32_bf16 v[56:59], v[208:211], v[208:211], v[56:59]\n\t"
        "s_waitcnt lgkmcnt(10)\n\t"
        "v_mfma_f32_16x16x32_bf16 v[52:55], v[24:27], v[212:215], v[52:55]\n\t"
        "v_mfma_f32_16x16x32_bf16 v[60:63], v[212:215], v[212:215], v[60:63]\n\t"
        "s_waitcnt lgkmcnt(9)\n\t"
        "v_mfma_f32_16x16x32_bf16 v[48:51], v[24:27], v[216:219], v[48:51]\n\t"
        "v_mfma_f32_16x16x32_bf16 v[56:59], v[216:219], v[216:219], v[56:59]\n\t"
        "s_waitcnt lgkmcnt(8)\n\t"
        "v_mfma_f32_16x16x32_bf16 v[52:55], v[24:27], v[220:223], v[52:55]\n\t"
        "v_mfma_f32_16x16x32_bf16 v[60:63], v[220:223], v[220:223], v[60:63]\n\t"
        "s_waitcnt lgkmcnt(7)\n\t"
        "v_mfma_f32_16x16x32_bf16 v[48:51], v[24:27], v[224:227], v[48:51]\n\t"
        "v_mfma_f32_16x16x32_bf16 v[56:59], v[224:227], v[224:227], v[56:59]\n\t"
        "s_waitcnt lgkmcnt(6)\n\t"
        "v_mfma_f32_16x16x32_bf16 v[52:55], v[24:27], v[228:231], v[52:55]\n\t"
        "v_mfma_f32_16x16x32_bf16 v[60:63], v[228:231], v[228:231], v[60:63]\n\t"
        "s_waitcnt lgkmcnt(5)\n\t"
        "v_mfma_f32_16x16x32_bf16 v[48:51], v[24:27], v[232:235], v[48:51]\n\t"
        "v_mfma_f32_16x16x32_bf16 v[56:59], v[232:235], v[232:235], v[56:59]\n\t"
        "s_waitcnt lgkmcnt(4)\n\t"
        "v_mfma_f32_16x16x32_bf16 v[52:55], v[24:27], v[236:239], v[52:55]\n\t"
        "v_mfma_f32_16x16x32_bf16 v[60:63], v[236:239], v[236:239], v[60:63]\n\t"
        "s_waitcnt lgkmcnt(3)\n\t"
        "v_mfma_f32_16x16x32_bf16 v[48:51], v[24:27], v[240:243], v[48:51]\n\t"
        "v_mfma_f32_16x16x32_bf16 v[56:59], v[240:243], v[240:243], v[56:59]\n\t"
        "s_waitcnt lgkmcnt(2)\n\t"
        "v_mfma_f32_16x16x32_bf16 v[52:55], v[24:27], v[244:247], v[52:55]\n\t"
        "v_mfma_f32_16x16x32_bf16 v[60:63], v[244:247], v[244:247], v[60:63]\n\t"
        "s_waitcnt lgkmcnt(1)\n\t"
        "v_mfma_f32_16x16x32_bf16 v[48:51], v[24:27], v[248:251], v[48:51]\n\t"
        "v_mfma_f32_16x16x32_bf16 v[56:59], v[248:251], v[248:251], v[56:59]\n\t"
        "s_waitcnt lgkmcnt(0)\n\t"
        "v_mfma_f32_16x16x32_bf16 v[52:55], v[24:27], v[252:255], v[52:55]\n\t"
        "v_mfma_f32_16x16x32_bf16 v[60:63], v[252:255], v[252:255], v[60:63]\n\t"
        "s_sub_u32 %[sc], %[sc], 1\n\t"
        "s_cmp_lg_u32 %[sc], 0\n\t"
        "s_cbranch_scc1 Ll_%=\n\t"
        "s_nop 7\n\t"
        "v_mov_b32 %[x0], v48\n\t"
        "v_cndmask_b32 v56, v56, v57, %[spar]\n\t"
        "v_cndmask_b32 v58, v58, v59, %[spar]\n\t"
        "v_cndmask_b32 %[q0], v56, v58, %[sl2]\n\t"
        "v_mov_b32 %[x1], v52\n\t"
        "v_cndmask_b32 v60, v60, v61, %[spar]\n\t"
        "v_cndmask_b32 v62, v62, v63, %[spar]\n\t"
        "v_cndmask_b32 %[q1], v60, v62, %[sl2]\n\t"
        : [xa] "+v"(xa), [xb] "=&v"(xb), [sc] "=&s"(sc), [x0] "=&v"(sx[0]), [q0] "=&v"(sq[0]), [x1] "=&v"(sx[1]), [q1] "=&v"(sq[1])
        : [spar] "s"(0xAAAAAAAAAAAAAAAAull), [sl2] "s"(0xCCCCCCCCCCCCCCCCull)
        : "memory", "scc", "v24", "v25", "v26", "v27");
}

DEV void xgather(u32x4 srd, u32 xo, u32 xp, u32 ld, u32 lh, u32& cn) {
    u32 cr, g0, g1, g2;
    asm volatile(
        "s_nop 4\n\t"
        "s_sleep 8\n\t"
        "s_add_u32 m0, %[sld], 0\n\t"
        "s_add_u32 %[scr], %[sxo], m0\n\t"
        "buffer_load_dwordx4 v107, %[ssrd], %[scr] offen offset:0 sc1 lds\n\t"
        "buffer_load_dwordx4 v107, %[ssrd], %[scr] offen offset:1024 sc1 lds\n\t"
        "buffer_load_dwordx4 v107, %[ssrd], %[scr] offen offset:2048 sc1 lds\n\t"
        "buffer_load_dwordx4 v107, %[ssrd], %[scr] offen offset:3072 sc1 lds\n\t"
        "s_add_u32 m0, %[sld], 4096\n\t"
        "s_add_u32 %[scr], %[sxo], m0\n\t"
        "buffer_load_dwordx4 v107, %[ssrd], %[scr] offen offset:0 sc1 lds\n\t"
        "buffer_load_dwordx4 v107, %[ssrd], %[scr] offen offset:1024 sc1 lds\n\t"
        "buffer_load_dwordx4 v107, %[ssrd], %[scr] offen offset:2048 sc1 lds\n\t"
        "buffer_load_dwordx4 v107, %[ssrd], %[scr] offen offset:3072 sc1 lds\n\t"
        "v_and_b32 %[g0], 0x300, v107\n\t"
        "v_and_b32 %[g1], 0xf0, v107\n\t"
        "v_lshl_add_u32 %[g0], %[g0], 2, %[g1]\n\t"
        "v_add_u32 %[g0], 768, %[g0]\n\t"
        "v_add_u32 %[g1], %[sld], v107\n\t"
        "v_mov_b32 %[g2], 153984\n\t"
        "s_sleep 4\n\t"
        "s_add_u32 m0, %[sld], 16384\n\t"
        "s_add_u32 %[scr], %[sxo], m0\n\t"
        "buffer_load_dwordx4 v107, %[ssrd], %[scr] offen offset:0 sc1 lds\n\t"
        "buffer_load_dwordx4 v107, %[ssrd], %[scr] offen offset:1024 sc1 lds\n\t"
        "buffer_load_dwordx4 v107, %[ssrd], %[scr] offen offset:2048 sc1 lds\n\t"
        "buffer_load_dwordx4 v107, %[ssrd], %[scr] offen offset:3072 sc1 lds\n\t"
        "s_add_u32 m0, %[sld], 20480\n\t"
        "s_add_u32 %[scr], %[sxo], m0\n\t"
        "buffer_load_dwordx4 v107, %[ssrd], %[scr] offen offset:0 sc1 lds\n\t"
        "buffer_load_dwordx4 v107, %[ssrd], %[scr] offen offset:1024 sc1 lds\n\t"
        "buffer_load_dwordx4 v107, %[ssrd], %[scr] offen offset:2048 sc1 lds\n\t"
        "buffer_load_dwordx4 v107, %[ssrd], %[scr] offen offset:3072 sc1 lds\n\t"
        "s_waitcnt vmcnt(8)\n\t"
        "ds_read_b128 v[48:51], %[g1] offset:0\n\t"
        "ds_read_b128 v[52:55], %[g1] offset:1024\n\t"
        "ds_read_b128 v[56:59], %[g1] offset:2048\n\t"
        "ds_read_b128 v[60:63], %[g1] offset:3072\n\t"
        "ds_read_b128 v[64:67], %[g1] offset:4096\n\t"
        "ds_read_b128 v[68:71], %[g1] offset:5120\n\t"
        "ds_read_b128 v[72:75], %[g1] offset:6144\n\t"
        "ds_read_b128 v[76:79], %[g1] offset:7168\n\t"
        "Lh0_%=:\n\t"
        "s_waitcnt lgkmcnt(0)\n\t"
        "v_max3_u32 v48, v48, v49, v50\n\t"
        "v_max3_u32 v48, v48, v51, v52\n\t"
        "v_max3_u32 v48, v48, v53, v54\n\t"
        "v_max3_u32 v48, v48, v55, v56\n\t"
        "v_max3_u32 v48, v48, v57, v58\n\t"
        "v_max3_u32 v48, v48, v59, v60\n\t"
        "v_max3_u32 v48, v48, v61, v62\n\t"
        "v_max3_u32 v48, v48, v63, v64\n\t"
        "v_max3_u32 v48, v48, v65, v66\n\t"
        "v_max3_u32 v48, v48, v67, v68\n\t"
        "v_max3_u32 v48, v48, v69, v70\n\t"
        "v_max3_u32 v48, v48, v71, v72\n\t"
        "v_max3_u32 v48, v48, v73, v74\n\t"
        "v_max3_u32 v48, v48, v75, v76\n\t"
        "v_max3_u32 v48, v48, v77, v78\n\t"
        "v_max_u32 v48, v48, v79\n\t"
        "v_cmp_eq_u32 vcc, -1, v48\n\t"
        "v_mov_b32 v49, 1\n\t"
        "s_cbranch_vccnz Lx0_%=\n\t"
        "Ld0_%=:\n\t"
        "s_mov_b64 exec, 1\n\t"
        "ds_add_u32 %[g2], v49 offset:%[og0]+0\n\t"
        "s_mov_b64 exec, -1\n\t"
        "s_add_u32 m0, %[sld], 32768\n\t"
        "s_add_u32 %[scr], %[sxo], m0\n\t"
        "buffer_load_dwordx4 v107, %[ssrd], %[scr] offen offset:0 sc1 lds\n\t"
        "buffer_load_dwordx4 v107, %[ssrd], %[scr] offen offset:1024 sc1 lds\n\t"
        "buffer_load_dwordx4 v107, %[ssrd], %[scr] offen offset:2048 sc1 lds\n\t"
        "buffer_load_dwordx4 v107, %[ssrd], %[scr] offen offset:3072 sc1 lds\n\t"
        "s_add_u32 m0, %[sld], 36864\n\t"
        "s_add_u32 %[scr], %[sxo], m0\n\t"
        "buffer_load_dwordx4 v107, %[ssrd], %[scr] offen offset:0 sc1 lds\n\t"
        "buffer_load_dwordx4 v107, %[ssrd], %[scr] offen offset:1024 sc1 lds\n\t"
        "buffer_load_dwordx4 v107, %[ssrd], %[scr] offen offset:2048 sc1 lds\n\t"
        "buffer_load_dwordx4 v107, %[ssrd], %[scr] offen offset:3072 sc1 lds\n\t"
        "s_waitcnt vmcnt(8)\n\t"
        "ds_read_b128 v[48:51], %[g1] offset:16384\n\t"
        "ds_read_b128 v[52:55], %[g1] offset:17408\n\t"
        "ds_read_b128 v[56:59], %[g1] offset:18432\n\t"
        "ds_read_b128 v[60:63], %[g1] offset:19456\n\t"
        "ds_read_b128 v[64:67], %[g1] offset:20480\n\t"
        "ds_read_b128 v[68:71], %[g1] offset:21504\n\t"
        "ds_read_b128 v[72:75], %[g1] offset:22528\n\t"
        "ds_read_b128 v[76:79], %[g1] offset:23552\n\t"
        "s_add_u32 m0, %[sld], 49152\n\t"
        "s_add_u32 %[scr], %[sxo], m0\n\t"
        "buffer_load_dwordx4 v107, %[ssrd], %[scr] offen offset:0 sc1 lds\n\t"
        "buffer_load_dwordx4 v107, %[ssrd], %[scr] offen offset:1024 sc1 lds\n\t"
        "buffer_load_dwordx4 v107, %[ssrd], %[scr] offen offset:2048 sc1 lds\n\t"
        "buffer_load_dwordx4 v107, %[ssrd], %[scr] offen offset:3072 sc1 lds\n\t"
        "s_add_u32 m0, %[sld], 53248\n\t"
        "s_add_u32 %[scr], %[sxo], m0\n\t"
        "buffer_load_dwordx4 v107, %[ssrd], %[scr] offen offset:0 sc1 lds\n\t"
        "buffer_load_dwordx4 v107, %[ssrd], %[scr] offen offset:1024 sc1 lds\n\t"
        "buffer_load_dwordx4 v107, %[ssrd], %[scr] offen offset:2048 sc1 lds\n\t"
        "buffer_load_dwordx4 v107, %[ssrd], %[scr] offen offset:3072 sc1 lds\n\t"
        "Lh1_%=:\n\t"
        "s_waitcnt lgkmcnt(0)\n\t"
        "v_max3_u32 v48, v48, v49, v50\n\t"
        "v_max3_u32 v48, v48, v51, v52\n\t"
        "v_max3_u32 v48, v48, v53, v54\n\t"
        "v_max3_u32 v48, v48, v55, v56\n\t"
        "v_max3_u32 v48, v48, v57, v58\n\t"
        "v_max3_u32 v48, v48, v59, v60\n\t"
        "v_max3_u32 v48, v48, v61, v62\n\t"
        "v_max3_u32 v48, v48, v63, v64\n\t"
        "v_max3_u32 v48, v48, v65, v66\n\t"
        "v_max3_u32 v48, v48, v67, v68\n\t"
        "v_max3_u32 v48, v48, v69, v70\n\t"
        "v_max3_u32 v48, v48, v71, v72\n\t"
        "v_max3_u32 v48, v48, v73, v74\n\t"
        "v_max3_u32 v48, v48, v75, v76\n\t"
        "v_max3_u32 v48, v48, v77, v78\n\t"
        "v_max_u32 v48, v48, v79\n\t"
        "v_cmp_eq_u32 vcc, -1, v48\n\t"
        "v_mov_b32 v49, 1\n\t"
        "s_cbranch_vccnz Lx1_%=\n\t"
        "Ld1_%=:\n\t"
        "s_mov_b64 exec, 1\n\t"
        "ds_add_u32 %[g2], v49 offset:%[og0]+4\n\t"
        "s_mov_b64 exec, -1\n\t"
        "s_waitcnt vmcnt(8)\n\t"
        "ds_read_b128 v[48:51], %[g1] offset:32768\n\t"
        "ds_read_b128 v[52:55], %[g1] offset:33792\n\t"
        "ds_read_b128 v[56:59], %[g1] offset:34816\n\t"
        "ds_read_b128 v[60:63], %[g1] offset:35840\n\t"
        "ds_read_b128 v[64:67], %[g1] offset:36864\n\t"
        "ds_read_b128 v[68:71], %[g1] offset:37888\n\t"
        "ds_read_b128 v[72:75], %[g1] offset:38912\n\t"
        "ds_read_b128 v[76:79], %[g1] offset:39936\n\t"
        "s_lshr_b32 m0, %[sld], 1\n\t"
        "s_bitset1_b32 m0, 16\n\t"
        "s_add_u32 %[scr], %[sxo], m0\n\t"
        "buffer_load_dwordx4 v107, %[ssrd], %[scr] offen offset:0 sc1 lds\n\t"
        "buffer_load_dwordx4 v107, %[ssrd], %[scr] offen offset:1024 sc1 lds\n\t"
        "buffer_load_dwordx4 v107, %[ssrd], %[scr] offen offset:2048 sc1 lds\n\t"
        "buffer_load_dwordx4 v107, %[ssrd], %[scr] offen offset:3072 sc1 lds\n\t"
        "Lh2_%=:\n\t"
        "s_waitcnt lgkmcnt(0)\n\t"
        "v_max3_u32 v48, v48, v49, v50\n\t"
        "v_max3_u32 v48, v48, v51, v52\n\t"
        "v_max3_u32 v48, v48, v53, v54\n\t"
        "v_max3_u32 v48, v48, v55, v56\n\t"
        "v_max3_u32 v48, v48, v57, v58\n\t"
        "v_max3_u32 v48, v48, v59, v60\n\t"
        "v_max3_u32 v48, v48, v61, v62\n\t"
        "v_max3_u32 v48, v48, v63, v64\n\t"
        "v_max3_u32 v48, v48, v65, v66\n\t"
        "v_max3_u32 v48, v48, v67, v68\n\t"
        "v_max3_u32 v48, v48, v69, v70\n\t"
        "v_max3_u32 v48, v48, v71, v72\n\t"
        "v_max3_u32 v48, v48, v73, v74\n\t"
        "v_max3_u32 v48, v48, v75, v76\n\t"
        "v_max3_u32 v48, v48, v77, v78\n\t"
        "v_max_u32 v48, v48, v79\n\t"
        "v_cmp_eq_u32 vcc, -1, v48\n\t"
        "v_mov_b32 v49, 1\n\t"
        "s_cbranch_vccnz Lx2_%=\n\t"
        "Ld2_%=:\n\t"
        "s_mov_b64 exec, 1\n\t"
        "ds_add_u32 %[g2], v49 offset:%[og0]+8\n\t"
        "s_mov_b64 exec, -1\n\t"
        "s_waitcnt vmcnt(4)\n\t"
        "ds_read_b128 v[48:51], %[g1] offset:49152\n\t"
        "ds_read_b128 v[52:55], %[g1] offset:50176\n\t"
        "ds_read_b128 v[56:59], %[g1] offset:51200\n\t"
        "ds_read_b128 v[60:63], %[g1] offset:52224\n\t"
        "ds_read_b128 v[64:67], %[g1] offset:53248\n\t"
        "ds_read_b128 v[68:71], %[g1] offset:54272\n\t"
        "ds_read_b128 v[72:75], %[g1] offset:55296\n\t"
        "ds_read_b128 v[76:79], %[g1] offset:56320\n\t"
        "s_add_u32 m0, %[slh], 0\n\t"
        "s_add_u32 %[scr], %[sxp], 0\n\t"
        "buffer_load_dwordx4 %[g0], %[ssrd], %[scr] offen sc1 lds\n\t"
        "s_add_u32 m0, %[slh], 1024\n\t"
        "s_add_u32 %[scr], %[sxp], 4096\n\t"
        "buffer_load_dwordx4 %[g0], %[ssrd], %[scr] offen sc1 lds\n\t"
        "s_add_u32 m0, %[slh], 2048\n\t"
        "s_add_u32 %[scr], %[sxp], 8192\n\t"
        "buffer_load_dwordx4 %[g0], %[ssrd], %[scr] offen sc1 lds\n\t"
        "s_add_u32 m0, %[slh], 3072\n\t"
        "s_add_u32 %[scr], %[sxp], 12288\n\t"
        "buffer_load_dwordx4 %[g0], %[ssrd], %[scr] offen sc1 lds\n\t"
        "s_add_u32 m0, %[slh], 4096\n\t"
        "s_add_u32 %[scr], %[sxp], 16384\n\t"
        "buffer_load_dwordx4 %[g0], %[ssrd], %[scr] offen sc1 lds\n\t"
        "Lh3_%=:\n\t"
        "s_waitcnt lgkmcnt(0)\n\t"
        "v_max3_u32 v48, v48, v49, v50\n\t"
        "v_max3_u32 v48, v48, v51, v52\n\t"
        "v_max3_u32 v48, v48, v53, v54\n\t"
        "v_max3_u32 v48, v48, v55, v56\n\t"
        "v_max3_u32 v48, v48, v57, v58\n\t"
        "v_max3_u32 v48, v48, v59, v60\n\t"
        "v_max3_u32 v48, v48, v61, v62\n\t"
        "v_max3_u32 v48, v48, v63, v64\n\t"
        "v_max3_u32 v48, v48, v65, v66\n\t"
        "v_max3_u32 v48, v48, v67, v68\n\t"
        "v_max3_u32 v48, v48, v69, v70\n\t"
        "v_max3_u32 v48, v48, v71, v72\n\t"
        "v_max3_u32 v48, v48, v73, v74\n\t"
        "v_max3_u32 v48, v48, v75, v76\n\t"
        "v_max3_u32 v48, v48, v77, v78\n\t"
        "v_max_u32 v48, v48, v79\n\t"
        "v_cmp_eq_u32 vcc, -1, v48\n\t"
        "v_mov_b32 v49, 1\n\t"
        "s_cbranch_vccnz Lx3_%=\n\t"
        "Ld3_%=:\n\t"
        "s_waitcnt vmcnt(5)\n\t"
        "s_lshr_b32 %[scr], %[sld], 1\n\t"
        "s_bitset1_b32 %[scr], 16\n\t"
        "v_add_u32 %[g1], %[scr], v107\n\t"
        "ds_read_b128 v[48:51], %[g1] offset:0\n\t"
        "ds_read_b128 v[52:55], %[g1] offset:1024\n\t"
        "ds_read_b128 v[56:59], %[g1] offset:2048\n\t"
        "ds_read_b128 v[60:63], %[g1] offset:3072\n\t"
        "s_add_u32 m0, %[slh], 5120\n\t"
        "s_add_u32 %[scr], %[sxp], 20480\n\t"
        "buffer_load_dwordx4 %[g0], %[ssrd], %[scr] offen sc1 lds\n\t"
        "s_add_u32 m0, %[slh], 6144\n\t"
        "s_add_u32 %[scr], %[sxp], 24576\n\t"
        "buffer_load_dwordx4 %[g0], %[ssrd], %[scr] offen sc1 lds\n\t"
        "s_add_u32 m0, %[slh], 7168\n\t"
        "s_add_u32 %[scr], %[sxp], 28672\n\t"
        "buffer_load_dwordx4 %[g0], %[ssrd], %[scr] offen sc1 lds\n\t"
        "s_add_u32 m0, %[slh], 8192\n\t"
        "s_add_u32 %[scr], %[sxp], 32768\n\t"
        "buffer_load_dwordx4 %[g0], %[ssrd], %[scr] offen sc1 lds\n\t"
        "Lh4_%=:\n\t"
        "s_waitcnt lgkmcnt(0)\n\t"
        "v_max3_u32 v48, v48, v49, v50\n\t"
        "v_max3_u32 v48, v48, v51, v52\n\t"
        "v_max3_u32 v48, v48, v53, v54\n\t"
        "v_max3_u32 v48, v48, v55, v56\n\t"
        "v_max3_u32 v48, v48, v57, v58\n\t"
        "v_max3_u32 v48, v48, v59, v60\n\t"
        "v_max3_u32 v48, v48, v61, v62\n\t"
        "v_max_u32 v48, v48, v63\n\t"
        "v_cmp_eq_u32 vcc, -1, v48\n\t"
        "v_mov_b32 v49, 1\n\t"
        "s_cbranch_vccnz Lx4_%=\n\t"
        "Ld4_%=:\n\t"
        "s_mov_b64 exec, 1\n\t"
        "ds_add_u32 %[g2], v49 offset:%[og0]+12\n\t"
        "s_mov_b64 exec, -1\n\t"
        "s_waitcnt vmcnt(4)\n\t"
        "v_add_u32 %[g1], %[slh], v107\n\t"
        "ds_read_b128 v[48:51], %[g1] offset:0\n\t"
        "ds_read_b128 v[52:55], %[g1] offset:1024\n\t"
        "ds_read_b128 v[56:59], %[g1] offset:2048\n\t"
        "ds_read_b128 v[60:63], %[g1] offset:3072\n\t"
        "ds_read_b128 v[64:67], %[g1] offset:4096\n\t"
        "Lh5_%=:\n\t"
        "s_waitcnt lgkmcnt(0)\n\t"
        "v_max3_u32 v48, v48, v49, v50\n\t"
        "v_max3_u32 v48, v48, v51, v52\n\t"
        "v_max3_u32 v48, v48, v53, v54\n\t"
        "v_max3_u32 v48, v48, v55, v56\n\t"
        "v_max3_u32 v48, v48, v57, v58\n\t"
        "v_max3_u32 v48, v48, v59, v60\n\t"
        "v_max3_u32 v48, v48, v61, v62\n\t"
        "v_max3_u32 v48, v48, v63, v64\n\t"
        "v_max3_u32 v48, v48, v65, v66\n\t"
        "v_max_u32 v48, v48, v67\n\t"
        "v_cmp_eq_u32 vcc, -1, v48\n\t"
        "v_mov_b32 v49, 1\n\t"
        "s_cbranch_vccnz Lx5_%=\n\t"
        "Ld5_%=:\n\t"
        "s_waitcnt vmcnt(0)\n\t"
        "ds_read_b128 v[48:51], %[g1] offset:5120\n\t"
        "ds_read_b128 v[52:55], %[g1] offset:6144\n\t"
        "ds_read_b128 v[56:59], %[g1] offset:7168\n\t"
        "ds_read_b128 v[60:63], %[g1] offset:8192\n\t"
        "Lh6_%=:\n\t"
        "s_waitcnt lgkmcnt(0)\n\t"
        "v_max3_u32 v48, v48, v49, v50\n\t"
        "v_max3_u32 v48, v48, v51, v52\n\t"
        "v_max3_u32 v48, v48, v53, v54\n\t"
        "v_max3_u32 v48, v48, v55, v56\n\t"
        "v_max3_u32 v48, v48, v57, v58\n\t"
        "v_max3_u32 v48, v48, v59, v60\n\t"
        "v_max3_u32 v48, v48, v61, v62\n\t"
        "v_max_u32 v48, v48, v63\n\t"
        "v_cmp_eq_u32 vcc, -1, v48\n\t"
        "v_mov_b32 v49, 1\n\t"
        "s_cbranch_vccnz Lx6_%=\n\t"
        "Ld6_%=:\n\t"
        "s_mov_b64 exec, 1\n\t"
        "ds_add_u32 %[g2], v49 offset:%[og0]+16\n\t"
        "s_mov_b64 exec, -1\n\t"
        "s_branch Le_%=\n\t"
        "Lx0_%=:\n\t"
        "s_cmp_lt_i32 %[scn], 1\n\t"
        "s_cbranch_scc1 Ld0_%=\n\t"
        "s_sub_u32 %[scn], %[scn], 1\n\t"
        "s_add_u32 m0, %[sld], 0\n\t"
        "s_add_u32 %[scr], %[sxo], m0\n\t"
        "buffer_load_dwordx4 v107, %[ssrd], %[scr] offen offset:0 sc1 lds\n\t"
        "buffer_load_dwordx4 v107, %[ssrd], %[scr] offen offset:1024 sc1 lds\n\t"
        "buffer_load_dwordx4 v107, %[ssrd], %[scr] offen offset:2048 sc1 lds\n\t"
        "buffer_load_dwordx4 v107, %[ssrd], %[scr] offen offset:3072 sc1 lds\n\t"
        "s_add_u32 m0, %[sld], 4096\n\t"
        "s_add_u32 %[scr], %[sxo], m0\n\t"
        "buffer_load_dwordx4 v107, %[ssrd], %[scr] offen offset:0 sc1 lds\n\t"
        "buffer_load_dwordx4 v107, %[ssrd], %[scr] offen offset:1024 sc1 lds\n\t"
        "buffer_load_dwordx4 v107, %[ssrd], %[scr] offen offset:2048 sc1 lds\n\t"
        "buffer_load_dwordx4 v107, %[ssrd], %[scr] offen offset:3072 sc1 lds\n\t"
        "s_waitcnt vmcnt(0)\n\t"
        "ds_read_b128 v[48:51], %[g1] offset:0\n\t"
        "ds_read_b128 v[52:55], %[g1] offset:1024\n\t"
        "ds_read_b128 v[56:59], %[g1] offset:2048\n\t"
        "ds_read_b128 v[60:63], %[g1] offset:3072\n\t"
        "ds_read_b128 v[64:67], %[g1] offset:4096\n\t"
        "ds_read_b128 v[68:71], %[g1] offset:5120\n\t"
        "ds_read_b128 v[72:75], %[g1] offset:6144\n\t"
        "ds_read_b128 v[76:79], %[g1] offset:7168\n\t"
        "s_branch Lh0_%=\n\t"
        "Lx1_%=:\n\t"
        "s_cmp_lt_i32 %[scn], 1\n\t"
        "s_cbranch_scc1 Ld1_%=\n\t"
        "s_sub_u32 %[scn], %[scn], 1\n\t"
        "s_add_u32 m0, %[sld], 16384\n\t"
        "s_add_u32 %[scr], %[sxo], m0\n\t"
        "buffer_load_dwordx4 v107, %[ssrd], %[scr] offen offset:0 sc1 lds\n\t"
        "buffer_load_dwordx4 v107, %[ssrd], %[scr] offen offset:1024 sc1 lds\n\t"
        "buffer_load_dwordx4 v107, %[ssrd], %[scr] offen offset:2048 sc1 lds\n\t"
        "buffer_load_dwordx4 v107, %[ssrd], %[scr] offen offset:3072 sc1 lds\n\t"
        "s_add_u32 m0, %[sld], 20480\n\t"
        "s_add_u32 %[scr], %[sxo], m0\n\t"
        "buffer_load_dwordx4 v107, %[ssrd], %[scr] offen offset:0 sc1 lds\n\t"
        "buffer_load_dwordx4 v107, %[ssrd], %[scr] offen offset:1024 sc1 lds\n\t"
        "buffer_load_dwordx4 v107, %[ssrd], %[scr] offen offset:2048 sc1 lds\n\t"
        "buffer_load_dwordx4 v107, %[ssrd], %[scr] offen offset:3072 sc1 lds\n\t"
        "s_waitcnt vmcnt(0)\n\t"
        "ds_read_b128 v[48:51], %[g1] offset:16384\n\t"
        "ds_read_b128 v[52:55], %[g1] offset:17408\n\t"
        "ds_read_b128 v[56:59], %[g1] offset:18432\n\t"
        "ds_read_b128 v[60:63], %[g1] offset:19456\n\t"
        "ds_read_b128 v[64:67], %[g1] offset:20480\n\t"
        "ds_read_b128 v[68:71], %[g1] offset:21504\n\t"
        "ds_read_b128 v[72:75], %[g1] offset:22528\n\t"
        "ds_read_b128 v[76:79], %[g1] offset:23552\n\t"
        "s_branch Lh1_%=\n\t"
        "Lx2_%=:\n\t"
        "s_cmp_lt_i32 %[scn], 1\n\t"
        "s_cbranch_scc1 Ld2_%=\n\t"
        "s_sub_u32 %[scn], %[scn], 1\n\t"
        "s_add_u32 m0, %[sld], 32768\n\t"
        "s_add_u32 %[scr], %[sxo], m0\n\t"
        "buffer_load_dwordx4 v107, %[ssrd], %[scr] offen offset:0 sc1 lds\n\t"
        "buffer_load_dwordx4 v107, %[ssrd], %[scr] offen offset:1024 sc1 lds\n\t"
        "buffer_load_dwordx4 v107, %[ssrd], %[scr] offen offset:2048 sc1 lds\n\t"
        "buffer_load_dwordx4 v107, %[ssrd], %[scr] offen offset:3072 sc1 lds\n\t"
        "s_add_u32 m0, %[sld], 36864\n\t"
        "s_add_u32 %[scr], %[sxo], m0\n\t"
        "buffer_load_dwordx4 v107, %[ssrd], %[scr] offen offset:0 sc1 lds\n\t"
        "buffer_load_dwordx4 v107, %[ssrd], %[scr] offen offset:1024 sc1 lds\n\t"
        "buffer_load_dwordx4 v107, %[ssrd], %[scr] offen offset:2048 sc1 lds\n\t"
        "buffer_load_dwordx4 v107, %[ssrd], %[scr] offen offset:3072 sc1 lds\n\t"
        "s_waitcnt vmcnt(0)\n\t"
        "ds_read_b128 v[48:51], %[g1] offset:32768\n\t"
        "ds_read_b128 v[52:55], %[g1] offset:33792\n\t"
        "ds_read_b128 v[56:59], %[g1] offset:34816\n\t"
        "ds_read_b128 v[60:63], %[g1] offset:35840\n\t"
        "ds_read_b128 v[64:67], %[g1] offset:36864\n\t"
        "ds_read_b128 v[68:71], %[g1] offset:37888\n\t"
        "ds_read_b128 v[72:75], %[g1] offset:38912\n\t"
        "ds_read_b128 v[76:79], %[g1] offset:39936\n\t"
        "s_branch Lh2_%=\n\t"
        "Lx3_%=:\n\t"
        "s_cmp_lt_i32 %[scn], 1\n\t"
        "s_cbranch_scc1 Ld3_%=\n\t"
        "s_sub_u32 %[scn], %[scn], 1\n\t"
        "s_add_u32 m0, %[sld], 49152\n\t"
        "s_add_u32 %[scr], %[sxo], m0\n\t"
        "buffer_load_dwordx4 v107, %[ssrd], %[scr] offen offset:0 sc1 lds\n\t"
        "buffer_load_dwordx4 v107, %[ssrd], %[scr] offen offset:1024 sc1 lds\n\t"
        "buffer_load_dwordx4 v107, %[ssrd], %[scr] offen offset:2048 sc1 lds\n\t"
        "buffer_load_dwordx4 v107, %[ssrd], %[scr] offen offset:3072 sc1 lds\n\t"
        "s_add_u32 m0, %[sld], 53248\n\t"
        "s_add_u32 %[scr], %[sxo], m0\n\t"
        "buffer_load_dwordx4 v107, %[ssrd], %[scr] offen offset:0 sc1 lds\n\t"
        "buffer_load_dwordx4 v107, %[ssrd], %[scr] offen offset:1024 sc1 lds\n\t"
        "buffer_load_dwordx4 v107, %[ssrd], %[scr] offen offset:2048 sc1 lds\n\t"
        "buffer_load_dwordx4 v107, %[ssrd], %[scr] offen offset:3072 sc1 lds\n\t"
        "s_waitcnt vmcnt(0)\n\t"
        "ds_read_b128 v[48:51], %[g1] offset:49152\n\t"
        "ds_read_b128 v[52:55], %[g1] offset:50176\n\t"
        "ds_read_b128 v[56:59], %[g1] offset:51200\n\t"
        "ds_read_b128 v[60:63], %[g1] offset:52224\n\t"
        "ds_read_b128 v[64:67], %[g1] offset:53248\n\t"
        "ds_read_b128 v[68:71], %[g1] offset:54272\n\t"
        "ds_read_b128 v[72:75], %[g1] offset:55296\n\t"
        "ds_read_b128 v[76:79], %[g1] offset:56320\n\t"
        "s_branch Lh3_%=\n\t"
        "Lx4_%=:\n\t"
        "s_cmp_lt_i32 %[scn], 1\n\t"
        "s_cbranch_scc1 Ld4_%=\n\t"
        "s_sub_u32 %[scn], %[scn], 1\n\t"
        "s_lshr_b32 m0, %[sld], 1\n\t"
        "s_bitset1_b32 m0, 16\n\t"
        "s_add_u32 %[scr], %[sxo], m0\n\t"
        "buffer_load_dwordx4 v107, %[ssrd], %[scr] offen offset:0 sc1 lds\n\t"
        "buffer_load_dwordx4 v107, %[ssrd], %[scr] offen offset:1024 sc1 lds\n\t"
        "buffer_load_dwordx4 v107, %[ssrd], %[scr] offen offset:2048 sc1 lds\n\t"
        "buffer_load_dwordx4 v107, %[ssrd], %[scr] offen offset:3072 sc1 lds\n\t"
        "s_waitcnt vmcnt(0)\n\t"
        "s_lshr_b32 %[scr], %[sld], 1\n\t"
        "s_bitset1_b32 %[scr], 16\n\t"
        "v_add_u32 %[g1], %[scr], v107\n\t"
        "ds_read_b128 v[48:51], %[g1] offset:0\n\t"
        "ds_read_b128 v[52:55], %[g1] offset:1024\n\t"
        "ds_read_b128 v[56:59], %[g1] offset:2048\n\t"
        "ds_read_b128 v[60:63], %[g1] offset:3072\n\t"
        "s_branch Lh4_%=\n\t"
        "Lx5_%=:\n\t"
        "s_cmp_lt_i32 %[scn], 1\n\t"
        "s_cbranch_scc1 Ld5_%=\n\t"
        "s_sub_u32 %[scn], %[scn], 1\n\t"
        "s_add_u32 m0, %[slh], 0\n\t"
        "s_add_u32 %[scr], %[sxp], 0\n\t"
        "buffer_load_dwordx4 %[g0], %[ssrd], %[scr] offen sc1 lds\n\t"
        "s_add_u32 m0, %[slh], 1024\n\t"
        "s_add_u32 %[scr], %[sxp], 4096\n\t"
        "buffer_load_dwordx4 %[g0], %[ssrd], %[scr] offen sc1 lds\n\t"
        "s_add_u32 m0, %[slh], 2048\n\t"
        "s_add_u32 %[scr], %[sxp], 8192\n\t"
        "buffer_load_dwordx4 %[g0], %[ssrd], %[scr] offen sc1 lds\n\t"
        "s_add_u32 m0, %[slh], 3072\n\t"
        "s_add_u32 %[scr], %[sxp], 12288\n\t"
        "buffer_load_dwordx4 %[g0], %[ssrd], %[scr] offen sc1 lds\n\t"
        "s_add_u32 m0, %[slh], 4096\n\t"
        "s_add_u32 %[scr], %[sxp], 16384\n\t"
        "buffer_load_dwordx4 %[g0], %[ssrd], %[scr] offen sc1 lds\n\t"
        "s_waitcnt vmcnt(0)\n\t"
        "v_add_u32 %[g1], %[slh], v107\n\t"
        "ds_read_b128 v[48:51], %[g1] offset:0\n\t"
        "ds_read_b128 v[52:55], %[g1] offset:1024\n\t"
        "ds_read_b128 v[56:59], %[g1] offset:2048\n\t"
        "ds_read_b128 v[60:63], %[g1] offset:3072\n\t"
        "ds_read_b128 v[64:67], %[g1] offset:4096\n\t"
        "s_branch Lh5_%=\n\t"
        "Lx6_%=:\n\t"
        "s_cmp_lt_i32 %[scn], 1\n\t"
        "s_cbranch_scc1 Ld6_%=\n\t"
        "s_sub_u32 %[scn], %[scn], 1\n\t"
        "s_add_u32 m0, %[slh], 5120\n\t"
        "s_add_u32 %[scr], %[sxp], 20480\n\t"
        "buffer_load_dwordx4 %[g0], %[ssrd], %[scr] offen sc1 lds\n\t"
        "s_add_u32 m0, %[slh], 6144\n\t"
        "s_add_u32 %[scr], %[sxp], 24576\n\t"
        "buffer_load_dwordx4 %[g0], %[ssrd], %[scr] offen sc1 lds\n\t"
        "s_add_u32 m0, %[slh], 7168\n\t"
        "s_add_u32 %[scr], %[sxp], 28672\n\t"
        "buffer_load_dwordx4 %[g0], %[ssrd], %[scr] offen sc1 lds\n\t"
        "s_add_u32 m0, %[slh], 8192\n\t"
        "s_add_u32 %[scr], %[sxp], 32768\n\t"
        "buffer_load_dwordx4 %[g0], %[ssrd], %[scr] offen sc1 lds\n\t"
        "s_waitcnt vmcnt(0)\n\t"
        "ds_read_b128 v[48:51], %[g1] offset:5120\n\t"
        "ds_read_b128 v[52:55], %[g1] offset:6144\n\t"
        "ds_read_b128 v[56:59], %[g1] offset:7168\n\t"
        "ds_read_b128 v[60:63], %[g1] offset:8192\n\t"
        "s_branch Lh6_%=\n\t"
        "Le_%=:\n\t"
        : [scr] "=&s"(cr), [scn] "+s"(cn), [g0] "=&v"(g0), [g1] "=&v"(g1), [g2] "=&v"(g2)
        : [ssrd] "s"(srd), [sxo] "s"(xo), [sxp] "s"(xp), [sld] "s"(ld), [slh] "s"(lh), [og0] "n"(BC_G0)
        : "memory", "scc", "vcc");
}

DEV void ffn_help(u32x4 srd, u32 c0, int g0, int g1, int g2, int pa, u32 fa, u32 ca, u32 l1, u32& cn) {
    u32 cr;
    asm volatile(
        "ds_read_b64_tr_b16 v[40:41], %[g0] offset:0\n\t"
        "ds_read_b64_tr_b16 v[42:43], %[g0] offset:384\n\t"
        "ds_read_b64_tr_b16 v[44:45], %[g1] offset:0\n\t"
        "ds_read_b64_tr_b16 v[46:47], %[g1] offset:384\n\t"
        "ds_read_b64_tr_b16 v[48:49], %[g2] offset:0\n\t"
        "ds_read_b64_tr_b16 v[50:51], %[g2] offset:384\n\t"
        "ds_read_b64_tr_b16 v[52:53], %[g0] offset:1536\n\t"
        "ds_read_b64_tr_b16 v[54:55], %[g0] offset:1920\n\t"
        "ds_read_b64_tr_b16 v[56:57], %[g1] offset:1536\n\t"
        "ds_read_b64_tr_b16 v[58:59], %[g1] offset:1920\n\t"
        "ds_read_b64_tr_b16 v[60:61], %[g2] offset:1536\n\t"
        "ds_read_b64_tr_b16 v[62:63], %[g2] offset:1920\n\t"
        "ds_read_b64_tr_b16 v[64:65], %[g0] offset:3072\n\t"
        "ds_read_b64_tr_b16 v[66:67], %[g0] offset:3456\n\t"
        "ds_read_b64_tr_b16 v[68:69], %[g1] offset:3072\n\t"
        "ds_read_b64_tr_b16 v[70:71], %[g1] offset:3456\n\t"
        "ds_read_b64_tr_b16 v[72:73], %[g2] offset:3072\n\t"
        "ds_read_b64_tr_b16 v[74:75], %[g2] offset:3456\n\t"
        "ds_read_b64_tr_b16 v[76:77], %[g0] offset:4608\n\t"
        "ds_read_b64_tr_b16 v[78:79], %[g0] offset:4992\n\t"
        "ds_read_b64_tr_b16 v[80:81], %[g1] offset:4608\n\t"
        "ds_read_b64_tr_b16 v[82:83], %[g1] offset:4992\n\t"
        "ds_read_b64_tr_b16 v[84:85], %[g2] offset:4608\n\t"
        "ds_read_b64_tr_b16 v[86:87], %[g2] offset:4992\n\t"
        "ds_read_b64_tr_b16 v[88:89], %[g0] offset:6144\n\t"
        "ds_read_b64_tr_b16 v[90:91], %[g0] offset:6528\n\t"
        "ds_read_b64_tr_b16 v[92:93], %[g1] offset:6144\n\t"
        "ds_read_b64_tr_b16 v[94:95], %[g1] offset:6528\n\t"
        "ds_read_b64_tr_b16 v[96:97], %[g2] offset:6144\n\t"
        "ds_read_b64_tr_b16 v[98:99], %[g2] offset:6528\n\t"
        "v_mov_b32 v20, 0x3f803f80\n\t"
        "v_mov_b32 v21, 0x3f803f80\n\t"
        "v_mov_b32 v22, 0x3f803f80\n\t"
        "v_mov_b32 v23, 0x3f803f80\n\t"
        "v_mov_b32 v24, 0x3f803f80\n\t"
        "v_mov_b32 v25, 0x3f803f80\n\t"
        "v_mov_b32 v26, 0x3f803f80\n\t"
        "v_mov_b32 v27, 0x3f803f80\n\t"
        "v_mov_b32 v28, 0x3f803f80\n\t"
        "v_mov_b32 v29, 0x3f803f80\n\t"
        "v_mov_b32 v30, 0x3f803f80\n\t"
        "v_mov_b32 v31, 0x3f803f80\n\t"
        "v_mov_b32 v32, 0x3f803f80\n\t"
        "v_mov_b32 v33, 0x3f803f80\n\t"
        "v_mov_b32 v34, 0x3f803f80\n\t"
        "v_mov_b32 v35, 0x3f803f80\n\t"
        "v_mov_b32 v36, 0x3f803f80\n\t"
        "v_mov_b32 v37, 0x3f803f80\n\t"
        "v_mov_b32 v38, 0x3f803f80\n\t"
        "v_mov_b32 v39, 0x3f803f80\n\t"
        "v_mov_b32 %[g1], %[sl1]\n\t"
        "v_mov_b32 %[g2], %[sfa]\n\t"
        "v_mov_b32 v8, %[sca]\n\t"
        "Lw_%=:\n\t"
        "s_sleep 1\n\t"
        "ds_read_b32 %[g0], v8\n\t"
        "s_sub_u32 %[scn], %[scn], 1\n\t"
        "s_waitcnt lgkmcnt(0)\n\t"
        "v_readfirstlane_b32 %[scr], %[g0]\n\t"
        "s_cmp_lt_i32 %[scn], 1\n\t"
        "s_cbranch_scc1 Lr_%=\n\t"
        "s_cmp_eq_u32 %[scr], 0\n\t"
        "s_cbranch_scc1 Lw_%=\n\t"
        "Lr_%=:\n\t"
        "s_nop 1\n\t"
        "s_mov_b64 exec, %[sm4]\n\t"
        "buffer_load_dwordx4 v[20:23], %[c0], %[ssrd], %[scr] offen offset:0 sc1\n\t"
        "buffer_load_dwordx4 v[24:27], %[c0], %[ssrd], %[scr] offen offset:64 sc1\n\t"
        "buffer_load_dwordx4 v[28:31], %[c0], %[ssrd], %[scr] offen offset:128 sc1\n\t"
        "buffer_load_dwordx4 v[32:35], %[c0], %[ssrd], %[scr] offen offset:192 sc1\n\t"
        "buffer_load_dwordx4 v[36:39], %[c0], %[ssrd], %[scr] offen offset:256 sc1\n\t"
        "s_mov_b64 exec, -1\n\t"
        "s_waitcnt vmcnt(4)\n\t"
        "v_mfma_f32_16x16x32_bf16 v[8:11], v[20:23], v[40:43], 0\n\t"
        "v_max3_u32 %[g0], v22, v20, v21\n\t"
        "v_mfma_f32_16x16x32_bf16 v[12:15], v[20:23], v[44:47], 0\n\t"
        "v_max3_u32 %[g0], %[g0], v22, v23\n\t"
        "v_mfma_f32_16x16x32_bf16 v[16:19], v[20:23], v[48:51], 0\n\t"
        "s_waitcnt vmcnt(3)\n\t"
        "v_mfma_f32_16x16x32_bf16 v[8:11], v[24:27], v[52:55], v[8:11]\n\t"
        "v_max3_u32 %[g0], %[g0], v24, v25\n\t"
        "v_mfma_f32_16x16x32_bf16 v[12:15], v[24:27], v[56:59], v[12:15]\n\t"
        "v_max3_u32 %[g0], %[g0], v26, v27\n\t"
        "v_mfma_f32_16x16x32_bf16 v[16:19], v[24:27], v[60:63], v[16:19]\n\t"
        "s_waitcnt vmcnt(2)\n\t"
        "v_mfma_f32_16x16x32_bf16 v[8:11], v[28:31], v[64:67], v[8:11]\n\t"
        "v_max3_u32 %[g0], %[g0], v28, v29\n\t"
        "v_mfma_f32_16x16x32_bf16 v[12:15], v[28:31], v[68:71], v[12:15]\n\t"
        "v_max3_u32 %[g0], %[g0], v30, v31\n\t"
        "v_mfma_f32_16x16x32_bf16 v[16:19], v[28:31], v[72:75], v[16:19]\n\t"
        "s_waitcnt vmcnt(1)\n\t"
        "v_mfma_f32_16x16x32_bf16 v[8:11], v[32:35], v[76:79], v[8:11]\n\t"
        "v_max3_u32 %[g0], %[g0], v32, v33\n\t"
        "v_mfma_f32_16x16x32_bf16 v[12:15], v[32:35], v[80:83], v[12:15]\n\t"
        "v_max3_u32 %[g0], %[g0], v34, v35\n\t"
        "v_mfma_f32_16x16x32_bf16 v[16:19], v[32:35], v[84:87], v[16:19]\n\t"
        "s_waitcnt vmcnt(0)\n\t"
        "v_mfma_f32_16x16x32_bf16 v[8:11], v[36:39], v[88:91], v[8:11]\n\t"
        "v_max3_u32 %[g0], %[g0], v36, v37\n\t"
        "v_mfma_f32_16x16x32_bf16 v[12:15], v[36:39], v[92:95], v[12:15]\n\t"
        "v_max3_u32 %[g0], %[g0], v38, v39\n\t"
        "v_mfma_f32_16x16x32_bf16 v[16:19], v[36:39], v[96:99], v[16:19]\n\t"
        "v_cmp_eq_u32 vcc, -1, %[g0]\n\t"
        "s_cbranch_vccz Ld_%=\n\t"
        "s_cmp_lt_i32 %[scn], 1\n\t"
        "s_cbranch_scc1 Ld_%=\n\t"
        "s_sub_u32 %[scn], %[scn], 1\n\t"
        "s_branch Lr_%=\n\t"
        "Ld_%=:\n\t"
        "s_nop 6\n\t"
        "s_mov_b64 exec, 0xffff\n\t"
        "ds_write_b64 %[pa], v[8:9] offset:0\n\t"
        "ds_write_b64 %[pa], v[12:13] offset:128\n\t"
        "ds_write_b64 %[pa], v[16:17] offset:256\n\t"
        "ds_write_b32 %[g2], %[g1]\n\t"
        "s_mov_b64 exec, -1\n\t"
        : [scr] "=&s"(cr), [scn] "+s"(cn), [g0] "+v"(g0), [g1] "+v"(g1), [g2] "+v"(g2)
        : [ssrd] "s"(srd), [c0] "v"(c0), [pa] "v"(pa), [sfa] "s"(fa), [sca] "s"(ca), [sl1] "s"(l1), [sm4] "s"(0x1111111111111111ull)
        : "memory", "scc", "vcc", "v8", "v9", "v10", "v11", "v12", "v13", "v14", "v15", "v16", "v17", "v18", "v19", "v20", "v21", "v22", "v23", "v24", "v25", "v26", "v27", "v28", "v29", "v30", "v31", "v32", "v33", "v34", "v35", "v36", "v37", "v38", "v39", "v40", "v41", "v42", "v43", "v44", "v45", "v46", "v47");
}

DEV void ffn(u32x4 srd, u32 cr, u32 c0, u32 c1, int g0, int g1, int g2, int pa, int ff, int pg, int pu, u32 ca, u32 l1, u32 ya, u32 ye, u32 yb, float& m, float& rs, u32& cn) {
    u32 ap[12];
    asm volatile(
        "s_cmp_eq_u32 %[sl1], 1\n\t"
        "s_cbranch_scc0 Lt_%=\n\t"
        "s_nop 2\n\t"
        "Ls_%=:\n\t"
        "buffer_load_dwordx4 v[24:27], v107, %[ssrd], %[scr] offen sc1\n\t"
        "buffer_load_dwordx4 v[28:31], %[c1], %[ssrd], %[scr] offen sc1\n\t"
        "v_add_u32 v45, 157472, v107\n\t"
        "v_lshrrev_b32 v47, 4, v107\n\t"
        "v_and_b32 v47, 0x30, v47\n\t"
        "v_add_u32 v44, 157472, v47\n\t"
        "v_add_u32 v47, 155168, v47\n\t"
        "v_cndmask_b32 v47, v47, v44, %[sm4]\n\t"
        "s_waitcnt vmcnt(0)\n\t"
        "ds_write_b128 v45, v[24:27]\n\t"
        "v_max3_u32 v44, v24, v25, v26\n\t"
        "v_max3_u32 v44, v44, v27, v28\n\t"
        "s_mov_b64 exec, 0xff\n\t"
        "ds_write_b128 v45, v[28:31] offset:1024\n\t"
        "s_mov_b64 exec, -1\n\t"
        "v_max3_u32 v44, v44, v29, v30\n\t"
        "v_max_u32 v44, v44, v31\n\t"
        "v_cmp_eq_u32 vcc, -1, v44\n\t"
        "ds_read_b128 v[84:87], v47 offset:960\n\t"
        "ds_read_b128 v[88:91], v47 offset:1024\n\t"
        "ds_read_b128 v[92:95], v47 offset:1088\n\t"
        "ds_read_b128 v[96:99], v47 offset:0\n\t"
        "s_cbranch_vccnz Ly_%=\n\t"
        "Lz_%=:\n\t"
        "v_readlane_b32 %[sa0], v28, 8\n\t"
        "v_readlane_b32 %[sa1], v29, 8\n\t"
        "s_waitcnt lgkmcnt(3)\n\t"
        "v_mfma_f32_16x16x32_bf16 v[32:35], v[84:87], v[220:223], 0\n\t"
        "ds_read_b64_tr_b16 v[220:221], %[g0] offset:4608\n\t"
        "ds_read_b64_tr_b16 v[222:223], %[g0] offset:4992\n\t"
        "v_mfma_f32_16x16x32_bf16 v[36:39], v[84:87], v[224:227], 0\n\t"
        "ds_read_b64_tr_b16 v[224:225], %[g1] offset:4608\n\t"
        "ds_read_b64_tr_b16 v[226:227], %[g1] offset:4992\n\t"
        "v_mfma_f32_16x16x32_bf16 v[40:43], v[84:87], v[228:231], 0\n\t"
        "s_waitcnt lgkmcnt(6)\n\t"
        "ds_read_b64_tr_b16 v[228:229], %[g2] offset:4608\n\t"
        "ds_read_b64_tr_b16 v[230:231], %[g2] offset:4992\n\t"
        "v_mfma_f32_16x16x32_bf16 v[32:35], v[88:91], v[232:235], v[32:35]\n\t"
        "ds_read_b128 v[84:87], v47 offset:64\n\t"
        "ds_read_b64_tr_b16 v[232:233], %[g0] offset:6144\n\t"
        "ds_read_b64_tr_b16 v[234:235], %[g0] offset:6528\n\t"
        "v_mfma_f32_16x16x32_bf16 v[36:39], v[88:91], v[236:239], v[36:39]\n\t"
        "ds_read_b64_tr_b16 v[236:237], %[g1] offset:6144\n\t"
        "ds_read_b64_tr_b16 v[238:239], %[g1] offset:6528\n\t"
        "v_mfma_f32_16x16x32_bf16 v[40:43], v[88:91], v[240:243], v[40:43]\n\t"
        "s_waitcnt lgkmcnt(12)\n\t"
        "ds_read_b64_tr_b16 v[240:241], %[g2] offset:6144\n\t"
        "ds_read_b64_tr_b16 v[242:243], %[g2] offset:6528\n\t"
        "v_mfma_f32_16x16x32_bf16 v[32:35], v[92:95], v[244:247], v[32:35]\n\t"
        "ds_read_b128 v[88:91], v47 offset:128\n\t"
        "ds_read_b64_tr_b16 v[244:245], %[g0] offset:7680\n\t"
        "ds_read_b64_tr_b16 v[246:247], %[g0] offset:8064\n\t"
        "v_mfma_f32_16x16x32_bf16 v[36:39], v[92:95], v[248:251], v[36:39]\n\t"
        "ds_read_b64_tr_b16 v[248:249], %[g1] offset:7680\n\t"
        "ds_read_b64_tr_b16 v[250:251], %[g1] offset:8064\n\t"
        "v_mfma_f32_16x16x32_bf16 v[40:43], v[92:95], v[252:255], v[40:43]\n\t"
        "s_waitcnt lgkmcnt(15)\n\t"
        "ds_read_b64_tr_b16 v[252:253], %[g2] offset:7680\n\t"
        "ds_read_b64_tr_b16 v[254:255], %[g2] offset:8064\n\t"
        "v_mfma_f32_16x16x32_bf16 v[32:35], v[96:99], v[48:51], v[32:35]\n\t"
        "ds_read_b128 v[92:95], v47 offset:192\n\t"
        "ds_read_b64_tr_b16 v[48:49], %[g0] offset:9216\n\t"
        "ds_read_b64_tr_b16 v[50:51], %[g0] offset:9600\n\t"
        "v_mfma_f32_16x16x32_bf16 v[36:39], v[96:99], v[52:55], v[36:39]\n\t"
        "ds_read_b64_tr_b16 v[52:53], %[g1] offset:9216\n\t"
        "ds_read_b64_tr_b16 v[54:55], %[g1] offset:9600\n\t"
        "v_mfma_f32_16x16x32_bf16 v[40:43], v[96:99], v[56:59], v[40:43]\n\t"
        "s_waitcnt lgkmcnt(15)\n\t"
        "ds_read_b64_tr_b16 v[56:57], %[g2] offset:9216\n\t"
        "ds_read_b64_tr_b16 v[58:59], %[g2] offset:9600\n\t"
        "v_mfma_f32_16x16x32_bf16 v[32:35], v[84:87], v[60:63], v[32:35]\n\t"
        "ds_read_b128 v[96:99], v47 offset:256\n\t"
        "ds_read_b64_tr_b16 v[60:61], %[g0] offset:10752\n\t"
        "ds_read_b64_tr_b16 v[62:63], %[g0] offset:11136\n\t"
        "v_mfma_f32_16x16x32_bf16 v[36:39], v[84:87], v[64:67], v[36:39]\n\t"
        "ds_read_b64_tr_b16 v[64:65], %[g1] offset:10752\n\t"
        "ds_read_b64_tr_b16 v[66:67], %[g1] offset:11136\n\t"
        "v_mfma_f32_16x16x32_bf16 v[40:43], v[84:87], v[68:71], v[40:43]\n\t"
        "s_waitcnt lgkmcnt(15)\n\t"
        "ds_read_b64_tr_b16 v[68:69], %[g2] offset:10752\n\t"
        "ds_read_b64_tr_b16 v[70:71], %[g2] offset:11136\n\t"
        "v_mfma_f32_16x16x32_bf16 v[32:35], v[88:91], v[72:75], v[32:35]\n\t"
        "ds_read_b128 v[84:87], v47 offset:320\n\t"
        "ds_read_b64_tr_b16 v[72:73], %[g0] offset:12288\n\t"
        "ds_read_b64_tr_b16 v[74:75], %[g0] offset:12672\n\t"
        "v_mfma_f32_16x16x32_bf16 v[36:39], v[88:91], v[76:79], v[36:39]\n\t"
        "ds_read_b64_tr_b16 v[76:77], %[g1] offset:12288\n\t"
        "ds_read_b64_tr_b16 v[78:79], %[g1] offset:12672\n\t"
        "v_mfma_f32_16x16x32_bf16 v[40:43], v[88:91], v[80:83], v[40:43]\n\t"
        "s_waitcnt lgkmcnt(15)\n\t"
        "ds_read_b64_tr_b16 v[80:81], %[g2] offset:12288\n\t"
        "ds_read_b64_tr_b16 v[82:83], %[g2] offset:12672\n\t"
        "v_mfma_f32_16x16x32_bf16 v[32:35], v[92:95], v[220:223], v[32:35]\n\t"
        "ds_read_b128 v[88:91], v47 offset:384\n\t"
        "ds_read_b64_tr_b16 v[220:221], %[g0] offset:13824\n\t"
        "ds_read_b64_tr_b16 v[222:223], %[g0] offset:14208\n\t"
        "v_mfma_f32_16x16x32_bf16 v[36:39], v[92:95], v[224:227], v[36:39]\n\t"
        "ds_read_b64_tr_b16 v[224:225], %[g1] offset:13824\n\t"
        "ds_read_b64_tr_b16 v[226:227], %[g1] offset:14208\n\t"
        "v_mfma_f32_16x16x32_bf16 v[40:43], v[92:95], v[228:231], v[40:43]\n\t"
        "s_waitcnt lgkmcnt(15)\n\t"
        "ds_read_b64_tr_b16 v[228:229], %[g2] offset:13824\n\t"
        "ds_read_b64_tr_b16 v[230:231], %[g2] offset:14208\n\t"
        "v_mfma_f32_16x16x32_bf16 v[32:35], v[96:99], v[232:235], v[32:35]\n\t"
        "ds_read_b128 v[92:95], v47 offset:448\n\t"
        "ds_read_b64_tr_b16 v[232:233], %[g0] offset:15360\n\t"
        "ds_read_b64_tr_b16 v[234:235], %[g0] offset:15744\n\t"
        "v_mfma_f32_16x16x32_bf16 v[36:39], v[96:99], v[236:239], v[36:39]\n\t"
        "ds_read_b64_tr_b16 v[236:237], %[g1] offset:15360\n\t"
        "ds_read_b64_tr_b16 v[238:239], %[g1] offset:15744\n\t"
        "v_mfma_f32_16x16x32_bf16 v[40:43], v[96:99], v[240:243], v[40:43]\n\t"
        "s_waitcnt lgkmcnt(15)\n\t"
        "ds_read_b64_tr_b16 v[240:241], %[g2] offset:15360\n\t"
        "ds_read_b64_tr_b16 v[242:243], %[g2] offset:15744\n\t"
        "v_mfma_f32_16x16x32_bf16 v[32:35], v[84:87], v[244:247], v[32:35]\n\t"
        "ds_read_b128 v[96:99], v47 offset:512\n\t"
        "ds_read_b64_tr_b16 v[244:245], %[g0] offset:16896\n\t"
        "ds_read_b64_tr_b16 v[246:247], %[g0] offset:17280\n\t"
        "v_mfma_f32_16x16x32_bf16 v[36:39], v[84:87], v[248:251], v[36:39]\n\t"
        "ds_read_b64_tr_b16 v[248:249], %[g1] offset:16896\n\t"
        "ds_read_b64_tr_b16 v[250:251], %[g1] offset:17280\n\t"
        "v_mfma_f32_16x16x32_bf16 v[40:43], v[84:87], v[252:255], v[40:43]\n\t"
        "s_waitcnt lgkmcnt(15)\n\t"
        "ds_read_b64_tr_b16 v[252:253], %[g2] offset:16896\n\t"
        "ds_read_b64_tr_b16 v[254:255], %[g2] offset:17280\n\t"
        "v_mfma_f32_16x16x32_bf16 v[32:35], v[88:91], v[48:51], v[32:35]\n\t"
        "ds_read_b128 v[84:87], v47 offset:576\n\t"
        "ds_read_b64_tr_b16 v[48:49], %[g0] offset:18432\n\t"
        "ds_read_b64_tr_b16 v[50:51], %[g0] offset:18816\n\t"
        "v_mfma_f32_16x16x32_bf16 v[36:39], v[88:91], v[52:55], v[36:39]\n\t"
        "ds_read_b64_tr_b16 v[52:53], %[g1] offset:18432\n\t"
        "ds_read_b64_tr_b16 v[54:55], %[g1] offset:18816\n\t"
        "v_mfma_f32_16x16x32_bf16 v[40:43], v[88:91], v[56:59], v[40:43]\n\t"
        "s_waitcnt lgkmcnt(15)\n\t"
        "ds_read_b64_tr_b16 v[56:57], %[g2] offset:18432\n\t"
        "ds_read_b64_tr_b16 v[58:59], %[g2] offset:18816\n\t"
        "v_mfma_f32_16x16x32_bf16 v[32:35], v[92:95], v[60:63], v[32:35]\n\t"
        "ds_read_b128 v[88:91], v47 offset:640\n\t"
        "ds_read_b64_tr_b16 v[60:61], %[g0] offset:19968\n\t"
        "ds_read_b64_tr_b16 v[62:63], %[g0] offset:20352\n\t"
        "v_mfma_f32_16x16x32_bf16 v[36:39], v[92:95], v[64:67], v[36:39]\n\t"
        "ds_read_b64_tr_b16 v[64:65], %[g1] offset:19968\n\t"
        "ds_read_b64_tr_b16 v[66:67], %[g1] offset:20352\n\t"
        "v_mfma_f32_16x16x32_bf16 v[40:43], v[92:95], v[68:71], v[40:43]\n\t"
        "s_waitcnt lgkmcnt(15)\n\t"
        "ds_read_b64_tr_b16 v[68:69], %[g2] offset:19968\n\t"
        "ds_read_b64_tr_b16 v[70:71], %[g2] offset:20352\n\t"
        "v_mfma_f32_16x16x32_bf16 v[32:35], v[96:99], v[72:75], v[32:35]\n\t"
        "ds_read_b128 v[92:95], v47 offset:704\n\t"
        "ds_read_b64_tr_b16 v[72:73], %[g0] offset:21504\n\t"
        "ds_read_b64_tr_b16 v[74:75], %[g0] offset:21888\n\t"
        "v_mfma_f32_16x16x32_bf16 v[36:39], v[96:99], v[76:79], v[36:39]\n\t"
        "ds_read_b64_tr_b16 v[76:77], %[g1] offset:21504\n\t"
        "ds_read_b64_tr_b16 v[78:79], %[g1] offset:21888\n\t"
        "v_mfma_f32_16x16x32_bf16 v[40:43], v[96:99], v[80:83], v[40:43]\n\t"
        "s_waitcnt lgkmcnt(15)\n\t"
        "ds_read_b64_tr_b16 v[80:81], %[g2] offset:21504\n\t"
        "ds_read_b64_tr_b16 v[82:83], %[g2] offset:21888\n\t"
        "v_mfma_f32_16x16x32_bf16 v[32:35], v[84:87], v[220:223], v[32:35]\n\t"
        "ds_read_b128 v[96:99], v47 offset:768\n\t"
        "v_mfma_f32_16x16x32_bf16 v[36:39], v[84:87], v[224:227], v[36:39]\n\t"
        "v_mfma_f32_16x16x32_bf16 v[40:43], v[84:87], v[228:231], v[40:43]\n\t"
        "s_waitcnt lgkmcnt(14)\n\t"
        "v_mfma_f32_16x16x32_bf16 v[32:35], v[88:91], v[232:235], v[32:35]\n\t"
        "ds_read_b128 v[84:87], v47 offset:832\n\t"
        "v_mfma_f32_16x16x32_bf16 v[36:39], v[88:91], v[236:239], v[36:39]\n\t"
        "v_mfma_f32_16x16x32_bf16 v[40:43], v[88:91], v[240:243], v[40:43]\n\t"
        "s_waitcnt lgkmcnt(8)\n\t"
        "v_mfma_f32_16x16x32_bf16 v[32:35], v[92:95], v[244:247], v[32:35]\n\t"
        "ds_read_b128 v[88:91], v47 offset:896\n\t"
        "v_mfma_f32_16x16x32_bf16 v[36:39], v[92:95], v[248:251], v[36:39]\n\t"
        "v_mfma_f32_16x16x32_bf16 v[40:43], v[92:95], v[252:255], v[40:43]\n\t"
        "s_waitcnt lgkmcnt(2)\n\t"
        "v_mfma_f32_16x16x32_bf16 v[32:35], v[96:99], v[48:51], v[32:35]\n\t"
        "v_mfma_f32_16x16x32_bf16 v[36:39], v[96:99], v[52:55], v[36:39]\n\t"
        "v_mfma_f32_16x16x32_bf16 v[40:43], v[96:99], v[56:59], v[40:43]\n\t"
        "s_waitcnt lgkmcnt(1)\n\t"
        "v_mfma_f32_16x16x32_bf16 v[32:35], v[84:87], v[60:63], v[32:35]\n\t"
        "v_mfma_f32_16x16x32_bf16 v[36:39], v[84:87], v[64:67], v[36:39]\n\t"
        "v_mfma_f32_16x16x32_bf16 v[40:43], v[84:87], v[68:71], v[40:43]\n\t"
        "s_waitcnt lgkmcnt(0)\n\t"
        "v_mfma_f32_16x16x32_bf16 v[32:35], v[88:91], v[72:75], v[32:35]\n\t"
        "v_mfma_f32_16x16x32_bf16 v[36:39], v[88:91], v[76:79], v[36:39]\n\t"
        "v_mfma_f32_16x16x32_bf16 v[40:43], v[88:91], v[80:83], v[40:43]\n\t"
        "s_nop 7\n\t"
        "v_mov_b32_dpp v36, v40 row_shl:8 row_mask:0x2 bank_mask:0xf\n\t"
        "v_mov_b32_dpp v37, v41 row_shl:8 row_mask:0x2 bank_mask:0xf\n\t"
        "v_mov_b32_dpp v32, v40 quad_perm:[0,1,2,3] row_mask:0x2 bank_mask:0xf\n\t"
        "v_mov_b32_dpp v33, v41 quad_perm:[0,1,2,3] row_mask:0x2 bank_mask:0xf\n\t"
        "v_fma_f32 v43, -%[sa0], v37, v36\n\t"
        "v_fma_f32 v42, -%[sa0], v33, v32\n\t"
        "s_branch Lc_%=\n\t"
        "Ly_%=:\n\t"
        "s_cmp_lt_i32 %[scn], 1\n\t"
        "s_cbranch_scc1 Lz_%=\n\t"
        "s_sub_u32 %[scn], %[scn], 1\n\t"
        "s_waitcnt lgkmcnt(0)\n\t"
        "s_branch Ls_%=\n\t"
        "Lt_%=:\n\t"
        "v_mov_b32 v44, %[sca]\n\t"
        "v_mov_b32 v45, %[scr]\n\t"
        "ds_write_b32 v44, v45\n\t"
        "v_add_u32 v45, 157472, v107\n\t"
        "Lr_%=:\n\t"
        "s_mov_b64 exec, %[sm4]\n\t"
        "buffer_load_dwordx4 v[60:63], %[c0], %[ssrd], %[scr] offen offset:0 sc1\n\t"
        "buffer_load_dwordx4 v[64:67], %[c0], %[ssrd], %[scr] offen offset:64 sc1\n\t"
        "buffer_load_dwordx4 v[68:71], %[c0], %[ssrd], %[scr] offen offset:128 sc1\n\t"
        "s_mov_b64 exec, -1\n\t"
        "buffer_load_dwordx4 v[24:27], v107, %[ssrd], %[scr] offen sc1\n\t"
        "buffer_load_dwordx4 v[28:31], %[c1], %[ssrd], %[scr] offen sc1\n\t"
        "s_waitcnt vmcnt(4)\n\t"
        "v_mfma_f32_16x16x32_bf16 v[84:87], v[60:63], v[220:223], 0\n\t"
        "v_max3_u32 v44, v62, v60, v61\n\t"
        "v_mfma_f32_16x16x32_bf16 v[88:91], v[60:63], v[224:227], 0\n\t"
        "v_max3_u32 v44, v44, v62, v63\n\t"
        "v_mfma_f32_16x16x32_bf16 v[92:95], v[60:63], v[228:231], 0\n\t"
        "s_waitcnt vmcnt(3)\n\t"
        "v_mfma_f32_16x16x32_bf16 v[84:87], v[64:67], v[232:235], v[84:87]\n\t"
        "v_max3_u32 v44, v44, v64, v65\n\t"
        "v_mfma_f32_16x16x32_bf16 v[88:91], v[64:67], v[236:239], v[88:91]\n\t"
        "v_max3_u32 v44, v44, v66, v67\n\t"
        "v_mfma_f32_16x16x32_bf16 v[92:95], v[64:67], v[240:243], v[92:95]\n\t"
        "s_waitcnt vmcnt(2)\n\t"
        "v_mfma_f32_16x16x32_bf16 v[84:87], v[68:71], v[244:247], v[84:87]\n\t"
        "v_max3_u32 v44, v44, v68, v69\n\t"
        "v_mfma_f32_16x16x32_bf16 v[88:91], v[68:71], v[248:251], v[88:91]\n\t"
        "v_max3_u32 v44, v44, v70, v71\n\t"
        "v_mfma_f32_16x16x32_bf16 v[92:95], v[68:71], v[252:255], v[92:95]\n\t"
        "v_cmp_eq_u32 vcc, -1, v44\n\t"
        "s_cbranch_vccnz Lx_%=\n\t"
        "s_waitcnt vmcnt(0)\n\t"
        "ds_write_b128 v45, v[24:27]\n\t"
        "v_max3_u32 v44, v24, v25, v26\n\t"
        "v_max3_u32 v44, v44, v27, v28\n\t"
        "s_mov_b64 exec, 0xff\n\t"
        "ds_write_b128 v45, v[28:31] offset:1024\n\t"
        "s_mov_b64 exec, -1\n\t"
        "v_max3_u32 v44, v44, v29, v30\n\t"
        "v_max_u32 v44, v44, v31\n\t"
        "v_cmp_eq_u32 vcc, -1, v44\n\t"
        "s_cbranch_vccnz Lx_%=\n\t"
        "Ld_%=:\n\t"
        "s_nop 1\n\t"
        "s_mov_b64 exec, 0xffff\n\t"
        "ds_write_b64 %[pa], v[84:85] offset:0\n\t"
        "ds_write_b64 %[pa], v[88:89] offset:128\n\t"
        "ds_write_b64 %[pa], v[92:93] offset:256\n\t"
        "s_mov_b64 exec, -1\n\t"
        "v_readlane_b32 %[sa0], v28, 8\n\t"
        "v_readlane_b32 %[sa1], v29, 8\n\t"
        "Lw_%=:\n\t"
        "ds_read_b128 v[76:79], %[ff]\n\t"
        "ds_read_b64 v[60:61], %[pg] offset:0\n\t"
        "ds_read_b64 v[62:63], %[pu] offset:0\n\t"
        "ds_read_b64 v[64:65], %[pg] offset:384\n\t"
        "ds_read_b64 v[66:67], %[pu] offset:384\n\t"
        "ds_read_b64 v[68:69], %[pg] offset:768\n\t"
        "ds_read_b64 v[70:71], %[pu] offset:768\n\t"
        "ds_read_b64 v[72:73], %[pg] offset:1152\n\t"
        "ds_read_b64 v[74:75], %[pu] offset:1152\n\t"
        "s_sub_u32 %[scn], %[scn], 1\n\t"
        "s_waitcnt lgkmcnt(8)\n\t"
        "v_min3_u32 v44, v76, v77, v78\n\t"
        "v_cmp_gt_u32 vcc, %[sl1], v44\n\t"
        "s_cmp_lt_i32 %[scn], 1\n\t"
        "s_cbranch_scc1 Lg_%=\n\t"
        "s_cbranch_vccnz Lw_%=\n\t"
        "Lg_%=:\n\t"
        "s_waitcnt lgkmcnt(0)\n\t"
        "v_add_f32 v60, v60, v64\n\t"
        "v_add_f32 v61, v61, v65\n\t"
        "v_add_f32 v62, v62, v66\n\t"
        "v_add_f32 v63, v63, v67\n\t"
        "v_add_f32 v68, v68, v72\n\t"
        "v_add_f32 v69, v69, v73\n\t"
        "v_add_f32 v70, v70, v74\n\t"
        "v_add_f32 v71, v71, v75\n\t"
        "v_add_f32 v60, v60, v68\n\t"
        "v_add_f32 v61, v61, v69\n\t"
        "v_add_f32 v62, v62, v70\n\t"
        "v_add_f32 v63, v63, v71\n\t"
        "v_fma_f32 v42, -%[sa0], v61, v60\n\t"
        "v_fma_f32 v43, -%[sa0], v63, v62\n\t"
        "Lc_%=:\n\t"
        "v_mul_f32 v42, %[sa1], v42\n\t"
        "v_mul_f32 v43, %[sa1], v43\n\t"
        "v_mul_f32 v46, 0xbfb8aa3b, v42\n\t"
        "v_exp_f32 v46, v46\n\t"
        "v_mul_f32 v42, v42, v43\n\t"
        "v_add_f32 v46, 1.0, v46\n\t"
        "v_rcp_f32 v46, v46\n\t"
        "s_mov_b32 %[sm], %[sa0]\n\t"
        "s_mov_b32 %[srs], %[sa1]\n\t"
        "v_mul_f32 v42, v42, v46\n\t"
        "v_mov_b32 v60, 0\n\t"
        "v_mov_b32 v61, 0\n\t"
        "v_mov_b32_dpp v43, v42 row_shl:1 row_mask:0xf bank_mask:0xf bound_ctrl:1\n\t"
        "v_cvt_pk_bf16_f32 v42, v42, v43\n\t"
        "v_mov_b32 v62, 0\n\t"
        "v_readlane_b32 %[sa0], v42, 0\n\t"
        "v_mov_b32 v63, 0\n\t"
        "v_readlane_b32 %[sa1], v42, 2\n\t"
        "v_mov_b32 v64, 0\n\t"
        "v_readlane_b32 %[sa2], v42, 4\n\t"
        "v_mov_b32 v65, 0\n\t"
        "v_readlane_b32 %[sa3], v42, 6\n\t"
        "v_mov_b32 v66, 0\n\t"
        "v_readlane_b32 %[sa4], v42, 8\n\t"
        "v_mov_b32 v67, 0\n\t"
        "v_readlane_b32 %[sa5], v42, 10\n\t"
        "v_mov_b32 v68, 0\n\t"
        "v_readlane_b32 %[sa6], v42, 12\n\t"
        "v_readlane_b32 %[sa7], v42, 14\n\t"
        "v_readlane_b32 %[sa8], v42, 16\n\t"
        "v_readlane_b32 %[sa9], v42, 18\n\t"
        "v_readlane_b32 %[sa10], v42, 20\n\t"
        "v_readlane_b32 %[sa11], v42, 22\n\t"
        "v_dot2c_f32_bf16 v60, %[sa0], v112\n\t"
        "v_dot2c_f32_bf16 v61, %[sa0], v116\n\t"
        "v_dot2c_f32_bf16 v62, %[sa0], v113\n\t"
        "v_dot2c_f32_bf16 v63, %[sa0], v117\n\t"
        "v_dot2c_f32_bf16 v60, %[sa1], v120\n\t"
        "v_dot2c_f32_bf16 v61, %[sa1], v124\n\t"
        "v_dot2c_f32_bf16 v62, %[sa1], v121\n\t"
        "v_dot2c_f32_bf16 v63, %[sa1], v125\n\t"
        "v_dot2c_f32_bf16 v60, %[sa2], v128\n\t"
        "v_dot2c_f32_bf16 v61, %[sa2], v132\n\t"
        "v_dot2c_f32_bf16 v62, %[sa2], v129\n\t"
        "v_dot2c_f32_bf16 v63, %[sa2], v133\n\t"
        "v_dot2c_f32_bf16 v60, %[sa3], v136\n\t"
        "v_dot2c_f32_bf16 v61, %[sa3], v140\n\t"
        "v_dot2c_f32_bf16 v62, %[sa3], v137\n\t"
        "v_dot2c_f32_bf16 v63, %[sa3], v141\n\t"
        "v_dot2c_f32_bf16 v60, %[sa4], v144\n\t"
        "v_dot2c_f32_bf16 v61, %[sa4], v148\n\t"
        "v_dot2c_f32_bf16 v62, %[sa4], v145\n\t"
        "v_dot2c_f32_bf16 v63, %[sa4], v149\n\t"
        "v_dot2c_f32_bf16 v60, %[sa5], v152\n\t"
        "v_dot2c_f32_bf16 v61, %[sa5], v156\n\t"
        "v_dot2c_f32_bf16 v62, %[sa5], v153\n\t"
        "v_dot2c_f32_bf16 v63, %[sa5], v157\n\t"
        "v_dot2c_f32_bf16 v60, %[sa6], v160\n\t"
        "v_dot2c_f32_bf16 v61, %[sa6], v164\n\t"
        "v_dot2c_f32_bf16 v62, %[sa6], v161\n\t"
        "v_dot2c_f32_bf16 v63, %[sa6], v165\n\t"
        "v_dot2c_f32_bf16 v60, %[sa7], v168\n\t"
        "v_dot2c_f32_bf16 v61, %[sa7], v172\n\t"
        "v_dot2c_f32_bf16 v62, %[sa7], v169\n\t"
        "v_dot2c_f32_bf16 v63, %[sa7], v173\n\t"
        "v_dot2c_f32_bf16 v60, %[sa8], v176\n\t"
        "v_dot2c_f32_bf16 v61, %[sa8], v180\n\t"
        "v_dot2c_f32_bf16 v62, %[sa8], v177\n\t"
        "v_dot2c_f32_bf16 v63, %[sa8], v181\n\t"
        "v_dot2c_f32_bf16 v60, %[sa9], v184\n\t"
        "v_dot2c_f32_bf16 v61, %[sa9], v188\n\t"
        "v_dot2c_f32_bf16 v62, %[sa9], v185\n\t"
        "v_dot2c_f32_bf16 v63, %[sa9], v189\n\t"
        "v_dot2c_f32_bf16 v60, %[sa10], v192\n\t"
        "v_dot2c_f32_bf16 v61, %[sa10], v196\n\t"
        "v_dot2c_f32_bf16 v62, %[sa10], v193\n\t"
        "v_dot2c_f32_bf16 v63, %[sa10], v197\n\t"
        "v_dot2c_f32_bf16 v60, %[sa11], v200\n\t"
        "v_dot2c_f32_bf16 v61, %[sa11], v204\n\t"
        "v_dot2c_f32_bf16 v62, %[sa11], v201\n\t"
        "v_dot2c_f32_bf16 v63, %[sa11], v205\n\t"
        "v_dot2c_f32_bf16 v64, %[sa0], v114\n\t"
        "v_dot2c_f32_bf16 v65, %[sa0], v118\n\t"
        "v_dot2c_f32_bf16 v66, %[sa0], v115\n\t"
        "v_dot2c_f32_bf16 v67, %[sa0], v119\n\t"
        "v_dot2c_f32_bf16 v68, %[sa0], v208\n\t"
        "v_min_u32 v60, -2, v60\n\t"
        "v_min_u32 v61, -2, v61\n\t"
        "v_min_u32 v62, -2, v62\n\t"
        "v_min_u32 v63, -2, v63\n\t"
        "buffer_store_dwordx4 v[60:63], %[ya], %[ssrd], %[syb] offen sc1\n\t"
        "v_dot2c_f32_bf16 v64, %[sa1], v122\n\t"
        "v_dot2c_f32_bf16 v65, %[sa1], v126\n\t"
        "v_dot2c_f32_bf16 v66, %[sa1], v123\n\t"
        "v_dot2c_f32_bf16 v67, %[sa1], v127\n\t"
        "v_dot2c_f32_bf16 v68, %[sa1], v209\n\t"
        "v_dot2c_f32_bf16 v64, %[sa2], v130\n\t"
        "v_dot2c_f32_bf16 v65, %[sa2], v134\n\t"
        "v_dot2c_f32_bf16 v66, %[sa2], v131\n\t"
        "v_dot2c_f32_bf16 v67, %[sa2], v135\n\t"
        "v_dot2c_f32_bf16 v68, %[sa2], v210\n\t"
        "v_dot2c_f32_bf16 v64, %[sa3], v138\n\t"
        "v_dot2c_f32_bf16 v65, %[sa3], v142\n\t"
        "v_dot2c_f32_bf16 v66, %[sa3], v139\n\t"
        "v_dot2c_f32_bf16 v67, %[sa3], v143\n\t"
        "v_dot2c_f32_bf16 v68, %[sa3], v211\n\t"
        "v_dot2c_f32_bf16 v64, %[sa4], v146\n\t"
        "v_dot2c_f32_bf16 v65, %[sa4], v150\n\t"
        "v_dot2c_f32_bf16 v66, %[sa4], v147\n\t"
        "v_dot2c_f32_bf16 v67, %[sa4], v151\n\t"
        "v_dot2c_f32_bf16 v68, %[sa4], v212\n\t"
        "v_dot2c_f32_bf16 v64, %[sa5], v154\n\t"
        "v_dot2c_f32_bf16 v65, %[sa5], v158\n\t"
        "v_dot2c_f32_bf16 v66, %[sa5], v155\n\t"
        "v_dot2c_f32_bf16 v67, %[sa5], v159\n\t"
        "v_dot2c_f32_bf16 v68, %[sa5], v213\n\t"
        "v_dot2c_f32_bf16 v64, %[sa6], v162\n\t"
        "v_dot2c_f32_bf16 v65, %[sa6], v166\n\t"
        "v_dot2c_f32_bf16 v66, %[sa6], v163\n\t"
        "v_dot2c_f32_bf16 v67, %[sa6], v167\n\t"
        "v_dot2c_f32_bf16 v68, %[sa6], v214\n\t"
        "v_dot2c_f32_bf16 v64, %[sa7], v170\n\t"
        "v_dot2c_f32_bf16 v65, %[sa7], v174\n\t"
        "v_dot2c_f32_bf16 v66, %[sa7], v171\n\t"
        "v_dot2c_f32_bf16 v67, %[sa7], v175\n\t"
        "v_dot2c_f32_bf16 v68, %[sa7], v215\n\t"
        "v_dot2c_f32_bf16 v64, %[sa8], v178\n\t"
        "v_dot2c_f32_bf16 v65, %[sa8], v182\n\t"
        "v_dot2c_f32_bf16 v66, %[sa8], v179\n\t"
        "v_dot2c_f32_bf16 v67, %[sa8], v183\n\t"
        "v_dot2c_f32_bf16 v68, %[sa8], v216\n\t"
        "v_dot2c_f32_bf16 v64, %[sa9], v186\n\t"
        "v_dot2c_f32_bf16 v65, %[sa9], v190\n\t"
        "v_dot2c_f32_bf16 v66, %[sa9], v187\n\t"
        "v_dot2c_f32_bf16 v67, %[sa9], v191\n\t"
        "v_dot2c_f32_bf16 v68, %[sa9], v217\n\t"
        "v_dot2c_f32_bf16 v64, %[sa10], v194\n\t"
        "v_dot2c_f32_bf16 v65, %[sa10], v198\n\t"
        "v_dot2c_f32_bf16 v66, %[sa10], v195\n\t"
        "v_dot2c_f32_bf16 v67, %[sa10], v199\n\t"
        "v_dot2c_f32_bf16 v68, %[sa10], v218\n\t"
        "v_dot2c_f32_bf16 v64, %[sa11], v202\n\t"
        "v_dot2c_f32_bf16 v65, %[sa11], v206\n\t"
        "v_dot2c_f32_bf16 v66, %[sa11], v203\n\t"
        "v_dot2c_f32_bf16 v67, %[sa11], v207\n\t"
        "v_dot2c_f32_bf16 v68, %[sa11], v219\n\t"
        "v_min_u32 v64, -2, v64\n\t"
        "v_min_u32 v65, -2, v65\n\t"
        "v_min_u32 v66, -2, v66\n\t"
        "v_min_u32 v67, -2, v67\n\t"
        "v_min_u32 v68, -2, v68\n\t"
        "buffer_store_dwordx4 v[64:67], %[ya], %[ssrd], %[syc] offen sc1\n\t"
        "buffer_store_dword v68, %[ye], %[ssrd], %[syb] offen sc1\n\t"
        "s_branch Le_%=\n\t"
        "Lx_%=:\n\t"
        "s_waitcnt vmcnt(0)\n\t"
        "s_cmp_lt_i32 %[scn], 1\n\t"
        "s_cbranch_scc1 Ld_%=\n\t"
        "s_sub_u32 %[scn], %[scn], 1\n\t"
        "s_branch Lr_%=\n\t"
        "Le_%=:\n\t"
        : [sa0] "=&s"(ap[0]), [sa1] "=&s"(ap[1]), [sa2] "=&s"(ap[2]), [sa3] "=&s"(ap[3]), [sa4] "=&s"(ap[4]), [sa5] "=&s"(ap[5]), [sa6] "=&s"(ap[6]), [sa7] "=&s"(ap[7]), [sa8] "=&s"(ap[8]), [sa9] "=&s"(ap[9]), [sa10] "=&s"(ap[10]), [sa11] "=&s"(ap[11]), [sm] "=&s"(m), [srs] "=&s"(rs), [scn] "+s"(cn)
        : [ssrd] "s"(srd), [scr] "s"(cr), [c0] "v"(c0), [c1] "v"(c1), [g0] "v"(g0), [g1] "v"(g1), [g2] "v"(g2), [pa] "v"(pa), [ff] "v"(ff), [pg] "v"(pg), [pu] "v"(pu), [ya] "v"(ya), [ye] "v"(ye),
          [syb] "s"(yb), [syc] "s"(yb + 262144), [sca] "s"(ca), [sl1] "s"(l1), [sm4] "s"(0x1111111111111111ull)
        : "memory", "scc", "vcc", "v24", "v25", "v26", "v27", "v28", "v29", "v30", "v31", "v32", "v33", "v34", "v35", "v36", "v37", "v38", "v39", "v40", "v41", "v42", "v43", "v44", "v45", "v46", "v47");
}

DEV void poll_stats(u32x4 srd, u32 va, u32 so, float (&o)[3], u32& cn) {
    u32 t;
    asm volatile(
        "s_nop 4\n\t"
        "buffer_load_dwordx4 v[48:51], %[va], %[ssrd], %[so] offen offset:0 sc1\n\t"
        "buffer_load_dwordx4 v[52:55], %[va], %[ssrd], %[so] offen offset:1024 sc1\n\t"
        "buffer_load_dwordx4 v[56:59], %[va], %[ssrd], %[so] offen offset:256 sc1\n\t"
        "buffer_load_dwordx4 v[60:63], %[va], %[ssrd], %[so] offen offset:1280 sc1\n\t"
        "buffer_load_dwordx4 v[64:67], %[va], %[ssrd], %[so] offen offset:512 sc1\n\t"
        "buffer_load_dwordx4 v[68:71], %[va], %[ssrd], %[so] offen offset:1536 sc1\n\t"
        "buffer_load_dwordx4 v[72:75], %[va], %[ssrd], %[so] offen offset:768 sc1\n\t"
        "buffer_load_dwordx4 v[76:79], %[va], %[ssrd], %[so] offen offset:1792 sc1\n\t"
        "Lp_%=:\n\t"
        "s_waitcnt vmcnt(0)\n\t"
        "v_max3_u32 v51, v48, v49, v50\n\t"
        "v_max3_u32 v55, v52, v53, v54\n\t"
        "v_max3_u32 v59, v56, v57, v58\n\t"
        "v_max3_u32 v63, v60, v61, v62\n\t"
        "v_max3_u32 v67, v64, v65, v66\n\t"
        "v_max3_u32 v71, v68, v69, v70\n\t"
        "v_max3_u32 v75, v72, v73, v74\n\t"
        "v_max3_u32 v79, v76, v77, v78\n\t"
        "v_add_f32 v48, v48, v52\n\t"
        "v_add_f32 v56, v56, v60\n\t"
        "v_add_f32 v64, v64, v68\n\t"
        "v_add_f32 v72, v72, v76\n\t"
        "v_add_f32 v49, v49, v53\n\t"
        "v_add_f32 v57, v57, v61\n\t"
        "v_add_f32 v65, v65, v69\n\t"
        "v_add_f32 v73, v73, v77\n\t"
        "v_add_f32 v50, v50, v54\n\t"
        "v_add_f32 v58, v58, v62\n\t"
        "v_add_f32 v66, v66, v70\n\t"
        "v_add_f32 v74, v74, v78\n\t"
        "v_max3_u32 %[t], v51, v55, v59\n\t"
        "v_max3_u32 v63, v63, v67, v71\n\t"
        "v_max3_u32 %[t], %[t], v75, v79\n\t"
        "v_max_u32 %[t], %[t], v63\n\t"
        "v_cmp_eq_u32 vcc, -1, %[t]\n\t"
        "v_add_f32 v48, v48, v56\n\t"
        "v_add_f32 v64, v64, v72\n\t"
        "v_add_f32 v49, v49, v57\n\t"
        "v_add_f32 v65, v65, v73\n\t"
        "v_add_f32 v50, v50, v58\n\t"
        "v_add_f32 v66, v66, v74\n\t"
        "s_cbranch_vccz Lf0_%=\n\t"
        "buffer_load_dwordx4 v[48:51], %[va], %[ssrd], %[so] offen offset:0 sc1\n\t"
        "buffer_load_dwordx4 v[52:55], %[va], %[ssrd], %[so] offen offset:1024 sc1\n\t"
        "buffer_load_dwordx4 v[56:59], %[va], %[ssrd], %[so] offen offset:256 sc1\n\t"
        "buffer_load_dwordx4 v[60:63], %[va], %[ssrd], %[so] offen offset:1280 sc1\n\t"
        "buffer_load_dwordx4 v[64:67], %[va], %[ssrd], %[so] offen offset:512 sc1\n\t"
        "buffer_load_dwordx4 v[68:71], %[va], %[ssrd], %[so] offen offset:1536 sc1\n\t"
        "buffer_load_dwordx4 v[72:75], %[va], %[ssrd], %[so] offen offset:768 sc1\n\t"
        "buffer_load_dwordx4 v[76:79], %[va], %[ssrd], %[so] offen offset:1792 sc1\n\t"
        "s_sub_u32 %[scn], %[scn], 1\n\t"
        "s_cmp_lg_u32 %[scn], 0\n\t"
        "s_cbranch_scc1 Lp_%=\n\t"
        "s_waitcnt vmcnt(0)\n\t"
        "Lf0_%=:\n\t"
        "v_add_f32 %[o0], v48, v64\n\t"
        "v_add_f32 %[o1], v49, v65\n\t"
        "v_add_f32 %[o2], v50, v66\n\t"
        "Le_%=:\n\t"
        : [o0] "=&v"(o[0]), [o1] "=&v"(o[1]), [o2] "=&v"(o[2]), [t] "=&v"(t), [scn] "+s"(cn)
        : [ssrd] "s"(srd), [va] "v"(va), [so] "s"(so)
        : "memory", "scc", "vcc");
}
DEV void poll_stats1(u32x4 srd, u32 so, float (&o)[3], u32& cn) {
    u32 t;
    asm volatile(
        "s_nop 4\n\t"
        "buffer_load_dwordx4 v[60:63], v107, %[ssrd], %[so] offen sc1\n\t"
        "buffer_load_dwordx4 v[64:67], v107, %[ssrd], %[so] offen offset:1024 sc1\n\t"
        "Lp_%=:\n\t"
        "s_waitcnt vmcnt(0)\n\t"
        "v_max3_u32 %[t], v60, v61, v62\n\t"
        "v_max3_u32 %[t], %[t], v63, v64\n\t"
        "v_max3_u32 %[t], %[t], v65, v66\n\t"
        "v_max_u32 %[t], %[t], v67\n\t"
        "v_cmp_eq_u32 vcc, -1, %[t]\n\t"
        "s_cbranch_vccz Lf0_%=\n\t"
        "buffer_load_dwordx4 v[60:63], v107, %[ssrd], %[so] offen sc1\n\t"
        "buffer_load_dwordx4 v[64:67], v107, %[ssrd], %[so] offen offset:1024 sc1\n\t"
        "s_sub_u32 %[scn], %[scn], 1\n\t"
        "s_cmp_lg_u32 %[scn], 0\n\t"
        "s_cbranch_scc1 Lp_%=\n\t"
        "s_waitcnt vmcnt(0)\n\t"
        "Lf0_%=:\n\t"
        "v_add_f32 %[o0], v60, v64\n\t"
        "v_add_f32 %[o1], v61, v65\n\t"
        "v_add_f32 %[o2], v62, v66\n\t"
        "Le_%=:\n\t"
        : [o0] "=&v"(o[0]), [o1] "=&v"(o[1]), [o2] "=&v"(o[2]), [t] "=&v"(t), [scn] "+s"(cn)
        : [ssrd] "s"(srd), [so] "s"(so)
        : "memory", "scc", "vcc");
}
DEV void poll_keys2(u32x4 srd, u32 so, u32 tag, u32& o, u32& cn) {
    u32 t;
    asm volatile(
        "s_nop 4\n\t"
        "buffer_load_dwordx4 v[48:51], v107, %[ssrd], %[so] offen sc1\n\t"
        "Lp_%=:\n\t"
        "s_waitcnt vmcnt(0)\n\t"
        "v_xor_b32 %[t], %[stag], v49\n\t"
        "v_xor_b32 %[o0], %[stag], v51\n\t"
        "v_or_b32 %[t], %[t], %[o0]\n\t"
        "v_cmp_ne_u32 vcc, 0, %[t]\n\t"
        "s_cbranch_vccz Lf0_%=\n\t"
        "buffer_load_dwordx4 v[48:51], v107, %[ssrd], %[so] offen sc1\n\t"
        "s_sub_u32 %[scn], %[scn], 1\n\t"
        "s_cmp_lg_u32 %[scn], 0\n\t"
        "s_cbranch_scc1 Lp_%=\n\t"
        "s_waitcnt vmcnt(0)\n\t"
        "Lf0_%=:\n\t"
        "v_max_u32 %[o0], v48, v50\n\t"
        "Le_%=:\n\t"
        : [o0] "=&v"(o), [t] "=&v"(t), [scn] "+s"(cn)
        : [ssrd] "s"(srd), [so] "s"(so), [stag] "s"(tag)
        : "memory", "scc", "vcc");
}
DEV void poll_keys(u32x4 srd, u32 so, u32 tag, u32 layer, u64 lm, int g0, int g1, int g2, u32& o, u32& cn) {
    u32 t;
    asm volatile(
        "s_nop 4\n\t"
        "buffer_load_dwordx4 v[96:99], v107, %[ssrd], %[so] offen sc1\n\t"
        "ds_read_b64_tr_b16 v[220:221], %[g0] offset:23040\n\t"
        "ds_read_b64_tr_b16 v[222:223], %[g0] offset:23424\n\t"
        "ds_read_b64_tr_b16 v[224:225], %[g1] offset:23040\n\t"
        "ds_read_b64_tr_b16 v[226:227], %[g1] offset:23424\n\t"
        "ds_read_b64_tr_b16 v[228:229], %[g2] offset:23040\n\t"
        "ds_read_b64_tr_b16 v[230:231], %[g2] offset:23424\n\t"
        "ds_read_b64_tr_b16 v[232:233], %[g0] offset:24576\n\t"
        "ds_read_b64_tr_b16 v[234:235], %[g0] offset:24960\n\t"
        "ds_read_b64_tr_b16 v[236:237], %[g1] offset:24576\n\t"
        "ds_read_b64_tr_b16 v[238:239], %[g1] offset:24960\n\t"
        "ds_read_b64_tr_b16 v[240:241], %[g2] offset:24576\n\t"
        "ds_read_b64_tr_b16 v[242:243], %[g2] offset:24960\n\t"
        "ds_read_b64_tr_b16 v[244:245], %[g0] offset:26112\n\t"
        "ds_read_b64_tr_b16 v[246:247], %[g0] offset:26496\n\t"
        "ds_read_b64_tr_b16 v[248:249], %[g1] offset:26112\n\t"
        "ds_read_b64_tr_b16 v[250:251], %[g1] offset:26496\n\t"
        "ds_read_b64_tr_b16 v[252:253], %[g2] offset:26112\n\t"
        "ds_read_b64_tr_b16 v[254:255], %[g2] offset:26496\n\t"
        "s_cmp_eq_u32 %[slay], 0\n\t"
        "s_cbranch_scc1 La_%=\n\t"
        "v_mov_b32 v60, 0x3f803f80\n\t"
        "v_mov_b32 v61, 0x3f803f80\n\t"
        "v_mov_b32 v62, 0x3f803f80\n\t"
        "v_mov_b32 v63, 0x3f803f80\n\t"
        "v_mov_b32 v64, 0x3f803f80\n\t"
        "v_mov_b32 v65, 0x3f803f80\n\t"
        "v_mov_b32 v66, 0x3f803f80\n\t"
        "v_mov_b32 v67, 0x3f803f80\n\t"
        "v_mov_b32 v68, 0x3f803f80\n\t"
        "v_mov_b32 v69, 0x3f803f80\n\t"
        "v_mov_b32 v70, 0x3f803f80\n\t"
        "v_mov_b32 v71, 0x3f803f80\n\t"
        "s_branch Lb_%=\n\t"
        "La_%=:\n\t"
        "v_mov_b32 v84, 0x3f803f80\n\t"
        "v_mov_b32 v85, 0x3f803f80\n\t"
        "v_mov_b32 v86, 0x3f803f80\n\t"
        "v_mov_b32 v87, 0x3f803f80\n\t"
        "v_add_u32 v88, 155168, v107\n\t"
        "ds_write_b128 v88, v[84:87]\n\t"
        "ds_write_b128 v88, v[84:87] offset:1024\n\t"
        "ds_read_b64_tr_b16 v[48:49], %[g0] offset:0\n\t"
        "ds_read_b64_tr_b16 v[50:51], %[g0] offset:384\n\t"
        "ds_read_b64_tr_b16 v[52:53], %[g1] offset:0\n\t"
        "ds_read_b64_tr_b16 v[54:55], %[g1] offset:384\n\t"
        "ds_read_b64_tr_b16 v[56:57], %[g2] offset:0\n\t"
        "ds_read_b64_tr_b16 v[58:59], %[g2] offset:384\n\t"
        "ds_read_b64_tr_b16 v[60:61], %[g0] offset:1536\n\t"
        "ds_read_b64_tr_b16 v[62:63], %[g0] offset:1920\n\t"
        "ds_read_b64_tr_b16 v[64:65], %[g1] offset:1536\n\t"
        "ds_read_b64_tr_b16 v[66:67], %[g1] offset:1920\n\t"
        "ds_read_b64_tr_b16 v[68:69], %[g2] offset:1536\n\t"
        "ds_read_b64_tr_b16 v[70:71], %[g2] offset:1920\n\t"
        "ds_read_b64_tr_b16 v[72:73], %[g0] offset:3072\n\t"
        "ds_read_b64_tr_b16 v[74:75], %[g0] offset:3456\n\t"
        "ds_read_b64_tr_b16 v[76:77], %[g1] offset:3072\n\t"
        "ds_read_b64_tr_b16 v[78:79], %[g1] offset:3456\n\t"
        "ds_read_b64_tr_b16 v[80:81], %[g2] offset:3072\n\t"
        "ds_read_b64_tr_b16 v[82:83], %[g2] offset:3456\n\t"
        "Lb_%=:\n\t"
        "s_waitcnt lgkmcnt(0)\n\t"
        "Lp_%=:\n\t"
        "s_waitcnt vmcnt(0)\n\t"
        "v_xor_b32 %[t], %[stag], v97\n\t"
        "v_xor_b32 %[o0], %[stag], v99\n\t"
        "v_or_b32 %[t], %[t], %[o0]\n\t"
        "v_max_u32 %[o0], v96, v98\n\t"
        "v_cndmask_b32 %[t], %[t], 0, %[slm]\n\t"
        "v_cmp_ne_u32 vcc, 0, %[t]\n\t"
        "s_cbranch_vccz Lf0_%=\n\t"
        "buffer_load_dwordx4 v[96:99], v107, %[ssrd], %[so] offen sc1\n\t"
        "s_sub_u32 %[scn], %[scn], 1\n\t"
        "s_cmp_lg_u32 %[scn], 0\n\t"
        "s_cbranch_scc1 Lp_%=\n\t"
        "s_waitcnt vmcnt(0)\n\t"
        "Lf0_%=:\n\t"
        "v_cndmask_b32 %[o0], %[o0], 0, %[slm]\n\t"
        "Le_%=:\n\t"
        : [o0] "=&v"(o), [t] "=&v"(t), [scn] "+s"(cn)
        : [ssrd] "s"(srd), [so] "s"(so), [stag] "s"(tag), [slay] "s"(layer), [slm] "s"(lm), [g0] "v"(g0), [g1] "v"(g1), [g2] "v"(g2)
        : "memory", "scc", "vcc");
}
DEV void poll_flags(u32x4 srd, u32 fo, u32 tag, u32& cn) {
    u32 t, d0;
    asm volatile(
        "s_nop 4\n\t"
        "s_sleep 5\n\t"
        "buffer_load_dword v48, %[fo], %[ssrd], 0 offen sc1\n\t"
        "Lp_%=:\n\t"
        "s_waitcnt vmcnt(0)\n\t"
        "v_cmp_ne_u32 vcc, %[stag], v48\n\t"
        "s_cbranch_vccz Lf0_%=\n\t"
        "buffer_load_dword v48, %[fo], %[ssrd], 0 offen sc1\n\t"
        "s_sub_u32 %[scn], %[scn], 1\n\t"
        "s_cmp_lg_u32 %[scn], 0\n\t"
        "s_cbranch_scc1 Lp_%=\n\t"
        "s_waitcnt vmcnt(0)\n\t"
        "Lf0_%=:\n\t"
        "s_nop 0\n\t"
        "Le_%=:\n\t"
        : [d0] "=&s"(d0), [t] "=&v"(t), [scn] "+s"(cn)
        : [ssrd] "s"(srd), [fo] "v"(fo), [stag] "s"(tag)
        : "memory", "scc", "vcc");
}
DEV void poll_flagh(u32x4 srd, u32 fo, u32 tag, u32& cn) {
    u32 t, d0;
    asm volatile(
        "s_nop 4\n\t"
        "buffer_load_dword v48, %[fo], %[ssrd], 0 offen sc1\n\t"
        "Lp_%=:\n\t"
        "s_waitcnt vmcnt(0)\n\t"
        "v_cmp_ne_u32 vcc, %[stag], v48\n\t"
        "s_cbranch_vccz Lf0_%=\n\t"
        "buffer_load_dword v48, %[fo], %[ssrd], 0 offen sc1\n\t"
        "s_sub_u32 %[scn], %[scn], 1\n\t"
        "s_cmp_lg_u32 %[scn], 0\n\t"
        "s_cbranch_scc1 Lp_%=\n\t"
        "s_waitcnt vmcnt(0)\n\t"
        "Lf0_%=:\n\t"
        "s_nop 0\n\t"
        "Le_%=:\n\t"
        : [d0] "=&s"(d0), [t] "=&v"(t), [scn] "+s"(cn)
        : [ssrd] "s"(srd), [fo] "v"(fo), [stag] "s"(tag)
        : "memory", "scc", "vcc");
}
DEV void poll_yp(u32x4 srd, u32 va, u32 so, float (&o)[4], u32& cn) {
    u32 t;
    asm volatile(
        "s_nop 4\n\t"
        "s_sleep 6\n\t"
        "buffer_load_dwordx4 v[48:51], %[va], %[ssrd], %[so0] offen sc1\n\t"
        "buffer_load_dwordx4 v[52:55], %[va], %[ssrd], %[so1] offen sc1\n\t"
        "buffer_load_dwordx4 v[56:59], %[va], %[ssrd], %[so2] offen sc1\n\t"
        "buffer_load_dwordx4 v[60:63], %[va], %[ssrd], %[so3] offen sc1\n\t"
        "Lp_%=:\n\t"
        "s_waitcnt vmcnt(0)\n\t"
        "v_max3_u32 %[t], v48, v49, v50\n\t"
        "v_max3_u32 %[t], %[t], v51, v52\n\t"
        "v_max3_u32 %[t], %[t], v53, v54\n\t"
        "v_max3_u32 %[t], %[t], v55, v56\n\t"
        "v_max3_u32 %[t], %[t], v57, v58\n\t"
        "v_max3_u32 %[t], %[t], v59, v60\n\t"
        "v_max3_u32 %[t], %[t], v61, v62\n\t"
        "v_max_u32 %[t], %[t], v63\n\t"
        "v_cmp_eq_u32 vcc, -1, %[t]\n\t"
        "s_cbranch_vccz Lf0_%=\n\t"
        "buffer_load_dwordx4 v[48:51], %[va], %[ssrd], %[so0] offen sc1\n\t"
        "buffer_load_dwordx4 v[52:55], %[va], %[ssrd], %[so1] offen sc1\n\t"
        "buffer_load_dwordx4 v[56:59], %[va], %[ssrd], %[so2] offen sc1\n\t"
        "buffer_load_dwordx4 v[60:63], %[va], %[ssrd], %[so3] offen sc1\n\t"
        "s_sub_u32 %[scn], %[scn], 1\n\t"
        "s_cmp_lg_u32 %[scn], 0\n\t"
        "s_cbranch_scc1 Lp_%=\n\t"
        "s_waitcnt vmcnt(0)\n\t"
        "Lf0_%=:\n\t"
        "v_add_f32 v48, v48, v52\n\t"
        "v_add_f32 v49, v49, v53\n\t"
        "v_add_f32 v50, v50, v54\n\t"
        "v_add_f32 v51, v51, v55\n\t"
        "v_add_f32 v56, v56, v60\n\t"
        "v_add_f32 v57, v57, v61\n\t"
        "v_add_f32 v58, v58, v62\n\t"
        "v_add_f32 v59, v59, v63\n\t"
        "v_add_f32 %[o0], v48, v56\n\t"
        "v_add_f32 %[o1], v49, v57\n\t"
        "v_add_f32 %[o2], v50, v58\n\t"
        "v_add_f32 %[o3], v51, v59\n\t"
        "Le_%=:\n\t"
        : [o0] "=&v"(o[0]), [o1] "=&v"(o[1]), [o2] "=&v"(o[2]), [o3] "=&v"(o[3]), [t] "=&v"(t), [scn] "+s"(cn)
        : [ssrd] "s"(srd), [va] "v"(va), [so0] "s"(so + 0), [so1] "s"(so + 4096), [so2] "s"(so + 8192), [so3] "s"(so + 12288)
        : "memory", "scc", "vcc");
}
DEV void poll_row(u32x4 srd, u32 v1, u32 v2, u32 (&o)[6], u32& cn) {
    u32 t;
    asm volatile(
        "s_nop 4\n\t"
        "buffer_load_dwordx2 v[48:49], %[v1], %[ssrd], 0 offen sc1\n\t"
        "buffer_load_dwordx2 v[50:51], %[v1], %[ssrd], 0 offen offset:2048 sc1\n\t"
        "buffer_load_dwordx2 v[52:53], %[v2], %[ssrd], 0 offen sc1\n\t"
        "Lp_%=:\n\t"
        "s_waitcnt vmcnt(0)\n\t"
        "v_max3_u32 %[t], v48, v49, v50\n\t"
        "v_max3_u32 %[t], %[t], v51, v52\n\t"
        "v_max_u32 %[t], %[t], v53\n\t"
        "v_cmp_eq_u32 vcc, -1, %[t]\n\t"
        "s_cbranch_vccz Lf0_%=\n\t"
        "buffer_load_dwordx2 v[48:49], %[v1], %[ssrd], 0 offen sc1\n\t"
        "buffer_load_dwordx2 v[50:51], %[v1], %[ssrd], 0 offen offset:2048 sc1\n\t"
        "buffer_load_dwordx2 v[52:53], %[v2], %[ssrd], 0 offen sc1\n\t"
        "s_sub_u32 %[scn], %[scn], 1\n\t"
        "s_cmp_lg_u32 %[scn], 0\n\t"
        "s_cbranch_scc1 Lp_%=\n\t"
        "s_waitcnt vmcnt(0)\n\t"
        "Lf0_%=:\n\t"
        "v_mov_b32 %[o0], v48\n\t"
        "v_mov_b32 %[o1], v49\n\t"
        "v_mov_b32 %[o2], v50\n\t"
        "v_mov_b32 %[o3], v51\n\t"
        "v_mov_b32 %[o4], v52\n\t"
        "v_mov_b32 %[o5], v53\n\t"
        "Le_%=:\n\t"
        : [o0] "=&v"(o[0]), [o1] "=&v"(o[1]), [o2] "=&v"(o[2]), [o3] "=&v"(o[3]), [o4] "=&v"(o[4]), [o5] "=&v"(o[5]), [t] "=&v"(t), [scn] "+s"(cn)
        : [ssrd] "s"(srd), [v1] "v"(v1), [v2] "v"(v2)
        : "memory", "scc", "vcc");
}

DEV int ffn_slice(int b, int& nu) {
    const int j = b >> 3, t = j - 20, t3 = (t * 43) >> 7, r = t - 3 * t3;
    nu = (j >= 20 && r < 2) ? 24 : 16;
    return (b & 7) * 576 + (j < 20 ? j * 16 : 320 + t3 * 64 + r * 24);
}
DEV void gu_dma(const u16* wg, const u16* wu, int lane, int b, int i0, int i1) {
    int nu;
    const int f0 = ffn_slice(b, nu);
    u32 gv[3];
#pragma unroll
    for (int m = 0; m < 3; ++m) {
        const int q = 64 * m + lane, sg = (q * 171) >> 9, part = q - 3 * sg;
        const int kin = 32 * (sg >> 5) + 16 * ((sg >> 4) & 1) + 8 * ((sg >> 2) & 1) + 4 * ((sg >> 3) & 1) + (sg & 3);
        gv[m] = (u32)kin * 9216u + f0 * 2u + ((part == 2 && nu == 16) ? 16u : part * 16u);
    }
#pragma nounroll
    for (int i = i0; i < i1; ++i) {
        int gi = i + ((b >> 3) & 3) * 4; if (gi >= 18) gi -= 18;
        const u32x4 sd = mk_srd(gi < 9 ? wg : wu, 5308416);
        asm volatile("" : "+s"(gi));
        int g9 = gi - 9 * ((gi * 57) >> 9);
        asm volatile("" : "+s"(g9));
        const u32 so = g9 * (64 * 9216), ld = L_G + gi * 3072;
#pragma unroll
        for (int m = 0; m < 3; ++m) dma16g(sd, gv[m], so, ld + m * 1024);
    }
}

DEV void xt_upd(u32 (&XT)[10], float rm, float rrs) {
#pragma unroll
    for (int i = 0; i < 10; ++i) {
        const float a = lo16(XT[i]), c = hi16(XT[i]);
        const u32 h = pk2((a - rm) * rrs, (c - rm) * rrs);
        XT[i] = pk2(a + lo16(h), c + hi16(h));
    }
}

DEV void gu_pf(const u16* wg, const u16* wu, int lane, int b, int i) {
    const int j = b >> 3;
    const u32x4 sg = mk_srd(j < 16 ? wg : wu, 5308416);
    const int q = i * 54 + lane, r = (q * 57) >> 9;
    const u32 o = lane < 54 ? (u32)((j & 15) * 36 + r) * 9216u + (b & 7) * 1152u + (q - 9 * r) * 128u : (u32)OOB;
    asm volatile("s_nop 4\n\tbuffer_load_dword v98, %0, %1, 0 offen" :: "v"(o), "s"(sg) : "memory");
}

DEV u32 w_voff(int wp, int r16, int g, int cb) {
    int ch = 16 * wp + r16; if (ch > 107) ch = 107;
    const int sec = (ch * 57) >> 11, c = ch - 36 * sec;
    return (u32)(8 * g) * 3456u + (sec == 2 ? 0u : (u32)(sec + 1) * 1152u) + cb * 144u + c * 4u;
}

__global__ void NOPK __attribute__((amdgpu_num_vgpr(24))) __launch_bounds__(512, 2)
mega(const u16* __restrict__ x0, const u16* __restrict__ wqkv, const u16* __restrict__ wo, const u16* __restrict__ wr,
     const u16* __restrict__ wg, const u16* __restrict__ wu, const u16* __restrict__ wd, u16* __restrict__ out, char* __restrict__ ws) {
    const int w_ = __builtin_amdgcn_readfirstlane(threadIdx.x >> 6);
    const int b_ = blockIdx.x;
    {
        u64 pc; u32 off;
        asm volatile("s_getpc_b64 %0\n"
                     "Lcode0:\n\t"
                     "v_lshlrev_b32 %1, 7, %2\n\t"
                     "v_and_b32 v99, 3, %2\n\t"
                     "v_cmp_gt_u32 vcc, Lcode1-Lcode0, %1\n\t"
                     "s_mov_b64 exec, vcc\n\t"
                     "v_cmp_eq_u32 vcc, %3, v99\n\t"
                     "s_and_b64 exec, exec, vcc\n\t"
                     "s_cbranch_execz Lcode2\n\t"
                     "global_load_dword v99, %1, %0\n"
                     "Lcode2:\n\t"
                     "s_mov_b64 exec, -1"
                     : "=&s"(pc), "=&v"(off) : "v"(threadIdx.x), "s"((b_ >> 3) & 3) : "vcc", "scc", "memory");
    }
    const __amdgpu_buffer_rsrc_t rs = __builtin_amdgcn_make_buffer_rsrc((void*)ws, 0, WS_BYTES, 0x00020000);
    u32 tag_, tmo = 0;
    {
        IDS
        int u = rg * 4 + (w & 3);
        const bool uok = u < 126;
        if (u > 125) u = 125;
        const int su = (u * 147) >> 10, wp = u - 7 * su;
        u32 wv = w_voff(wp, r16, g, cb), so = (u32)su * 110592u;
        sts32(L_BC + lane * 4, 0u);
        asm volatile("" : "+v"(wv), "+s"(so));
        asm volatile("" :: "s"(x0), "s"(wqkv), "s"(wo), "s"(ws));
        wld8p(mk_srd(wqkv, 1990656), wv, so, w);
        bar();
        { const char* ep_ = ws; asm volatile("" : "+s"(ep_)); tag_ = *(const __attribute__((address_space(4))) u32*)(ep_ + OFF_EPOCH) + 1u; }
        wunit_st(mk_srd(ws, WS_BYTES), (uok && !(wp == 6 && r16 >= 12)) ? OFF_WI + ((cb * 7 + wp) * 18 + su) * 2048 + (wp == 6 ? g * 12 + r16 : lane) * 16 : OOB, w);
        x_dma(mk_srd(x0 - 9216, 2359296 + 18432), (u32)((rg * 64 + (lane >> 3)) * 1152) + (u32)(((lane ^ (lane >> 4)) & 7) << 4), rg == 0 ? OOB : 0,
              ((XCI >> (4 * w)) & 15) + cb, (XCN >> (4 * w)) & 15);
        if (w < 4) {
            const int fa = lane < WREP ? OFF_FLAG + ((lane * 8 + cb) * 32 + rg) * FST : OOB;
            if (w > 0) { VMWAIT(10); } else { VMWAIT(0); }
            BST32(tag_, lds_inc_rtn(L_BC + BC_FC) == 3u ? fa : OOB, 0);
            __builtin_amdgcn_s_setprio(0);
        }
#define PFI(w) ((w) >= 4 ? (w) - 4 : (w) + 2)
#define PFON(w) ((w) > 0 && (w) < 7 && PFI(w) < PFA)
        if (PFON(w)) gu_pf(wg, wu, lane, b, PFI(w));
        if (w == 6) {
#pragma unroll
            for (int i = 0; i < 3; ++i) {
                const int q = lane + 64 * i;
                if (q < 144) sts128((q >> 4) * XCH + (q & 15) * 16, (u32x4){ONES2, ONES2, ONES2, ONES2});
            }
        }
        lc_set(lane_consts(w_, b_));
    }
    MidS a;
    {
        IDS
        const int rt4 = w & 3, kh = w >> 2, hp = 2 * cb + kh;
        const u32 obase = OFF_OT + (rg * 4 + rt4) * 18432;
        const u32 wl = OFF_WOI + cb * 82944;
        const u32 lo = w < 4 ? 16384 + w * 6144 : (w < 7 ? 64512 + (w - 4) * 6144 : 57344), eo = kh * 41472 + rt4 * 4096;
        a.n7 = rt4 == 3 ? 1024 : 0;
        a.ob = obase + kh * 9216; a.st = obase + hp * 1024; a.tt = obase + 16384 + hp * 128;
        a.c0 = (rg == 0 && rt4 == 0) ? 0xff61b1e6u : 0u;
        a.l1d = kh ? (u32)-(L_WOE + 9 * 4608) : 5 * 4608; a.tvd = kh ? 128 : 8;
        a.wi = wl + lo; a.ld = lo >= 64512 ? lo - 64512 : L_WOE + lo; a.wx = wl + eo; a.ldx = L_WOE + eo;
        a.msk = (u32)(kh - 1); a.scr = rt4 == 2 ? L_SCR3 : L_SCR + (rt4 == 3 ? 2 : rt4) * 4608; a.pk = L_PK(rt4);
        a.stat = OFF_STATS + (rg * 4 + rt4) * 2048 + cb * 256;
        a.x1 = OFF_X1G + rg * 73728 + 9 * cb * 1024 + rt4 * 256;
        asm volatile("" : "+s"(a.ob), "+s"(a.st), "+s"(a.tt), "+s"(a.c0), "+s"(a.l1d), "+s"(a.tvd), "+s"(a.wi), "+s"(a.ld), "+s"(a.wx), "+s"(a.ldx), "+s"(a.msk),
                          "+s"(a.scr), "+s"(a.stat), "+s"(a.x1), "+s"(a.pk), "+s"(a.n7));
    }
    asm volatile("" : "+s"(tag_));
#define VA_MASK (CSL(w) < 6 ? ~0ull : 0x0000FFFFFFFFFFFFull)
#define V0_MASK (CSL(w) < 4 ? ~0ull : (w == 4 ? 0xFFFFFFFFull : 0ull))
    a.lb = L_WOE + (w_ >> 2) * 9 * 4608;
    a.skip = 0; a.wc = L_BC + BC_WC + (w_ >> 2) * 4;
    u64 lm0 = 0;
#pragma nounroll
    for (int layer = 0; layer < 2; ++layer) {
    u32 tag = tag_; asm volatile("" : "+s"(tag));
    if (layer == 0) {
    if (w_ < 7) {
        IDS
        const u32x4 sws = mk_srd(ws, WS_BYTES);
        const u32 fo = w > 0 ? OOB : OFF_GFLAG + (lane < 18 ? lane * 8 + cb : (((lane & 8) && rg > 0) ? rg - 1 : rg) * 8 + (lane & 7)) * 16;
        int xt[3];
        {
            const int row = 16 + 16 * (w & 3) + r16;
#pragma unroll
            for (int p = 0; p < 3; ++p) {
                const int pc = 9 * cb + 4 * p + (p < 2 ? g : 0);
                xt[p] = (pc >> 3) * XCH + row * 128 + (((pc ^ (row >> 1)) & 7) << 4) + (p < 2 ? 0 : (g & 1) * 8);
            }
            asm volatile("" : "+v"(xt[0]), "+v"(xt[1]), "+v"(xt[2]));
        }
        if (w >= 1) {
            if (PFON(w)) { VMWAIT(1); } else { VMWAIT(0); }
            lds_inc(L_BC + BC_XC, lane);
        }
        if (w >= 4) {
            LWAIT0(L_BC + BC_XC, 6, 22);
            float xs[2], xq[2];
            xstats((w - 4) * 2048 + r16 * 128 + (((g ^ (r16 >> 1)) & 7) << 4), xs, xq);
#pragma unroll
            for (int t = 0; t < 2; ++t) {
                const int row = 16 * (w - 4 + 3 * t) + r16;
                float m = xs[t] * (1.f / 576.f);
                float rsd = ln_rs(fmaxf(xq[t] * (1.f / 576.f) - m * m, 0.f));
                m = ln_m(m);
                if (row < 2) { m = 0.f; rsd = 0.f; }
                if (g == (r16 >> 2) && row < 80) sts64(L_MR + row * 8, (u32x2){__float_as_uint(m), __float_as_uint(rsd)});
            }
            lds_inc(L_BC + BC_MC, lane);
        }
        if (w == 0) {
            u32 cn = SPIN_MAX;
            poll_flags(sws, OFF_FLAG + ((rg & (WREP - 1)) * 256 + cb * 32 + (lane & 31)) * FST, tag, cn);
            sts32v(L_BC + BC_GC, 1u);
            if (cn == 0) TMO(3);
        } else {
            LWAIT0(L_BC + BC_GC, 1, 23);
        }
        LWAIT0(L_BC + BC_XC, 6, 22);
        {
            const u32 ib = OFF_WI + (cb * 7 + CSL(w)) * 36864;
            u32 cn = SPIN_MAX;
            gemm0(sws, r16 * 128 + (((g ^ (r16 >> 1)) & 7) << 4), (CSL(w) == 6 ? g * 12 + (r16 < 12 ? r16 : 11) : lane) * 16, ib, xt, fo, w > 0 ? 0u : tag, L_CS + w * 128, VA_MASK, V0_MASK, cn);
            if ((int)cn < 1) TMO(25);
        }
    } else {
        IDS
        __builtin_amdgcn_s_setprio(3);
        {
            u32 U[3][8];
            const int ks = rg < 18 ? rg : 0;
            const u32 rb = rg < 18 ? (u32)(ks < 16 ? 36 * ks + 4 * g : 36 * (8 * (ks - 16) + 2 * g) + 16) * 1152u + cb * 144u : (u32)OOB;
#pragma unroll
            for (int p = 0; p < 3; ++p) old8(U[p], mk_srd(wo, 663552), rb + p * 64u + (p < 2 ? r16 * 4u : ((r16 & 7) >> 1) * 4u), (ks < 16 ? 20 : 36) * 1152);
            u32 sw_ = SENT; asm volatile("" : "+v"(sw_));
            const u32x4 sv = {sw_, sw_, sw_, sw_};
            const int ob = OFF_OT + rg * 4 * 18432;
            LWAIT(L_BC + BC_FC, 4, 43);
#pragma unroll
            for (int i = 0; i < 8; ++i) BST128(sv, lane * 16 + (i & 1) * 1024, ob + (i >> 1) * 18432 + cb * 2048);
            BST128(sv, g * 18432 + r16 * 16, ob + 16384 + cb * 256);
            BST128(sv, g * 2048 + r16 * 16, OFF_STATS + rg * 4 * 2048 + cb * 256);
            const int wb = rg < 18 ? OFF_WOI + cb * 82944 + ks * 4608 : OOB;
#pragma unroll
            for (int p = 0; p < 3; ++p) {
                vmwait8n(U[p], 10);
                u32x4 f0, f1;
                wfrag(U[p], f0, f1);
                if (p < 2) { BST128(f0, wb + p * 2048 + lane * 16, 0); BST128(f1, wb + p * 2048 + 1024 + lane * 16, 0); }
                else BST128((r16 & 1) ? f1 : f0, r16 < 8 ? wb + 4096 + (g * 8 + r16) * 16 : OOB, 0);
            }
        }
        u32 wq[6];
        {
            const u32x4 sr = mk_srd(wr, 1152);
#pragma unroll
            for (int i = 0; i < 5; ++i) bld32(wq[i], sr, lane * 4u, 0u, 256 * i);
            bld32(wq[5], sr, lane < 32 ? cb * 144u + lane * 4u : ((lane < 40 && !(lane & 2)) ? cb * 144u + 128u + ((lane >> 2) & 1) * 8u + (lane & 1) * 4u : (u32)OOB), 0u, 0);
        }
        { int nu; const int f0 = ffn_slice(b, nu); dload(mk_srd(wd, (f0 + nu) * 1152), lane * 2u, f0 * 1152u); }
        asm volatile("s_waitcnt vmcnt(%6)" : "+v"(wq[0]), "+v"(wq[1]), "+v"(wq[2]), "+v"(wq[3]), "+v"(wq[4]), "+v"(wq[5]) : "n"(48) : "memory");
        if (lane == 0) BST32(tag, OFF_GFLAG + (rg * 8 + cb) * 16, 0);
        {
            float sacc = 0.f;
#pragma unroll
            for (int q = 0; q < 5; ++q) sacc = dot2(wq[q], ONES2, sacc);
            sacc = gsum(rsum(sacc));
            if (lane < 48) sts32(L_WR + lane * 4, wq[5]);
            if (lane < 36) sts32(L_WR + 192 + lane * 4, ONES2);
            if (lane == 0) sts32(L_BC + BC_WRS, __float_as_uint(sacc));
        }
        asm volatile("s_nop 4\n\tbuffer_load_dword v98, %0, %1, 0 offen sc1" :: "v"(lane < 2 ? OFF_KEY + b * 256 + lane * 128 : (lane == 2 ? OFF_XFLAG + b * 128 : OOB)), "s"(mk_srd(ws, WS_BYTES)) : "memory");
        LWAIT(L_BC + BC_GC, 1, 31);
        __builtin_amdgcn_s_sleep(30);
        __builtin_amdgcn_s_setprio(3);
#pragma unroll
        for (int i = PFA; i < 6; ++i) gu_pf(wg, wu, lane, b, i);
        VMWAIT(0);
        __builtin_amdgcn_s_setprio(3);
        gu_dma(wg, wu, lane, b, 0, GN1);
        {
            u32 sw_ = SENT; asm volatile("" : "+v"(sw_));
            const u32x4 sv = {sw_, sw_, sw_, sw_};
            const int l16 = lane * 16;
#pragma unroll
            for (int l = 0; l < 2; ++l) {
                BST128(sv, (r16 < 9 ? g * 1280 + cb * 144 + r16 * 16 : ((r16 == 9 && cb == 0) ? g * 1280 + 1152 : OOB)), OFF_CROW + (l * 128 + rg * 4) * 1280);
            }
            if (b < 144) {
#pragma unroll
                for (int i = 0; i < 4; ++i) BST128(sv, l16 + i * 1024, OFF_YP + b * 4096);
            }
            if (b >= 251) BST128(sv, l16, OFF_X2R + (b - 251) * 1024);
#pragma unroll
            for (int i = 0; i < 9; ++i) BST128(sv, l16 + (i & 3) * 1024, OFF_X1G + rg * 73728 + cb * 9216 + (i >> 2) * 4096);
            BST128(sv, g * 2048 + r16 * 16, OFF_STATS + STATS_B + rg * 4 * 2048 + cb * 256);
        }
        __builtin_amdgcn_s_setprio(0);
    }
    }

#define WO_PRE() do { const u32x4 sws = mk_srd(ws, WS_BYTES); dma16x4_sc1(sws, lane * 16u, a.wx, a.ldx); dma16x4_sc1(sws, lane * 16u, a.wi, a.ld); \
    dma16x4_sc1(sws, lane * 16u, a.wi + 2048, a.ld + 2048); a.skip = 1; } while (0)
#define FFN_ADDR(kb) \
            const int q4 = r16 & 3; \
            const int sl = (16 * (g >> 1) + 4 * (g & 1) + (r16 >> 2)) * 48 + (kb) * 7680; \
            int g0 = L_G + sl + q4 * 8, g1 = L_U + sl + q4 * 8, g2 = (q4 < 2 ? L_G + q4 * 8 : L_U + (q4 - 2) * 8) + sl + 32; \
            u32 c0 = (w - 4) * 320 + g * 16; \
            int pa = L_FP + (w - 4) * 384 + r16 * 8;
        {
            IDS
            u32 cn = LSPIN_MAX / 4;
            a.srd = mk_srd(ws, WS_BYTES); a.layer = layer;
            att(a);
            if (layer == 0) {
                BST32(tag, (w == 7 && lane == 0) ? OFF_XFLAG + (rg * 8 + cb) * XFS : OOB, 0);
                gu_dma(wg, wu, lane, b, GN1 + (((18 - GN1) * w) >> 3), GN1 + (((18 - GN1) * (w + 1)) >> 3));
            }
            mid(a, cn);
            a.stat += STATS_B;
            if (cn == 0) TMO(4);
        }
        if (w_ == (layer == 0 ? 6 : 3)) {
            IDS
            const int tile = rg * 4 + g;
            __builtin_amdgcn_s_setprio(3);
            float wrsum = __uint_as_float(lds32(L_BC + BC_WRS));
            const int cr = OFF_CROW + (layer * 128 + tile) * 1280;
            int ka = r16 < 4 ? OFF_KEY + (layer * 32 + cb * 4 + r16) * 1024 + tile * 8 : OOB;
            int pa_ = L_PK(g) + (r16 < 9 ? r16 : 0) * 256 + 240, ca_ = r16 < 9 ? cr + cb * 144 + r16 * 16 : OOB, sa_ = cb == 0 ? cr + 1152 : OOB;
            u32 idx = 2047 - (rg * 64 + lane), tv = tag, sv = g * 2048 + r16 * 16;
            asm volatile("" : "+v"(wrsum), "+v"(ka), "+v"(pa_), "+v"(ca_), "+v"(sa_), "+v"(idx), "+v"(tv), "+v"(sv));
            float t[3];
            u32 cn = SPIN_MAX;
            LWAIT0(L_BC + BC_PK, 4 * (layer + 1), 27);
            if (layer == 0) __builtin_amdgcn_s_sleep(10);
            if (layer == 1) __builtin_amdgcn_s_sleep(8);
            poll_stats(mk_srd(ws, WS_BYTES), sv, OFF_STATS + layer * STATS_B + rg * 8192, t, cn);
            const float rm0 = t[0] * (1.f / 576.f);
            const float var = fmaxf(t[1] * (1.f / 576.f) - rm0 * rm0, 0.f);
            const float rm = ln_m(rm0), rrs = ln_rs(var);
            const float score = rrs * (t[2] - rm * wrsum);
            const u32 sb = pk2(0.f, score);
            const u32 key = ((sb ^ ((u32)((int)sb >> 31) | 0x80000000u)) & 0xffff0000u) | idx;
            const u32 best = rmax(key);
            BST64(((u32x2){best, tv}), ka, 0);
            CFENCE();
            {
                const u32x4 v = lds128(pa_ - (int)(best & 15u) * 16);
                sts64(L_RS + lane * 8, (u32x2){__float_as_uint(rm), __float_as_uint(rrs)});
                sts32(L_BC + BC_TB + g * 4, best);
                CFENCE();
                sts32v(L_BC + BC_KC, layer + 1);
                BST128(((u32x4){san(v.x), san(v.y), san(v.z), san(v.w)}), ca_, 0);
                BST128(((u32x4){san(__float_as_uint(rm)), san(__float_as_uint(rrs)), san(__float_as_uint(rm)), san(__float_as_uint(rrs))}), key == best ? sa_ : OOB, 0);
            }
            if (cn == 0) TMO(5);
            if (layer == 0) {
                sts64(L_MR + (16 + lane) * 8, ln_next(rm0, var, rm, rrs));
                lds_inc(L_BC + BC_MC, lane);
                cn = SPIN_MAX;
                poll_stats1(mk_srd(ws, WS_BYTES), OFF_STATS + (rg > 0 ? rg * 4 - 1 : 3) * 2048, t, cn);
                if (cn == 0) TMO(15);
                const float hm = gsum(t[0]) * (1.f / 576.f);
                const float hv = fmaxf(gsum(t[1]) * (1.f / 576.f) - hm * hm, 0.f);
                sts64(L_MR + r16 * 8, ln_next(hm, hv, ln_m(hm), ln_rs(hv)));
                lds_inc(L_BC + BC_MC, lane);
            }
            __builtin_amdgcn_s_setprio(0);
        }
        if (layer == 0 && (w_ == 4 || w_ == 5)) {
            IDS
            u32 cn = SPIN_MAX;
            const int h = w - 4;
            poll_flagh(mk_srd(ws, WS_BYTES), OFF_XFLAG + ((((lane & 8) && rg > 0) ? rg - 1 : rg) * 8 + (lane & 7)) * XFS, tag, cn);
            if (cn == 0) TMO(6);
            const u32 own = OFF_X1G + rg * 73728;
            LWAIT0(L_BC + BC_PK, 4, 27);
            __builtin_amdgcn_s_setprio(3);
            cn = SPIN_MAX;
            xgather(mk_srd(ws, WS_BYTES), own, own - (rg > 0 ? 73728 : 0) + h * 36864, h * 8192, L_XH + h * 9216, cn);
            if ((int)cn < 1) TMO(39);
            __builtin_amdgcn_s_setprio(0);
        }
        if (w_ < 4) {
            if (layer == 1) {
                IDS
                const int grow = rg * 64 + w * 16 + r16;
                LWAIT(L_BC + BC_KC, 2, 24);
                const u32 best = lds32(L_BC + BC_TB + w * 4);
                const bool isb = (best & 0x7ffu) == (u32)(2047 - grow);
                const u32x2 mrs = lds64(L_RS + (16 * w + r16) * 8);
                u32 XT[10];
                xt_get(XT); xt_upd(XT, __uint_as_float(mrs.x), __uint_as_float(mrs.y));
                const __amdgpu_buffer_rsrc_t ro = __builtin_amdgcn_make_buffer_rsrc((void*)out, 0, 2359296, 0x00020000);
                const int ob = grow * 1152 + cb * 144;
                const u32x4 v0 = {XT[0], XT[1], XT[2], XT[3]}, v1 = {XT[4], XT[5], XT[6], XT[7]};
                const u32x2 v2 = {XT[8], XT[9]};
                if (!isb) {
                    __builtin_amdgcn_raw_buffer_store_b128(v0, ro, ob + g * 16, 0, 0);
                    __builtin_amdgcn_raw_buffer_store_b128(v1, ro, ob + 64 + g * 16, 0, 0);
                    if (g < 2) __builtin_amdgcn_raw_buffer_store_b64(v2, ro, ob + 128 + g * 8, 0, 0);
                }
                LWAIT(L_BC + BC_FIN, 1, 13);
                if (isb && best != lds32v(L_BC + BC_BEST1)) {
                    __builtin_amdgcn_raw_buffer_store_b128(v0, ro, ob + g * 16, 0, 0);
                    __builtin_amdgcn_raw_buffer_store_b128(v1, ro, ob + 64 + g * 16, 0, 0);
                    if (g < 2) __builtin_amdgcn_raw_buffer_store_b64(v2, ro, ob + 128 + g * 8, 0, 0);
                }
            }
        } else if (w_ < 7) {
            u32 cn = SPIN_MAX;
            if (layer == 1) {
                __builtin_amdgcn_s_setprio(3);
#pragma nounroll
                for (int rd = 1; rd < 3; ++rd) {
                    IDS
                    FFN_ADDR(w - 4)
                    asm volatile("" : "+v"(g0), "+v"(g1), "+v"(g2), "+v"(c0), "+v"(pa));
                    cn = LSPIN_MAX;
                    ffn_help(mk_srd(ws, WS_BYTES), c0, g0, g1, g2, pa, L_BC + BC_HF + (w - 4) * 4, L_BC + BC_CR - 4 + rd * 4, rd + 1, cn);
                    if ((int)cn < 1) TMO(38);
                    if (rd == 2) break;
                    if (w == 4) {
                        u32 k;
                        cn = SPIN_MAX;
                        poll_keys2(mk_srd(ws, WS_BYTES), OFF_KEY + (32 + (b & 31)) * 1024, tag, k, cn);
                        if (cn == 0) TMO(9);
                        k = rmax(k);
                        const u32 best = umax2(umax2(__builtin_amdgcn_readlane(k, 0), __builtin_amdgcn_readlane(k, 16)), umax2(__builtin_amdgcn_readlane(k, 32), __builtin_amdgcn_readlane(k, 48)));
                        sts32v(L_BC + BC_BEST1, best);
                        sts32v(L_BC + BC_FIN, best == lds32v(L_BC + BC_BEST) ? 1u : 2u);
                    }
                    LWAIT(L_BC + BC_FIN, 1, 17);
                    if (__builtin_amdgcn_readfirstlane(lds32v(L_BC + BC_FIN)) != 2u) break;
                }
            }
            __builtin_amdgcn_s_setprio(0);
        }
        if (w_ < 7) {
            if (layer == 0) {
                u32 cn = LSPIN_MAX;
                {
                    IDS
                    int fb = L_BC;
                    asm volatile("" : "+v"(fb));
                    LWAIT0(L_BC + BC_G0, 2, 16);
                    gemm1a(g * 1024 + r16 * 16, fb, L_CS + w * 128, VA_MASK, V0_MASK, cn);
                }
                IDS
                if (w < 4) {
                    u32 XT[10];
                    const u32x2 mrs = lds64(L_RS + (16 * w + r16) * 8);
                    xt_get(XT); xt_upd(XT, __uint_as_float(mrs.x), __uint_as_float(mrs.y)); xt_set(XT);
                }
                if (w < 3) {
                    LWAIT0(L_BC + BC_C1, 7, 48);
                    WO_PRE();
                }
                LWAIT(L_BC + BC_BEST, 1, 28);
                const int ridx = 2047 - (int)(__builtin_amdgcn_readfirstlane(lds32v(L_BC + BC_BEST)) & 0x7ffu);
                const u64 lm = LATE_MASK(ridx);
                if (w == 3) {
                    if (b == 255) BST32(tag, lane == 0 ? OFF_EPOCH : OOB, 0);
                    u32 sw_ = SENT; asm volatile("" : "+v"(sw_));
                    const u32x4 sv = {sw_, sw_, sw_, sw_};
#pragma nounroll
                    for (int t = 0; t < 3; ++t) {
                        const bool on = t == 2 ? b >= 112 : (b < 144 && ((lm >> (2 * (rg ^ (16 * t)))) & 1) == (u64)t);
                        const int o = t == 2 ? OFF_YP2 + (b - 112) * 4096 : OFF_YP + YP_B + (b ^ (4 * t)) * 4096;
                        if (on) {
#pragma unroll
                            for (int i = 0; i < 4; ++i) BST128(sv, lane * 16 + i * 1024, o);
                        }
                    }
                }
                if ((lm >> (2 * rg)) & 1)
                    gemm1b(L_BC, L_XR + cb * 144 + g * 16, L_XR + cb * 144 + 128 + (g & 1) * 8, w + 1, L_CS + w * 128, VA_MASK, V0_MASK, cn);
                if ((int)cn < 1) TMO(16);
            }
        } else {
            IDS
            if (layer == 0) dperm();
            __builtin_amdgcn_s_setprio(3);
            int ridx;
#pragma nounroll
            for (int rd = layer; ; ++rd) {
                IDS
                FFN_ADDR(0)
                u32 c1 = 1024 + (lane < 8 ? lane : 8) * 16;
                u32 ya = (lane >> 2) * 16384u + (lane & 3) * 16u, ye = (32 + g) * 16384u + r16 * 4u;
                int pg = L_FP + (g == 0 ? r16 * 8 : 256 + (r16 & 7) * 8), pu = L_FP + (g == 0 ? 128 + r16 * 8 : 320 + (r16 & 7) * 8), ff = L_BC + BC_HF;
                asm volatile("" : "+v"(g0), "+v"(g1), "+v"(g2), "+v"(c0), "+v"(c1), "+v"(ya), "+v"(ye), "+v"(pa), "+v"(pg), "+v"(pu), "+v"(ff));
                const u32x4 sws = mk_srd(ws, WS_BYTES);
                u32 yp = rd == 2 ? OFF_YP2 : OFF_YP + rd * YP_B;
                asm volatile("" : "+s"(yp));
                u32 best;
                {
                    u32 k, cn = SPIN_MAX;
                    const bool late = (lm0 >> (2 * rg)) & 1;
                    if (!late) {
                        LWAIT(L_BC + BC_KC, layer + 1, 29);
                        if (layer == 0) __builtin_amdgcn_s_sleep(9);
                        if (layer == 1) __builtin_amdgcn_s_sleep(9);
                    }
                    poll_keys(sws, OFF_KEY + (layer * 32 + (b & 31)) * 1024, tag, layer, lm0, g0, g1, g2, k, cn);
                    if (cn == 0) TMO(7);
                    if (late) LWAIT0(L_BC + BC_PK, 8, 30);
                    k = rmax(k);
                    best = umax2(umax2(__builtin_amdgcn_readlane(k, 0), __builtin_amdgcn_readlane(k, 16)), umax2(__builtin_amdgcn_readlane(k, 32), __builtin_amdgcn_readlane(k, 48)));
                }
                ridx = 2047 - (int)(best & 0x7ffu);
                sts32v(L_BC + BC_BEST, best);
                float fm, frs;
                {
                    u32 cn = LSPIN_MAX;
                    ffn(sws, OFF_CROW + (layer * 128 + (ridx >> 4)) * 1280, c0, c1, g0, g1, g2, pa, ff, pg, pu, L_BC + BC_CR - 4 + rd * 4, rd + 1,
                        ya, ye, yp + b * 64, fm, frs, cn);
                    if ((int)cn < 1) TMO(14);
                }
                u32x2 res = {0u, 0u};
                int xo = OOB, oo = OOB;
                u32 fin = 0u;
                const __amdgpu_buffer_rsrc_t ro = __builtin_amdgcn_make_buffer_rsrc((void*)out, 0, 2359296, 0x00020000);
                if (b < 144) {
                    const int q = (b & 7) * 18 + (b >> 3), c0 = q < 128 ? 8 * (q & 63) + 4 * (q >> 6) : 4 * q;
                    const int cidx = c0 + 2 * (g & 1) + (g >> 1);
                    float xv = __uint_as_float((u32)*(const u16*)(smem + L_XR + cidx * 2) << 16);
                    float hv = rbf(__builtin_fmaf(xv, frs, -fm * frs));
                    const u32 sbits = best >> 16;
                    const u32 sraw = (sbits & 0x8000u) ? (sbits & 0x7fffu) : (~sbits & 0xffffu);
                    const float sc = __uint_as_float(sraw << 16);
                    float sg = rbf(__builtin_amdgcn_rcpf(1.f + __builtin_amdgcn_exp2f(-sc * 1.4426950408889634f)));
                    xo = (lane == 0 && layer == 0) ? OFF_X2R + 32 * q : OOB; oo = (lane == 0 && layer == 1) ? ridx * 1152 + 2 * c0 : OOB;
                    asm volatile("" : "+v"(xv), "+v"(hv), "+v"(sg), "+v"(xo), "+v"(oo));
                    float yf[4];
                    u32 cn = SPIN_MAX;
                    poll_yp(mk_srd(ws, WS_BYTES), lane * 64u, yp + (q >> 2) * 16384 + (q & 3) * 16, yf, cn);
                    fin = lds32v(L_BC + BC_FIN);
                    if (cn == 0) TMO(8);
                    const u32 ys[4] = {__float_as_uint(yf[0]), __float_as_uint(yf[1]), __float_as_uint(yf[2]), __float_as_uint(yf[3])};
                    auto p0 = __builtin_amdgcn_permlane32_swap(ys[0], ys[1], false, false);
                    auto p1 = __builtin_amdgcn_permlane32_swap(ys[2], ys[3], false, false);
                    const u32 ya = __float_as_uint(__uint_as_float(p0[0]) + __uint_as_float(p0[1])), yc = __float_as_uint(__uint_as_float(p1[0]) + __uint_as_float(p1[1]));
                    auto p2 = __builtin_amdgcn_permlane16_swap(ya, yc, false, false);
                    const float y = rsum(__uint_as_float(p2[0]) + __uint_as_float(p2[1]));
                    const u32 r1 = pk2(xv + rbf(hv + rbf(rbf(y) * sg)), 0.f);
                    auto p3 = __builtin_amdgcn_permlane32_swap(r1, r1, false, false);
                    const u32 rr = p3[0] | (p3[1] << 16);
                    auto p4 = __builtin_amdgcn_permlane16_swap(rr, rr, false, false);
                    res = (u32x2){p4[0], p4[1]};
                    BST64(((u32x2){san(res.x), san(res.y)}), xo, 0);
                }
                if (rd == 1) {
                    lm0 = 0;
                    if (__builtin_amdgcn_readfirstlane(fin) != 1u) {
                        LWAIT0(L_BC + BC_FIN, 1, 18);
                        if (__builtin_amdgcn_readfirstlane(lds32v(L_BC + BC_FIN)) == 2u) continue;
                    }
                }
                __builtin_amdgcn_raw_buffer_store_b64(res, ro, oo, 0, 0);
                break;
            }
            lm0 = LATE_MASK(ridx);
            const int prow = ridx - (rg * 64 - 16);
            const bool affected = (lm0 >> (2 * rg)) & 1;
            if (layer == 0 && affected) {
                LWAIT0(L_BC + BC_G0 + 16, 2, 41);
                u32 o[6], cn = SPIN_MAX;
                int h0 = L_XH + lane * 256, h1 = L_XH + 16384 + (r16 >> 1) * 256 + (r16 & 1) * 8;
                u32 gw = 1u | (u32)prow << 8;
                asm volatile("" : "+v"(h0), "+v"(h1), "+v"(gw));
                poll_row(mk_srd(ws, WS_BYTES), OFF_X2R + lane * 32, OFF_X2R + 4096 + r16 * 32, o, cn);
                if (cn == 0) TMO(2);
                u32x4 c0 = {o[0], o[1], o[2], o[3]}; u32x2 c1 = {o[4], o[5]};
                sts128(h0, c0);
                sts64(h1, c1);
                CFENCE();
                sts32v(L_BC + BC_GO, gw);
                CFENCE();
                sts128(L_XR + lane * 16, c0);
                sts64(L_XR + 1024 + r16 * 8, c1);
                if (lane >= 16) c1 = (u32x2){0u, 0u};
                float psx = 0.f, psq = 0.f;
#pragma unroll
                for (int i = 0; i < 4; ++i) {
                    psx = dot2(c0[i], ONES2, psx); psq = dot2(c0[i], c0[i], psq);
                    if (i < 2) { psx = dot2(c1[i], ONES2, psx); psq = dot2(c1[i], c1[i], psq); }
                }
                psx = gsum(rsum(psx)); psq = gsum(rsum(psq));
                CFENCE();
                const float pm0 = psx * (1.f / 576.f);
                const float prs = ln_rs(fmaxf(psq * (1.f / 576.f) - pm0 * pm0, 0.f)), pm = ln_m(pm0);
                sts32v(L_MR, __float_as_uint(pm)); sts32v(L_MR + 4, __float_as_uint(prs)); sts32v(L_BC + BC_PST, 255u);
            }
            __builtin_amdgcn_s_setprio(0);
        }
    }
    if (tmo != 0u) BST32(tmo, OFF_TMO, 0);
    asm volatile("Lcode1:");
}

extern "C" void kernel_launch(void* const* d_in, const int* in_sizes, int n_in, void* d_out, int out_size, void* d_ws, size_t ws_size,
                              hipStream_t stream) {
    (void)in_sizes; (void)n_in; (void)out_size; (void)ws_size;
    hipFuncSetAttribute((const void*)mega, hipFuncAttributeMaxDynamicSharedMemorySize, LDS_BYTES);
    mega<<<256, 512, LDS_BYTES, stream>>>((const u16*)d_in[0], (const u16*)d_in[1], (const u16*)d_in[2], (const u16*)d_in[4],
                                          (const u16*)d_in[5], (const u16*)d_in[6], (const u16*)d_in[7], (u16*)d_out, (char*)d_ws);
}
```

```cpp
#include <hip/hip_runtime.h>
#include <stdint.h>

#define WREP 8
#define FST 32
#define NTD " nt"
#define XFS 32
#define XCI 0x04208760u
#define XCN 0x02221110u
#define PFA 3

#if defined(__HIP_DEVICE_COMPILE__)
#define NOPK __attribute__((target("no-packed-fp32-ops")))
#else
#define NOPK
#endif
#define DEV __device__ __forceinline__ NOPK
typedef unsigned short u16;
typedef unsigned int u32;
typedef unsigned long long u64;
#define __uint_as_float(x) __builtin_bit_cast(float, (u32)(x))
#define __float_as_uint(x) __builtin_bit_cast(u32, (float)(x))
#define fmaxf(a, b) __builtin_fmaxf((a), (b))
typedef __attribute__((ext_vector_type(2))) float f32x2;
typedef __attribute__((ext_vector_type(2))) __bf16 bf16x2;
typedef __attribute__((ext_vector_type(2))) u32 u32x2;
typedef __attribute__((ext_vector_type(4))) u32 u32x4;

extern __shared__ __attribute__((aligned(16))) char smem[];

#define SPIN_MAX 20000
#define LSPIN_MAX 400000

#define WS_BYTES 0x1000000
#define OFF_EPOCH 0
#define OFF_TMO 256
#define OFF_FLAG 0xF0000
#define OFF_GFLAG 4096
#define OFF_XFLAG 0x18000
#define OFF_KEY 16384
#define OFF_X2R 81920
#define OFF_STATS 0x20000
#define STATS_B 0x40000
#define OFF_CROW 0xA0000
#define OFF_YP 0x100000
#define YP_B 589824
#define OFF_OT 0x240000
#define OFF_X1G 0x6C0000
#define OFF_WI 0x900000
#define OFF_WOI 0xB00000
#define OFF_YP2 0xD00000
#define OOB 0x7FFFFF00

#define LDS_BYTES 163840
#define XCH 10240
#define L_XH 73728
#define L_KV 0
#define KVS 304
#define L_Q 24320
#define L_WOE 33536
#define L_SCR 18432
#define L_G 98048
#define L_U 125696
#define L_MISC 153344
#define L_MR (L_MISC)
#define L_BC (L_MISC + 640)
#define BC_BEST 0
#define BC_SP 8
#define BC_WRS 4
#define BC_GC 16
#define BC_C1 20
#define BC_GO 28
#define BC_PST 32
#define BC_FIN 36
#define BC_BEST1 40
#define BC_XC 44
#define BC_ZERO 48
#define BC_G0 96
#define BC_TB 168
#define BC_KC 64
#define BC_MC 160
#define BC_FC 76
#define BC_PP 56
#define BC_PK 84
#define BC_PC 152
#define BC_WC 144
#define L_RS 92160
#define L_PK(t) ((t) < 2 ? 92672 + (t) * 2304 : ((t) == 2 ? L_SCR3 : L_HROW))
#define L_WR (L_MISC + 9888)
#define L_CS (L_MISC + 928)
#define L_HROW (L_MISC + 1824)
#define L_XR (L_MISC + 4128)
#define L_SCR3 (L_MISC + 5280)

#define SENT 0xFFFFFFFFu

DEV void bar() {
    asm volatile("s_waitcnt lgkmcnt(0)" ::: "memory");
    __builtin_amdgcn_s_barrier();
    asm volatile("" ::: "memory");
}
#define VMWAIT(n) asm volatile("s_waitcnt vmcnt(" #n ")" ::: "memory")
DEV void vmwaitn(const int n) { asm volatile("s_waitcnt vmcnt(%0)" :: "n"(n) : "memory"); }
DEV void vmwait8n(u32 (&d)[8], const int n) {
    asm volatile("s_waitcnt vmcnt(%8)" : "+v"(d[0]), "+v"(d[1]), "+v"(d[2]), "+v"(d[3]), "+v"(d[4]), "+v"(d[5]), "+v"(d[6]), "+v"(d[7]) : "n"(n) : "memory");
}
DEV u32x4 mk_srd(const void* p, u32 bytes) {
    u64 a = (u64)p;
    asm volatile("" : "+s"(a));
    return (u32x4){(u32)a, (u32)(a >> 32) & 0xffffu, bytes, 0x00020000u};
}
DEV void dma16g(u32x4 srd, u32 voff, u32 soff, u32 lds_dst) {
    asm volatile("s_mov_b32 m0, %0\n\ts_nop 3\n\tbuffer_load_dwordx4 %1, %2, %3 offen lds" :: "s"(lds_dst), "v"(voff), "s"(srd), "s"(soff) : "memory");
}
DEV void dma16x4_sc1(u32x4 srd, u32 voff, u32 soff, u32 lds_dst) {
    asm volatile("s_mov_b32 m0, %0\n\ts_nop 3\n\tbuffer_load_dwordx4 %1, %2, %3 offen sc1 lds\n\tbuffer_load_dwordx4 %1, %2, %3 offen offset:1024 sc1 lds\n\t"
                 "buffer_load_dwordx4 %1, %2, %3 offen offset:2048 sc1 lds\n\tbuffer_load_dwordx4 %1, %2, %3 offen offset:3072 sc1 lds"
                 :: "s"(lds_dst), "v"(voff), "s"(srd), "s"(soff) : "memory");
}
DEV void dma16x2_sc1(u32x4 srd, u32 voff, u32 soff, u32 lds_dst) {
    asm volatile("s_mov_b32 m0, %0\n\ts_nop 3\n\tbuffer_load_dwordx4 %1, %2, %3 offen sc1 lds\n\tbuffer_load_dwordx4 %1, %2, %3 offen offset:1024 sc1 lds"
                 :: "s"(lds_dst), "v"(voff), "s"(srd), "s"(soff) : "memory");
}
DEV void bld32(u32& d, u32x4 srd, u32 voff, u32 soff, const int imm) {
    asm volatile("s_nop 4\n\tbuffer_load_dword %0, %1, %2, %3 offen offset:%4" : "=v"(d) : "v"(voff), "s"(srd), "s"(soff), "n"(imm));
}
DEV void old8(u32 (&d)[8], u32x4 srd, u32 voff, u32 s2) {
    asm volatile("s_nop 4\n\tbuffer_load_dword %0, %8, %9, 0 offen nt\n\tbuffer_load_dword %1, %8, %9, 0 offen offset:1152 nt\n\t"
                 "buffer_load_dword %2, %8, %9, 0 offen offset:2304 nt\n\tbuffer_load_dword %3, %8, %9, 0 offen offset:3456 nt\n\t"
                 "buffer_load_dword %4, %8, %9, %10 offen nt\n\tbuffer_load_dword %5, %8, %9, %10 offen offset:1152 nt\n\t"
                 "buffer_load_dword %6, %8, %9, %10 offen offset:2304 nt\n\tbuffer_load_dword %7, %8, %9, %10 offen offset:3456 nt"
                 : "=&v"(d[0]), "=&v"(d[1]), "=&v"(d[2]), "=&v"(d[3]), "=&v"(d[4]), "=&v"(d[5]), "=&v"(d[6]), "=&v"(d[7])
                 : "v"(voff), "s"(srd), "s"(s2));
}
DEV void wld8p(u32x4 srd, u32 voff, u32 soff, int w) {
    u32 t;
    asm volatile("s_cmp_gt_u32 %4, 3\n\ts_cbranch_scc1 Lw8_%=\n\ts_setprio 3\n\ts_nop 1\n\t"
                 "buffer_load_dword v48, %1, %2, %3 offen nt\n\tbuffer_load_dword v49, %1, %2, %3 offen offset:3456 nt\n\ts_add_u32 %0, %3, 6912\n\t"
                 "buffer_load_dword v50, %1, %2, %0 offen nt\n\tbuffer_load_dword v51, %1, %2, %0 offen offset:3456 nt\n\ts_add_u32 %0, %3, 13824\n\t"
                 "buffer_load_dword v52, %1, %2, %0 offen nt\n\tbuffer_load_dword v53, %1, %2, %0 offen offset:3456 nt\n\ts_add_u32 %0, %3, 20736\n\t"
                 "buffer_load_dword v54, %1, %2, %0 offen nt\n\tbuffer_load_dword v55, %1, %2, %0 offen offset:3456 nt\n"
                 "Lw8_%=:"
                 : "=&s"(t) : "v"(voff), "s"(srd), "s"(soff), "s"(w) : "scc", "memory");
}
DEV void x_dma(u32x4 srd, u32 xv, u32 orm, u32 c, u32 n) {
    u32 z1, z2, z3, y0, y1, so, t;
    asm volatile("s_cmp_eq_u32 %8, 0\n\ts_cbranch_scc1 Lx1_%=\n\t"
                 "v_xor_b32 %2, 64, %9\n\tv_or_b32 %3, %11, %9\n\tv_add_u32 %0, 0x2000, %2\n\tv_add_u32 %1, 0x4000, %9\n\tv_or_b32 %4, %11, %0\n\tv_add_u32 %2, 0x6000, %2\n"
                 "Lx0_%=:\n\t"
                 "s_cmp_gt_u32 %7, 8\n\ts_cselect_b32 %6, 9, 0\n\ts_sub_u32 %7, %7, %6\n\ts_mul_i32 m0, %7, 10240\n\ts_lshl_b32 %5, %7, 7\n\t"
                 "s_mov_b32 exec_lo, 0xffff0000\n\tbuffer_load_dwordx4 %3, %10, %5 offen lds\n\ts_mov_b32 exec_lo, -1\n\t"
                 "buffer_load_dwordx4 %4, %10, %5 offen offset:1024 lds\n\tbuffer_load_dwordx4 %1, %10, %5 offen offset:2048 lds\n\tbuffer_load_dwordx4 %2, %10, %5 offen offset:3072 lds\n\t"
                 "s_add_u32 m0, m0, 4096\n\ts_add_u32 %5, %5, 36864\n\t"
                 "buffer_load_dwordx4 %9, %10, %5 offen lds\n\tbuffer_load_dwordx4 %0, %10, %5 offen offset:1024 lds\n\t"
                 "buffer_load_dwordx4 %1, %10, %5 offen offset:2048 lds\n\tbuffer_load_dwordx4 %2, %10, %5 offen offset:3072 lds\n\t"
                 "s_add_u32 m0, m0, 4096\n\ts_add_u32 %5, %5, 36864\n\t"
                 "buffer_load_dwordx4 %9, %10, %5 offen lds\n\tbuffer_load_dwordx4 %0, %10, %5 offen offset:1024 lds\n\t"
                 "s_add_u32 %7, %7, 1\n\ts_sub_u32 %8, %8, 1\n\ts_cmp_lg_u32 %8, 0\n\ts_cbranch_scc1 Lx0_%=\n"
                 "Lx1_%=:"
                 : "=&v"(z1), "=&v"(z2), "=&v"(z3), "=&v"(y0), "=&v"(y1), "=&s"(so), "=&s"(t), "+s"(c), "+s"(n) : "v"(xv), "s"(srd), "s"(orm) : "scc", "memory");
}
DEV void wunit_st(u32x4 srd, u32 ib, int w) {
    asm volatile("s_cmp_gt_u32 %2, 3\n\ts_cbranch_scc1 Lu_%=\n\ts_waitcnt vmcnt(0)\n\t"
                 "v_perm_b32 v56, v49, v48, %3\n\tv_perm_b32 v57, v51, v50, %3\n\tv_perm_b32 v58, v53, v52, %3\n\tv_perm_b32 v59, v55, v54, %3\n\t"
                 "buffer_store_dwordx4 v[56:59], %0, %1, 0 offen sc1\n\t"
                 "v_perm_b32 v60, v49, v48, %4\n\tv_perm_b32 v61, v51, v50, %4\n\tv_perm_b32 v62, v53, v52, %4\n\tv_perm_b32 v63, v55, v54, %4\n\t"
                 "buffer_store_dwordx4 v[60:63], %0, %1, 0 offen offset:1024 sc1\n"
                 "Lu_%=:" :: "v"(ib), "s"(srd), "s"(w), "s"(0x05040100u), "s"(0x07060302u) : "scc", "memory");
}
DEV u32 plo(u32 hi, u32 lo) { return __builtin_amdgcn_perm(hi, lo, 0x05040100u); }
DEV u32 phi(u32 hi, u32 lo) { return __builtin_amdgcn_perm(hi, lo, 0x07060302u); }
DEV void wfrag(const u32 (&d)[8], u32x4& f0, u32x4& f1) {
    f0 = (u32x4){plo(d[1], d[0]), plo(d[3], d[2]), plo(d[5], d[4]), plo(d[7], d[6])};
    f1 = (u32x4){phi(d[1], d[0]), phi(d[3], d[2]), phi(d[5], d[4]), phi(d[7], d[6])};
}
DEV u32 pk2(float a, float b) { f32x2 v = {a, b}; return __builtin_bit_cast(u32, __builtin_convertvector(v, bf16x2)); }
DEV float rbf(float a) { return (float)(__bf16)a; }
DEV float ln_rs(float var) { return __builtin_amdgcn_rcpf(rbf(__builtin_amdgcn_sqrtf(rbf(rbf(var) + 1e-5f)))); }
DEV float ln_m(float m) { return rbf(m); }
DEV u32x2 ln_next(float m, float var, float mb, float rs) {
    const float c = 1.f + rs;
    const float mb2 = ln_m(c * m - mb * rs);
    return (u32x2){__float_as_uint((mb * rs + mb2) * __builtin_amdgcn_rcpf(c)), __float_as_uint(c * ln_rs(c * c * var))};
}
DEV float lo16(u32 v) { return __uint_as_float(v << 16); }
DEV float hi16(u32 v) { return __uint_as_float(v & 0xffff0000u); }
DEV u32 san(u32 v) { return v < 0xFFFFFFFEu ? v : 0xFFFFFFFEu; }
DEV u32 umax2(u32 a, u32 b) { return a > b ? a : b; }
DEV float dot2(u32 a, u32 b, float c) {
    return __builtin_amdgcn_fdot2_f32_bf16(__builtin_bit_cast(bf16x2, a), __builtin_bit_cast(bf16x2, b), c, false);
}
DEV float gsum(float v) {
    u32 u = __float_as_uint(v);
    auto a = __builtin_amdgcn_permlane16_swap(u, u, false, false);
    v = __uint_as_float(a[0]) + __uint_as_float(a[1]);
    u = __float_as_uint(v);
    auto c = __builtin_amdgcn_permlane32_swap(u, u, false, false);
    return __uint_as_float(c[0]) + __uint_as_float(c[1]);
}
#define DPP(v, ctrl) ((u32)__builtin_amdgcn_update_dpp(0, (int)(v), (ctrl), 0xf, 0xf, false))
DEV u32 rmax(u32 v) {
    v = umax2(v, DPP(v, 0x128)); v = umax2(v, DPP(v, 0x124)); v = umax2(v, DPP(v, 0x122)); v = umax2(v, DPP(v, 0x121));
    return v;
}
DEV float rsum(float v) {
    v += __uint_as_float(DPP(__float_as_uint(v), 0x128)); v += __uint_as_float(DPP(__float_as_uint(v), 0x124));
    v += __uint_as_float(DPP(__float_as_uint(v), 0x122)); v += __uint_as_float(DPP(__float_as_uint(v), 0x121));
    return v;
}
DEV u32 lds32(int off) { return *(const u32*)(smem + off); }
typedef __attribute__((address_space(3))) volatile u32* lds_vu32;
DEV u32 lds32v(int off) { return *(lds_vu32)(smem + off); }
DEV u32x2 lds64(int off) { return *(const u32x2*)(smem + off); }
DEV u32x4 lds128(int off) { return *(const u32x4*)(smem + off); }
DEV void sts32(int off, u32 v) { *(u32*)(smem + off) = v; }
DEV void sts32v(int off, u32 v) { *(lds_vu32)(smem + off) = v; }
DEV void sts64(int off, u32x2 v) { *(u32x2*)(smem + off) = v; }
DEV void sts128(int off, u32x4 v) { *(u32x4*)(smem + off) = v; }
DEV void lds_inc(int off, int lane) {
    (void)lane;
    asm volatile("s_mov_b64 exec, 1\n\tds_add_u32 %0, %1\n\ts_mov_b64 exec, -1" :: "v"(off), "v"(1u) : "memory");
}
DEV u32 lds_inc_rtn(int off) {
    u32 old;
    asm volatile("s_mov_b64 exec, 1\n\tds_add_rtn_u32 %0, %1, %2\n\ts_mov_b64 exec, -1\n\ts_waitcnt lgkmcnt(0)" : "=&v"(old) : "v"(off), "v"(1u) : "memory");
    return __builtin_amdgcn_readfirstlane(old);
}
#define BST128(v, off, soff) __builtin_amdgcn_raw_buffer_store_b128((v), rs, (off) + (soff), 0, 16)
#define BST64(v, off, soff) __builtin_amdgcn_raw_buffer_store_b64((v), rs, (off), (soff), 16)
#define BST32(v, off, soff) __builtin_amdgcn_raw_buffer_store_b32((v), rs, (off), (soff), 16)
#define CFENCE() asm volatile("" ::: "memory")
#define SB() __builtin_amdgcn_sched_barrier(0)
#define TMO(code) (tmo = (code))
#define LWAIT(off, target, code) do { int it_ = 0; _Pragma("nounroll") for (; it_ < LSPIN_MAX; ++it_) { if (lds32v(off) >= (u32)(target)) break; __builtin_amdgcn_s_sleep(1); } \
    if (it_ == LSPIN_MAX) TMO(code); CFENCE(); } while (0)

#define LWAIT0(off, target, code) do { int it_ = 0; _Pragma("nounroll") for (; it_ < LSPIN_MAX; ++it_) { if (lds32v(off) >= (u32)(target)) break; } \
    if (it_ == LSPIN_MAX) TMO(code); CFENCE(); } while (0)

#define ONES2 0x3F803F80u
#define LATE_MASK(r) ((((r) & 63) >= 50 ? 15ull : 3ull) << (((r) >> 6) * 2))
#define RG_OF(b) (((b) & 7) * 4 + (((b) >> 3) & 3))
#define CB_OF(b) ((b) >> 5)
#define IDS \
    int lane; asm volatile("v_mbcnt_lo_u32_b32 %0, -1, 0\n\tv_mbcnt_hi_u32_b32 %0, -1, %0" : "=v"(lane)); \
    int w = w_; asm volatile("" : "+s"(w)); \
    int b = b_; asm volatile("" : "+s"(b)); \
    const int rg = RG_OF(b), cb = CB_OF(b); \
    const int r16 = lane & 15, g = lane >> 4; \
    (void)rg; (void)cb; (void)r16; (void)g; (void)w;

#define CSL(w) ((0x4653210 >> (4 * (w))) & 15)
struct LaneC { int ka, kt, qa, qt, va, ta, l0; u32 av, tv0, ot; int sd, sstr; };
DEV LaneC lane_consts(int w_, int b_) {
    IDS
    LaneC c;
    const int rt4 = w & 3, pp = w >> 2;
    const int kvb = L_KV + 16 * rt4 * KVS + 72 * pp;
    const int qb = L_Q + (16 * rt4 + r16) * 144 + 72 * pp;
    const int trr = kvb + (4 * g + (r16 >> 2)) * KVS + 144;
    c.kt = kvb + r16 * KVS + 32; c.ka = kvb + r16 * KVS + g * 8;
    c.qa = qb + g * 8; c.qt = g == 2 ? qb + 32 : L_BC + BC_ZERO;
    c.va = trr + (r16 & 3) * 8; c.ta = trr + 32;
    c.l0 = 4096 + (8 * g + (r16 & 7)) * 16;
    c.av = lane * 16;
    c.tv0 = pp ? 7168 + g * 256 + r16 * 8 : 7168 + lane * 16;
    c.ot = 0;
    const int P = 4 * CSL(w) + g;
    c.sstr = P >= 18 ? 144 : KVS;
    c.sd = (P >= 18 ? (L_Q - 16 * 144 + (P - 18) * 16) : (L_KV + P * 16)) + r16 * c.sstr;
    c.sstr *= 16;
    return c;
}

struct MidS { u32x4 srd; u32 ob, st, tt, c0, l1d, tvd, wi, ld, wx, ldx, msk, scr, stat, x1, layer, pk, n7, skip, wc, lb; };

DEV void lc_set(const LaneC& c) {
    asm volatile("v_mov_b32 v100, %0\n\t" "v_mov_b32 v101, %1\n\t" "v_mov_b32 v102, %2\n\t" "v_mov_b32 v103, %3\n\t" "v_mov_b32 v104, %4\n\t" "v_mov_b32 v105, %5\n\t" "v_mov_b32 v106, %6\n\t" "v_mov_b32 v107, %7\n\t" "v_mov_b32 v108, %8\n\t" "v_mov_b32 v110, %10\n\t" "v_mov_b32 v111, %11\n\t"
        :: "v"(c.ka), "v"(c.kt), "v"(c.qa), "v"(c.qt), "v"(c.va), "v"(c.ta), "v"(c.l0), "v"(c.av), "v"(c.tv0), "v"(c.ot), "v"(c.sd), "v"(c.sstr));
}
DEV void xt_get(u32 (&x)[10]) {
    asm volatile("v_mov_b32 %0, v88\n\t" "v_mov_b32 %1, v89\n\t" "v_mov_b32 %2, v90\n\t" "v_mov_b32 %3, v91\n\t" "v_mov_b32 %4, v92\n\t" "v_mov_b32 %5, v93\n\t" "v_mov_b32 %6, v94\n\t" "v_mov_b32 %7, v95\n\t" "v_mov_b32 %8, v96\n\t" "v_mov_b32 %9, v97\n\t"
        : "=v"(x[0]), "=v"(x[1]), "=v"(x[2]), "=v"(x[3]), "=v"(x[4]), "=v"(x[5]), "=v"(x[6]), "=v"(x[7]), "=v"(x[8]), "=v"(x[9]));
}
DEV void rs_get(int a) { asm volatile("ds_read_b64 v[98:99], %0" :: "v"(a) : "memory"); }

DEV void gemm0(u32x4 srd, int xa, u32 iv, u32 si, int (&xt)[3], u32 ro, u32 cm, u32 c0, u32 c1, u32 fo, u32 fexp, u32 cs, u64 va, u64 v0, u32& cn) {
    int xb; u32 fl, sf;
    asm volatile(
        "s_nop 4\n\t"
        "s_setprio 2\n\t"
        "buffer_load_dwordx4 v[112:115], %[iv], %[ssrd], %[si] offen offset:0 sc1\n\t"
        "buffer_load_dwordx4 v[116:119], %[iv], %[ssrd], %[si] offen offset:1024 sc1\n\t"
        "buffer_load_dwordx4 v[120:123], %[iv], %[ssrd], %[si] offen offset:2048 sc1\n\t"
        "buffer_load_dwordx4 v[124:127], %[iv], %[ssrd], %[si] offen offset:3072 sc1\n\t"
        "s_add_u32 %[si], %[si], 4096\n\t"
        "buffer_load_dwordx4 v[128:131], %[iv], %[ssrd], %[si] offen offset:0 sc1\n\t"
        "buffer_load_dwordx4 v[132:135], %[iv], %[ssrd], %[si] offen offset:1024 sc1\n\t"
        "buffer_load_dwordx4 v[136:139], %[iv], %[ssrd], %[si] offen offset:2048 sc1\n\t"
        "buffer_load_dwordx4 v[140:143], %[iv], %[ssrd], %[si] offen offset:3072 sc1\n\t"
        "s_add_u32 %[si], %[si], 4096\n\t"
        "buffer_load_dwordx4 v[144:147], %[iv], %[ssrd], %[si] offen offset:0 sc1\n\t"
        "buffer_load_dwordx4 v[148:151], %[iv], %[ssrd], %[si] offen offset:1024 sc1\n\t"
        "ds_read_b128 v[88:91], %[x0]\n\t"
        "ds_read_b128 v[92:95], %[x1]\n\t"
        "ds_read_b64 v[96:97], %[x2]\n\t"
        "v_xor_b32 %[xb], 64, %[xa]\n\t"
        "v_subrev_u32 %[x0], %[sro], %[x0]\n\t"
        "v_subrev_u32 %[x1], %[sro], %[x1]\n\t"
        "ds_read_b128 v[24:27], %[x0] offset:0\n\t"
        "ds_read_b128 v[28:31], %[x0] offset:2048\n\t"
        "ds_read_b128 v[32:35], %[x0] offset:4096\n\t"
        "ds_read_b128 v[36:39], %[x0] offset:6144\n\t"
        "ds_read_b128 v[40:43], %[x0] offset:8192\n\t"
        "ds_read_b128 v[44:47], %[x1] offset:0\n\t"
        "s_waitcnt vmcnt(8) lgkmcnt(5)\n\t"
        "v_mfma_f32_16x16x32_bf16 v[48:51], v[112:115], v[24:27], 0\n\t"
        "v_mfma_f32_16x16x32_bf16 v[68:71], v[116:119], v[24:27], 0\n\t"
        "ds_read_b128 v[24:27], %[x1] offset:2048\n\t"
        "s_waitcnt lgkmcnt(5)\n\t"
        "v_mfma_f32_16x16x32_bf16 v[52:55], v[112:115], v[28:31], 0\n\t"
        "v_mfma_f32_16x16x32_bf16 v[72:75], v[116:119], v[28:31], 0\n\t"
        "buffer_load_dwordx4 v[152:155], %[iv], %[ssrd], %[si] offen offset:2048 sc1\n\t"
        "buffer_load_dwordx4 v[156:159], %[iv], %[ssrd], %[si] offen offset:3072 sc1\n\t"
        "ds_read_b128 v[28:31], %[x1] offset:4096\n\t"
        "s_waitcnt lgkmcnt(5)\n\t"
        "v_mfma_f32_16x16x32_bf16 v[56:59], v[112:115], v[32:35], 0\n\t"
        "v_mfma_f32_16x16x32_bf16 v[76:79], v[116:119], v[32:35], 0\n\t"
        "ds_read_b128 v[32:35], %[x1] offset:6144\n\t"
        "s_waitcnt lgkmcnt(5)\n\t"
        "v_mfma_f32_16x16x32_bf16 v[60:63], v[112:115], v[36:39], 0\n\t"
        "v_mfma_f32_16x16x32_bf16 v[80:83], v[116:119], v[36:39], 0\n\t"
        "ds_read_b128 v[36:39], %[x1] offset:8192\n\t"
        "s_waitcnt lgkmcnt(5)\n\t"
        "v_mfma_f32_16x16x32_bf16 v[64:67], v[112:115], v[40:43], 0\n\t"
        "v_mfma_f32_16x16x32_bf16 v[84:87], v[116:119], v[40:43], 0\n\t"
        "s_bitcmp1_b32 %[scm], 0\n\t"
        "s_cbranch_scc1 Lc0_%=\n\t"
        "Ld0_%=:\n\t"
        "ds_read_b128 v[40:43], %[xa] offset:0\n\t"
        "s_waitcnt vmcnt(8) lgkmcnt(5)\n\t"
        "v_mfma_f32_16x16x32_bf16 v[48:51], v[120:123], v[44:47], v[48:51]\n\t"
        "v_mfma_f32_16x16x32_bf16 v[68:71], v[124:127], v[44:47], v[68:71]\n\t"
        "ds_read_b128 v[44:47], %[xa] offset:2048\n\t"
        "s_waitcnt lgkmcnt(5)\n\t"
        "v_mfma_f32_16x16x32_bf16 v[52:55], v[120:123], v[24:27], v[52:55]\n\t"
        "v_mfma_f32_16x16x32_bf16 v[72:75], v[124:127], v[24:27], v[72:75]\n\t"
        "s_add_u32 %[si], %[si], 4096\n\t"
        "buffer_load_dwordx4 v[160:163], %[iv], %[ssrd], %[si] offen offset:0 sc1\n\t"
        "buffer_load_dwordx4 v[164:167], %[iv], %[ssrd], %[si] offen offset:1024 sc1\n\t"
        "ds_read_b128 v[24:27], %[xa] offset:4096\n\t"
        "s_waitcnt lgkmcnt(5)\n\t"
        "v_mfma_f32_16x16x32_bf16 v[56:59], v[120:123], v[28:31], v[56:59]\n\t"
        "v_mfma_f32_16x16x32_bf16 v[76:79], v[124:127], v[28:31], v[76:79]\n\t"
        "ds_read_b128 v[28:31], %[xa] offset:6144\n\t"
        "s_waitcnt lgkmcnt(5)\n\t"
        "v_mfma_f32_16x16x32_bf16 v[60:63], v[120:123], v[32:35], v[60:63]\n\t"
        "v_mfma_f32_16x16x32_bf16 v[80:83], v[124:127], v[32:35], v[80:83]\n\t"
        "ds_read_b128 v[32:35], %[xa] offset:8192\n\t"
        "s_waitcnt lgkmcnt(5)\n\t"
        "v_mfma_f32_16x16x32_bf16 v[64:67], v[120:123], v[36:39], v[64:67]\n\t"
        "v_mfma_f32_16x16x32_bf16 v[84:87], v[124:127], v[36:39], v[84:87]\n\t"
        "ds_read_b128 v[36:39], %[xb] offset:0\n\t"
        "s_waitcnt vmcnt(8) lgkmcnt(5)\n\t"
        "v_mfma_f32_16x16x32_bf16 v[48:51], v[128:131], v[40:43], v[48:51]\n\t"
        "v_mfma_f32_16x16x32_bf16 v[68:71], v[132:135], v[40:43], v[68:71]\n\t"
        "ds_read_b128 v[40:43], %[xb] offset:2048\n\t"
        "s_waitcnt lgkmcnt(5)\n\t"
        "v_mfma_f32_16x16x32_bf16 v[52:55], v[128:131], v[44:47], v[52:55]\n\t"
        "v_mfma_f32_16x16x32_bf16 v[72:75], v[132:135], v[44:47], v[72:75]\n\t"
        "buffer_load_dwordx4 v[168:171], %[iv], %[ssrd], %[si] offen offset:2048 sc1\n\t"
        "buffer_load_dwordx4 v[172:175], %[iv], %[ssrd], %[si] offen offset:3072 sc1\n\t"
        "ds_read_b128 v[44:47], %[xb] offset:4096\n\t"
        "s_waitcnt lgkmcnt(5)\n\t"
        "v_mfma_f32_16x16x32_bf16 v[56:59], v[128:131], v[24:27], v[56:59]\n\t"
        "v_mfma_f32_16x16x32_bf16 v[76:79], v[132:135], v[24:27], v[76:79]\n\t"
        "ds_read_b128 v[24:27], %[xb] offset:6144\n\t"
        "s_waitcnt lgkmcnt(5)\n\t"
        "v_mfma_f32_16x16x32_bf16 v[60:63], v[128:131], v[28:31], v[60:63]\n\t"
        "v_mfma_f32_16x16x32_bf16 v[80:83], v[132:135], v[28:31], v[80:83]\n\t"
        "ds_read_b128 v[28:31], %[xb] offset:8192\n\t"
        "s_waitcnt lgkmcnt(5)\n\t"
        "v_mfma_f32_16x16x32_bf16 v[64:67], v[128:131], v[32:35], v[64:67]\n\t"
        "v_mfma_f32_16x16x32_bf16 v[84:87], v[132:135], v[32:35], v[84:87]\n\t"
        "s_bitcmp1_b32 %[scm], 1\n\t"
        "s_cbranch_scc1 Lc1_%=\n\t"
        "Ld1_%=:\n\t"
        "ds_read_b128 v[32:35], %[xa] offset:10240\n\t"
        "s_waitcnt vmcnt(8) lgkmcnt(5)\n\t"
        "v_mfma_f32_16x16x32_bf16 v[48:51], v[136:139], v[36:39], v[48:51]\n\t"
        "v_mfma_f32_16x16x32_bf16 v[68:71], v[140:143], v[36:39], v[68:71]\n\t"
        "ds_read_b128 v[36:39], %[xa] offset:12288\n\t"
        "s_waitcnt lgkmcnt(5)\n\t"
        "v_mfma_f32_16x16x32_bf16 v[52:55], v[136:139], v[40:43], v[52:55]\n\t"
        "v_mfma_f32_16x16x32_bf16 v[72:75], v[140:143], v[40:43], v[72:75]\n\t"
        "s_add_u32 %[si], %[si], 4096\n\t"
        "buffer_load_dwordx4 v[176:179], %[iv], %[ssrd], %[si] offen offset:0 sc1\n\t"
        "buffer_load_dwordx4 v[180:183], %[iv], %[ssrd], %[si] offen offset:1024 sc1\n\t"
        "ds_read_b128 v[40:43], %[xa] offset:14336\n\t"
        "s_waitcnt lgkmcnt(5)\n\t"
        "v_mfma_f32_16x16x32_bf16 v[56:59], v[136:139], v[44:47], v[56:59]\n\t"
        "v_mfma_f32_16x16x32_bf16 v[76:79], v[140:143], v[44:47], v[76:79]\n\t"
        "ds_read_b128 v[44:47], %[xa] offset:16384\n\t"
        "s_waitcnt lgkmcnt(5)\n\t"
        "v_mfma_f32_16x16x32_bf16 v[60:63], v[136:139], v[24:27], v[60:63]\n\t"
        "v_mfma_f32_16x16x32_bf16 v[80:83], v[140:143], v[24:27], v[80:83]\n\t"
        "ds_read_b128 v[24:27], %[xa] offset:18432\n\t"
        "s_waitcnt lgkmcnt(5)\n\t"
        "v_mfma_f32_16x16x32_bf16 v[64:67], v[136:139], v[28:31], v[64:67]\n\t"
        "v_mfma_f32_16x16x32_bf16 v[84:87], v[140:143], v[28:31], v[84:87]\n\t"
        "ds_read_b128 v[28:31], %[xb] offset:10240\n\t"
        "s_waitcnt vmcnt(8) lgkmcnt(5)\n\t"
        "v_mfma_f32_16x16x32_bf16 v[48:51], v[144:147], v[32:35], v[48:51]\n\t"
        "v_mfma_f32_16x16x32_bf16 v[68:71], v[148:151], v[32:35], v[68:71]\n\t"
        "ds_read_b128 v[32:35], %[xb] offset:12288\n\t"
        "s_waitcnt lgkmcnt(5)\n\t"
        "v_mfma_f32_16x16x32_bf16 v[52:55], v[144:147], v[36:39], v[52:55]\n\t"
        "v_mfma_f32_16x16x32_bf16 v[72:75], v[148:151], v[36:39], v[72:75]\n\t"
        "buffer_load_dwordx4 v[184:187], %[iv], %[ssrd], %[si] offen offset:2048 sc1\n\t"
        "buffer_load_dwordx4 v[188:191], %[iv], %[ssrd], %[si] offen offset:3072 sc1\n\t"
        "ds_read_b128 v[36:39], %[xb] offset:14336\n\t"
        "s_waitcnt lgkmcnt(5)\n\t"
        "v_mfma_f32_16x16x32_bf16 v[56:59], v[144:147], v[40:43], v[56:59]\n\t"
        "v_mfma_f32_16x16x32_bf16 v[76:79], v[148:151], v[40:43], v[76:79]\n\t"
        "ds_read_b128 v[40:43], %[xb] offset:16384\n\t"
        "s_waitcnt lgkmcnt(5)\n\t"
        "v_mfma_f32_16x16x32_bf16 v[60:63], v[144:147], v[44:47], v[60:63]\n\t"
        "v_mfma_f32_16x16x32_bf16 v[80:83], v[148:151], v[44:47], v[80:83]\n\t"
        "ds_read_b128 v[44:47], %[xb] offset:18432\n\t"
        "s_waitcnt lgkmcnt(5)\n\t"
        "v_mfma_f32_16x16x32_bf16 v[64:67], v[144:147], v[24:27], v[64:67]\n\t"
        "v_mfma_f32_16x16x32_bf16 v[84:87], v[148:151], v[24:27], v[84:87]\n\t"
        "s_bitcmp1_b32 %[scm], 2\n\t"
        "s_cbranch_scc1 Lc2_%=\n\t"
        "Ld2_%=:\n\t"
        "ds_read_b128 v[24:27], %[xa] offset:20480\n\t"
        "s_waitcnt vmcnt(8) lgkmcnt(5)\n\t"
        "v_mfma_f32_16x16x32_bf16 v[48:51], v[152:155], v[28:31], v[48:51]\n\t"
        "v_mfma_f32_16x16x32_bf16 v[68:71], v[156:159], v[28:31], v[68:71]\n\t"
        "ds_read_b128 v[28:31], %[xa] offset:22528\n\t"
        "s_waitcnt lgkmcnt(5)\n\t"
        "v_mfma_f32_16x16x32_bf16 v[52:55], v[152:155], v[32:35], v[52:55]\n\t"
        "v_mfma_f32_16x16x32_bf16 v[72:75], v[156:159], v[32:35], v[72:75]\n\t"
        "s_add_u32 %[si], %[si], 4096\n\t"
        "buffer_load_dwordx4 v[192:195], %[iv], %[ssrd], %[si] offen offset:0 sc1\n\t"
        "buffer_load_dwordx4 v[196:199], %[iv], %[ssrd], %[si] offen offset:1024 sc1\n\t"
        "ds_read_b128 v[32:35], %[xa] offset:24576\n\t"
        "s_waitcnt lgkmcnt(5)\n\t"
        "v_mfma_f32_16x16x32_bf16 v[56:59], v[152:155], v[36:39], v[56:59]\n\t"
        "v_mfma_f32_16x16x32_bf16 v[76:79], v[156:159], v[36:39], v[76:79]\n\t"
        "ds_read_b128 v[36:39], %[xa] offset:26624\n\t"
        "s_waitcnt lgkmcnt(5)\n\t"
        "v_mfma_f32_16x16x32_bf16 v[60:63], v[152:155], v[40:43], v[60:63]\n\t"
        "v_mfma_f32_16x16x32_bf16 v[80:83], v[156:159], v[40:43], v[80:83]\n\t"
        "ds_read_b128 v[40:43], %[xa] offset:28672\n\t"
        "s_waitcnt lgkmcnt(5)\n\t"
        "v_mfma_f32_16x16x32_bf16 v[64:67], v[152:155], v[44:47], v[64:67]\n\t"
        "v_mfma_f32_16x16x32_bf16 v[84:87], v[156:159], v[44:47], v[84:87]\n\t"
        "ds_read_b128 v[44:47], %[xb] offset:20480\n\t"
        "s_setprio 1\n\t"
        "s_waitcnt vmcnt(8) lgkmcnt(5)\n\t"
        "v_mfma_f32_16x16x32_bf16 v[48:51], v[160:163], v[24:27], v[48:51]\n\t"
        "v_mfma_f32_16x16x32_bf16 v[68:71], v[164:167], v[24:27], v[68:71]\n\t"
        "ds_read_b128 v[24:27], %[xb] offset:22528\n\t"
        "s_waitcnt lgkmcnt(5)\n\t"
        "v_mfma_f32_16x16x32_bf16 v[52:55], v[160:163], v[28:31], v[52:55]\n\t"
        "v_mfma_f32_16x16x32_bf16 v[72:75], v[164:167], v[28:31], v[72:75]\n\t"
        "buffer_load_dwordx4 v[200:203], %[iv], %[ssrd], %[si] offen offset:2048 sc1\n\t"
        "buffer_load_dwordx4 v[204:207], %[iv], %[ssrd], %[si] offen offset:3072 sc1\n\t"
        "ds_read_b128 v[28:31], %[xb] offset:24576\n\t"
        "s_waitcnt lgkmcnt(5)\n\t"
        "v_mfma_f32_16x16x32_bf16 v[56:59], v[160:163], v[32:35], v[56:59]\n\t"
        "v_mfma_f32_16x16x32_bf16 v[76:79], v[164:167], v[32:35], v[76:79]\n\t"
        "ds_read_b128 v[32:35], %[xb] offset:26624\n\t"
        "s_waitcnt lgkmcnt(5)\n\t"
        "v_mfma_f32_16x16x32_bf16 v[60:63], v[160:163], v[36:39], v[60:63]\n\t"
        "v_mfma_f32_16x16x32_bf16 v[80:83], v[164:167], v[36:39], v[80:83]\n\t"
        "ds_read_b128 v[36:39], %[xb] offset:28672\n\t"
        "s_waitcnt lgkmcnt(5)\n\t"
        "v_mfma_f32_16x16x32_bf16 v[64:67], v[160:163], v[40:43], v[64:67]\n\t"
        "v_mfma_f32_16x16x32_bf16 v[84:87], v[164:167], v[40:43], v[84:87]\n\t"
        "s_bitcmp1_b32 %[scm], 3\n\t"
        "s_cbranch_scc1 Lc3_%=\n\t"
        "Ld3_%=:\n\t"
        "ds_read_b128 v[40:43], %[xa] offset:30720\n\t"
        "s_waitcnt vmcnt(8) lgkmcnt(5)\n\t"
        "v_mfma_f32_16x16x32_bf16 v[48:51], v[168:171], v[44:47], v[48:51]\n\t"
        "v_mfma_f32_16x16x32_bf16 v[68:71], v[172:175], v[44:47], v[68:71]\n\t"
        "ds_read_b128 v[44:47], %[xa] offset:32768\n\t"
        "s_waitcnt lgkmcnt(5)\n\t"
        "v_mfma_f32_16x16x32_bf16 v[52:55], v[168:171], v[24:27], v[52:55]\n\t"
        "v_mfma_f32_16x16x32_bf16 v[72:75], v[172:175], v[24:27], v[72:75]\n\t"
        "s_add_u32 %[si], %[si], 4096\n\t"
        "buffer_load_dwordx4 v[208:211], %[iv], %[ssrd], %[si] offen offset:0 sc1\n\t"
        "buffer_load_dwordx4 v[212:215], %[iv], %[ssrd], %[si] offen offset:1024 sc1\n\t"
        "ds_read_b128 v[24:27], %[xa] offset:34816\n\t"
        "s_waitcnt lgkmcnt(5)\n\t"
        "v_mfma_f32_16x16x32_bf16 v[56:59], v[168:171], v[28:31], v[56:59]\n\t"
        "v_mfma_f32_16x16x32_bf16 v[76:79], v[172:175], v[28:31], v[76:79]\n\t"
        "ds_read_b128 v[28:31], %[xa] offset:36864\n\t"
        "s_waitcnt lgkmcnt(5)\n\t"
        "v_mfma_f32_16x16x32_bf16 v[60:63], v[168:171], v[32:35], v[60:63]\n\t"
        "v_mfma_f32_16x16x32_bf16 v[80:83], v[172:175], v[32:35], v[80:83]\n\t"
        "ds_read_b128 v[32:35], %[xa] offset:38912\n\t"
        "s_waitcnt lgkmcnt(5)\n\t"
        "v_mfma_f32_16x16x32_bf16 v[64:67], v[168:171], v[36:39], v[64:67]\n\t"
        "v_mfma_f32_16x16x32_bf16 v[84:87], v[172:175], v[36:39], v[84:87]\n\t"
        "ds_read_b128 v[36:39], %[xb] offset:30720\n\t"
        "s_waitcnt vmcnt(8) lgkmcnt(5)\n\t"
        "v_mfma_f32_16x16x32_bf16 v[48:51], v[176:179], v[40:43], v[48:51]\n\t"
        "v_mfma_f32_16x16x32_bf16 v[68:71], v[180:183], v[40:43], v[68:71]\n\t"
        "ds_read_b128 v[40:43], %[xb] offset:32768\n\t"
        "s_waitcnt lgkmcnt(5)\n\t"
        "v_mfma_f32_16x16x32_bf16 v[52:55], v[176:179], v[44:47], v[52:55]\n\t"
        "v_mfma_f32_16x16x32_bf16 v[72:75], v[180:183], v[44:47], v[72:75]\n\t"
        "buffer_load_dwordx4 v[216:219], %[iv], %[ssrd], %[si] offen offset:2048 sc1\n\t"
        "buffer_load_dwordx4 v[220:223], %[iv], %[ssrd], %[si] offen offset:3072 sc1\n\t"
        "ds_read_b128 v[44:47], %[xb] offset:34816\n\t"
        "s_waitcnt lgkmcnt(5)\n\t"
        "v_mfma_f32_16x16x32_bf16 v[56:59], v[176:179], v[24:27], v[56:59]\n\t"
        "v_mfma_f32_16x16x32_bf16 v[76:79], v[180:183], v[24:27], v[76:79]\n\t"
        "ds_read_b128 v[24:27], %[xb] offset:36864\n\t"
        "s_waitcnt lgkmcnt(5)\n\t"
        "v_mfma_f32_16x16x32_bf16 v[60:63], v[176:179], v[28:31], v[60:63]\n\t"
        "v_mfma_f32_16x16x32_bf16 v[80:83], v[180:183], v[28:31], v[80:83]\n\t"
        "ds_read_b128 v[28:31], %[xb] offset:38912\n\t"
        "s_waitcnt lgkmcnt(5)\n\t"
        "v_mfma_f32_16x16x32_bf16 v[64:67], v[176:179], v[32:35], v[64:67]\n\t"
        "v_mfma_f32_16x16x32_bf16 v[84:87], v[180:183], v[32:35], v[84:87]\n\t"
        "s_bitcmp1_b32 %[scm], 4\n\t"
        "s_cbranch_scc1 Lc4_%=\n\t"
        "Ld4_%=:\n\t"
        "ds_read_b128 v[32:35], %[xa] offset:40960\n\t"
        "v_mov_b32 %[x2], 153984\n\t"
        "v_mov_b32 %[x0], 1\n\t"
        "s_mov_b64 exec, 1\n\t"
        "ds_add_u32 %[x2], %[x0] offset:%[omc]+4\n\t"
        "s_mov_b64 exec, -1\n\t"
        "s_waitcnt vmcnt(8) lgkmcnt(6)\n\t"
        "v_mfma_f32_16x16x32_bf16 v[48:51], v[184:187], v[36:39], v[48:51]\n\t"
        "v_mfma_f32_16x16x32_bf16 v[68:71], v[188:191], v[36:39], v[68:71]\n\t"
        "ds_read_b128 v[36:39], %[xa] offset:43008\n\t"
        "s_waitcnt lgkmcnt(6)\n\t"
        "v_mfma_f32_16x16x32_bf16 v[52:55], v[184:187], v[40:43], v[52:55]\n\t"
        "v_mfma_f32_16x16x32_bf16 v[72:75], v[188:191], v[40:43], v[72:75]\n\t"
        "s_add_u32 %[si], %[si], 4096\n\t"
        "buffer_load_dwordx4 v[224:227], %[iv], %[ssrd], %[si] offen offset:0 sc1\n\t"
        "buffer_load_dwordx4 v[228:231], %[iv], %[ssrd], %[si] offen offset:1024 sc1\n\t"
        "ds_read_b128 v[40:43], %[xa] offset:45056\n\t"
        "s_waitcnt lgkmcnt(6)\n\t"
        "v_mfma_f32_16x16x32_bf16 v[56:59], v[184:187], v[44:47], v[56:59]\n\t"
        "v_mfma_f32_16x16x32_bf16 v[76:79], v[188:191], v[44:47], v[76:79]\n\t"
        "ds_read_b128 v[44:47], %[xa] offset:47104\n\t"
        "s_waitcnt lgkmcnt(6)\n\t"
        "v_mfma_f32_16x16x32_bf16 v[60:63], v[184:187], v[24:27], v[60:63]\n\t"
        "v_mfma_f32_16x16x32_bf16 v[80:83], v[188:191], v[24:27], v[80:83]\n\t"
        "ds_read_b128 v[24:27], %[xa] offset:49152\n\t"
        "s_waitcnt lgkmcnt(6)\n\t"
        "v_mfma_f32_16x16x32_bf16 v[64:67], v[184:187], v[28:31], v[64:67]\n\t"
        "v_mfma_f32_16x16x32_bf16 v[84:87], v[188:191], v[28:31], v[84:87]\n\t"
        "ds_read_b128 v[28:31], %[xb] offset:40960\n\t"
        "s_waitcnt vmcnt(8) lgkmcnt(6)\n\t"
        "v_mfma_f32_16x16x32_bf16 v[48:51], v[192:195], v[32:35], v[48:51]\n\t"
        "v_mfma_f32_16x16x32_bf16 v[68:71], v[196:199], v[32:35], v[68:71]\n\t"
        "ds_read_b128 v[32:35], %[xb] offset:43008\n\t"
        "s_waitcnt lgkmcnt(5)\n\t"
        "v_mfma_f32_16x16x32_bf16 v[52:55], v[192:195], v[36:39], v[52:55]\n\t"
        "v_mfma_f32_16x16x32_bf16 v[72:75], v[196:199], v[36:39], v[72:75]\n\t"
        "buffer_load_dwordx4 v[232:235], %[iv], %[ssrd], %[si] offen offset:2048 sc1\n\t"
        "buffer_load_dwordx4 v[236:239], %[iv], %[ssrd], %[si] offen offset:3072 sc1\n\t"
        "ds_read_b128 v[36:39], %[xb] offset:45056\n\t"
        "s_waitcnt lgkmcnt(5)\n\t"
        "v_mfma_f32_16x16x32_bf16 v[56:59], v[192:195], v[40:43], v[56:59]\n\t"
        "v_mfma_f32_16x16x32_bf16 v[76:79], v[196:199], v[40:43], v[76:79]\n\t"
        "ds_read_b128 v[40:43], %[xb] offset:47104\n\t"
        "s_waitcnt lgkmcnt(5)\n\t"
        "v_mfma_f32_16x16x32_bf16 v[60:63], v[192:195], v[44:47], v[60:63]\n\t"
        "v_mfma_f32_16x16x32_bf16 v[80:83], v[196:199], v[44:47], v[80:83]\n\t"
        "ds_read_b128 v[44:47], %[xb] offset:49152\n\t"
        "s_waitcnt lgkmcnt(5)\n\t"
        "v_mfma_f32_16x16x32_bf16 v[64:67], v[192:195], v[24:27], v[64:67]\n\t"
        "v_mfma_f32_16x16x32_bf16 v[84:87], v[196:199], v[24:27], v[84:87]\n\t"
        "s_bitcmp1_b32 %[scm], 5\n\t"
        "s_cbranch_scc1 Lc5_%=\n\t"
        "Ld5_%=:\n\t"
        "v_add_u32 %[xa], 51200, %[xa]\n\t"
        "ds_read_b128 v[24:27], %[xa] offset:0\n\t"
        "s_waitcnt vmcnt(8) lgkmcnt(5)\n\t"
        "v_mfma_f32_16x16x32_bf16 v[48:51], v[200:203], v[28:31], v[48:51]\n\t"
        "v_mfma_f32_16x16x32_bf16 v[68:71], v[204:207], v[28:31], v[68:71]\n\t"
        "ds_read_b128 v[28:31], %[xa] offset:2048\n\t"
        "s_waitcnt lgkmcnt(5)\n\t"
        "v_mfma_f32_16x16x32_bf16 v[52:55], v[200:203], v[32:35], v[52:55]\n\t"
        "v_mfma_f32_16x16x32_bf16 v[72:75], v[204:207], v[32:35], v[72:75]\n\t"
        "s_add_u32 %[si], %[si], 4096\n\t"
        "buffer_load_dwordx4 v[240:243], %[iv], %[ssrd], %[si] offen offset:0 sc1\n\t"
        "buffer_load_dwordx4 v[244:247], %[iv], %[ssrd], %[si] offen offset:1024 sc1\n\t"
        "ds_read_b128 v[32:35], %[xa] offset:4096\n\t"
        "s_waitcnt lgkmcnt(5)\n\t"
        "v_mfma_f32_16x16x32_bf16 v[56:59], v[200:203], v[36:39], v[56:59]\n\t"
        "v_mfma_f32_16x16x32_bf16 v[76:79], v[204:207], v[36:39], v[76:79]\n\t"
        "ds_read_b128 v[36:39], %[xa] offset:6144\n\t"
        "s_waitcnt lgkmcnt(5)\n\t"
        "v_mfma_f32_16x16x32_bf16 v[60:63], v[200:203], v[40:43], v[60:63]\n\t"
        "v_mfma_f32_16x16x32_bf16 v[80:83], v[204:207], v[40:43], v[80:83]\n\t"
        "ds_read_b128 v[40:43], %[xa] offset:8192\n\t"
        "s_waitcnt lgkmcnt(5)\n\t"
        "v_mfma_f32_16x16x32_bf16 v[64:67], v[200:203], v[44:47], v[64:67]\n\t"
        "v_mfma_f32_16x16x32_bf16 v[84:87], v[204:207], v[44:47], v[84:87]\n\t"
        "v_add_u32 %[xb], 51200, %[xb]\n\t"
        "ds_read_b128 v[44:47], %[xb] offset:0\n\t"
        "s_setprio 0\n\t"
        "s_waitcnt vmcnt(8) lgkmcnt(5)\n\t"
        "v_mfma_f32_16x16x32_bf16 v[48:51], v[208:211], v[24:27], v[48:51]\n\t"
        "v_mfma_f32_16x16x32_bf16 v[68:71], v[212:215], v[24:27], v[68:71]\n\t"
        "ds_read_b128 v[24:27], %[xb] offset:2048\n\t"
        "s_waitcnt lgkmcnt(5)\n\t"
        "v_mfma_f32_16x16x32_bf16 v[52:55], v[208:211], v[28:31], v[52:55]\n\t"
        "v_mfma_f32_16x16x32_bf16 v[72:75], v[212:215], v[28:31], v[72:75]\n\t"
        "buffer_load_dwordx4 v[248:251], %[iv], %[ssrd], %[si] offen offset:2048 sc1\n\t"
        "buffer_load_dwordx4 v[252:255], %[iv], %[ssrd], %[si] offen offset:3072 sc1\n\t"
        "ds_read_b128 v[28:31], %[xb] offset:4096\n\t"
        "s_waitcnt lgkmcnt(5)\n\t"
        "v_mfma_f32_16x16x32_bf16 v[56:59], v[208:211], v[32:35], v[56:59]\n\t"
        "v_mfma_f32_16x16x32_bf16 v[76:79], v[212:215], v[32:35], v[76:79]\n\t"
        "ds_read_b128 v[32:35], %[xb] offset:6144\n\t"
        "s_waitcnt lgkmcnt(5)\n\t"
        "v_mfma_f32_16x16x32_bf16 v[60:63], v[208:211], v[36:39], v[60:63]\n\t"
        "v_mfma_f32_16x16x32_bf16 v[80:83], v[212:215], v[36:39], v[80:83]\n\t"
        "ds_read_b128 v[36:39], %[xb] offset:8192\n\t"
        "s_waitcnt lgkmcnt(5)\n\t"
        "v_mfma_f32_16x16x32_bf16 v[64:67], v[208:211], v[40:43], v[64:67]\n\t"
        "v_mfma_f32_16x16x32_bf16 v[84:87], v[212:215], v[40:43], v[84:87]\n\t"
        "s_bitcmp1_b32 %[scm], 6\n\t"
        "s_cbranch_scc1 Lc6_%=\n\t"
        "Ld6_%=:\n\t"
        "ds_read_b128 v[40:43], %[xa] offset:10240\n\t"
        "s_waitcnt vmcnt(8) lgkmcnt(5)\n\t"
        "v_mfma_f32_16x16x32_bf16 v[48:51], v[216:219], v[44:47], v[48:51]\n\t"
        "v_mfma_f32_16x16x32_bf16 v[68:71], v[220:223], v[44:47], v[68:71]\n\t"
        "ds_read_b128 v[44:47], %[xa] offset:12288\n\t"
        "s_waitcnt lgkmcnt(5)\n\t"
        "v_mfma_f32_16x16x32_bf16 v[52:55], v[216:219], v[24:27], v[52:55]\n\t"
        "v_mfma_f32_16x16x32_bf16 v[72:75], v[220:223], v[24:27], v[72:75]\n\t"
        "buffer_load_dword %[fl], %[fo], %[ssrd], 0 offen sc1\n\t"
        "ds_read_b128 v[24:27], %[xa] offset:14336\n\t"
        "s_waitcnt lgkmcnt(5)\n\t"
        "v_mfma_f32_16x16x32_bf16 v[56:59], v[216:219], v[28:31], v[56:59]\n\t"
        "v_mfma_f32_16x16x32_bf16 v[76:79], v[220:223], v[28:31], v[76:79]\n\t"
        "ds_read_b128 v[28:31], %[xa] offset:16384\n\t"
        "s_waitcnt lgkmcnt(5)\n\t"
        "v_mfma_f32_16x16x32_bf16 v[60:63], v[216:219], v[32:35], v[60:63]\n\t"
        "v_mfma_f32_16x16x32_bf16 v[80:83], v[220:223], v[32:35], v[80:83]\n\t"
        "ds_read_b128 v[32:35], %[xa] offset:18432\n\t"
        "s_waitcnt lgkmcnt(5)\n\t"
        "v_mfma_f32_16x16x32_bf16 v[64:67], v[216:219], v[36:39], v[64:67]\n\t"
        "v_mfma_f32_16x16x32_bf16 v[84:87], v[220:223], v[36:39], v[84:87]\n\t"
        "ds_read_b128 v[36:39], %[xb] offset:10240\n\t"
        "s_waitcnt vmcnt(7) lgkmcnt(5)\n\t"
        "v_mfma_f32_16x16x32_bf16 v[48:51], v[224:227], v[40:43], v[48:51]\n\t"
        "v_mfma_f32_16x16x32_bf16 v[68:71], v[228:231], v[40:43], v[68:71]\n\t"
        "ds_read_b128 v[40:43], %[xb] offset:12288\n\t"
        "s_waitcnt lgkmcnt(5)\n\t"
        "v_mfma_f32_16x16x32_bf16 v[52:55], v[224:227], v[44:47], v[52:55]\n\t"
        "v_mfma_f32_16x16x32_bf16 v[72:75], v[228:231], v[44:47], v[72:75]\n\t"
        "ds_read_b128 v[44:47], %[xb] offset:14336\n\t"
        "s_waitcnt lgkmcnt(5)\n\t"
        "v_mfma_f32_16x16x32_bf16 v[56:59], v[224:227], v[24:27], v[56:59]\n\t"
        "v_mfma_f32_16x16x32_bf16 v[76:79], v[228:231], v[24:27], v[76:79]\n\t"
        "ds_read_b128 v[24:27], %[xb] offset:16384\n\t"
        "s_waitcnt lgkmcnt(5)\n\t"
        "v_mfma_f32_16x16x32_bf16 v[60:63], v[224:227], v[28:31], v[60:63]\n\t"
        "v_mfma_f32_16x16x32_bf16 v[80:83], v[228:231], v[28:31], v[80:83]\n\t"
        "ds_read_b128 v[28:31], %[xb] offset:18432\n\t"
        "s_waitcnt lgkmcnt(5)\n\t"
        "v_mfma_f32_16x16x32_bf16 v[64:67], v[224:227], v[32:35], v[64:67]\n\t"
        "v_mfma_f32_16x16x32_bf16 v[84:87], v[228:231], v[32:35], v[84:87]\n\t"
        "s_bitcmp1_b32 %[scm], 7\n\t"
        "s_cbranch_scc1 Lc7_%=\n\t"
        "Ld7_%=:\n\t"
        "ds_read_b128 v[32:35], %[xa] offset:20480\n\t"
        "s_waitcnt vmcnt(5) lgkmcnt(5)\n\t"
        "v_mfma_f32_16x16x32_bf16 v[48:51], v[232:235], v[36:39], v[48:51]\n\t"
        "v_mfma_f32_16x16x32_bf16 v[68:71], v[236:239], v[36:39], v[68:71]\n\t"
        "ds_read_b128 v[36:39], %[xa] offset:22528\n\t"
        "s_waitcnt lgkmcnt(5)\n\t"
        "v_mfma_f32_16x16x32_bf16 v[52:55], v[232:235], v[40:43], v[52:55]\n\t"
        "v_mfma_f32_16x16x32_bf16 v[72:75], v[236:239], v[40:43], v[72:75]\n\t"
        "ds_read_b128 v[40:43], %[xa] offset:24576\n\t"
        "s_waitcnt lgkmcnt(5)\n\t"
        "v_mfma_f32_16x16x32_bf16 v[56:59], v[232:235], v[44:47], v[56:59]\n\t"
        "v_mfma_f32_16x16x32_bf16 v[76:79], v[236:239], v[44:47], v[76:79]\n\t"
        "ds_read_b128 v[44:47], %[xa] offset:26624\n\t"
        "s_waitcnt lgkmcnt(5)\n\t"
        "v_mfma_f32_16x16x32_bf16 v[60:63], v[232:235], v[24:27], v[60:63]\n\t"
        "v_mfma_f32_16x16x32_bf16 v[80:83], v[236:239], v[24:27], v[80:83]\n\t"
        "ds_read_b128 v[24:27], %[xa] offset:28672\n\t"
        "s_waitcnt lgkmcnt(5)\n\t"
        "v_mfma_f32_16x16x32_bf16 v[64:67], v[232:235], v[28:31], v[64:67]\n\t"
        "v_mfma_f32_16x16x32_bf16 v[84:87], v[236:239], v[28:31], v[84:87]\n\t"
        "ds_read_b128 v[28:31], %[xb] offset:20480\n\t"
        "s_waitcnt vmcnt(3) lgkmcnt(5)\n\t"
        "v_mfma_f32_16x16x32_bf16 v[48:51], v[240:243], v[32:35], v[48:51]\n\t"
        "v_mfma_f32_16x16x32_bf16 v[68:71], v[244:247], v[32:35], v[68:71]\n\t"
        "ds_read_b128 v[32:35], %[xb] offset:22528\n\t"
        "s_waitcnt lgkmcnt(5)\n\t"
        "v_mfma_f32_16x16x32_bf16 v[52:55], v[240:243], v[36:39], v[52:55]\n\t"
        "v_mfma_f32_16x16x32_bf16 v[72:75], v[244:247], v[36:39], v[72:75]\n\t"
        "ds_read_b128 v[36:39], %[xb] offset:24576\n\t"
        "v_and_b32 %[x0], 0xf0, v107\n\t"
        "v_lshrrev_b32 %[x0], 1, %[x0]\n\t"
        "v_add_u32 %[x0], 153344, %[x0]\n\t"
        "v_lshrrev_b32 %[x1], 8, v107\n\t"
        "v_lshl_add_u32 %[x1], %[x1], 5, %[scs]\n\t"
        "s_waitcnt lgkmcnt(5)\n\t"
        "v_mfma_f32_16x16x32_bf16 v[56:59], v[240:243], v[40:43], v[56:59]\n\t"
        "v_mfma_f32_16x16x32_bf16 v[76:79], v[244:247], v[40:43], v[76:79]\n\t"
        "ds_read_b128 v[40:43], %[xb] offset:26624\n\t"
        "s_waitcnt lgkmcnt(5)\n\t"
        "v_mfma_f32_16x16x32_bf16 v[60:63], v[240:243], v[44:47], v[60:63]\n\t"
        "v_mfma_f32_16x16x32_bf16 v[80:83], v[244:247], v[44:47], v[80:83]\n\t"
        "ds_read_b128 v[44:47], %[xb] offset:28672\n\t"
        "s_waitcnt lgkmcnt(5)\n\t"
        "v_mfma_f32_16x16x32_bf16 v[64:67], v[240:243], v[24:27], v[64:67]\n\t"
        "v_mfma_f32_16x16x32_bf16 v[84:87], v[244:247], v[24:27], v[84:87]\n\t"
        "s_waitcnt vmcnt(1) lgkmcnt(4)\n\t"
        "v_mfma_f32_16x16x32_bf16 v[48:51], v[248:251], v[28:31], v[48:51]\n\t"
        "v_mfma_f32_16x16x32_bf16 v[68:71], v[252:255], v[28:31], v[68:71]\n\t"
        "s_waitcnt lgkmcnt(3)\n\t"
        "v_mfma_f32_16x16x32_bf16 v[52:55], v[248:251], v[32:35], v[52:55]\n\t"
        "v_mfma_f32_16x16x32_bf16 v[72:75], v[252:255], v[32:35], v[72:75]\n\t"
        "ds_read_b64 v[34:35], %[x2] offset:%[omc]\n\t"
        "ds_read_b64 v[24:25], %[x0] offset:0\n\t"
        "ds_read_b64 v[26:27], %[x0] offset:128\n\t"
        "ds_read_b64 v[28:29], %[x0] offset:256\n\t"
        "ds_read_b64 v[30:31], %[x0] offset:384\n\t"
        "ds_read_b64 v[32:33], %[x0] offset:512\n\t"
        "s_waitcnt lgkmcnt(8)\n\t"
        "v_mfma_f32_16x16x32_bf16 v[56:59], v[248:251], v[36:39], v[56:59]\n\t"
        "v_mfma_f32_16x16x32_bf16 v[76:79], v[252:255], v[36:39], v[76:79]\n\t"
        "s_waitcnt lgkmcnt(7)\n\t"
        "v_mfma_f32_16x16x32_bf16 v[60:63], v[248:251], v[40:43], v[60:63]\n\t"
        "v_mfma_f32_16x16x32_bf16 v[80:83], v[252:255], v[40:43], v[80:83]\n\t"
        "s_waitcnt lgkmcnt(6)\n\t"
        "v_mfma_f32_16x16x32_bf16 v[64:67], v[248:251], v[44:47], v[64:67]\n\t"
        "v_mfma_f32_16x16x32_bf16 v[84:87], v[252:255], v[44:47], v[84:87]\n\t"
        "v_mov_b32_dpp v36, v48 row_newbcast:0 row_mask:0xf bank_mask:0xf bound_ctrl:1\n\t"
        "v_mov_b32_dpp v37, v49 row_newbcast:0 row_mask:0xf bank_mask:0xf bound_ctrl:1\n\t"
        "v_mov_b32_dpp v38, v50 row_newbcast:0 row_mask:0xf bank_mask:0xf bound_ctrl:1\n\t"
        "v_mov_b32_dpp v39, v51 row_newbcast:0 row_mask:0xf bank_mask:0xf bound_ctrl:1\n\t"
        "v_mov_b32_dpp v40, v68 row_newbcast:0 row_mask:0xf bank_mask:0xf bound_ctrl:1\n\t"
        "v_mov_b32_dpp v41, v69 row_newbcast:0 row_mask:0xf bank_mask:0xf bound_ctrl:1\n\t"
        "v_mov_b32_dpp v42, v70 row_newbcast:0 row_mask:0xf bank_mask:0xf bound_ctrl:1\n\t"
        "v_mov_b32_dpp v43, v71 row_newbcast:0 row_mask:0xf bank_mask:0xf bound_ctrl:1\n\t"
        "Lm_%=:\n\t"
        "s_waitcnt vmcnt(0) lgkmcnt(0)\n\t"
        "v_readfirstlane_b32 %[sf], v34\n\t"
        "v_cmp_ne_u32 vcc, %[sexp], %[fl]\n\t"
        "s_cmp_lt_u32 %[sf], 3\n\t"
        "s_cbranch_scc1 Ln_%=\n\t"
        "s_cbranch_vccnz Ln_%=\n\t"
        "Lo_%=:\n\t"
        "s_mov_b64 exec, %[sm16]\n\t"
        "ds_write_b128 %[x1], v[36:39]\n\t"
        "ds_write_b128 %[x1], v[40:43] offset:16\n\t"
        "s_mov_b64 exec, -1\n\t"
        "v_fma_f32 v48, -v24, v36, v48\n\t"
        "v_fma_f32 v49, -v24, v37, v49\n\t"
        "v_fma_f32 v50, -v24, v38, v50\n\t"
        "v_fma_f32 v51, -v24, v39, v51\n\t"
        "v_fma_f32 v68, -v24, v40, v68\n\t"
        "v_fma_f32 v69, -v24, v41, v69\n\t"
        "v_fma_f32 v70, -v24, v42, v70\n\t"
        "v_fma_f32 v71, -v24, v43, v71\n\t"
        "v_mul_f32 v48, v25, v48\n\t"
        "v_mul_f32 v49, v25, v49\n\t"
        "v_mul_f32 v50, v25, v50\n\t"
        "v_mul_f32 v51, v25, v51\n\t"
        "v_mul_f32 v68, v25, v68\n\t"
        "v_mul_f32 v69, v25, v69\n\t"
        "v_mul_f32 v70, v25, v70\n\t"
        "v_mul_f32 v71, v25, v71\n\t"
        "v_cvt_pk_bf16_f32 v48, v48, v68\n\t"
        "v_cvt_pk_bf16_f32 v49, v49, v69\n\t"
        "v_cvt_pk_bf16_f32 v50, v50, v70\n\t"
        "v_cvt_pk_bf16_f32 v51, v51, v71\n\t"
        "v_add_u32 v68, v110, v111\n\t"
        "v_add_u32 v69, v68, v111\n\t"
        "v_add_u32 v70, v69, v111\n\t"
        "v_add_u32 v71, v70, v111\n\t"
        "v_fma_f32 v52, -v26, v36, v52\n\t"
        "v_fma_f32 v53, -v26, v37, v53\n\t"
        "v_fma_f32 v54, -v26, v38, v54\n\t"
        "v_fma_f32 v55, -v26, v39, v55\n\t"
        "v_fma_f32 v72, -v26, v40, v72\n\t"
        "v_fma_f32 v73, -v26, v41, v73\n\t"
        "v_fma_f32 v74, -v26, v42, v74\n\t"
        "v_fma_f32 v75, -v26, v43, v75\n\t"
        "v_mul_f32 v52, v27, v52\n\t"
        "v_mul_f32 v53, v27, v53\n\t"
        "v_mul_f32 v54, v27, v54\n\t"
        "v_mul_f32 v55, v27, v55\n\t"
        "v_mul_f32 v72, v27, v72\n\t"
        "v_mul_f32 v73, v27, v73\n\t"
        "v_mul_f32 v74, v27, v74\n\t"
        "v_mul_f32 v75, v27, v75\n\t"
        "v_cvt_pk_bf16_f32 v52, v52, v72\n\t"
        "v_cvt_pk_bf16_f32 v53, v53, v73\n\t"
        "v_cvt_pk_bf16_f32 v54, v54, v74\n\t"
        "v_cvt_pk_bf16_f32 v55, v55, v75\n\t"
        "v_fma_f32 v56, -v28, v36, v56\n\t"
        "v_fma_f32 v57, -v28, v37, v57\n\t"
        "v_fma_f32 v58, -v28, v38, v58\n\t"
        "v_fma_f32 v59, -v28, v39, v59\n\t"
        "v_fma_f32 v76, -v28, v40, v76\n\t"
        "v_fma_f32 v77, -v28, v41, v77\n\t"
        "v_fma_f32 v78, -v28, v42, v78\n\t"
        "v_fma_f32 v79, -v28, v43, v79\n\t"
        "v_mul_f32 v56, v29, v56\n\t"
        "v_mul_f32 v57, v29, v57\n\t"
        "v_mul_f32 v58, v29, v58\n\t"
        "v_mul_f32 v59, v29, v59\n\t"
        "v_mul_f32 v76, v29, v76\n\t"
        "v_mul_f32 v77, v29, v77\n\t"
        "v_mul_f32 v78, v29, v78\n\t"
        "v_mul_f32 v79, v29, v79\n\t"
        "v_cvt_pk_bf16_f32 v56, v56, v76\n\t"
        "v_cvt_pk_bf16_f32 v57, v57, v77\n\t"
        "v_cvt_pk_bf16_f32 v58, v58, v78\n\t"
        "v_cvt_pk_bf16_f32 v59, v59, v79\n\t"
        "v_fma_f32 v60, -v30, v36, v60\n\t"
        "v_fma_f32 v61, -v30, v37, v61\n\t"
        "v_fma_f32 v62, -v30, v38, v62\n\t"
        "v_fma_f32 v63, -v30, v39, v63\n\t"
        "v_fma_f32 v80, -v30, v40, v80\n\t"
        "v_fma_f32 v81, -v30, v41, v81\n\t"
        "v_fma_f32 v82, -v30, v42, v82\n\t"
        "v_fma_f32 v83, -v30, v43, v83\n\t"
        "v_mul_f32 v60, v31, v60\n\t"
        "v_mul_f32 v61, v31, v61\n\t"
        "v_mul_f32 v62, v31, v62\n\t"
        "v_mul_f32 v63, v31, v63\n\t"
        "v_mul_f32 v80, v31, v80\n\t"
        "v_mul_f32 v81, v31, v81\n\t"
        "v_mul_f32 v82, v31, v82\n\t"
        "v_mul_f32 v83, v31, v83\n\t"
        "v_cvt_pk_bf16_f32 v60, v60, v80\n\t"
        "v_cvt_pk_bf16_f32 v61, v61, v81\n\t"
        "v_cvt_pk_bf16_f32 v62, v62, v82\n\t"
        "v_cvt_pk_bf16_f32 v63, v63, v83\n\t"
        "v_fma_f32 v64, -v32, v36, v64\n\t"
        "v_fma_f32 v65, -v32, v37, v65\n\t"
        "v_fma_f32 v66, -v32, v38, v66\n\t"
        "v_fma_f32 v67, -v32, v39, v67\n\t"
        "v_fma_f32 v84, -v32, v40, v84\n\t"
        "v_fma_f32 v85, -v32, v41, v85\n\t"
        "v_fma_f32 v86, -v32, v42, v86\n\t"
        "v_fma_f32 v87, -v32, v43, v87\n\t"
        "v_mul_f32 v64, v33, v64\n\t"
        "v_mul_f32 v65, v33, v65\n\t"
        "v_mul_f32 v66, v33, v66\n\t"
        "v_mul_f32 v67, v33, v67\n\t"
        "v_mul_f32 v84, v33, v84\n\t"
        "v_mul_f32 v85, v33, v85\n\t"
        "v_mul_f32 v86, v33, v86\n\t"
        "v_mul_f32 v87, v33, v87\n\t"
        "v_cvt_pk_bf16_f32 v64, v64, v84\n\t"
        "v_cvt_pk_bf16_f32 v65, v65, v85\n\t"
        "v_cvt_pk_bf16_f32 v66, v66, v86\n\t"
        "v_cvt_pk_bf16_f32 v67, v67, v87\n\t"
        "Lu_%=:\n\t"
        "v_readfirstlane_b32 %[sf], v35\n\t"
        "s_cmp_ge_u32 %[sf], 7\n\t"
        "s_cbranch_scc1 Lv_%=\n\t"
        "s_cmp_lt_i32 %[scn], 1\n\t"
        "s_cbranch_scc1 Lv_%=\n\t"
        "s_sub_u32 %[scn], %[scn], 1\n\t"
        "s_sleep 4\n\t"
        "ds_read_b32 v35, %[x2] offset:%[omc]+4\n\t"
        "s_waitcnt lgkmcnt(0)\n\t"
        "s_branch Lu_%=\n\t"
        "Lv_%=:\n\t"
        "s_mov_b64 exec, %[sva]\n\t"
        "ds_write_b128 v68, v[52:55]\n\t"
        "ds_write_b128 v69, v[56:59]\n\t"
        "ds_write_b128 v70, v[60:63]\n\t"
        "ds_write_b128 v71, v[64:67]\n\t"
        "s_mov_b64 exec, %[sv0]\n\t"
        "ds_write_b128 v110, v[48:51]\n\t"
        "s_mov_b64 exec, -1\n\t"
        "s_branch Le_%=\n\t"
        "Ln_%=:\n\t"
        "s_cmp_lt_i32 %[scn], 1\n\t"
        "s_cbranch_scc1 Lo_%=\n\t"
        "s_sub_u32 %[scn], %[scn], 1\n\t"
        "buffer_load_dword %[fl], %[fo], %[ssrd], 0 offen sc1\n\t"
        "ds_read_b64 v[34:35], %[x2] offset:%[omc]\n\t"
        "ds_read_b64 v[24:25], %[x0] offset:0\n\t"
        "ds_read_b64 v[26:27], %[x0] offset:128\n\t"
        "ds_read_b64 v[28:29], %[x0] offset:256\n\t"
        "ds_read_b64 v[30:31], %[x0] offset:384\n\t"
        "ds_read_b64 v[32:33], %[x0] offset:512\n\t"
        "s_branch Lm_%=\n\t"
        "Lc0_%=:\n\t"
        "v_add_u32 %[xa], %[c0], %[xa]\n\t"
        "v_add_u32 %[xb], %[c1], %[xb]\n\t"
        "v_xor_b32 %[c0], 10240, %[c0]\n\t"
        "v_xor_b32 %[c1], 10240, %[c1]\n\t"
        "s_branch Ld0_%=\n\t"
        "Lc1_%=:\n\t"
        "v_add_u32 %[xa], %[c0], %[xa]\n\t"
        "v_add_u32 %[xb], %[c1], %[xb]\n\t"
        "v_xor_b32 %[c0], 10240, %[c0]\n\t"
        "v_xor_b32 %[c1], 10240, %[c1]\n\t"
        "s_branch Ld1_%=\n\t"
        "Lc2_%=:\n\t"
        "v_add_u32 %[xa], %[c0], %[xa]\n\t"
        "v_add_u32 %[xb], %[c1], %[xb]\n\t"
        "v_xor_b32 %[c0], 10240, %[c0]\n\t"
        "v_xor_b32 %[c1], 10240, %[c1]\n\t"
        "s_branch Ld2_%=\n\t"
        "Lc3_%=:\n\t"
        "v_add_u32 %[xa], %[c0], %[xa]\n\t"
        "v_add_u32 %[xb], %[c1], %[xb]\n\t"
        "v_xor_b32 %[c0], 10240, %[c0]\n\t"
        "v_xor_b32 %[c1], 10240, %[c1]\n\t"
        "s_branch Ld3_%=\n\t"
        "Lc4_%=:\n\t"
        "v_add_u32 %[xa], %[c0], %[xa]\n\t"
        "v_add_u32 %[xb], %[c1], %[xb]\n\t"
        "v_xor_b32 %[c0], 10240, %[c0]\n\t"
        "v_xor_b32 %[c1], 10240, %[c1]\n\t"
        "s_branch Ld4_%=\n\t"
        "Lc5_%=:\n\t"
        "v_add_u32 %[xa], %[c0], %[xa]\n\t"
        "v_add_u32 %[xb], %[c1], %[xb]\n\t"
        "v_xor_b32 %[c0], 10240, %[c0]\n\t"
        "v_xor_b32 %[c1], 10240, %[c1]\n\t"
        "s_branch Ld5_%=\n\t"
        "Lc6_%=:\n\t"
        "v_add_u32 %[xa], %[c0], %[xa]\n\t"
        "v_add_u32 %[xb], %[c1], %[xb]\n\t"
        "v_xor_b32 %[c0], 10240, %[c0]\n\t"
        "v_xor_b32 %[c1], 10240, %[c1]\n\t"
        "s_branch Ld6_%=\n\t"
        "Lc7_%=:\n\t"
        "v_add_u32 %[xa], %[c0], %[xa]\n\t"
        "v_add_u32 %[xb], %[c1], %[xb]\n\t"
        "v_xor_b32 %[c0], 10240, %[c0]\n\t"
        "v_xor_b32 %[c1], 10240, %[c1]\n\t"
        "s_branch Ld7_%=\n\t"
        "Le_%=:\n\t"
        : [xa] "+v"(xa), [xb] "=&v"(xb), [c0] "+v"(c0), [c1] "+v"(c1), [si] "+s"(si), [fl] "=&v"(fl), [sf] "=&s"(sf), [scn] "+s"(cn), [x0] "+v"(xt[0]), [x1] "+v"(xt[1]), [x2] "+v"(xt[2])
        : [ssrd] "s"(srd), [iv] "v"(iv), [sro] "s"(ro), [scm] "s"(cm), [fo] "v"(fo), [sexp] "s"(fexp), [scs] "s"(cs), [sva] "s"(va), [sv0] "s"(v0), [sm16] "s"(0x0001000100010001ull), [omc] "n"(BC_MC)
        : "memory", "scc", "vcc", "v255", "v24", "v25", "v26", "v27", "v28", "v29", "v30", "v31", "v32", "v33", "v34", "v35", "v36", "v37", "v38", "v39", "v40", "v41", "v42", "v43", "v44", "v45", "v46", "v47");
}

DEV void gemm1a(int xo0, int fb, u32 cs, u64 va, u64 v0, u32 thr, u32& sf, u32& cn) {
    int xa, xh; u32 one, fl;
    asm volatile(
        "v_add_u32 %[xa], 36864, %[xo0]\n\t"
        "v_mov_b32 %[one], 1\n\t"
        "s_setprio 2\n\t"
        "ds_read_b128 v[24:27], %[xo0] offset:8192\n\t"
        "ds_read_b128 v[28:31], %[xo0] offset:8448\n\t"
        "ds_read_b128 v[32:35], %[xo0] offset:8704\n\t"
        "ds_read_b128 v[36:39], %[xo0] offset:8960\n\t"
        "ds_read_b128 v[40:43], %[xo0] offset:12288\n\t"
        "ds_read_b128 v[44:47], %[xo0] offset:12544\n\t"
        "ds_read_b128 v[8:11], %[xo0] offset:12800\n\t"
        "ds_read_b128 v[12:15], %[xo0] offset:13056\n\t"
        "ds_read_b128 v[16:19], %[xo0] offset:16384\n\t"
        "ds_read_b128 v[20:23], %[xo0] offset:16640\n\t"
        "ds_read_b128 v[48:51], %[xo0] offset:16896\n\t"
        "ds_read_b128 v[68:71], %[xo0] offset:17152\n\t"
        "s_waitcnt lgkmcnt(11)\n\t"
        "v_mfma_f32_16x16x32_bf16 v[52:55], v[128:131], v[24:27], v[52:55]\n\t"
        "v_mfma_f32_16x16x32_bf16 v[72:75], v[132:135], v[24:27], v[72:75]\n\t"
        "s_waitcnt lgkmcnt(10)\n\t"
        "v_mfma_f32_16x16x32_bf16 v[56:59], v[128:131], v[28:31], v[56:59]\n\t"
        "ds_read_b128 v[24:27], %[xo0] offset:20480\n\t"
        "v_mfma_f32_16x16x32_bf16 v[76:79], v[132:135], v[28:31], v[76:79]\n\t"
        "s_waitcnt lgkmcnt(10)\n\t"
        "v_mfma_f32_16x16x32_bf16 v[60:63], v[128:131], v[32:35], v[60:63]\n\t"
        "ds_read_b128 v[28:31], %[xo0] offset:20736\n\t"
        "v_mfma_f32_16x16x32_bf16 v[80:83], v[132:135], v[32:35], v[80:83]\n\t"
        "s_waitcnt lgkmcnt(10)\n\t"
        "v_mfma_f32_16x16x32_bf16 v[64:67], v[128:131], v[36:39], v[64:67]\n\t"
        "ds_read_b128 v[32:35], %[xo0] offset:20992\n\t"
        "v_mfma_f32_16x16x32_bf16 v[84:87], v[132:135], v[36:39], v[84:87]\n\t"
        "s_waitcnt lgkmcnt(10)\n\t"
        "v_mfma_f32_16x16x32_bf16 v[52:55], v[136:139], v[40:43], v[52:55]\n\t"
        "ds_read_b128 v[36:39], %[xo0] offset:21248\n\t"
        "v_mfma_f32_16x16x32_bf16 v[72:75], v[140:143], v[40:43], v[72:75]\n\t"
        "s_waitcnt lgkmcnt(10)\n\t"
        "v_mfma_f32_16x16x32_bf16 v[56:59], v[136:139], v[44:47], v[56:59]\n\t"
        "ds_read_b32 %[fl], %[fb] offset:%[og0]+4\n\t"
        "v_mfma_f32_16x16x32_bf16 v[76:79], v[140:143], v[44:47], v[76:79]\n\t"
        "s_waitcnt lgkmcnt(10)\n\t"
        "v_mfma_f32_16x16x32_bf16 v[60:63], v[136:139], v[8:11], v[60:63]\n\t"
        "v_mfma_f32_16x16x32_bf16 v[80:83], v[140:143], v[8:11], v[80:83]\n\t"
        "s_waitcnt lgkmcnt(9)\n\t"
        "v_mfma_f32_16x16x32_bf16 v[64:67], v[136:139], v[12:15], v[64:67]\n\t"
        "v_mfma_f32_16x16x32_bf16 v[84:87], v[140:143], v[12:15], v[84:87]\n\t"
        "s_waitcnt lgkmcnt(8)\n\t"
        "v_mfma_f32_16x16x32_bf16 v[52:55], v[144:147], v[16:19], v[52:55]\n\t"
        "v_mfma_f32_16x16x32_bf16 v[72:75], v[148:151], v[16:19], v[72:75]\n\t"
        "s_waitcnt lgkmcnt(7)\n\t"
        "v_mfma_f32_16x16x32_bf16 v[56:59], v[144:147], v[20:23], v[56:59]\n\t"
        "v_mfma_f32_16x16x32_bf16 v[76:79], v[148:151], v[20:23], v[76:79]\n\t"
        "s_waitcnt lgkmcnt(6)\n\t"
        "v_mfma_f32_16x16x32_bf16 v[60:63], v[144:147], v[48:51], v[60:63]\n\t"
        "s_waitcnt lgkmcnt(0)\n\t"
        "v_cmp_gt_u32 vcc, 2, %[fl]\n\t"
        "v_mfma_f32_16x16x32_bf16 v[80:83], v[148:151], v[48:51], v[80:83]\n\t"
        "v_mfma_f32_16x16x32_bf16 v[64:67], v[144:147], v[68:71], v[64:67]\n\t"
        "s_cbranch_vccnz Ls16_%=\n\t"
        "Lg16_%=:\n\t"
        "ds_read_b128 v[40:43], %[xo0] offset:24576\n\t"
        "v_mfma_f32_16x16x32_bf16 v[84:87], v[148:151], v[68:71], v[84:87]\n\t"
        "ds_read_b128 v[44:47], %[xo0] offset:24832\n\t"
        "v_mfma_f32_16x16x32_bf16 v[52:55], v[152:155], v[24:27], v[52:55]\n\t"
        "ds_read_b128 v[8:11], %[xo0] offset:25088\n\t"
        "v_mfma_f32_16x16x32_bf16 v[72:75], v[156:159], v[24:27], v[72:75]\n\t"
        "ds_read_b128 v[12:15], %[xo0] offset:25344\n\t"
        "v_mfma_f32_16x16x32_bf16 v[56:59], v[152:155], v[28:31], v[56:59]\n\t"
        "ds_read_b128 v[16:19], %[xo0] offset:28672\n\t"
        "v_mfma_f32_16x16x32_bf16 v[76:79], v[156:159], v[28:31], v[76:79]\n\t"
        "ds_read_b128 v[20:23], %[xo0] offset:28928\n\t"
        "v_mfma_f32_16x16x32_bf16 v[60:63], v[152:155], v[32:35], v[60:63]\n\t"
        "ds_read_b128 v[48:51], %[xo0] offset:29184\n\t"
        "v_mfma_f32_16x16x32_bf16 v[80:83], v[156:159], v[32:35], v[80:83]\n\t"
        "ds_read_b128 v[68:71], %[xo0] offset:29440\n\t"
        "v_mfma_f32_16x16x32_bf16 v[64:67], v[152:155], v[36:39], v[64:67]\n\t"
        "ds_read_b128 v[24:27], %[xo0] offset:32768\n\t"
        "v_mfma_f32_16x16x32_bf16 v[84:87], v[156:159], v[36:39], v[84:87]\n\t"
        "ds_read_b128 v[28:31], %[xo0] offset:33024\n\t"
        "s_waitcnt lgkmcnt(9)\n\t"
        "v_mfma_f32_16x16x32_bf16 v[52:55], v[160:163], v[40:43], v[52:55]\n\t"
        "ds_read_b128 v[32:35], %[xo0] offset:33280\n\t"
        "v_mfma_f32_16x16x32_bf16 v[72:75], v[164:167], v[40:43], v[72:75]\n\t"
        "ds_read_b128 v[36:39], %[xo0] offset:33536\n\t"
        "s_mov_b64 exec, 1\n\t"
        "ds_add_u32 %[fb], %[one] offset:%[omc]+4\n\t"
        "s_mov_b64 exec, -1\n\t"
        "s_waitcnt lgkmcnt(11)\n\t"
        "v_and_b32 %[xo0], 0xf0, v107\n\t"
        "v_lshrrev_b32 %[xo0], 1, %[xo0]\n\t"
        "v_mfma_f32_16x16x32_bf16 v[56:59], v[160:163], v[44:47], v[56:59]\n\t"
        "ds_read_b128 v[40:43], %[xa] offset:0\n\t"
        "v_add_u32 %[xo0], 153344, %[xo0]\n\t"
        "v_lshrrev_b32 %[xh], 8, v107\n\t"
        "v_mfma_f32_16x16x32_bf16 v[76:79], v[164:167], v[44:47], v[76:79]\n\t"
        "s_waitcnt lgkmcnt(11)\n\t"
        "v_lshl_add_u32 %[xh], %[xh], 5, %[scs]\n\t"
        "v_mfma_f32_16x16x32_bf16 v[60:63], v[160:163], v[8:11], v[60:63]\n\t"
        "ds_read_b128 v[44:47], %[xa] offset:256\n\t"
        "v_mfma_f32_16x16x32_bf16 v[80:83], v[164:167], v[8:11], v[80:83]\n\t"
        "s_waitcnt lgkmcnt(11)\n\t"
        "v_mfma_f32_16x16x32_bf16 v[64:67], v[160:163], v[12:15], v[64:67]\n\t"
        "ds_read_b128 v[8:11], %[xa] offset:512\n\t"
        "v_mfma_f32_16x16x32_bf16 v[84:87], v[164:167], v[12:15], v[84:87]\n\t"
        "s_waitcnt lgkmcnt(11)\n\t"
        "v_mfma_f32_16x16x32_bf16 v[52:55], v[168:171], v[16:19], v[52:55]\n\t"
        "ds_read_b128 v[12:15], %[xa] offset:768\n\t"
        "v_mfma_f32_16x16x32_bf16 v[72:75], v[172:175], v[16:19], v[72:75]\n\t"
        "s_waitcnt lgkmcnt(11)\n\t"
        "v_mfma_f32_16x16x32_bf16 v[56:59], v[168:171], v[20:23], v[56:59]\n\t"
        "ds_read_b32 %[fl], %[fb] offset:%[og0]+8\n\t"
        "v_mfma_f32_16x16x32_bf16 v[76:79], v[172:175], v[20:23], v[76:79]\n\t"
        "s_waitcnt lgkmcnt(11)\n\t"
        "v_mfma_f32_16x16x32_bf16 v[60:63], v[168:171], v[48:51], v[60:63]\n\t"
        "v_mfma_f32_16x16x32_bf16 v[80:83], v[172:175], v[48:51], v[80:83]\n\t"
        "s_waitcnt lgkmcnt(10)\n\t"
        "v_mfma_f32_16x16x32_bf16 v[64:67], v[168:171], v[68:71], v[64:67]\n\t"
        "v_mfma_f32_16x16x32_bf16 v[84:87], v[172:175], v[68:71], v[84:87]\n\t"
        "s_waitcnt lgkmcnt(9)\n\t"
        "v_mfma_f32_16x16x32_bf16 v[52:55], v[176:179], v[24:27], v[52:55]\n\t"
        "v_mfma_f32_16x16x32_bf16 v[72:75], v[180:183], v[24:27], v[72:75]\n\t"
        "s_waitcnt lgkmcnt(8)\n\t"
        "v_mfma_f32_16x16x32_bf16 v[56:59], v[176:179], v[28:31], v[56:59]\n\t"
        "v_mfma_f32_16x16x32_bf16 v[76:79], v[180:183], v[28:31], v[76:79]\n\t"
        "s_waitcnt lgkmcnt(7)\n\t"
        "v_mfma_f32_16x16x32_bf16 v[60:63], v[176:179], v[32:35], v[60:63]\n\t"
        "s_waitcnt lgkmcnt(0)\n\t"
        "v_cmp_gt_u32 vcc, 2, %[fl]\n\t"
        "v_mfma_f32_16x16x32_bf16 v[80:83], v[180:183], v[32:35], v[80:83]\n\t"
        "v_mfma_f32_16x16x32_bf16 v[64:67], v[176:179], v[36:39], v[64:67]\n\t"
        "s_cbranch_vccnz Ls32_%=\n\t"
        "Lg32_%=:\n\t"
        "ds_read_b128 v[16:19], %[xa] offset:4096\n\t"
        "v_mfma_f32_16x16x32_bf16 v[84:87], v[180:183], v[36:39], v[84:87]\n\t"
        "ds_read_b128 v[20:23], %[xa] offset:4352\n\t"
        "v_mfma_f32_16x16x32_bf16 v[52:55], v[184:187], v[40:43], v[52:55]\n\t"
        "ds_read_b128 v[48:51], %[xa] offset:4608\n\t"
        "v_mfma_f32_16x16x32_bf16 v[72:75], v[188:191], v[40:43], v[72:75]\n\t"
        "ds_read_b128 v[68:71], %[xa] offset:4864\n\t"
        "v_mfma_f32_16x16x32_bf16 v[56:59], v[184:187], v[44:47], v[56:59]\n\t"
        "ds_read_b128 v[24:27], %[xa] offset:8192\n\t"
        "v_mfma_f32_16x16x32_bf16 v[76:79], v[188:191], v[44:47], v[76:79]\n\t"
        "ds_read_b128 v[28:31], %[xa] offset:8448\n\t"
        "v_mfma_f32_16x16x32_bf16 v[60:63], v[184:187], v[8:11], v[60:63]\n\t"
        "ds_read_b128 v[32:35], %[xa] offset:8704\n\t"
        "v_mfma_f32_16x16x32_bf16 v[80:83], v[188:191], v[8:11], v[80:83]\n\t"
        "ds_read_b128 v[36:39], %[xa] offset:8960\n\t"
        "v_mfma_f32_16x16x32_bf16 v[64:67], v[184:187], v[12:15], v[64:67]\n\t"
        "ds_read_b32 %[fl], %[fb] offset:%[og0]+12\n\t"
        "ds_read_b128 v[40:43], %[xa] offset:12288\n\t"
        "v_mfma_f32_16x16x32_bf16 v[84:87], v[188:191], v[12:15], v[84:87]\n\t"
        "ds_read_b128 v[44:47], %[xa] offset:16384\n\t"
        "s_waitcnt lgkmcnt(10)\n\t"
        "v_mfma_f32_16x16x32_bf16 v[52:55], v[192:195], v[16:19], v[52:55]\n\t"
        "v_mfma_f32_16x16x32_bf16 v[72:75], v[196:199], v[16:19], v[72:75]\n\t"
        "s_waitcnt lgkmcnt(9)\n\t"
        "v_mfma_f32_16x16x32_bf16 v[56:59], v[192:195], v[20:23], v[56:59]\n\t"
        "v_mfma_f32_16x16x32_bf16 v[76:79], v[196:199], v[20:23], v[76:79]\n\t"
        "s_waitcnt lgkmcnt(8)\n\t"
        "v_mfma_f32_16x16x32_bf16 v[60:63], v[192:195], v[48:51], v[60:63]\n\t"
        "ds_read_b128 v[8:11], %[xh] offset:0\n\t"
        "v_mfma_f32_16x16x32_bf16 v[80:83], v[196:199], v[48:51], v[80:83]\n\t"
        "ds_read_b128 v[12:15], %[xh] offset:16\n\t"
        "s_waitcnt lgkmcnt(9)\n\t"
        "v_mfma_f32_16x16x32_bf16 v[64:67], v[192:195], v[68:71], v[64:67]\n\t"
        "ds_read_b64 v[98:99], %[fb] offset:%[omc]\n\t"
        "v_mfma_f32_16x16x32_bf16 v[84:87], v[196:199], v[68:71], v[84:87]\n\t"
        "ds_read_b64 v[16:17], %[xo0] offset:128\n\t"
        "s_waitcnt lgkmcnt(10)\n\t"
        "v_mfma_f32_16x16x32_bf16 v[52:55], v[200:203], v[24:27], v[52:55]\n\t"
        "s_waitcnt lgkmcnt(6)\n\t"
        "v_cmp_gt_u32 vcc, %[sthr], %[fl]\n\t"
        "ds_read_b64 v[18:19], %[xo0] offset:256\n\t"
        "v_mfma_f32_16x16x32_bf16 v[72:75], v[204:207], v[24:27], v[72:75]\n\t"
        "ds_read_b64 v[20:21], %[xo0] offset:384\n\t"
        "v_mfma_f32_16x16x32_bf16 v[56:59], v[200:203], v[28:31], v[56:59]\n\t"
        "s_cbranch_vccnz Ls42_%=\n\t"
        "Lg42_%=:\n\t"
        "ds_read_b128 v[48:51], %[xa] offset:20480\n\t"
        "v_mfma_f32_16x16x32_bf16 v[76:79], v[204:207], v[28:31], v[76:79]\n\t"
        "ds_read_b64 v[22:23], %[xo0] offset:512\n\t"
        "v_mfma_f32_16x16x32_bf16 v[60:63], v[200:203], v[32:35], v[60:63]\n\t"
        "ds_read_b128 v[68:71], %[xa] offset:24576\n\t"
        "v_mfma_f32_16x16x32_bf16 v[80:83], v[204:207], v[32:35], v[80:83]\n\t"
        "ds_read_b128 v[24:27], %[xa] offset:28672\n\t"
        "v_mfma_f32_16x16x32_bf16 v[64:67], v[200:203], v[36:39], v[64:67]\n\t"
        "ds_read_b128 v[28:31], %[xa] offset:32768\n\t"
        "v_mfma_f32_16x16x32_bf16 v[84:87], v[204:207], v[36:39], v[84:87]\n\t"
        "ds_read_b128 v[32:35], %[xa] offset:12544\n\t"
        "s_waitcnt lgkmcnt(13)\n\t"
        "v_mfma_f32_16x16x32_bf16 v[52:55], v[208:211], v[40:43], v[52:55]\n\t"
        "ds_read_b128 v[36:39], %[xa] offset:16640\n\t"
        "v_mfma_f32_16x16x32_bf16 v[72:75], v[212:215], v[40:43], v[72:75]\n\t"
        "s_waitcnt lgkmcnt(13)\n\t"
        "v_mfma_f32_16x16x32_bf16 v[52:55], v[216:219], v[44:47], v[52:55]\n\t"
        "ds_read_b128 v[40:43], %[xa] offset:20736\n\t"
        "v_mfma_f32_16x16x32_bf16 v[72:75], v[220:223], v[44:47], v[72:75]\n\t"
        "s_waitcnt lgkmcnt(7)\n\t"
        "v_mfma_f32_16x16x32_bf16 v[52:55], v[224:227], v[48:51], v[52:55]\n\t"
        "ds_read_b128 v[44:47], %[xa] offset:24832\n\t"
        "v_add_u32 %[xh], 73728, v107\n\t"
        "v_mfma_f32_16x16x32_bf16 v[72:75], v[228:231], v[48:51], v[72:75]\n\t"
        "s_waitcnt lgkmcnt(6)\n\t"
        "v_mfma_f32_16x16x32_bf16 v[52:55], v[232:235], v[68:71], v[52:55]\n\t"
        "ds_read_b128 v[48:51], %[xa] offset:28928\n\t"
        "v_mfma_f32_16x16x32_bf16 v[72:75], v[236:239], v[68:71], v[72:75]\n\t"
        "s_waitcnt lgkmcnt(6)\n\t"
        "v_mfma_f32_16x16x32_bf16 v[52:55], v[240:243], v[24:27], v[52:55]\n\t"
        "ds_read_b128 v[68:71], %[xa] offset:33024\n\t"
        "v_mfma_f32_16x16x32_bf16 v[72:75], v[244:247], v[24:27], v[72:75]\n\t"
        "s_waitcnt lgkmcnt(6)\n\t"
        "v_mfma_f32_16x16x32_bf16 v[52:55], v[248:251], v[28:31], v[52:55]\n\t"
        "v_cmp_gt_u32 vcc, 5, v98\n\t"
        "ds_read_b128 v[24:27], %[xa] offset:12800\n\t"
        "v_mfma_f32_16x16x32_bf16 v[72:75], v[252:255], v[28:31], v[72:75]\n\t"
        "s_waitcnt lgkmcnt(6)\n\t"
        "v_mfma_f32_16x16x32_bf16 v[56:59], v[208:211], v[32:35], v[56:59]\n\t"
        "s_cbranch_vccnz Ln_%=\n\t"
        "Lo_%=:\n\t"
        "ds_read_b128 v[28:31], %[xa] offset:16896\n\t"
        "v_mfma_f32_16x16x32_bf16 v[76:79], v[212:215], v[32:35], v[76:79]\n\t"
        "s_waitcnt lgkmcnt(6)\n\t"
        "v_mfma_f32_16x16x32_bf16 v[56:59], v[216:219], v[36:39], v[56:59]\n\t"
        "ds_read_b128 v[32:35], %[xa] offset:20992\n\t"
        "v_fma_f32 v52, -v16, v8, v52\n\t"
        "v_fma_f32 v53, -v16, v9, v53\n\t"
        "v_mfma_f32_16x16x32_bf16 v[76:79], v[220:223], v[36:39], v[76:79]\n\t"
        "s_waitcnt lgkmcnt(6)\n\t"
        "v_fma_f32 v54, -v16, v10, v54\n\t"
        "v_fma_f32 v55, -v16, v11, v55\n\t"
        "v_mfma_f32_16x16x32_bf16 v[56:59], v[224:227], v[40:43], v[56:59]\n\t"
        "ds_read_b128 v[36:39], %[xa] offset:25088\n\t"
        "v_fma_f32 v72, -v16, v12, v72\n\t"
        "v_fma_f32 v73, -v16, v13, v73\n\t"
        "v_mfma_f32_16x16x32_bf16 v[76:79], v[228:231], v[40:43], v[76:79]\n\t"
        "s_waitcnt lgkmcnt(6)\n\t"
        "v_fma_f32 v74, -v16, v14, v74\n\t"
        "v_fma_f32 v75, -v16, v15, v75\n\t"
        "v_mfma_f32_16x16x32_bf16 v[56:59], v[232:235], v[44:47], v[56:59]\n\t"
        "ds_read_b128 v[40:43], %[xa] offset:29184\n\t"
        "v_mul_f32 v52, v17, v52\n\t"
        "v_mul_f32 v53, v17, v53\n\t"
        "v_mfma_f32_16x16x32_bf16 v[76:79], v[236:239], v[44:47], v[76:79]\n\t"
        "s_waitcnt lgkmcnt(6)\n\t"
        "v_mul_f32 v54, v17, v54\n\t"
        "v_mul_f32 v55, v17, v55\n\t"
        "v_mfma_f32_16x16x32_bf16 v[56:59], v[240:243], v[48:51], v[56:59]\n\t"
        "ds_read_b128 v[44:47], %[xa] offset:33280\n\t"
        "v_mul_f32 v72, v17, v72\n\t"
        "v_mul_f32 v73, v17, v73\n\t"
        "v_mfma_f32_16x16x32_bf16 v[76:79], v[244:247], v[48:51], v[76:79]\n\t"
        "s_waitcnt lgkmcnt(6)\n\t"
        "v_mul_f32 v74, v17, v74\n\t"
        "v_mul_f32 v75, v17, v75\n\t"
        "v_mfma_f32_16x16x32_bf16 v[56:59], v[248:251], v[68:71], v[56:59]\n\t"
        "ds_read_b128 v[48:51], %[xa] offset:13056\n\t"
        "v_cvt_pk_bf16_f32 v52, v52, v72\n\t"
        "v_cvt_pk_bf16_f32 v53, v53, v73\n\t"
        "v_mfma_f32_16x16x32_bf16 v[76:79], v[252:255], v[68:71], v[76:79]\n\t"
        "s_waitcnt lgkmcnt(6)\n\t"
        "v_cvt_pk_bf16_f32 v54, v54, v74\n\t"
        "v_cvt_pk_bf16_f32 v55, v55, v75\n\t"
        "v_mfma_f32_16x16x32_bf16 v[60:63], v[208:211], v[24:27], v[60:63]\n\t"
        "v_add_u32 v16, v110, v111\n\t"
        "v_mfma_f32_16x16x32_bf16 v[80:83], v[212:215], v[24:27], v[80:83]\n\t"
        "ds_read_b128 v[72:75], %[xa] offset:17152\n\t"
        "s_waitcnt lgkmcnt(6)\n\t"
        "s_cmp_eq_u64 %[sv0], 0\n\t"
        "s_cbranch_scc1 Lq_%=\n\t"
        "v_mfma_f32_16x16x32_bf16 v[60:63], v[216:219], v[28:31], v[60:63]\n\t"
        "ds_read_b32 %[fl], %[fb] offset:%[og0]+16\n\t"
        "ds_read_b128 v[24:27], %[xa] offset:21248\n\t"
        "v_fma_f32 v56, -v18, v8, v56\n\t"
        "v_fma_f32 v57, -v18, v9, v57\n\t"
        "v_mfma_f32_16x16x32_bf16 v[80:83], v[220:223], v[28:31], v[80:83]\n\t"
        "s_waitcnt lgkmcnt(7)\n\t"
        "v_fma_f32 v58, -v18, v10, v58\n\t"
        "v_fma_f32 v59, -v18, v11, v59\n\t"
        "v_mfma_f32_16x16x32_bf16 v[60:63], v[224:227], v[32:35], v[60:63]\n\t"
        "ds_read_b128 v[28:31], %[xa] offset:25344\n\t"
        "v_fma_f32 v76, -v18, v12, v76\n\t"
        "v_fma_f32 v77, -v18, v13, v77\n\t"
        "v_mfma_f32_16x16x32_bf16 v[80:83], v[228:231], v[32:35], v[80:83]\n\t"
        "s_waitcnt lgkmcnt(7)\n\t"
        "v_fma_f32 v78, -v18, v14, v78\n\t"
        "v_fma_f32 v79, -v18, v15, v79\n\t"
        "v_mfma_f32_16x16x32_bf16 v[60:63], v[232:235], v[36:39], v[60:63]\n\t"
        "ds_read_b128 v[32:35], %[xa] offset:29440\n\t"
        "v_mul_f32 v56, v19, v56\n\t"
        "v_mul_f32 v57, v19, v57\n\t"
        "v_mfma_f32_16x16x32_bf16 v[80:83], v[236:239], v[36:39], v[80:83]\n\t"
        "s_waitcnt lgkmcnt(7)\n\t"
        "v_mul_f32 v58, v19, v58\n\t"
        "v_mul_f32 v59, v19, v59\n\t"
        "v_mfma_f32_16x16x32_bf16 v[60:63], v[240:243], v[40:43], v[60:63]\n\t"
        "ds_read_b128 v[36:39], %[xa] offset:33536\n\t"
        "s_mov_b64 exec, 1\n\t"
        "ds_add_u32 %[fb], %[one] offset:%[oc1]\n\t"
        "s_mov_b64 exec, -1\n\t"
        "v_mul_f32 v76, v19, v76\n\t"
        "v_mul_f32 v77, v19, v77\n\t"
        "v_mfma_f32_16x16x32_bf16 v[80:83], v[244:247], v[40:43], v[80:83]\n\t"
        "s_waitcnt lgkmcnt(8)\n\t"
        "v_mul_f32 v78, v19, v78\n\t"
        "v_mul_f32 v79, v19, v79\n\t"
        "v_mfma_f32_16x16x32_bf16 v[60:63], v[248:251], v[44:47], v[60:63]\n\t"
        "v_cvt_pk_bf16_f32 v56, v56, v76\n\t"
        "v_cvt_pk_bf16_f32 v57, v57, v77\n\t"
        "v_mfma_f32_16x16x32_bf16 v[80:83], v[252:255], v[44:47], v[80:83]\n\t"
        "s_waitcnt lgkmcnt(7)\n\t"
        "v_cvt_pk_bf16_f32 v58, v58, v78\n\t"
        "v_cvt_pk_bf16_f32 v59, v59, v79\n\t"
        "v_mfma_f32_16x16x32_bf16 v[64:67], v[208:211], v[48:51], v[64:67]\n\t"
        "s_waitcnt lgkmcnt(5)\n\t"
        "v_cmp_gt_u32 vcc, 2, %[fl]\n\t"
        "v_add_u32 v18, v16, v111\n\t"
        "v_mfma_f32_16x16x32_bf16 v[84:87], v[212:215], v[48:51], v[84:87]\n\t"
        "v_mfma_f32_16x16x32_bf16 v[64:67], v[216:219], v[72:75], v[64:67]\n\t"
        "s_cbranch_vccnz Ls64_%=\n\t"
        "Lg64_%=:\n\t"
        "ds_read_b128 v[40:43], %[xh] offset:0\n\t"
        "v_fma_f32 v60, -v20, v8, v60\n\t"
        "v_fma_f32 v61, -v20, v9, v61\n\t"
        "v_mfma_f32_16x16x32_bf16 v[84:87], v[220:223], v[72:75], v[84:87]\n\t"
        "ds_read_b128 v[44:47], %[xh] offset:1024\n\t"
        "s_waitcnt lgkmcnt(6)\n\t"
        "v_fma_f32 v62, -v20, v10, v62\n\t"
        "v_fma_f32 v63, -v20, v11, v63\n\t"
        "v_mfma_f32_16x16x32_bf16 v[64:67], v[224:227], v[24:27], v[64:67]\n\t"
        "ds_read_b128 v[76:79], %[xh] offset:2048\n\t"
        "v_fma_f32 v80, -v20, v12, v80\n\t"
        "v_fma_f32 v81, -v20, v13, v81\n\t"
        "v_mfma_f32_16x16x32_bf16 v[84:87], v[228:231], v[24:27], v[84:87]\n\t"
        "ds_read_b128 v[72:75], %[xh] offset:3072\n\t"
        "s_waitcnt lgkmcnt(7)\n\t"
        "v_fma_f32 v82, -v20, v14, v82\n\t"
        "v_fma_f32 v83, -v20, v15, v83\n\t"
        "v_mfma_f32_16x16x32_bf16 v[64:67], v[232:235], v[28:31], v[64:67]\n\t"
        "ds_read_b128 v[24:27], %[xh] offset:4096\n\t"
        "v_mul_f32 v60, v21, v60\n\t"
        "v_mul_f32 v61, v21, v61\n\t"
        "v_mfma_f32_16x16x32_bf16 v[84:87], v[236:239], v[28:31], v[84:87]\n\t"
        "s_waitcnt lgkmcnt(7)\n\t"
        "v_mul_f32 v62, v21, v62\n\t"
        "v_mul_f32 v63, v21, v63\n\t"
        "v_mfma_f32_16x16x32_bf16 v[64:67], v[240:243], v[32:35], v[64:67]\n\t"
        "ds_read_b128 v[28:31], %[xh] offset:5120\n\t"
        "v_mul_f32 v80, v21, v80\n\t"
        "v_mul_f32 v81, v21, v81\n\t"
        "v_mfma_f32_16x16x32_bf16 v[84:87], v[244:247], v[32:35], v[84:87]\n\t"
        "s_waitcnt lgkmcnt(7)\n\t"
        "v_mul_f32 v82, v21, v82\n\t"
        "v_mul_f32 v83, v21, v83\n\t"
        "v_mfma_f32_16x16x32_bf16 v[64:67], v[248:251], v[36:39], v[64:67]\n\t"
        "ds_read_b128 v[32:35], %[xh] offset:6144\n\t"
        "v_cvt_pk_bf16_f32 v60, v60, v80\n\t"
        "v_cvt_pk_bf16_f32 v61, v61, v81\n\t"
        "v_mfma_f32_16x16x32_bf16 v[84:87], v[252:255], v[36:39], v[84:87]\n\t"
        "s_waitcnt lgkmcnt(6)\n\t"
        "v_cvt_pk_bf16_f32 v62, v62, v82\n\t"
        "v_cvt_pk_bf16_f32 v63, v63, v83\n\t"
        "v_mfma_f32_16x16x32_bf16 v[48:51], v[112:115], v[40:43], 0\n\t"
        "ds_read_b128 v[36:39], %[xh] offset:7168\n\t"
        "v_add_u32 v20, v18, v111\n\t"
        "v_mfma_f32_16x16x32_bf16 v[68:71], v[116:119], v[40:43], 0\n\t"
        "ds_read_b128 v[80:83], %[xh] offset:8192\n\t"
        "s_waitcnt lgkmcnt(7)\n\t"
        "v_mfma_f32_16x16x32_bf16 v[48:51], v[120:123], v[44:47], v[48:51]\n\t"
        "ds_read_b128 v[40:43], %[xh] offset:9216\n\t"
        "v_fma_f32 v64, -v22, v8, v64\n\t"
        "v_fma_f32 v65, -v22, v9, v65\n\t"
        "v_mfma_f32_16x16x32_bf16 v[68:71], v[124:127], v[44:47], v[68:71]\n\t"
        "s_waitcnt lgkmcnt(7)\n\t"
        "v_fma_f32 v66, -v22, v10, v66\n\t"
        "v_fma_f32 v67, -v22, v11, v67\n\t"
        "v_mfma_f32_16x16x32_bf16 v[48:51], v[128:131], v[76:79], v[48:51]\n\t"
        "ds_read_b128 v[44:47], %[xh] offset:10240\n\t"
        "v_fma_f32 v84, -v22, v12, v84\n\t"
        "v_fma_f32 v85, -v22, v13, v85\n\t"
        "v_mfma_f32_16x16x32_bf16 v[68:71], v[132:135], v[76:79], v[68:71]\n\t"
        "s_waitcnt lgkmcnt(7)\n\t"
        "v_fma_f32 v86, -v22, v14, v86\n\t"
        "v_fma_f32 v87, -v22, v15, v87\n\t"
        "v_mfma_f32_16x16x32_bf16 v[48:51], v[136:139], v[72:75], v[48:51]\n\t"
        "ds_read_b128 v[76:79], %[xh] offset:11264\n\t"
        "v_mul_f32 v64, v23, v64\n\t"
        "v_mul_f32 v65, v23, v65\n\t"
        "v_mfma_f32_16x16x32_bf16 v[68:71], v[140:143], v[72:75], v[68:71]\n\t"
        "s_waitcnt lgkmcnt(7)\n\t"
        "v_mul_f32 v66, v23, v66\n\t"
        "v_mul_f32 v67, v23, v67\n\t"
        "v_mfma_f32_16x16x32_bf16 v[48:51], v[144:147], v[24:27], v[48:51]\n\t"
        "ds_read_b128 v[72:75], %[xh] offset:12288\n\t"
        "v_mul_f32 v84, v23, v84\n\t"
        "v_mul_f32 v85, v23, v85\n\t"
        "v_mfma_f32_16x16x32_bf16 v[68:71], v[148:151], v[24:27], v[68:71]\n\t"
        "s_waitcnt lgkmcnt(7)\n\t"
        "v_mul_f32 v86, v23, v86\n\t"
        "v_mul_f32 v87, v23, v87\n\t"
        "v_mfma_f32_16x16x32_bf16 v[48:51], v[152:155], v[28:31], v[48:51]\n\t"
        "ds_read_b128 v[24:27], %[xh] offset:13312\n\t"
        "v_cvt_pk_bf16_f32 v64, v64, v84\n\t"
        "v_cvt_pk_bf16_f32 v65, v65, v85\n\t"
        "v_mfma_f32_16x16x32_bf16 v[68:71], v[156:159], v[28:31], v[68:71]\n\t"
        "s_waitcnt lgkmcnt(7)\n\t"
        "v_cvt_pk_bf16_f32 v66, v66, v86\n\t"
        "v_cvt_pk_bf16_f32 v67, v67, v87\n\t"
        "v_mfma_f32_16x16x32_bf16 v[48:51], v[160:163], v[32:35], v[48:51]\n\t"
        "ds_read_b128 v[28:31], %[xh] offset:14336\n\t"
        "v_add_u32 v22, v20, v111\n\t"
        "v_mfma_f32_16x16x32_bf16 v[68:71], v[164:167], v[32:35], v[68:71]\n\t"
        "s_waitcnt lgkmcnt(7)\n\t"
        "v_mfma_f32_16x16x32_bf16 v[48:51], v[168:171], v[36:39], v[48:51]\n\t"
        "ds_read_b128 v[32:35], %[xh] offset:15360\n\t"
        "v_mfma_f32_16x16x32_bf16 v[68:71], v[172:175], v[36:39], v[68:71]\n\t"
        "s_waitcnt lgkmcnt(7)\n\t"
        "v_mfma_f32_16x16x32_bf16 v[48:51], v[176:179], v[80:83], v[48:51]\n\t"
        "ds_read_b128 v[36:39], %[xh] offset:16384\n\t"
        "v_mfma_f32_16x16x32_bf16 v[68:71], v[180:183], v[80:83], v[68:71]\n\t"
        "s_waitcnt lgkmcnt(7)\n\t"
        "v_mfma_f32_16x16x32_bf16 v[48:51], v[184:187], v[40:43], v[48:51]\n\t"
        "ds_read_b128 v[80:83], %[xh] offset:17408\n\t"
        "v_mfma_f32_16x16x32_bf16 v[68:71], v[188:191], v[40:43], v[68:71]\n\t"
        "ds_read_b64 v[84:85], %[xo0]\n\t"
        "s_waitcnt lgkmcnt(8)\n\t"
        "v_mfma_f32_16x16x32_bf16 v[48:51], v[192:195], v[44:47], v[48:51]\n\t"
        "ds_read_b32 v99, %[fb] offset:%[omc]+4\n\t"
        "v_mfma_f32_16x16x32_bf16 v[68:71], v[196:199], v[44:47], v[68:71]\n\t"
        "ds_read_b32 v98, %[fb]\n\t"
        "s_waitcnt lgkmcnt(9)\n\t"
        "v_mfma_f32_16x16x32_bf16 v[48:51], v[200:203], v[76:79], v[48:51]\n\t"
        "v_mfma_f32_16x16x32_bf16 v[68:71], v[204:207], v[76:79], v[68:71]\n\t"
        "s_waitcnt lgkmcnt(8)\n\t"
        "v_mfma_f32_16x16x32_bf16 v[48:51], v[208:211], v[72:75], v[48:51]\n\t"
        "v_mfma_f32_16x16x32_bf16 v[68:71], v[212:215], v[72:75], v[68:71]\n\t"
        "s_waitcnt lgkmcnt(7)\n\t"
        "v_mfma_f32_16x16x32_bf16 v[48:51], v[216:219], v[24:27], v[48:51]\n\t"
        "v_mfma_f32_16x16x32_bf16 v[68:71], v[220:223], v[24:27], v[68:71]\n\t"
        "s_waitcnt lgkmcnt(6)\n\t"
        "v_mfma_f32_16x16x32_bf16 v[48:51], v[224:227], v[28:31], v[48:51]\n\t"
        "v_mfma_f32_16x16x32_bf16 v[68:71], v[228:231], v[28:31], v[68:71]\n\t"
        "s_waitcnt lgkmcnt(5)\n\t"
        "v_mfma_f32_16x16x32_bf16 v[48:51], v[232:235], v[32:35], v[48:51]\n\t"
        "v_mfma_f32_16x16x32_bf16 v[68:71], v[236:239], v[32:35], v[68:71]\n\t"
        "s_waitcnt lgkmcnt(4)\n\t"
        "v_mfma_f32_16x16x32_bf16 v[48:51], v[240:243], v[36:39], v[48:51]\n\t"
        "v_mfma_f32_16x16x32_bf16 v[68:71], v[244:247], v[36:39], v[68:71]\n\t"
        "s_waitcnt lgkmcnt(3)\n\t"
        "v_mfma_f32_16x16x32_bf16 v[48:51], v[248:251], v[80:83], v[48:51]\n\t"
        "v_mfma_f32_16x16x32_bf16 v[68:71], v[252:255], v[80:83], v[68:71]\n\t"
        "s_waitcnt lgkmcnt(0)\n\t"
        "s_nop 5\n\t"
        "v_fma_f32 v48, -v84, v8, v48\n\t"
        "v_fma_f32 v49, -v84, v9, v49\n\t"
        "v_fma_f32 v50, -v84, v10, v50\n\t"
        "v_fma_f32 v51, -v84, v11, v51\n\t"
        "v_fma_f32 v68, -v84, v12, v68\n\t"
        "v_fma_f32 v69, -v84, v13, v69\n\t"
        "v_fma_f32 v70, -v84, v14, v70\n\t"
        "v_fma_f32 v71, -v84, v15, v71\n\t"
        "v_mul_f32 v48, v85, v48\n\t"
        "v_mul_f32 v49, v85, v49\n\t"
        "v_mul_f32 v50, v85, v50\n\t"
        "v_mul_f32 v51, v85, v51\n\t"
        "v_mul_f32 v68, v85, v68\n\t"
        "v_mul_f32 v69, v85, v69\n\t"
        "v_mul_f32 v70, v85, v70\n\t"
        "v_mul_f32 v71, v85, v71\n\t"
        "v_cvt_pk_bf16_f32 v48, v48, v68\n\t"
        "v_cvt_pk_bf16_f32 v49, v49, v69\n\t"
        "v_cvt_pk_bf16_f32 v50, v50, v70\n\t"
        "v_cvt_pk_bf16_f32 v51, v51, v71\n\t"
        "s_branch Lu_%=\n\t"
        "Lq_%=:\n\t"
        "v_mfma_f32_16x16x32_bf16 v[60:63], v[216:219], v[28:31], v[60:63]\n\t"
        "ds_read_b128 v[24:27], %[xa] offset:21248\n\t"
        "v_fma_f32 v56, -v18, v8, v56\n\t"
        "v_fma_f32 v57, -v18, v9, v57\n\t"
        "v_mfma_f32_16x16x32_bf16 v[80:83], v[220:223], v[28:31], v[80:83]\n\t"
        "s_waitcnt lgkmcnt(6)\n\t"
        "v_fma_f32 v58, -v18, v10, v58\n\t"
        "v_fma_f32 v59, -v18, v11, v59\n\t"
        "v_mfma_f32_16x16x32_bf16 v[60:63], v[224:227], v[32:35], v[60:63]\n\t"
        "ds_read_b128 v[28:31], %[xa] offset:25344\n\t"
        "v_fma_f32 v76, -v18, v12, v76\n\t"
        "v_fma_f32 v77, -v18, v13, v77\n\t"
        "v_mfma_f32_16x16x32_bf16 v[80:83], v[228:231], v[32:35], v[80:83]\n\t"
        "s_waitcnt lgkmcnt(6)\n\t"
        "v_fma_f32 v78, -v18, v14, v78\n\t"
        "v_fma_f32 v79, -v18, v15, v79\n\t"
        "v_mfma_f32_16x16x32_bf16 v[60:63], v[232:235], v[36:39], v[60:63]\n\t"
        "ds_read_b128 v[32:35], %[xa] offset:29440\n\t"
        "v_mul_f32 v56, v19, v56\n\t"
        "v_mul_f32 v57, v19, v57\n\t"
        "v_mfma_f32_16x16x32_bf16 v[80:83], v[236:239], v[36:39], v[80:83]\n\t"
        "s_waitcnt lgkmcnt(6)\n\t"
        "v_mul_f32 v58, v19, v58\n\t"
        "v_mul_f32 v59, v19, v59\n\t"
        "v_mfma_f32_16x16x32_bf16 v[60:63], v[240:243], v[40:43], v[60:63]\n\t"
        "ds_read_b128 v[36:39], %[xa] offset:33536\n\t"
        "s_mov_b64 exec, 1\n\t"
        "ds_add_u32 %[fb], %[one] offset:%[oc1]\n\t"
        "s_mov_b64 exec, -1\n\t"
        "v_mul_f32 v76, v19, v76\n\t"
        "v_mul_f32 v77, v19, v77\n\t"
        "v_mfma_f32_16x16x32_bf16 v[80:83], v[244:247], v[40:43], v[80:83]\n\t"
        "s_waitcnt lgkmcnt(7)\n\t"
        "v_mul_f32 v78, v19, v78\n\t"
        "v_mul_f32 v79, v19, v79\n\t"
        "v_mfma_f32_16x16x32_bf16 v[60:63], v[248:251], v[44:47], v[60:63]\n\t"
        "v_cvt_pk_bf16_f32 v56, v56, v76\n\t"
        "v_cvt_pk_bf16_f32 v57, v57, v77\n\t"
        "v_mfma_f32_16x16x32_bf16 v[80:83], v[252:255], v[44:47], v[80:83]\n\t"
        "s_waitcnt lgkmcnt(6)\n\t"
        "v_cvt_pk_bf16_f32 v58, v58, v78\n\t"
        "v_cvt_pk_bf16_f32 v59, v59, v79\n\t"
        "v_mfma_f32_16x16x32_bf16 v[64:67], v[208:211], v[48:51], v[64:67]\n\t"
        "v_add_u32 v18, v16, v111\n\t"
        "v_mfma_f32_16x16x32_bf16 v[84:87], v[212:215], v[48:51], v[84:87]\n\t"
        "s_waitcnt lgkmcnt(5)\n\t"
        "v_mfma_f32_16x16x32_bf16 v[64:67], v[216:219], v[72:75], v[64:67]\n\t"
        "v_fma_f32 v60, -v20, v8, v60\n\t"
        "v_fma_f32 v61, -v20, v9, v61\n\t"
        "v_mfma_f32_16x16x32_bf16 v[84:87], v[220:223], v[72:75], v[84:87]\n\t"
        "s_waitcnt lgkmcnt(4)\n\t"
        "v_fma_f32 v62, -v20, v10, v62\n\t"
        "v_fma_f32 v63, -v20, v11, v63\n\t"
        "v_mfma_f32_16x16x32_bf16 v[64:67], v[224:227], v[24:27], v[64:67]\n\t"
        "v_fma_f32 v80, -v20, v12, v80\n\t"
        "v_fma_f32 v81, -v20, v13, v81\n\t"
        "v_mfma_f32_16x16x32_bf16 v[84:87], v[228:231], v[24:27], v[84:87]\n\t"
        "s_waitcnt lgkmcnt(3)\n\t"
        "v_fma_f32 v82, -v20, v14, v82\n\t"
        "v_fma_f32 v83, -v20, v15, v83\n\t"
        "v_mfma_f32_16x16x32_bf16 v[64:67], v[232:235], v[28:31], v[64:67]\n\t"
        "v_mul_f32 v60, v21, v60\n\t"
        "v_mul_f32 v61, v21, v61\n\t"
        "v_mfma_f32_16x16x32_bf16 v[84:87], v[236:239], v[28:31], v[84:87]\n\t"
        "s_waitcnt lgkmcnt(2)\n\t"
        "v_mul_f32 v62, v21, v62\n\t"
        "v_mul_f32 v63, v21, v63\n\t"
        "v_mfma_f32_16x16x32_bf16 v[64:67], v[240:243], v[32:35], v[64:67]\n\t"
        "v_mul_f32 v80, v21, v80\n\t"
        "v_mul_f32 v81, v21, v81\n\t"
        "v_mfma_f32_16x16x32_bf16 v[84:87], v[244:247], v[32:35], v[84:87]\n\t"
        "s_waitcnt lgkmcnt(1)\n\t"
        "v_mul_f32 v82, v21, v82\n\t"
        "v_mul_f32 v83, v21, v83\n\t"
        "v_mfma_f32_16x16x32_bf16 v[64:67], v[248:251], v[36:39], v[64:67]\n\t"
        "v_cvt_pk_bf16_f32 v60, v60, v80\n\t"
        "v_cvt_pk_bf16_f32 v61, v61, v81\n\t"
        "v_mfma_f32_16x16x32_bf16 v[84:87], v[252:255], v[36:39], v[84:87]\n\t"
        "v_cvt_pk_bf16_f32 v62, v62, v82\n\t"
        "v_cvt_pk_bf16_f32 v63, v63, v83\n\t"
        "ds_read_b32 v99, %[fb] offset:%[omc]+4\n\t"
        "ds_read_b32 v98, %[fb]\n\t"
        "v_add_u32 v20, v18, v111\n\t"
        "v_fma_f32 v64, -v22, v8, v64\n\t"
        "v_fma_f32 v65, -v22, v9, v65\n\t"
        "v_fma_f32 v66, -v22, v10, v66\n\t"
        "v_fma_f32 v67, -v22, v11, v67\n\t"
        "v_fma_f32 v84, -v22, v12, v84\n\t"
        "v_fma_f32 v85, -v22, v13, v85\n\t"
        "v_fma_f32 v86, -v22, v14, v86\n\t"
        "v_fma_f32 v87, -v22, v15, v87\n\t"
        "v_mul_f32 v64, v23, v64\n\t"
        "v_mul_f32 v65, v23, v65\n\t"
        "v_mul_f32 v66, v23, v66\n\t"
        "v_mul_f32 v67, v23, v67\n\t"
        "v_mul_f32 v84, v23, v84\n\t"
        "v_mul_f32 v85, v23, v85\n\t"
        "v_mul_f32 v86, v23, v86\n\t"
        "v_mul_f32 v87, v23, v87\n\t"
        "v_cvt_pk_bf16_f32 v64, v64, v84\n\t"
        "v_cvt_pk_bf16_f32 v65, v65, v85\n\t"
        "v_cvt_pk_bf16_f32 v66, v66, v86\n\t"
        "v_cvt_pk_bf16_f32 v67, v67, v87\n\t"
        "v_add_u32 v22, v20, v111\n\t"
        "s_waitcnt lgkmcnt(0)\n\t"
        "Lu_%=:\n\t"
        "v_readfirstlane_b32 %[sf], v99\n\t"
        "s_cmp_ge_u32 %[sf], 14\n\t"
        "s_cbranch_scc1 Lv_%=\n\t"
        "s_cmp_lt_i32 %[scn], 1\n\t"
        "s_cbranch_scc1 Lv_%=\n\t"
        "s_sub_u32 %[scn], %[scn], 1\n\t"
        "s_sleep 4\n\t"
        "ds_read_b32 v99, %[fb] offset:%[omc]+4\n\t"
        "s_waitcnt lgkmcnt(0)\n\t"
        "s_branch Lu_%=\n\t"
        "Lv_%=:\n\t"
        "s_mov_b64 exec, %[sva]\n\t"
        "ds_write_b128 v16, v[52:55]\n\t"
        "ds_write_b128 v18, v[56:59]\n\t"
        "ds_write_b128 v20, v[60:63]\n\t"
        "ds_write_b128 v22, v[64:67]\n\t"
        "s_mov_b64 exec, %[sv0]\n\t"
        "ds_write_b128 v110, v[48:51]\n\t"
        "s_mov_b64 exec, -1\n\t"
        "v_readfirstlane_b32 %[sf], v98\n\t"
        "s_branch Le_%=\n\t"
        "Ls16_%=:\n\t"
        "s_cmp_lt_i32 %[scn], 1\n\t"
        "s_cbranch_scc1 Lg16_%=\n\t"
        "s_sub_u32 %[scn], %[scn], 1\n\t"
        "ds_read_b32 %[fl], %[fb] offset:%[og0]+4\n\t"
        "s_waitcnt lgkmcnt(0)\n\t"
        "v_cmp_gt_u32 vcc, 2, %[fl]\n\t"
        "s_cbranch_vccnz Ls16_%=\n\t"
        "s_branch Lg16_%=\n\t"
        "Ls32_%=:\n\t"
        "s_cmp_lt_i32 %[scn], 1\n\t"
        "s_cbranch_scc1 Lg32_%=\n\t"
        "s_sub_u32 %[scn], %[scn], 1\n\t"
        "ds_read_b32 %[fl], %[fb] offset:%[og0]+8\n\t"
        "s_waitcnt lgkmcnt(0)\n\t"
        "v_cmp_gt_u32 vcc, 2, %[fl]\n\t"
        "s_cbranch_vccnz Ls32_%=\n\t"
        "s_branch Lg32_%=\n\t"
        "Ls42_%=:\n\t"
        "s_cmp_lt_i32 %[scn], 1\n\t"
        "s_cbranch_scc1 Lg42_%=\n\t"
        "s_sub_u32 %[scn], %[scn], 1\n\t"
        "ds_read_b32 %[fl], %[fb] offset:%[og0]+12\n\t"
        "s_waitcnt lgkmcnt(0)\n\t"
        "v_cmp_gt_u32 vcc, %[sthr], %[fl]\n\t"
        "s_cbranch_vccnz Ls42_%=\n\t"
        "s_branch Lg42_%=\n\t"
        "Ls64_%=:\n\t"
        "s_cmp_lt_i32 %[scn], 1\n\t"
        "s_cbranch_scc1 Lg64_%=\n\t"
        "s_sub_u32 %[scn], %[scn], 1\n\t"
        "ds_read_b32 %[fl], %[fb] offset:%[og0]+16\n\t"
        "s_waitcnt lgkmcnt(0)\n\t"
        "v_cmp_gt_u32 vcc, 2, %[fl]\n\t"
        "s_cbranch_vccnz Ls64_%=\n\t"
        "s_branch Lg64_%=\n\t"
        "Ln_%=:\n\t"
        "s_cmp_lt_i32 %[scn], 1\n\t"
        "s_cbranch_scc1 Lo_%=\n\t"
        "s_sub_u32 %[scn], %[scn], 1\n\t"
        "s_sleep 1\n\t"
        "ds_read_b64 v[98:99], %[fb] offset:%[omc]\n\t"
        "ds_read_b64 v[16:17], %[xo0] offset:128\n\t"
        "ds_read_b64 v[18:19], %[xo0] offset:256\n\t"
        "ds_read_b64 v[20:21], %[xo0] offset:384\n\t"
        "ds_read_b64 v[22:23], %[xo0] offset:512\n\t"
        "s_waitcnt lgkmcnt(0)\n\t"
        "v_cmp_gt_u32 vcc, 5, v98\n\t"
        "s_cbranch_vccnz Ln_%=\n\t"
        "s_branch Lo_%=\n\t"
        "Le_%=:\n\t"
        : [xa] "=&v"(xa), [xh] "=&v"(xh), [xo0] "+v"(xo0), [one] "=&v"(one), [fl] "=&v"(fl), [sf] "=&s"(sf), [scn] "+s"(cn)
        : [fb] "v"(fb), [scs] "s"(cs), [sva] "s"(va), [sv0] "s"(v0), [sthr] "s"(thr), [omc] "n"(BC_MC), [oc1] "n"(BC_C1), [og0] "n"(BC_G0)
        : "memory", "scc", "vcc", "v8", "v9", "v10", "v11", "v12", "v13", "v14", "v15", "v16", "v17", "v18", "v19", "v20", "v21", "v22", "v23", "v24", "v25", "v26", "v27", "v28", "v29", "v30", "v31", "v32", "v33", "v34", "v35", "v36", "v37", "v38", "v39", "v40", "v41", "v42", "v43", "v44", "v45", "v46", "v47");
}

DEV void gemm1p() {
    int a0, a1;
    asm volatile(
        "v_add_u32 %[a0], 92672, v107\n\t"
        "v_add_u32 %[a1], 153344, v107\n\t"
        "ds_read_b128 v[24:27], %[a0] offset:0\n\t"
        "ds_read_b128 v[28:31], %[a0] offset:2304\n\t"
        "ds_read_b128 v[32:35], %[a1] offset:5280\n\t"
        "ds_read_b128 v[36:39], %[a1] offset:1824\n\t"
        "ds_read_b128 v[40:43], %[a0] offset:1024\n\t"
        "ds_read_b128 v[44:47], %[a0] offset:3328\n\t"
        "ds_read_b128 v[48:51], %[a1] offset:6304\n\t"
        "ds_read_b128 v[68:71], %[a1] offset:2848\n\t"
        "s_waitcnt lgkmcnt(7)\n\t"
        "v_mfma_f32_16x16x32_bf16 v[52:55], v[112:115], v[24:27], 0\n\t"
        "v_mfma_f32_16x16x32_bf16 v[72:75], v[116:119], v[24:27], 0\n\t"
        "s_waitcnt lgkmcnt(6)\n\t"
        "v_mfma_f32_16x16x32_bf16 v[56:59], v[112:115], v[28:31], 0\n\t"
        "v_mfma_f32_16x16x32_bf16 v[76:79], v[116:119], v[28:31], 0\n\t"
        "s_waitcnt lgkmcnt(5)\n\t"
        "v_mfma_f32_16x16x32_bf16 v[60:63], v[112:115], v[32:35], 0\n\t"
        "v_mfma_f32_16x16x32_bf16 v[80:83], v[116:119], v[32:35], 0\n\t"
        "s_waitcnt lgkmcnt(4)\n\t"
        "v_mfma_f32_16x16x32_bf16 v[64:67], v[112:115], v[36:39], 0\n\t"
        "v_mfma_f32_16x16x32_bf16 v[84:87], v[116:119], v[36:39], 0\n\t"
        "s_waitcnt lgkmcnt(3)\n\t"
        "v_mfma_f32_16x16x32_bf16 v[52:55], v[120:123], v[40:43], v[52:55]\n\t"
        "v_mfma_f32_16x16x32_bf16 v[72:75], v[124:127], v[40:43], v[72:75]\n\t"
        "s_waitcnt lgkmcnt(2)\n\t"
        "v_mfma_f32_16x16x32_bf16 v[56:59], v[120:123], v[44:47], v[56:59]\n\t"
        "v_mfma_f32_16x16x32_bf16 v[76:79], v[124:127], v[44:47], v[76:79]\n\t"
        "s_waitcnt lgkmcnt(1)\n\t"
        "v_mfma_f32_16x16x32_bf16 v[60:63], v[120:123], v[48:51], v[60:63]\n\t"
        "v_mfma_f32_16x16x32_bf16 v[80:83], v[124:127], v[48:51], v[80:83]\n\t"
        "s_waitcnt lgkmcnt(0)\n\t"
        "v_mfma_f32_16x16x32_bf16 v[64:67], v[120:123], v[68:71], v[64:67]\n\t"
        "v_mfma_f32_16x16x32_bf16 v[84:87], v[124:127], v[68:71], v[84:87]\n\t"
        "s_nop 7\n\t"
        : [a0] "=&v"(a0), [a1] "=&v"(a1) :: "memory", "v24", "v25", "v26", "v27", "v28", "v29", "v30", "v31", "v32", "v33", "v34", "v35", "v36", "v37", "v38", "v39", "v40", "v41", "v42", "v43", "v44", "v45", "v46", "v47");
}

DEV void gemm1b(int fb, int xr, int xq, u32 w1, u32 cs, u64 va, u64 v0, u32& cn) {
    u32 sf, st; u64 mw, mx;
    asm volatile(
        "v_add_u32 v36, 73728, v107\n\t"
        "v_mov_b32 v35, 153344\n\t"
        "v_lshrrev_b32 v37, 8, v107\n\t"
        "v_lshl_add_u32 v37, v37, 5, %[scs]\n\t"
        "s_waitcnt lgkmcnt(0)\n\t"
        "ds_read_b128 v[48:51], v37 offset:0\n\t"
        "ds_read_b128 v[68:71], v37 offset:16\n\t"
        "v_lshrrev_b32 v37, 4, v111\n\t"
        "Lw_%=:\n\t"
        "ds_read_b32 v34, %[fb] offset:%[ogo]\n\t"
        "ds_read_b128 v[52:55], v36 offset:0\n\t"
        "ds_read_b128 v[56:59], v36 offset:1024\n\t"
        "ds_read_b128 v[60:63], v36 offset:2048\n\t"
        "ds_read_b128 v[64:67], v36 offset:3072\n\t"
        "s_sub_u32 %[scn], %[scn], 1\n\t"
        "s_waitcnt lgkmcnt(4)\n\t"
        "v_readfirstlane_b32 %[sf], v34\n\t"
        "s_cmp_lt_i32 %[scn], 1\n\t"
        "s_cbranch_scc1 Lg_%=\n\t"
        "s_cmp_lg_u32 %[sf], 0\n\t"
        "s_cbranch_scc1 Lg_%=\n\t"
        "s_sleep 1\n\t"
        "s_branch Lw_%=\n\t"
        "Lg_%=:\n\t"
        "ds_read_b128 v[72:75], v36 offset:4096\n\t"
        "ds_read_b128 v[76:79], v36 offset:5120\n\t"
        "ds_read_b128 v[80:83], v36 offset:6144\n\t"
        "ds_read_b128 v[84:87], v36 offset:7168\n\t"
        "s_lshr_b32 %[sf], %[sf], 8\n\t"
        "s_and_b32 %[st], %[sf], 15\n\t"
        "s_lshl_b64 %[smx], %[sm16], %[st]\n\t"
        "s_cmp_ge_u32 %[sf], 16\n\t"
        "s_cselect_b64 %[smw], %[sva], %[sv0]\n\t"
        "s_and_b64 %[smw], %[smw], %[sm16]\n\t"
        "s_lshr_b32 %[st], %[sf], 4\n\t"
        "s_cmp_eq_u32 %[st], %[sw1]\n\t"
        "s_cselect_b64 %[smx], %[smx], 0\n\t"
        "s_cmp_eq_u64 %[smw], 0\n\t"
        "s_cbranch_scc1 Le_%=\n\t"
        "s_waitcnt lgkmcnt(7)\n\t"
        "v_mfma_f32_16x16x32_bf16 v[24:27], v[112:115], v[52:55], 0\n\t"
        "v_mfma_f32_16x16x32_bf16 v[28:31], v[116:119], v[52:55], 0\n\t"
        "ds_read_b128 v[52:55], v36 offset:8192\n\t"
        "s_waitcnt lgkmcnt(7)\n\t"
        "v_mfma_f32_16x16x32_bf16 v[24:27], v[120:123], v[56:59], v[24:27]\n\t"
        "v_mfma_f32_16x16x32_bf16 v[28:31], v[124:127], v[56:59], v[28:31]\n\t"
        "ds_read_b128 v[56:59], v36 offset:9216\n\t"
        "v_mad_u32_u24 v37, v37, %[sf], v110\n\t"
        "s_waitcnt lgkmcnt(7)\n\t"
        "v_mfma_f32_16x16x32_bf16 v[24:27], v[128:131], v[60:63], v[24:27]\n\t"
        "v_mfma_f32_16x16x32_bf16 v[28:31], v[132:135], v[60:63], v[28:31]\n\t"
        "ds_read_b128 v[60:63], v36 offset:10240\n\t"
        "s_waitcnt lgkmcnt(7)\n\t"
        "v_mfma_f32_16x16x32_bf16 v[24:27], v[136:139], v[64:67], v[24:27]\n\t"
        "v_mfma_f32_16x16x32_bf16 v[28:31], v[140:143], v[64:67], v[28:31]\n\t"
        "ds_read_b128 v[64:67], v36 offset:11264\n\t"
        "s_waitcnt lgkmcnt(7)\n\t"
        "v_mfma_f32_16x16x32_bf16 v[24:27], v[144:147], v[72:75], v[24:27]\n\t"
        "v_mfma_f32_16x16x32_bf16 v[28:31], v[148:151], v[72:75], v[28:31]\n\t"
        "ds_read_b128 v[72:75], v36 offset:12288\n\t"
        "s_waitcnt lgkmcnt(7)\n\t"
        "v_mfma_f32_16x16x32_bf16 v[24:27], v[152:155], v[76:79], v[24:27]\n\t"
        "v_mfma_f32_16x16x32_bf16 v[28:31], v[156:159], v[76:79], v[28:31]\n\t"
        "ds_read_b128 v[76:79], v36 offset:13312\n\t"
        "s_waitcnt lgkmcnt(7)\n\t"
        "v_mfma_f32_16x16x32_bf16 v[24:27], v[160:163], v[80:83], v[24:27]\n\t"
        "v_mfma_f32_16x16x32_bf16 v[28:31], v[164:167], v[80:83], v[28:31]\n\t"
        "ds_read_b128 v[80:83], v36 offset:14336\n\t"
        "s_waitcnt lgkmcnt(7)\n\t"
        "v_mfma_f32_16x16x32_bf16 v[24:27], v[168:171], v[84:87], v[24:27]\n\t"
        "v_mfma_f32_16x16x32_bf16 v[28:31], v[172:175], v[84:87], v[28:31]\n\t"
        "ds_read_b128 v[84:87], v36 offset:15360\n\t"
        "s_waitcnt lgkmcnt(7)\n\t"
        "v_mfma_f32_16x16x32_bf16 v[24:27], v[176:179], v[52:55], v[24:27]\n\t"
        "v_mfma_f32_16x16x32_bf16 v[28:31], v[180:183], v[52:55], v[28:31]\n\t"
        "ds_read_b128 v[52:55], v36 offset:16384\n\t"
        "s_waitcnt lgkmcnt(7)\n\t"
        "v_mfma_f32_16x16x32_bf16 v[24:27], v[184:187], v[56:59], v[24:27]\n\t"
        "v_mfma_f32_16x16x32_bf16 v[28:31], v[188:191], v[56:59], v[28:31]\n\t"
        "ds_read_b128 v[56:59], v36 offset:17408\n\t"
        "s_waitcnt lgkmcnt(7)\n\t"
        "v_mfma_f32_16x16x32_bf16 v[24:27], v[192:195], v[60:63], v[24:27]\n\t"
        "v_mfma_f32_16x16x32_bf16 v[28:31], v[196:199], v[60:63], v[28:31]\n\t"
        "s_waitcnt lgkmcnt(6)\n\t"
        "v_mfma_f32_16x16x32_bf16 v[24:27], v[200:203], v[64:67], v[24:27]\n\t"
        "v_mfma_f32_16x16x32_bf16 v[28:31], v[204:207], v[64:67], v[28:31]\n\t"
        "s_waitcnt lgkmcnt(5)\n\t"
        "v_mfma_f32_16x16x32_bf16 v[24:27], v[208:211], v[72:75], v[24:27]\n\t"
        "v_mfma_f32_16x16x32_bf16 v[28:31], v[212:215], v[72:75], v[28:31]\n\t"
        "ds_read_b32 v34, %[fb] offset:%[ops]\n\t"
        "ds_read_b64 v[32:33], v35\n\t"
        "s_waitcnt lgkmcnt(6)\n\t"
        "v_mfma_f32_16x16x32_bf16 v[24:27], v[216:219], v[76:79], v[24:27]\n\t"
        "v_mfma_f32_16x16x32_bf16 v[28:31], v[220:223], v[76:79], v[28:31]\n\t"
        "s_waitcnt lgkmcnt(5)\n\t"
        "v_mfma_f32_16x16x32_bf16 v[24:27], v[224:227], v[80:83], v[24:27]\n\t"
        "v_mfma_f32_16x16x32_bf16 v[28:31], v[228:231], v[80:83], v[28:31]\n\t"
        "s_waitcnt lgkmcnt(4)\n\t"
        "v_mfma_f32_16x16x32_bf16 v[24:27], v[232:235], v[84:87], v[24:27]\n\t"
        "v_mfma_f32_16x16x32_bf16 v[28:31], v[236:239], v[84:87], v[28:31]\n\t"
        "s_waitcnt lgkmcnt(3)\n\t"
        "v_mfma_f32_16x16x32_bf16 v[24:27], v[240:243], v[52:55], v[24:27]\n\t"
        "v_mfma_f32_16x16x32_bf16 v[28:31], v[244:247], v[52:55], v[28:31]\n\t"
        "s_waitcnt lgkmcnt(2)\n\t"
        "v_mfma_f32_16x16x32_bf16 v[24:27], v[248:251], v[56:59], v[24:27]\n\t"
        "v_mfma_f32_16x16x32_bf16 v[28:31], v[252:255], v[56:59], v[28:31]\n\t"
        "Lm_%=:\n\t"
        "s_waitcnt lgkmcnt(0)\n\t"
        "v_readfirstlane_b32 %[st], v34\n\t"
        "s_cmp_ge_u32 %[st], %[sf]\n\t"
        "s_cbranch_scc1 Lo_%=\n\t"
        "s_cmp_lt_i32 %[scn], 1\n\t"
        "s_cbranch_scc1 Lo_%=\n\t"
        "s_sub_u32 %[scn], %[scn], 1\n\t"
        "ds_read_b32 v34, %[fb] offset:%[ops]\n\t"
        "ds_read_b64 v[32:33], v35\n\t"
        "s_branch Lm_%=\n\t"
        "Lo_%=:\n\t"
        "s_mov_b64 exec, %[smx]\n\t"
        "ds_read_b128 v[88:91], %[xr]\n\t"
        "ds_read_b128 v[92:95], %[xr] offset:64\n\t"
        "ds_read_b64 v[96:97], %[xq]\n\t"
        "s_mov_b64 exec, -1\n\t"
        "v_fma_f32 v24, -v32, v48, v24\n\t"
        "v_fma_f32 v25, -v32, v49, v25\n\t"
        "v_fma_f32 v26, -v32, v50, v26\n\t"
        "v_fma_f32 v27, -v32, v51, v27\n\t"
        "v_fma_f32 v28, -v32, v68, v28\n\t"
        "v_fma_f32 v29, -v32, v69, v29\n\t"
        "v_fma_f32 v30, -v32, v70, v30\n\t"
        "v_fma_f32 v31, -v32, v71, v31\n\t"
        "v_mul_f32 v24, v33, v24\n\t"
        "v_mul_f32 v25, v33, v25\n\t"
        "v_mul_f32 v26, v33, v26\n\t"
        "v_mul_f32 v27, v33, v27\n\t"
        "v_mul_f32 v28, v33, v28\n\t"
        "v_mul_f32 v29, v33, v29\n\t"
        "v_mul_f32 v30, v33, v30\n\t"
        "v_mul_f32 v31, v33, v31\n\t"
        "v_cvt_pk_bf16_f32 v24, v24, v28\n\t"
        "v_cvt_pk_bf16_f32 v25, v25, v29\n\t"
        "v_cvt_pk_bf16_f32 v26, v26, v30\n\t"
        "v_cvt_pk_bf16_f32 v27, v27, v31\n\t"
        "s_mov_b64 exec, %[smw]\n\t"
        "ds_write_b128 v37, v[24:27]\n\t"
        "s_mov_b64 exec, -1\n\t"
        "Le_%=:\n\t"
        "s_waitcnt lgkmcnt(0)\n\t"
        : [sf] "=&s"(sf), [st] "=&s"(st), [smw] "=&s"(mw), [smx] "=&s"(mx), [scn] "+s"(cn)
        : [fb] "v"(fb), [xr] "v"(xr), [xq] "v"(xq), [sw1] "s"(w1), [scs] "s"(cs), [sva] "s"(va), [sv0] "s"(v0), [sm16] "s"(0x0001000100010001ull), [ogo] "n"(BC_GO), [ops] "n"(BC_PST)
        : "memory", "scc", "v24", "v25", "v26", "v27", "v28", "v29", "v30", "v31", "v32", "v33", "v34", "v35", "v36", "v37");
}

DEV void att(const MidS& a) {
    u32 tm;
    asm volatile(
        "s_mov_b32 m0, %[sldx]\n\t"
        "s_nop 3\n\t"
        "s_waitcnt lgkmcnt(0)\n\t"
        "s_barrier\n\t"
        "s_setprio 0\n\t"
        "ds_read_b64 v[36:37], v102 offset:0\n\t"
        "ds_read_b32 v38, v103 offset:0\n\t"
        "ds_read_b64 v[20:21], v100 offset:0\n\t"
        "ds_read_b32 v22, v101 offset:0\n\t"
        "ds_read_b64 v[24:25], v100 offset:4864\n\t"
        "ds_read_b32 v26, v101 offset:4864\n\t"
        "ds_read_b64 v[40:41], v102 offset:40\n\t"
        "ds_read_b32 v42, v103 offset:4\n\t"
        "ds_read_b64 v[28:29], v100 offset:40\n\t"
        "ds_read_b32 v30, v101 offset:4\n\t"
        "ds_read_b64 v[32:33], v100 offset:4904\n\t"
        "ds_read_b32 v34, v101 offset:4868\n\t"
        "ds_read_b64_tr_b16 v[52:53], v105\n\t"
        "ds_read_b64_tr_b16 v[54:55], v105 offset:4864\n\t"
        "s_bitcmp1_b32 %[sskip], 0\n\t"
        "s_cbranch_scc1 Le0_%=\n\t"
        "buffer_load_dwordx4 v107, %[ssrd], %[swx] offen sc1 lds\n\t"
        "buffer_load_dwordx4 v107, %[ssrd], %[swx] offen offset:1024 sc1 lds\n\t"
        "Le0_%=:\n\t"
        "v_mov_b32 v23, 0\n\t"
        "v_mov_b32 v27, 0\n\t"
        "v_mov_b32 v31, 0\n\t"
        "v_mov_b32 v35, 0\n\t"
        "v_mov_b32 v39, 0\n\t"
        "v_mov_b32 v43, 0\n\t"
        "v_mov_b32 v60, %[sc0]\n\t"
        "v_mov_b32 v61, %[sc0]\n\t"
        "v_mov_b32 v62, %[sc0]\n\t"
        "v_mov_b32 v63, %[sc0]\n\t"
        "v_mov_b32 v68, %[sc0]\n\t"
        "v_mov_b32 v69, %[sc0]\n\t"
        "v_mov_b32 v70, %[sc0]\n\t"
        "v_mov_b32 v71, %[sc0]\n\t"
        "s_waitcnt lgkmcnt(10)\n\t"
        "v_mfma_f32_16x16x32_bf16 v[60:63], v[20:23], v[36:39], v[60:63]\n\t"
        "ds_read_b64_tr_b16 v[44:45], v104\n\t"
        "ds_read_b64_tr_b16 v[46:47], v104 offset:4864\n\t"
        "s_waitcnt lgkmcnt(10)\n\t"
        "v_mfma_f32_16x16x32_bf16 v[64:67], v[24:27], v[36:39], 0\n\t"
        "ds_read_b64_tr_b16 v[48:49], v104 offset:40\n\t"
        "ds_read_b64_tr_b16 v[50:51], v104 offset:4904\n\t"
        "s_waitcnt lgkmcnt(8)\n\t"
        "v_mfma_f32_16x16x32_bf16 v[68:71], v[28:31], v[40:43], v[68:71]\n\t"
        "s_waitcnt lgkmcnt(6)\n\t"
        "v_mfma_f32_16x16x32_bf16 v[72:75], v[32:35], v[40:43], 0\n\t"
        "s_cbranch_scc1 Le1_%=\n\t"
        "buffer_load_dwordx4 v107, %[ssrd], %[swx] offen offset:2048 sc1 lds\n\t"
        "buffer_load_dwordx4 v107, %[ssrd], %[swx] offen offset:3072 sc1 lds\n\t"
        "Le1_%=:\n\t"
        "v_mov_b32 v56, 0x0c0c0100\n\t"
        "v_mov_b32 v57, 0x01000c0c\n\t"
        "v_mov_b32 v58, 0x0c0c0c0c\n\t"
        "v_cndmask_b32 v56, v56, v57, %[spar]\n\t"
        "v_add_u32 v57, 0x0202, v56\n\t"
        "v_add_u32 v59, 0x02020000, v56\n\t"
        "v_cndmask_b32 v57, v57, v59, %[spar]\n\t"
        "v_cndmask_b32 v76, v56, v58, %[ssa]\n\t"
        "v_cndmask_b32 v78, v58, v56, %[ssa]\n\t"
        "v_cndmask_b32 v77, v57, v58, %[ssb]\n\t"
        "v_cndmask_b32 v79, v58, v57, %[ssb]\n\t"
        "v_mov_b32 v17, 0x3f803f80\n\t"
        "v_cndmask_b32 v28, v60, v61, %[spar]\n\t"
        "v_cndmask_b32 v36, v68, v69, %[spar]\n\t"
        "v_cndmask_b32 v29, v62, v63, %[spar]\n\t"
        "v_cndmask_b32 v37, v70, v71, %[spar]\n\t"
        "v_cndmask_b32 v30, v64, v65, %[spar]\n\t"
        "v_cndmask_b32 v38, v72, v73, %[spar]\n\t"
        "v_cndmask_b32 v31, v66, v67, %[spar]\n\t"
        "v_cndmask_b32 v39, v74, v75, %[spar]\n\t"
        "v_cndmask_b32 v28, v28, v30, %[ssa]\n\t"
        "v_cndmask_b32 v36, v36, v38, %[ssa]\n\t"
        "v_cndmask_b32 v29, v29, v31, %[ssb]\n\t"
        "v_cndmask_b32 v37, v37, v39, %[ssb]\n\t"
        "v_max_f32 v30, v28, v29\n\t"
        "v_max_f32 v38, v36, v37\n\t"
        "v_max_f32 v31, v28, v29\n\t"
        "v_max_f32 v39, v36, v37\n\t"
        "s_waitcnt lgkmcnt(4)\n\t"
        "v_permlane16_swap_b32 v30, v31\n\t"
        "v_permlane16_swap_b32 v38, v39\n\t"
        "v_max_f32 v32, v30, v31\n\t"
        "v_max_f32 v40, v38, v39\n\t"
        "v_max_f32 v33, v30, v31\n\t"
        "v_max_f32 v41, v38, v39\n\t"
        "v_cndmask_b32 v56, v17, v52, %[sl2]\n\t"
        "v_permlane32_swap_b32 v32, v33\n\t"
        "v_permlane32_swap_b32 v40, v41\n\t"
        "v_max_f32 v30, v32, v33\n\t"
        "v_max_f32 v38, v40, v41\n\t"
        "v_sub_f32 v28, v28, v30\n\t"
        "v_sub_f32 v36, v36, v38\n\t"
        "v_sub_f32 v29, v29, v30\n\t"
        "v_sub_f32 v37, v37, v38\n\t"
        "v_mul_f32 v28, 0x3eae1a93, v28\n\t"
        "v_mul_f32 v36, 0x3eae1a93, v36\n\t"
        "v_mul_f32 v29, 0x3eae1a93, v29\n\t"
        "v_mul_f32 v37, 0x3eae1a93, v37\n\t"
        "v_exp_f32 v28, v28\n\t"
        "v_exp_f32 v36, v36\n\t"
        "v_exp_f32 v29, v29\n\t"
        "v_exp_f32 v37, v37\n\t"
        "v_cndmask_b32 v57, v17, v53, %[sl2]\n\t"
        "v_cndmask_b32 v58, v17, v54, %[sl2]\n\t"
        "v_cndmask_b32 v59, v17, v55, %[sl2]\n\t"
        "v_cndmask_b32 v52, v52, v17, %[sl2]\n\t"
        "v_cndmask_b32 v53, v53, v17, %[sl2]\n\t"
        "v_cndmask_b32 v54, v54, v17, %[sl2]\n\t"
        "v_cndmask_b32 v55, v55, v17, %[sl2]\n\t"
        "v_cvt_pk_bf16_f32 v28, v28, v29\n\t"
        "v_cvt_pk_bf16_f32 v36, v36, v37\n\t"
        "v_perm_b32 v20, v28, v28, v76\n\t"
        "v_perm_b32 v21, v28, v28, v77\n\t"
        "v_perm_b32 v22, v28, v28, v78\n\t"
        "v_perm_b32 v23, v28, v28, v79\n\t"
        "v_perm_b32 v24, v36, v36, v76\n\t"
        "v_perm_b32 v25, v36, v36, v77\n\t"
        "v_perm_b32 v26, v36, v36, v78\n\t"
        "v_perm_b32 v27, v36, v36, v79\n\t"
        "s_waitcnt lgkmcnt(0)\n\t"
        "v_mfma_f32_16x16x32_bf16 v[64:67], v[52:55], v[20:23], 0\n\t"
        "v_mfma_f32_16x16x32_bf16 v[60:63], v[44:47], v[20:23], 0\n\t"
        "v_mfma_f32_16x16x32_bf16 v[72:75], v[56:59], v[24:27], 0\n\t"
        "v_mfma_f32_16x16x32_bf16 v[68:71], v[48:51], v[24:27], 0\n\t"
        "v_lshrrev_b32 v17, 1, v107\n\t"
        "s_nop 3\n\t"
        "v_rcp_f32 v36, v66\n\t"
        "s_nop 0\n\t"
        "v_mul_f32 v60, v60, v36\n\t"
        "v_mul_f32 v61, v61, v36\n\t"
        "v_mul_f32 v62, v62, v36\n\t"
        "v_mul_f32 v63, v63, v36\n\t"
        "v_rcp_f32 v37, v72\n\t"
        "v_cvt_pk_bf16_f32 v80, v60, v61\n\t"
        "v_cvt_pk_bf16_f32 v81, v62, v63\n\t"
        "v_mul_f32 v68, v68, v37\n\t"
        "v_mul_f32 v69, v69, v37\n\t"
        "v_mul_f32 v70, v70, v37\n\t"
        "v_mul_f32 v71, v71, v37\n\t"
        "v_bfi_b32 v80, 1, %[slay], v80\n\t"
        "v_cvt_pk_bf16_f32 v82, v68, v69\n\t"
        "v_cvt_pk_bf16_f32 v83, v70, v71\n\t"
        "v_mul_f32 v64, v64, v36\n\t"
        "v_mul_f32 v65, v65, v36\n\t"
        "v_bfi_b32 v82, 1, %[slay], v82\n\t"
        "buffer_store_dwordx4 v[80:83], v107, %[ssrd], %[sst] offen sc1\n\t"
        "v_mul_f32 v74, v74, v37\n\t"
        "v_mul_f32 v75, v75, v37\n\t"
        "v_cvt_pk_bf16_f32 v84, v64, v65\n\t"
        "v_cvt_pk_bf16_f32 v85, v74, v75\n\t"
        "v_bfi_b32 v84, 1, %[slay], v84\n\t"
        "s_mov_b64 exec, 0xffff\n\t"
        "buffer_store_dwordx2 v[84:85], v17, %[ssrd], %[stt] offen sc1\n\t"
        "s_mov_b64 exec, -1\n\t"
        "s_waitcnt vmcnt(2)\n\t"
        "s_barrier\n\t"
        "s_cmp_eq_u32 %[smsk], 0\n\t"
        "s_cbranch_scc0 Lpr_%=\n\t"
        "s_setprio 1\n\t"
        "Lpr_%=:\n\t"
        : [stm] "=&s"(tm)
        : [ssrd] "s"(a.srd), [sst] "s"(a.st), [stt] "s"(a.tt), [sc0] "s"(a.c0), [swx] "s"(a.wx), [sldx] "s"(a.ldx), [sskip] "s"(a.skip), [smsk] "s"(a.msk), [swi] "s"(a.wi), [sld] "s"(a.ld), [sn7] "s"(a.n7), [slay] "s"(a.layer),
          [spar] "s"(0xAAAAAAAAAAAAAAAAull), [ssa] "s"(0xf000ff00fff0ffffull), [ssb] "s"(0xc000fc00ffc0fffcull), [sl2] "s"(0xCCCCCCCCCCCCCCCCull)
        : "memory", "scc", "vcc", "v14", "v15", "v16", "v17", "v18", "v19", "v20", "v21", "v22", "v23", "v24", "v25", "v26", "v27", "v28", "v29", "v30", "v31", "v32", "v33", "v34", "v35", "v36", "v37", "v38", "v39", "v40", "v41", "v42", "v43", "v44", "v45", "v46", "v47");
}
DEV void mid(const MidS& a, u32& cn) {
    u32 ob4, ob4x;
    asm volatile(
        "s_nop 4\n\t"
        "s_add_u32 %[sob4], %[sob], 4096\n\t"
        "v_add_u32 v19, %[stvd], v108\n\t"
        "s_bitcmp1_b32 %[sskip], 1\n\t"
        "s_cbranch_scc1 L6_%=\n\t"
        "s_mov_b32 m0, %[sld]\n\t"
        "s_add_u32 %[sob4x], %[swi], 4096\n\t"
        "buffer_load_dwordx4 v107, %[ssrd], %[swi] offen offset:0 sc1 lds\n\t"
        "buffer_load_dwordx4 v107, %[ssrd], %[swi] offen offset:1024 sc1 lds\n\t"
        "buffer_load_dwordx4 v107, %[ssrd], %[swi] offen offset:2048 sc1 lds\n\t"
        "buffer_load_dwordx4 v107, %[ssrd], %[swi] offen offset:3072 sc1 lds\n\t"
        "s_add_u32 m0, m0, 4096\n\t"
        "s_cmp_eq_u32 %[sn7], 0\n\t"
        "buffer_load_dwordx4 v107, %[ssrd], %[sob4x] offen offset:0 sc1 lds\n\t"
        "buffer_load_dwordx4 v107, %[ssrd], %[sob4x] offen offset:1024 sc1 lds\n\t"
        "s_cbranch_scc1 L6_%=\n\t"
        "buffer_load_dwordx4 v107, %[ssrd], %[sob4x] offen offset:2048 sc1 lds\n\t"
        "L6_%=:\n\t"
        "s_and_b32 %[sob4x], %[smsk], 0xffff0000\n\t"
        "buffer_load_dwordx4 v[40:43], v107, %[ssrd], %[sob] offen offset:0 sc1\n\t"
        "v_lshlrev_b32 v20, 16, v88\n\t"
        "v_and_b32 v24, %[sob4x], v88\n\t"
        "v_and_b32 v20, %[smsk], v20\n\t"
        "buffer_load_dwordx4 v[44:47], v107, %[ssrd], %[sob] offen offset:1024 sc1\n\t"
        "v_lshlrev_b32 v21, 16, v89\n\t"
        "v_and_b32 v25, %[sob4x], v89\n\t"
        "v_and_b32 v21, %[smsk], v21\n\t"
        "buffer_load_dwordx4 v[48:51], v107, %[ssrd], %[sob] offen offset:2048 sc1\n\t"
        "v_lshlrev_b32 v22, 16, v90\n\t"
        "v_and_b32 v26, %[sob4x], v90\n\t"
        "v_and_b32 v22, %[smsk], v22\n\t"
        "buffer_load_dwordx4 v[52:55], v107, %[ssrd], %[sob] offen offset:3072 sc1\n\t"
        "v_lshlrev_b32 v23, 16, v91\n\t"
        "v_and_b32 v27, %[sob4x], v91\n\t"
        "v_and_b32 v23, %[smsk], v23\n\t"
        "buffer_load_dwordx4 v[56:59], v107, %[ssrd], %[sob4] offen offset:0 sc1\n\t"
        "v_lshlrev_b32 v28, 16, v92\n\t"
        "v_and_b32 v32, %[sob4x], v92\n\t"
        "v_and_b32 v28, %[smsk], v28\n\t"
        "buffer_load_dwordx4 v[60:63], v107, %[ssrd], %[sob4] offen offset:1024 sc1\n\t"
        "v_lshlrev_b32 v29, 16, v93\n\t"
        "v_and_b32 v33, %[sob4x], v93\n\t"
        "v_and_b32 v29, %[smsk], v29\n\t"
        "buffer_load_dwordx4 v[64:67], v107, %[ssrd], %[sob4] offen offset:2048 sc1\n\t"
        "v_lshlrev_b32 v30, 16, v94\n\t"
        "v_and_b32 v34, %[sob4x], v94\n\t"
        "v_and_b32 v30, %[smsk], v30\n\t"
        "buffer_load_dwordx2 v[68:69], v108, %[ssrd], %[sob] offen offset:0 sc1\n\t"
        "buffer_load_dwordx2 v[70:71], v19, %[ssrd], %[sob] offen offset:0 sc1\n\t"
        "v_lshlrev_b32 v31, 16, v95\n\t"
        "v_and_b32 v35, %[sob4x], v95\n\t"
        "v_and_b32 v31, %[smsk], v31\n\t"
        "buffer_load_dwordx2 v[72:73], v108, %[ssrd], %[sob] offen offset:1024 sc1\n\t"
        "buffer_load_dwordx2 v[74:75], v19, %[ssrd], %[sob] offen offset:1024 sc1\n\t"
        "v_lshlrev_b32 v36, 16, v96\n\t"
        "v_and_b32 v37, %[sob4x], v96\n\t"
        "v_and_b32 v36, %[smsk], v36\n\t"
        "v_lshlrev_b32 v38, 16, v97\n\t"
        "v_and_b32 v39, %[sob4x], v97\n\t"
        "v_and_b32 v38, %[smsk], v38\n\t"
        "s_and_b32 %[sob4x], %[smsk], %[slay]\n\t"
        "s_cbranch_scc0 Lu_%=\n\t"
        "v_sub_f32 v88, v20, v98\n\t"
        "v_sub_f32 v86, v24, v98\n\t"
        "v_sub_f32 v89, v21, v98\n\t"
        "v_sub_f32 v87, v25, v98\n\t"
        "v_mul_f32 v88, v99, v88\n\t"
        "v_mul_f32 v86, v99, v86\n\t"
        "v_mul_f32 v89, v99, v89\n\t"
        "v_mul_f32 v87, v99, v87\n\t"
        "v_cvt_pk_bf16_f32 v86, v88, v86\n\t"
        "v_cvt_pk_bf16_f32 v87, v89, v87\n\t"
        "v_lshlrev_b32 v88, 16, v86\n\t"
        "v_and_b32 v86, 0xffff0000, v86\n\t"
        "v_lshlrev_b32 v89, 16, v87\n\t"
        "v_and_b32 v87, 0xffff0000, v87\n\t"
        "v_add_f32 v20, v20, v88\n\t"
        "v_add_f32 v24, v24, v86\n\t"
        "v_add_f32 v21, v21, v89\n\t"
        "v_add_f32 v25, v25, v87\n\t"
        "v_cvt_pk_bf16_f32 v88, v20, v24\n\t"
        "v_cvt_pk_bf16_f32 v89, v21, v25\n\t"
        "v_lshlrev_b32 v20, 16, v88\n\t"
        "v_and_b32 v24, 0xffff0000, v88\n\t"
        "v_lshlrev_b32 v21, 16, v89\n\t"
        "v_and_b32 v25, 0xffff0000, v89\n\t"
        "v_sub_f32 v90, v22, v98\n\t"
        "v_sub_f32 v86, v26, v98\n\t"
        "v_sub_f32 v91, v23, v98\n\t"
        "v_sub_f32 v87, v27, v98\n\t"
        "v_mul_f32 v90, v99, v90\n\t"
        "v_mul_f32 v86, v99, v86\n\t"
        "v_mul_f32 v91, v99, v91\n\t"
        "v_mul_f32 v87, v99, v87\n\t"
        "v_cvt_pk_bf16_f32 v86, v90, v86\n\t"
        "v_cvt_pk_bf16_f32 v87, v91, v87\n\t"
        "v_lshlrev_b32 v90, 16, v86\n\t"
        "v_and_b32 v86, 0xffff0000, v86\n\t"
        "v_lshlrev_b32 v91, 16, v87\n\t"
        "v_and_b32 v87, 0xffff0000, v87\n\t"
        "v_add_f32 v22, v22, v90\n\t"
        "v_add_f32 v26, v26, v86\n\t"
        "v_add_f32 v23, v23, v91\n\t"
        "v_add_f32 v27, v27, v87\n\t"
        "v_cvt_pk_bf16_f32 v90, v22, v26\n\t"
        "v_cvt_pk_bf16_f32 v91, v23, v27\n\t"
        "v_lshlrev_b32 v22, 16, v90\n\t"
        "v_and_b32 v26, 0xffff0000, v90\n\t"
        "v_lshlrev_b32 v23, 16, v91\n\t"
        "v_and_b32 v27, 0xffff0000, v91\n\t"
        "v_sub_f32 v92, v28, v98\n\t"
        "v_sub_f32 v86, v32, v98\n\t"
        "v_sub_f32 v93, v29, v98\n\t"
        "v_sub_f32 v87, v33, v98\n\t"
        "v_mul_f32 v92, v99, v92\n\t"
        "v_mul_f32 v86, v99, v86\n\t"
        "v_mul_f32 v93, v99, v93\n\t"
        "v_mul_f32 v87, v99, v87\n\t"
        "v_cvt_pk_bf16_f32 v86, v92, v86\n\t"
        "v_cvt_pk_bf16_f32 v87, v93, v87\n\t"
        "v_lshlrev_b32 v92, 16, v86\n\t"
        "v_and_b32 v86, 0xffff0000, v86\n\t"
        "v_lshlrev_b32 v93, 16, v87\n\t"
        "v_and_b32 v87, 0xffff0000, v87\n\t"
        "v_add_f32 v28, v28, v92\n\t"
        "v_add_f32 v32, v32, v86\n\t"
        "v_add_f32 v29, v29, v93\n\t"
        "v_add_f32 v33, v33, v87\n\t"
        "v_cvt_pk_bf16_f32 v92, v28, v32\n\t"
        "v_cvt_pk_bf16_f32 v93, v29, v33\n\t"
        "v_lshlrev_b32 v28, 16, v92\n\t"
        "v_and_b32 v32, 0xffff0000, v92\n\t"
        "v_lshlrev_b32 v29, 16, v93\n\t"
        "v_and_b32 v33, 0xffff0000, v93\n\t"
        "v_sub_f32 v94, v30, v98\n\t"
        "v_sub_f32 v86, v34, v98\n\t"
        "v_sub_f32 v95, v31, v98\n\t"
        "v_sub_f32 v87, v35, v98\n\t"
        "v_mul_f32 v94, v99, v94\n\t"
        "v_mul_f32 v86, v99, v86\n\t"
        "v_mul_f32 v95, v99, v95\n\t"
        "v_mul_f32 v87, v99, v87\n\t"
        "v_cvt_pk_bf16_f32 v86, v94, v86\n\t"
        "v_cvt_pk_bf16_f32 v87, v95, v87\n\t"
        "v_lshlrev_b32 v94, 16, v86\n\t"
        "v_and_b32 v86, 0xffff0000, v86\n\t"
        "v_lshlrev_b32 v95, 16, v87\n\t"
        "v_and_b32 v87, 0xffff0000, v87\n\t"
        "v_add_f32 v30, v30, v94\n\t"
        "v_add_f32 v34, v34, v86\n\t"
        "v_add_f32 v31, v31, v95\n\t"
        "v_add_f32 v35, v35, v87\n\t"
        "v_cvt_pk_bf16_f32 v94, v30, v34\n\t"
        "v_cvt_pk_bf16_f32 v95, v31, v35\n\t"
        "v_lshlrev_b32 v30, 16, v94\n\t"
        "v_and_b32 v34, 0xffff0000, v94\n\t"
        "v_lshlrev_b32 v31, 16, v95\n\t"
        "v_and_b32 v35, 0xffff0000, v95\n\t"
        "v_sub_f32 v96, v36, v98\n\t"
        "v_sub_f32 v86, v37, v98\n\t"
        "v_sub_f32 v97, v38, v98\n\t"
        "v_sub_f32 v87, v39, v98\n\t"
        "v_mul_f32 v96, v99, v96\n\t"
        "v_mul_f32 v86, v99, v86\n\t"
        "v_mul_f32 v97, v99, v97\n\t"
        "v_mul_f32 v87, v99, v87\n\t"
        "v_cvt_pk_bf16_f32 v86, v96, v86\n\t"
        "v_cvt_pk_bf16_f32 v87, v97, v87\n\t"
        "v_lshlrev_b32 v96, 16, v86\n\t"
        "v_and_b32 v86, 0xffff0000, v86\n\t"
        "v_lshlrev_b32 v97, 16, v87\n\t"
        "v_and_b32 v87, 0xffff0000, v87\n\t"
        "v_add_f32 v36, v36, v96\n\t"
        "v_add_f32 v37, v37, v86\n\t"
        "v_add_f32 v38, v38, v97\n\t"
        "v_add_f32 v39, v39, v87\n\t"
        "v_cvt_pk_bf16_f32 v96, v36, v37\n\t"
        "v_cvt_pk_bf16_f32 v97, v38, v39\n\t"
        "v_lshlrev_b32 v36, 16, v96\n\t"
        "v_and_b32 v37, 0xffff0000, v96\n\t"
        "v_lshlrev_b32 v38, 16, v97\n\t"
        "v_and_b32 v39, 0xffff0000, v97\n\t"
        "Lu_%=:\n\t"
        "v_mov_b32 v15, %[swc]\n\t"
        "v_mov_b32 v16, 1\n\t"
        "v_add_u32 v18, %[slb], v107\n\t"
        "v_add_u32 v14, %[slb], v106\n\t"
        "ds_read_b128 v[76:79], v18 offset:0\n\t"
        "ds_read_b128 v[80:83], v18 offset:1024\n\t"
        "ds_read_b128 v[84:87], v18 offset:2048\n\t"
        "ds_read_b128 v[88:91], v18 offset:3072\n\t"
        "ds_read_b128 v[92:95], v14 offset:0\n\t"
        "ds_read_b128 v[96:99], v18 offset:4608\n\t"
        "s_waitcnt vmcnt(11)\n\t"
        "s_mov_b64 exec, 1\n\t"
        "ds_add_u32 v15, v16\n\t"
        "s_mov_b64 exec, -1\n\t"
        "Lr0_%=:\n\t"
        "s_waitcnt vmcnt(10)\n\t"
        "v_bitop3_b32 v17, v40, v42, %[slay] bitop3:0x7e\n\t"
        "v_and_b32 v17, 1, v17\n\t"
        "v_cmp_eq_u32 vcc, 1, v17\n\t"
        "s_cbranch_vccnz Lx0_%=\n\t"
        "Ld0_%=:\n\t"
        "s_waitcnt lgkmcnt(6)\n\t"
        "v_mfma_f32_16x16x32_bf16 v[20:23], v[76:79], v[40:43], v[20:23]\n\t"
        "ds_read_b128 v[76:79], v18 offset:5632\n\t"
        "s_waitcnt lgkmcnt(6)\n\t"
        "v_mfma_f32_16x16x32_bf16 v[24:27], v[80:83], v[40:43], v[24:27]\n\t"
        "ds_read_b128 v[80:83], v18 offset:6656\n\t"
        "s_waitcnt lgkmcnt(6)\n\t"
        "v_mfma_f32_16x16x32_bf16 v[28:31], v[84:87], v[40:43], v[28:31]\n\t"
        "s_waitcnt vmcnt(9)\n\t"
        "v_bitop3_b32 v17, v44, v46, %[slay] bitop3:0x7e\n\t"
        "v_and_b32 v17, 1, v17\n\t"
        "v_cmp_eq_u32 vcc, 1, v17\n\t"
        "ds_read_b128 v[84:87], v18 offset:7680\n\t"
        "s_waitcnt lgkmcnt(6)\n\t"
        "v_mfma_f32_16x16x32_bf16 v[32:35], v[88:91], v[40:43], v[32:35]\n\t"
        "ds_read_b128 v[88:91], v14 offset:4608\n\t"
        "s_waitcnt lgkmcnt(6)\n\t"
        "v_mfma_f32_16x16x32_bf16 v[36:39], v[92:95], v[40:43], v[36:39]\n\t"
        "s_cbranch_vccnz Lx1_%=\n\t"
        "Ld1_%=:\n\t"
        "ds_read_b32 v16, v15\n\t"
        "ds_read_b128 v[92:95], v18 offset:9216\n\t"
        "ds_read_b128 v[40:43], v18 offset:10240\n\t"
        "s_waitcnt lgkmcnt(8)\n\t"
        "v_mfma_f32_16x16x32_bf16 v[20:23], v[96:99], v[44:47], v[20:23]\n\t"
        "ds_read_b128 v[96:99], v18 offset:11264\n\t"
        "s_waitcnt lgkmcnt(7)\n\t"
        "v_mfma_f32_16x16x32_bf16 v[24:27], v[76:79], v[44:47], v[24:27]\n\t"
        "ds_read_b128 v[76:79], v18 offset:12288\n\t"
        "s_waitcnt lgkmcnt(7)\n\t"
        "v_mfma_f32_16x16x32_bf16 v[28:31], v[80:83], v[44:47], v[28:31]\n\t"
        "s_waitcnt vmcnt(8)\n\t"
        "v_bitop3_b32 v17, v48, v50, %[slay] bitop3:0x7e\n\t"
        "v_and_b32 v17, 1, v17\n\t"
        "v_cmp_eq_u32 vcc, 1, v17\n\t"
        "ds_read_b128 v[80:83], v14 offset:9216\n\t"
        "s_waitcnt lgkmcnt(7)\n\t"
        "v_mfma_f32_16x16x32_bf16 v[32:35], v[84:87], v[44:47], v[32:35]\n\t"
        "s_waitcnt lgkmcnt(5)\n\t"
        "Lwc_%=:\n\t"
        "v_readfirstlane_b32 %[sob4x], v16\n\t"
        "s_cmp_lt_u32 %[sob4x], %[swt]\n\t"
        "s_cbranch_scc1 Lws_%=\n\t"
        "Lwg_%=:\n\t"
        "ds_read_b128 v[84:87], v18 offset:13824\n\t"
        "v_mfma_f32_16x16x32_bf16 v[36:39], v[88:91], v[44:47], v[36:39]\n\t"
        "s_cbranch_vccnz Lx2_%=\n\t"
        "Ld2_%=:\n\t"
        "ds_read_b128 v[88:91], v18 offset:14848\n\t"
        "ds_read_b128 v[44:47], v18 offset:15872\n\t"
        "s_waitcnt lgkmcnt(7)\n\t"
        "v_mfma_f32_16x16x32_bf16 v[20:23], v[92:95], v[48:51], v[20:23]\n\t"
        "ds_read_b128 v[92:95], v18 offset:16896\n\t"
        "s_waitcnt lgkmcnt(7)\n\t"
        "v_mfma_f32_16x16x32_bf16 v[24:27], v[40:43], v[48:51], v[24:27]\n\t"
        "ds_read_b128 v[40:43], v14 offset:13824\n\t"
        "s_waitcnt lgkmcnt(7)\n\t"
        "v_mfma_f32_16x16x32_bf16 v[28:31], v[96:99], v[48:51], v[28:31]\n\t"
        "s_waitcnt vmcnt(7)\n\t"
        "v_bitop3_b32 v17, v52, v54, %[slay] bitop3:0x7e\n\t"
        "v_and_b32 v17, 1, v17\n\t"
        "v_cmp_eq_u32 vcc, 1, v17\n\t"
        "ds_read_b128 v[96:99], v18 offset:18432\n\t"
        "s_waitcnt lgkmcnt(7)\n\t"
        "v_mfma_f32_16x16x32_bf16 v[32:35], v[76:79], v[48:51], v[32:35]\n\t"
        "ds_read_b128 v[76:79], v18 offset:19456\n\t"
        "s_waitcnt lgkmcnt(7)\n\t"
        "v_mfma_f32_16x16x32_bf16 v[36:39], v[80:83], v[48:51], v[36:39]\n\t"
        "s_cbranch_vccnz Lx3_%=\n\t"
        "Ld3_%=:\n\t"
        "ds_read_b128 v[80:83], v18 offset:20480\n\t"
        "ds_read_b128 v[48:51], v18 offset:21504\n\t"
        "s_waitcnt lgkmcnt(8)\n\t"
        "v_mfma_f32_16x16x32_bf16 v[20:23], v[84:87], v[52:55], v[20:23]\n\t"
        "ds_read_b128 v[84:87], v14 offset:18432\n\t"
        "s_waitcnt lgkmcnt(8)\n\t"
        "v_mfma_f32_16x16x32_bf16 v[24:27], v[88:91], v[52:55], v[24:27]\n\t"
        "v_add_u32 v18, %[sl1d], v18\n\t"
        "v_add_u32 v14, %[sl1d], v14\n\t"
        "ds_read_b128 v[88:91], v18 offset:0\n\t"
        "s_waitcnt lgkmcnt(8)\n\t"
        "v_mfma_f32_16x16x32_bf16 v[28:31], v[44:47], v[52:55], v[28:31]\n\t"
        "s_waitcnt vmcnt(6)\n\t"
        "v_bitop3_b32 v17, v56, v58, %[slay] bitop3:0x7e\n\t"
        "v_and_b32 v17, 1, v17\n\t"
        "v_cmp_eq_u32 vcc, 1, v17\n\t"
        "ds_read_b128 v[44:47], v18 offset:1024\n\t"
        "s_waitcnt lgkmcnt(8)\n\t"
        "v_mfma_f32_16x16x32_bf16 v[32:35], v[92:95], v[52:55], v[32:35]\n\t"
        "ds_read_b128 v[92:95], v18 offset:2048\n\t"
        "s_waitcnt lgkmcnt(8)\n\t"
        "v_mfma_f32_16x16x32_bf16 v[36:39], v[40:43], v[52:55], v[36:39]\n\t"
        "s_cbranch_vccnz Lx4_%=\n\t"
        "Ld4_%=:\n\t"
        "ds_read_b128 v[40:43], v18 offset:3072\n\t"
        "s_waitcnt lgkmcnt(8)\n\t"
        "v_mfma_f32_16x16x32_bf16 v[20:23], v[96:99], v[56:59], v[20:23]\n\t"
        "ds_read_b128 v[52:55], v14 offset:0\n\t"
        "s_waitcnt lgkmcnt(8)\n\t"
        "v_mfma_f32_16x16x32_bf16 v[24:27], v[76:79], v[56:59], v[24:27]\n\t"
        "ds_read_b128 v[96:99], v18 offset:4608\n\t"
        "s_waitcnt lgkmcnt(8)\n\t"
        "v_mfma_f32_16x16x32_bf16 v[28:31], v[80:83], v[56:59], v[28:31]\n\t"
        "s_waitcnt vmcnt(5)\n\t"
        "v_bitop3_b32 v17, v60, v62, %[slay] bitop3:0x7e\n\t"
        "v_and_b32 v17, 1, v17\n\t"
        "v_cmp_eq_u32 vcc, 1, v17\n\t"
        "ds_read_b128 v[76:79], v18 offset:5632\n\t"
        "s_waitcnt lgkmcnt(8)\n\t"
        "v_mfma_f32_16x16x32_bf16 v[32:35], v[48:51], v[56:59], v[32:35]\n\t"
        "ds_read_b128 v[80:83], v18 offset:6656\n\t"
        "s_waitcnt lgkmcnt(8)\n\t"
        "v_mfma_f32_16x16x32_bf16 v[36:39], v[84:87], v[56:59], v[36:39]\n\t"
        "s_cbranch_vccnz Lx5_%=\n\t"
        "Ld5_%=:\n\t"
        "ds_read_b128 v[48:51], v18 offset:7680\n\t"
        "s_waitcnt lgkmcnt(8)\n\t"
        "v_mfma_f32_16x16x32_bf16 v[20:23], v[88:91], v[60:63], v[20:23]\n\t"
        "ds_read_b128 v[84:87], v14 offset:4608\n\t"
        "s_waitcnt lgkmcnt(8)\n\t"
        "v_mfma_f32_16x16x32_bf16 v[24:27], v[44:47], v[60:63], v[24:27]\n\t"
        "ds_read_b128 v[56:59], v18 offset:9216\n\t"
        "s_waitcnt lgkmcnt(8)\n\t"
        "v_mfma_f32_16x16x32_bf16 v[28:31], v[92:95], v[60:63], v[28:31]\n\t"
        "s_waitcnt vmcnt(4)\n\t"
        "v_bitop3_b32 v17, v64, v66, %[slay] bitop3:0x7e\n\t"
        "v_and_b32 v17, 1, v17\n\t"
        "v_cmp_eq_u32 vcc, 1, v17\n\t"
        "ds_read_b128 v[88:91], v18 offset:10240\n\t"
        "s_waitcnt lgkmcnt(8)\n\t"
        "v_mfma_f32_16x16x32_bf16 v[32:35], v[40:43], v[60:63], v[32:35]\n\t"
        "ds_read_b128 v[44:47], v18 offset:11264\n\t"
        "s_waitcnt lgkmcnt(8)\n\t"
        "v_mfma_f32_16x16x32_bf16 v[36:39], v[52:55], v[60:63], v[36:39]\n\t"
        "s_cbranch_vccnz Lx6_%=\n\t"
        "Ld6_%=:\n\t"
        "ds_read_b128 v[92:95], v18 offset:12288\n\t"
        "s_waitcnt lgkmcnt(8)\n\t"
        "v_mfma_f32_16x16x32_bf16 v[20:23], v[96:99], v[64:67], v[20:23]\n\t"
        "ds_read_b128 v[40:43], v14 offset:9216\n\t"
        "s_waitcnt lgkmcnt(8)\n\t"
        "v_mfma_f32_16x16x32_bf16 v[24:27], v[76:79], v[64:67], v[24:27]\n\t"
        "ds_read_b128 v[52:55], v18 offset:13824\n\t"
        "s_waitcnt lgkmcnt(8)\n\t"
        "v_mfma_f32_16x16x32_bf16 v[28:31], v[80:83], v[64:67], v[28:31]\n\t"
        "s_waitcnt vmcnt(2)\n\t"
        "v_bitop3_b32 v17, v68, v70, %[slay] bitop3:0x7e\n\t"
        "v_and_b32 v17, 1, v17\n\t"
        "v_cmp_eq_u32 vcc, 1, v17\n\t"
        "ds_read_b128 v[60:63], v18 offset:14848\n\t"
        "s_waitcnt lgkmcnt(8)\n\t"
        "v_mfma_f32_16x16x32_bf16 v[32:35], v[48:51], v[64:67], v[32:35]\n\t"
        "ds_read_b128 v[96:99], v18 offset:15872\n\t"
        "s_waitcnt lgkmcnt(8)\n\t"
        "v_mfma_f32_16x16x32_bf16 v[36:39], v[84:87], v[64:67], v[36:39]\n\t"
        "s_cbranch_vccnz Lx7_%=\n\t"
        "Ld7_%=:\n\t"
        "ds_read_b128 v[76:79], v18 offset:16896\n\t"
        "s_waitcnt lgkmcnt(8)\n\t"
        "v_mfma_f32_16x16x32_bf16 v[20:23], v[56:59], v[68:71], v[20:23]\n\t"
        "ds_read_b128 v[80:83], v14 offset:13824\n\t"
        "s_waitcnt lgkmcnt(8)\n\t"
        "v_mfma_f32_16x16x32_bf16 v[24:27], v[88:91], v[68:71], v[24:27]\n\t"
        "s_waitcnt lgkmcnt(7)\n\t"
        "v_mfma_f32_16x16x32_bf16 v[28:31], v[44:47], v[68:71], v[28:31]\n\t"
        "s_waitcnt vmcnt(0)\n\t"
        "v_bitop3_b32 v17, v72, v74, %[slay] bitop3:0x7e\n\t"
        "v_and_b32 v17, 1, v17\n\t"
        "v_cmp_eq_u32 vcc, 1, v17\n\t"
        "s_waitcnt lgkmcnt(6)\n\t"
        "v_mfma_f32_16x16x32_bf16 v[32:35], v[92:95], v[68:71], v[32:35]\n\t"
        "s_waitcnt lgkmcnt(5)\n\t"
        "v_mfma_f32_16x16x32_bf16 v[36:39], v[40:43], v[68:71], v[36:39]\n\t"
        "s_cbranch_vccnz Lx8_%=\n\t"
        "Ld8_%=:\n\t"
        "s_waitcnt lgkmcnt(4)\n\t"
        "v_mfma_f32_16x16x32_bf16 v[20:23], v[52:55], v[72:75], v[20:23]\n\t"
        "s_waitcnt lgkmcnt(3)\n\t"
        "v_mfma_f32_16x16x32_bf16 v[24:27], v[60:63], v[72:75], v[24:27]\n\t"
        "v_add_u32 v18, %[sscr], v107\n\t"
        "v_mov_b32 v15, 153984\n\t"
        "s_cmp_eq_u32 %[smsk], 0\n\t"
        "s_waitcnt lgkmcnt(2)\n\t"
        "v_mfma_f32_16x16x32_bf16 v[28:31], v[96:99], v[72:75], v[28:31]\n\t"
        "s_waitcnt lgkmcnt(1)\n\t"
        "v_mfma_f32_16x16x32_bf16 v[32:35], v[76:79], v[72:75], v[32:35]\n\t"
        "s_waitcnt lgkmcnt(0)\n\t"
        "v_mfma_f32_16x16x32_bf16 v[36:39], v[80:83], v[72:75], v[36:39]\n\t"
        "s_cbranch_scc0 Lk0_%=\n\t"
        "v_mov_b32 v16, 1\n\t"
        "ds_write_b128 v18, v[20:23] offset:0\n\t"
        "ds_write_b128 v18, v[24:27] offset:1024\n\t"
        "ds_write_b128 v18, v[28:31] offset:2048\n\t"
        "s_nop 0\n\t"
        "ds_write_b128 v18, v[32:35] offset:3072\n\t"
        "s_mov_b32 exec_hi, 0\n\t"
        "ds_write_b128 v18, v[36:39] offset:4096\n\t"
        "s_mov_b32 exec_lo, 1\n\t"
        "ds_add_u32 v15, v16 offset:%[opc]\n\t"
        "s_mov_b64 exec, -1\n\t"
        "s_branch Lend_%=\n\t"
        "Lx0_%=:\n\t"
        "s_cmp_eq_u32 %[scn], 0\n\t"
        "s_cbranch_scc1 Ld0_%=\n\t"
        "s_sub_u32 %[scn], %[scn], 1\n\t"
        "buffer_load_dwordx4 v[40:43], v107, %[ssrd], %[sob] offen offset:0 sc1\n\t"
        "buffer_load_dwordx4 v[44:47], v107, %[ssrd], %[sob] offen offset:1024 sc1\n\t"
        "buffer_load_dwordx4 v[48:51], v107, %[ssrd], %[sob] offen offset:2048 sc1\n\t"
        "buffer_load_dwordx4 v[52:55], v107, %[ssrd], %[sob] offen offset:3072 sc1\n\t"
        "buffer_load_dwordx4 v[56:59], v107, %[ssrd], %[sob4] offen offset:0 sc1\n\t"
        "buffer_load_dwordx4 v[60:63], v107, %[ssrd], %[sob4] offen offset:1024 sc1\n\t"
        "buffer_load_dwordx4 v[64:67], v107, %[ssrd], %[sob4] offen offset:2048 sc1\n\t"
        "buffer_load_dwordx2 v[68:69], v108, %[ssrd], %[sob] offen offset:0 sc1\n\t"
        "buffer_load_dwordx2 v[70:71], v19, %[ssrd], %[sob] offen offset:0 sc1\n\t"
        "buffer_load_dwordx2 v[72:73], v108, %[ssrd], %[sob] offen offset:1024 sc1\n\t"
        "buffer_load_dwordx2 v[74:75], v19, %[ssrd], %[sob] offen offset:1024 sc1\n\t"
        "s_waitcnt vmcnt(10)\n\t"
        "v_bitop3_b32 v17, v40, v42, %[slay] bitop3:0x7e\n\t"
        "v_and_b32 v17, 1, v17\n\t"
        "v_cmp_eq_u32 vcc, 1, v17\n\t"
        "s_cbranch_vccnz Lx0_%=\n\t"
        "s_branch Ld0_%=\n\t"
        "Lx1_%=:\n\t"
        "s_cmp_eq_u32 %[scn], 0\n\t"
        "s_cbranch_scc1 Ld1_%=\n\t"
        "s_sub_u32 %[scn], %[scn], 1\n\t"
        "buffer_load_dwordx4 v[44:47], v107, %[ssrd], %[sob] offen offset:1024 sc1\n\t"
        "buffer_load_dwordx4 v[48:51], v107, %[ssrd], %[sob] offen offset:2048 sc1\n\t"
        "buffer_load_dwordx4 v[52:55], v107, %[ssrd], %[sob] offen offset:3072 sc1\n\t"
        "buffer_load_dwordx4 v[56:59], v107, %[ssrd], %[sob4] offen offset:0 sc1\n\t"
        "buffer_load_dwordx4 v[60:63], v107, %[ssrd], %[sob4] offen offset:1024 sc1\n\t"
        "buffer_load_dwordx4 v[64:67], v107, %[ssrd], %[sob4] offen offset:2048 sc1\n\t"
        "buffer_load_dwordx2 v[68:69], v108, %[ssrd], %[sob] offen offset:0 sc1\n\t"
        "buffer_load_dwordx2 v[70:71], v19, %[ssrd], %[sob] offen offset:0 sc1\n\t"
        "buffer_load_dwordx2 v[72:73], v108, %[ssrd], %[sob] offen offset:1024 sc1\n\t"
        "buffer_load_dwordx2 v[74:75], v19, %[ssrd], %[sob] offen offset:1024 sc1\n\t"
        "s_waitcnt vmcnt(9)\n\t"
        "v_bitop3_b32 v17, v44, v46, %[slay] bitop3:0x7e\n\t"
        "v_and_b32 v17, 1, v17\n\t"
        "v_cmp_eq_u32 vcc, 1, v17\n\t"
        "s_cbranch_vccnz Lx1_%=\n\t"
        "s_branch Ld1_%=\n\t"
        "Lx2_%=:\n\t"
        "s_cmp_eq_u32 %[scn], 0\n\t"
        "s_cbranch_scc1 Ld2_%=\n\t"
        "s_sub_u32 %[scn], %[scn], 1\n\t"
        "buffer_load_dwordx4 v[48:51], v107, %[ssrd], %[sob] offen offset:2048 sc1\n\t"
        "buffer_load_dwordx4 v[52:55], v107, %[ssrd], %[sob] offen offset:3072 sc1\n\t"
        "buffer_load_dwordx4 v[56:59], v107, %[ssrd], %[sob4] offen offset:0 sc1\n\t"
        "buffer_load_dwordx4 v[60:63], v107, %[ssrd], %[sob4] offen offset:1024 sc1\n\t"
        "buffer_load_dwordx4 v[64:67], v107, %[ssrd], %[sob4] offen offset:2048 sc1\n\t"
        "buffer_load_dwordx2 v[68:69], v108, %[ssrd], %[sob] offen offset:0 sc1\n\t"
        "buffer_load_dwordx2 v[70:71], v19, %[ssrd], %[sob] offen offset:0 sc1\n\t"
        "buffer_load_dwordx2 v[72:73], v108, %[ssrd], %[sob] offen offset:1024 sc1\n\t"
        "buffer_load_dwordx2 v[74:75], v19, %[ssrd], %[sob] offen offset:1024 sc1\n\t"
        "s_waitcnt vmcnt(8)\n\t"
        "v_bitop3_b32 v17, v48, v50, %[slay] bitop3:0x7e\n\t"
        "v_and_b32 v17, 1, v17\n\t"
        "v_cmp_eq_u32 vcc, 1, v17\n\t"
        "s_cbranch_vccnz Lx2_%=\n\t"
        "s_branch Ld2_%=\n\t"
        "Lx3_%=:\n\t"
        "s_cmp_eq_u32 %[scn], 0\n\t"
        "s_cbranch_scc1 Ld3_%=\n\t"
        "s_sub_u32 %[scn], %[scn], 1\n\t"
        "buffer_load_dwordx4 v[52:55], v107, %[ssrd], %[sob] offen offset:3072 sc1\n\t"
        "buffer_load_dwordx4 v[56:59], v107, %[ssrd], %[sob4] offen offset:0 sc1\n\t"
        "buffer_load_dwordx4 v[60:63], v107, %[ssrd], %[sob4] offen offset:1024 sc1\n\t"
        "buffer_load_dwordx4 v[64:67], v107, %[ssrd], %[sob4] offen offset:2048 sc1\n\t"
        "buffer_load_dwordx2 v[68:69], v108, %[ssrd], %[sob] offen offset:0 sc1\n\t"
        "buffer_load_dwordx2 v[70:71], v19, %[ssrd], %[sob] offen offset:0 sc1\n\t"
        "buffer_load_dwordx2 v[72:73], v108, %[ssrd], %[sob] offen offset:1024 sc1\n\t"
        "buffer_load_dwordx2 v[74:75], v19, %[ssrd], %[sob] offen offset:1024 sc1\n\t"
        "s_waitcnt vmcnt(7)\n\t"
        "v_bitop3_b32 v17, v52, v54, %[slay] bitop3:0x7e\n\t"
        "v_and_b32 v17, 1, v17\n\t"
        "v_cmp_eq_u32 vcc, 1, v17\n\t"
        "s_cbranch_vccnz Lx3_%=\n\t"
        "s_branch Ld3_%=\n\t"
        "Lx4_%=:\n\t"
        "s_cmp_eq_u32 %[scn], 0\n\t"
        "s_cbranch_scc1 Ld4_%=\n\t"
        "s_sub_u32 %[scn], %[scn], 1\n\t"
        "buffer_load_dwordx4 v[56:59], v107, %[ssrd], %[sob4] offen offset:0 sc1\n\t"
        "buffer_load_dwordx4 v[60:63], v107, %[ssrd], %[sob4] offen offset:1024 sc1\n\t"
        "buffer_load_dwordx4 v[64:67], v107, %[ssrd], %[sob4] offen offset:2048 sc1\n\t"
        "buffer_load_dwordx2 v[68:69], v108, %[ssrd], %[sob] offen offset:0 sc1\n\t"
        "buffer_load_dwordx2 v[70:71], v19, %[ssrd], %[sob] offen offset:0 sc1\n\t"
        "buffer_load_dwordx2 v[72:73], v108, %[ssrd], %[sob] offen offset:1024 sc1\n\t"
        "buffer_load_dwordx2 v[74:75], v19, %[ssrd], %[sob] offen offset:1024 sc1\n\t"
        "s_waitcnt vmcnt(6)\n\t"
        "v_bitop3_b32 v17, v56, v58, %[slay] bitop3:0x7e\n\t"
        "v_and_b32 v17, 1, v17\n\t"
        "v_cmp_eq_u32 vcc, 1, v17\n\t"
        "s_cbranch_vccnz Lx4_%=\n\t"
        "s_branch Ld4_%=\n\t"
        "Lx5_%=:\n\t"
        "s_cmp_eq_u32 %[scn], 0\n\t"
        "s_cbranch_scc1 Ld5_%=\n\t"
        "s_sub_u32 %[scn], %[scn], 1\n\t"
        "buffer_load_dwordx4 v[60:63], v107, %[ssrd], %[sob4] offen offset:1024 sc1\n\t"
        "buffer_load_dwordx4 v[64:67], v107, %[ssrd], %[sob4] offen offset:2048 sc1\n\t"
        "buffer_load_dwordx2 v[68:69], v108, %[ssrd], %[sob] offen offset:0 sc1\n\t"
        "buffer_load_dwordx2 v[70:71], v19, %[ssrd], %[sob] offen offset:0 sc1\n\t"
        "buffer_load_dwordx2 v[72:73], v108, %[ssrd], %[sob] offen offset:1024 sc1\n\t"
        "buffer_load_dwordx2 v[74:75], v19, %[ssrd], %[sob] offen offset:1024 sc1\n\t"
        "s_waitcnt vmcnt(5)\n\t"
        "v_bitop3_b32 v17, v60, v62, %[slay] bitop3:0x7e\n\t"
        "v_and_b32 v17, 1, v17\n\t"
        "v_cmp_eq_u32 vcc, 1, v17\n\t"
        "s_cbranch_vccnz Lx5_%=\n\t"
        "s_branch Ld5_%=\n\t"
        "Lx6_%=:\n\t"
        "s_cmp_eq_u32 %[scn], 0\n\t"
        "s_cbranch_scc1 Ld6_%=\n\t"
        "s_sub_u32 %[scn], %[scn], 1\n\t"
        "buffer_load_dwordx4 v[64:67], v107, %[ssrd], %[sob4] offen offset:2048 sc1\n\t"
        "buffer_load_dwordx2 v[68:69], v108, %[ssrd], %[sob] offen offset:0 sc1\n\t"
        "buffer_load_dwordx2 v[70:71], v19, %[ssrd], %[sob] offen offset:0 sc1\n\t"
        "buffer_load_dwordx2 v[72:73], v108, %[ssrd], %[sob] offen offset:1024 sc1\n\t"
        "buffer_load_dwordx2 v[74:75], v19, %[ssrd], %[sob] offen offset:1024 sc1\n\t"
        "s_waitcnt vmcnt(4)\n\t"
        "v_bitop3_b32 v17, v64, v66, %[slay] bitop3:0x7e\n\t"
        "v_and_b32 v17, 1, v17\n\t"
        "v_cmp_eq_u32 vcc, 1, v17\n\t"
        "s_cbranch_vccnz Lx6_%=\n\t"
        "s_branch Ld6_%=\n\t"
        "Lx7_%=:\n\t"
        "s_cmp_eq_u32 %[scn], 0\n\t"
        "s_cbranch_scc1 Ld7_%=\n\t"
        "s_sub_u32 %[scn], %[scn], 1\n\t"
        "buffer_load_dwordx2 v[68:69], v108, %[ssrd], %[sob] offen offset:0 sc1\n\t"
        "buffer_load_dwordx2 v[70:71], v19, %[ssrd], %[sob] offen offset:0 sc1\n\t"
        "buffer_load_dwordx2 v[72:73], v108, %[ssrd], %[sob] offen offset:1024 sc1\n\t"
        "buffer_load_dwordx2 v[74:75], v19, %[ssrd], %[sob] offen offset:1024 sc1\n\t"
        "s_waitcnt vmcnt(2)\n\t"
        "v_bitop3_b32 v17, v68, v70, %[slay] bitop3:0x7e\n\t"
        "v_and_b32 v17, 1, v17\n\t"
        "v_cmp_eq_u32 vcc, 1, v17\n\t"
        "s_cbranch_vccnz Lx7_%=\n\t"
        "s_branch Ld7_%=\n\t"
        "Lx8_%=:\n\t"
        "s_cmp_eq_u32 %[scn], 0\n\t"
        "s_cbranch_scc1 Ld8_%=\n\t"
        "s_sub_u32 %[scn], %[scn], 1\n\t"
        "buffer_load_dwordx2 v[72:73], v108, %[ssrd], %[sob] offen offset:1024 sc1\n\t"
        "buffer_load_dwordx2 v[74:75], v19, %[ssrd], %[sob] offen offset:1024 sc1\n\t"
        "s_waitcnt vmcnt(0)\n\t"
        "v_bitop3_b32 v17, v72, v74, %[slay] bitop3:0x7e\n\t"
        "v_and_b32 v17, 1, v17\n\t"
        "v_cmp_eq_u32 vcc, 1, v17\n\t"
        "s_cbranch_vccnz Lx8_%=\n\t"
        "s_branch Ld8_%=\n\t"
        "Lws_%=:\n\t"
        "s_cmp_eq_u32 %[scn], 0\n\t"
        "s_cbranch_scc1 Lwg_%=\n\t"
        "s_sub_u32 %[scn], %[scn], 1\n\t"
        "s_sleep 1\n\t"
        "ds_read_b32 v16, v15\n\t"
        "s_waitcnt lgkmcnt(0)\n\t"
        "s_branch Lwc_%=\n\t"
        "Lk0_%=:\n\t"
        "s_waitcnt vmcnt(0)\n\t"
        "Lp_%=:\n\t"
        "ds_read_b32 v16, v15 offset:%[opc]\n\t"
        "ds_read_b128 v[52:55], v18 offset:0\n\t"
        "ds_read_b128 v[56:59], v18 offset:1024\n\t"
        "ds_read_b128 v[60:63], v18 offset:2048\n\t"
        "ds_read_b128 v[64:67], v18 offset:3072\n\t"
        "ds_read_b128 v[68:71], v18 offset:4096\n\t"
        "v_lshrrev_b32 v19, 8, v107\n\t"
        "v_lshl_add_u32 v19, v19, 4, %[swr]\n\t"
        "v_mov_b32 v17, 163424\n\t"
        "v_cndmask_b32 v19, v17, v19, %[sl1]\n\t"
        "ds_read_b128 v[40:43], v19 offset:0\n\t"
        "ds_read_b128 v[44:47], v19 offset:64\n\t"
        "ds_read_b128 v[48:51], v19 offset:128\n\t"
        "v_mov_b32 v98, 0\n\t"
        "v_mov_b32 v99, 0\n\t"
        "v_and_b32 v84, 0xf0, v107\n\t"
        "v_mov_b32 v83, 0\n\t"
        "s_waitcnt lgkmcnt(8)\n\t"
        "v_readfirstlane_b32 %[sob4x], v16\n\t"
        "s_cmp_ge_u32 %[sob4x], %[swt]\n\t"
        "s_cbranch_scc1 Lq_%=\n\t"
        "s_cmp_eq_u32 %[scn], 0\n\t"
        "s_cbranch_scc1 Lq_%=\n\t"
        "s_sub_u32 %[scn], %[scn], 1\n\t"
        "s_waitcnt lgkmcnt(0)\n\t"
        "s_branch Lp_%=\n\t"
        "Lq_%=:\n\t"
        "s_waitcnt lgkmcnt(6)\n\t"
        "v_add_f32 v20, v20, v52\n\t"
        "v_add_f32 v21, v21, v53\n\t"
        "v_add_f32 v22, v22, v54\n\t"
        "v_add_f32 v23, v23, v55\n\t"
        "v_add_f32 v24, v24, v56\n\t"
        "v_add_f32 v25, v25, v57\n\t"
        "v_add_f32 v26, v26, v58\n\t"
        "v_add_f32 v27, v27, v59\n\t"
        "v_cvt_pk_bf16_f32 v88, v20, v24\n\t"
        "v_cvt_pk_bf16_f32 v89, v21, v25\n\t"
        "v_cvt_pk_bf16_f32 v90, v22, v26\n\t"
        "v_cvt_pk_bf16_f32 v91, v23, v27\n\t"
        "s_waitcnt lgkmcnt(4)\n\t"
        "v_add_f32 v28, v28, v60\n\t"
        "v_add_f32 v29, v29, v61\n\t"
        "v_add_f32 v30, v30, v62\n\t"
        "v_add_f32 v31, v31, v63\n\t"
        "v_add_f32 v32, v32, v64\n\t"
        "v_add_f32 v33, v33, v65\n\t"
        "v_add_f32 v34, v34, v66\n\t"
        "v_add_f32 v35, v35, v67\n\t"
        "v_cvt_pk_bf16_f32 v92, v28, v32\n\t"
        "v_cvt_pk_bf16_f32 v93, v29, v33\n\t"
        "v_cvt_pk_bf16_f32 v94, v30, v34\n\t"
        "v_cvt_pk_bf16_f32 v95, v31, v35\n\t"
        "s_waitcnt lgkmcnt(0)\n\t"
        "v_mfma_f32_16x16x32_bf16 v[72:75], v[40:43], v[88:91], 0\n\t"
        "v_mfma_f32_16x16x32_bf16 v[76:79], v[88:91], v[88:91], 0\n\t"
        "s_waitcnt lgkmcnt(3)\n\t"
        "v_add_f32 v36, v36, v68\n\t"
        "v_add_f32 v37, v37, v69\n\t"
        "v_add_f32 v38, v38, v70\n\t"
        "v_add_f32 v39, v39, v71\n\t"
        "v_cvt_pk_bf16_f32 v96, v36, v37\n\t"
        "v_cvt_pk_bf16_f32 v97, v38, v39\n\t"
        "s_mov_b32 exec_lo, 0\n\t"
        "v_mov_b32 v96, 0\n\t"
        "v_mov_b32 v97, 0\n\t"
        "s_mov_b32 exec_lo, -1\n\t"
        "v_mfma_f32_16x16x32_bf16 v[72:75], v[44:47], v[92:95], v[72:75]\n\t"
        "v_mfma_f32_16x16x32_bf16 v[76:79], v[92:95], v[92:95], v[76:79]\n\t"
        "v_mfma_f32_16x16x32_bf16 v[72:75], v[48:51], v[96:99], v[72:75]\n\t"
        "v_mfma_f32_16x16x32_bf16 v[76:79], v[96:99], v[96:99], v[76:79]\n\t"
        "v_and_b32 v17, 0x300, v107\n\t"
        "v_lshrrev_b32 v19, 5, v17\n\t"
        "v_lshl_add_u32 v17, v17, 2, v84\n\t"
        "v_add_u32 v19, v19, v84\n\t"
        "s_add_u32 %[sob4], %[sx1], 4096\n\t"
        "s_add_u32 %[sob4x], %[sx1], 8192\n\t"
        "s_cmp_lg_u32 %[slay], 0\n\t"
        "v_min_u32 v80, -2, v72\n\t"
        "v_min_u32 v82, -2, v73\n\t"
        "v_cndmask_b32 v76, v76, v77, %[spar]\n\t"
        "v_cndmask_b32 v78, v78, v79, %[spar]\n\t"
        "v_cndmask_b32 v76, v76, v78, %[sl2]\n\t"
        "v_min_u32 v81, -2, v76\n\t"
        "s_mov_b64 exec, %[sdiag]\n\t"
        "buffer_store_dwordx4 v[80:83], v84, %[ssrd], %[sstat] offen sc1\n\t"
        "s_mov_b64 exec, -1\n\t"
        "s_cbranch_scc1 Lpk_%=\n\t"
        "v_min_u32 v88, -2, v88\n\t"
        "v_min_u32 v89, -2, v89\n\t"
        "v_min_u32 v90, -2, v90\n\t"
        "v_min_u32 v91, -2, v91\n\t"
        "buffer_store_dwordx4 v[88:91], v17, %[ssrd], %[sx1] offen sc1\n\t"
        "v_min_u32 v92, -2, v92\n\t"
        "v_min_u32 v93, -2, v93\n\t"
        "v_min_u32 v94, -2, v94\n\t"
        "v_min_u32 v95, -2, v95\n\t"
        "v_min_u32 v96, -2, v96\n\t"
        "v_min_u32 v97, -2, v97\n\t"
        "buffer_store_dwordx4 v[92:95], v17, %[ssrd], %[sob4] offen sc1\n\t"
        "s_mov_b32 exec_hi, 0\n\t"
        "buffer_store_dwordx2 v[96:97], v19, %[ssrd], %[sob4x] offen sc1\n\t"
        "s_mov_b32 exec_hi, -1\n\t"
        "Lpk_%=:\n\t"
        "v_add_u32 v18, %[spk], v107\n\t"
        "v_add_u32 v19, %[spk], v19\n\t"
        "v_mov_b32 v16, 1\n\t"
        "ds_write_b128 v18, v[88:91]\n\t"
        "ds_write_b128 v18, v[92:95] offset:1024\n\t"
        "s_mov_b32 exec_hi, 0\n\t"
        "ds_write_b64 v19, v[96:97] offset:2048\n\t"
        "s_mov_b32 exec_lo, 1\n\t"
        "ds_add_u32 v15, v16 offset:%[opk]\n\t"
        "s_mov_b64 exec, -1\n\t"
        "Lend_%=:\n\t"
        "s_setprio 0\n\t"
        : [sob4] "=&s"(ob4), [sob4x] "=&s"(ob4x), [scn] "+s"(cn)
        : [ssrd] "s"(a.srd), [sob] "s"(a.ob), [sl1d] "s"(a.l1d), [stvd] "s"(a.tvd), [swi] "s"(a.wi), [sld] "s"(a.ld),
          [smsk] "s"(a.msk), [sscr] "s"(a.scr), [sstat] "s"(a.stat), [sx1] "s"(a.x1), [slay] "s"(a.layer), [spk] "s"(a.pk), [sn7] "s"(a.n7), [sskip] "s"(a.skip), [swc] "s"(a.wc), [swt] "s"(4 * a.layer + 4), [slb] "s"(a.lb), [opc] "n"(BC_PC), [opk] "n"(BC_PK), [swr] "s"(163232),
          [spar] "s"(0xAAAAAAAAAAAAAAAAull), [sl2] "s"(0xCCCCCCCCCCCCCCCCull), [sl1] "s"(0x2222222222222222ull), [sdiag] "s"(0xf0000f0000f0000full)
        : "memory", "scc", "vcc", "v14", "v15", "v16", "v17", "v18", "v19", "v20", "v21", "v22", "v23", "v24", "v25", "v26", "v27", "v28", "v29", "v30", "v31", "v32", "v33", "v34", "v35", "v36", "v37", "v38", "v39", "v40", "v41", "v42", "v43", "v44", "v45", "v46", "v47");
}

DEV void dload(u32x4 srd, u32 a2, u32 so) {
    asm volatile(
        "s_nop 4\n\t"
        "buffer_load_dwordx4 v[112:115], v107, %[ssrd], %[so] offen offset:0" NTD "\n\t"
        "buffer_load_dwordx4 v[116:119], v107, %[ssrd], %[so] offen offset:1152" NTD "\n\t"
        "buffer_load_dwordx4 v[120:123], v107, %[ssrd], %[so] offen offset:2304" NTD "\n\t"
        "buffer_load_ushort v220, %[a2], %[ssrd], %[so] offen offset:1024" NTD "\n\t"
        "buffer_load_ushort v221, %[a2], %[ssrd], %[so] offen offset:2176" NTD "\n\t"
        "buffer_load_ushort v222, %[a2], %[ssrd], %[so] offen offset:3328" NTD "\n\t"
        "s_add_u32 %[so], %[so], 3456\n\t"
        "buffer_load_dwordx4 v[124:127], v107, %[ssrd], %[so] offen offset:0" NTD "\n\t"
        "buffer_load_dwordx4 v[128:131], v107, %[ssrd], %[so] offen offset:1152" NTD "\n\t"
        "buffer_load_dwordx4 v[132:135], v107, %[ssrd], %[so] offen offset:2304" NTD "\n\t"
        "buffer_load_ushort v223, %[a2], %[ssrd], %[so] offen offset:1024" NTD "\n\t"
        "buffer_load_ushort v224, %[a2], %[ssrd], %[so] offen offset:2176" NTD "\n\t"
        "buffer_load_ushort v225, %[a2], %[ssrd], %[so] offen offset:3328" NTD "\n\t"
        "s_add_u32 %[so], %[so], 3456\n\t"
        "buffer_load_dwordx4 v[136:139], v107, %[ssrd], %[so] offen offset:0" NTD "\n\t"
        "buffer_load_dwordx4 v[140:143], v107, %[ssrd], %[so] offen offset:1152" NTD "\n\t"
        "buffer_load_dwordx4 v[144:147], v107, %[ssrd], %[so] offen offset:2304" NTD "\n\t"
        "buffer_load_ushort v226, %[a2], %[ssrd], %[so] offen offset:1024" NTD "\n\t"
        "buffer_load_ushort v227, %[a2], %[ssrd], %[so] offen offset:2176" NTD "\n\t"
        "buffer_load_ushort v228, %[a2], %[ssrd], %[so] offen offset:3328" NTD "\n\t"
        "s_add_u32 %[so], %[so], 3456\n\t"
        "buffer_load_dwordx4 v[148:151], v107, %[ssrd], %[so] offen offset:0" NTD "\n\t"
        "buffer_load_dwordx4 v[152:155], v107, %[ssrd], %[so] offen offset:1152" NTD "\n\t"
        "buffer_load_dwordx4 v[156:159], v107, %[ssrd], %[so] offen offset:2304" NTD "\n\t"
        "buffer_load_ushort v229, %[a2], %[ssrd], %[so] offen offset:1024" NTD "\n\t"
        "buffer_load_ushort v230, %[a2], %[ssrd], %[so] offen offset:2176" NTD "\n\t"
        "buffer_load_ushort v231, %[a2], %[ssrd], %[so] offen offset:3328" NTD "\n\t"
        "s_add_u32 %[so], %[so], 3456\n\t"
        "buffer_load_dwordx4 v[160:163], v107, %[ssrd], %[so] offen offset:0" NTD "\n\t"
        "buffer_load_dwordx4 v[164:167], v107, %[ssrd], %[so] offen offset:1152" NTD "\n\t"
        "buffer_load_dwordx4 v[168:171], v107, %[ssrd], %[so] offen offset:2304" NTD "\n\t"
        "buffer_load_ushort v232, %[a2], %[ssrd], %[so] offen offset:1024" NTD "\n\t"
        "buffer_load_ushort v233, %[a2], %[ssrd], %[so] offen offset:2176" NTD "\n\t"
        "buffer_load_ushort v234, %[a2], %[ssrd], %[so] offen offset:3328" NTD "\n\t"
        "s_add_u32 %[so], %[so], 3456\n\t"
        "buffer_load_dwordx4 v[172:175], v107, %[ssrd], %[so] offen offset:0" NTD "\n\t"
        "buffer_load_dwordx4 v[176:179], v107, %[ssrd], %[so] offen offset:1152" NTD "\n\t"
        "buffer_load_dwordx4 v[180:183], v107, %[ssrd], %[so] offen offset:2304" NTD "\n\t"
        "buffer_load_ushort v235, %[a2], %[ssrd], %[so] offen offset:1024" NTD "\n\t"
        "buffer_load_ushort v236, %[a2], %[ssrd], %[so] offen offset:2176" NTD "\n\t"
        "buffer_load_ushort v237, %[a2], %[ssrd], %[so] offen offset:3328" NTD "\n\t"
        "s_add_u32 %[so], %[so], 3456\n\t"
        "buffer_load_dwordx4 v[184:187], v107, %[ssrd], %[so] offen offset:0" NTD "\n\t"
        "buffer_load_dwordx4 v[188:191], v107, %[ssrd], %[so] offen offset:1152" NTD "\n\t"
        "buffer_load_dwordx4 v[192:195], v107, %[ssrd], %[so] offen offset:2304" NTD "\n\t"
        "buffer_load_ushort v238, %[a2], %[ssrd], %[so] offen offset:1024" NTD "\n\t"
        "buffer_load_ushort v239, %[a2], %[ssrd], %[so] offen offset:2176" NTD "\n\t"
        "buffer_load_ushort v240, %[a2], %[ssrd], %[so] offen offset:3328" NTD "\n\t"
        "s_add_u32 %[so], %[so], 3456\n\t"
        "buffer_load_dwordx4 v[196:199], v107, %[ssrd], %[so] offen offset:0" NTD "\n\t"
        "buffer_load_dwordx4 v[200:203], v107, %[ssrd], %[so] offen offset:1152" NTD "\n\t"
        "buffer_load_dwordx4 v[204:207], v107, %[ssrd], %[so] offen offset:2304" NTD "\n\t"
        "buffer_load_ushort v241, %[a2], %[ssrd], %[so] offen offset:1024" NTD "\n\t"
        "buffer_load_ushort v242, %[a2], %[ssrd], %[so] offen offset:2176" NTD "\n\t"
        "buffer_load_ushort v243, %[a2], %[ssrd], %[so] offen offset:3328" NTD "\n\t"
        "s_add_u32 %[so], %[so], 3456\n\t"
        : [so] "+s"(so) : [ssrd] "s"(srd), [a2] "v"(a2) : "memory", "scc");
}

DEV void dpermx() {
    asm volatile(
        "v_perm_b32 v208, v221, v220, %[slo]\n\t"
        "v_perm_b32 v209, v223, v222, %[slo]\n\t"
        "v_perm_b32 v210, v225, v224, %[slo]\n\t"
        "v_perm_b32 v211, v227, v226, %[slo]\n\t"
        "v_perm_b32 v212, v229, v228, %[slo]\n\t"
        "v_perm_b32 v213, v231, v230, %[slo]\n\t"
        "v_perm_b32 v214, v233, v232, %[slo]\n\t"
        "v_perm_b32 v215, v235, v234, %[slo]\n\t"
        "v_perm_b32 v216, v237, v236, %[slo]\n\t"
        "v_perm_b32 v217, v239, v238, %[slo]\n\t"
        "v_perm_b32 v218, v241, v240, %[slo]\n\t"
        "v_perm_b32 v219, v243, v242, %[slo]\n\t"
        :: [slo] "s"(0x05040100u) : "memory");
}
DEV void dperm() {
    u32 t;
    asm volatile(
        "v_perm_b32 %[t], v116, v112, %[slo]\n\t"
        "v_perm_b32 v116, v116, v112, %[shi]\n\t"
        "v_mov_b32 v112, %[t]\n\t"
        "v_perm_b32 %[t], v117, v113, %[slo]\n\t"
        "v_perm_b32 v117, v117, v113, %[shi]\n\t"
        "v_mov_b32 v113, %[t]\n\t"
        "v_perm_b32 %[t], v118, v114, %[slo]\n\t"
        "v_perm_b32 v118, v118, v114, %[shi]\n\t"
        "v_mov_b32 v114, %[t]\n\t"
        "v_perm_b32 %[t], v119, v115, %[slo]\n\t"
        "v_perm_b32 v119, v119, v115, %[shi]\n\t"
        "v_mov_b32 v115, %[t]\n\t"
        "v_perm_b32 %[t], v124, v120, %[slo]\n\t"
        "v_perm_b32 v124, v124, v120, %[shi]\n\t"
        "v_mov_b32 v120, %[t]\n\t"
        "v_perm_b32 %[t], v125, v121, %[slo]\n\t"
        "v_perm_b32 v125, v125, v121, %[shi]\n\t"
        "v_mov_b32 v121, %[t]\n\t"
        "v_perm_b32 %[t], v126, v122, %[slo]\n\t"
        "v_perm_b32 v126, v126, v122, %[shi]\n\t"
        "v_mov_b32 v122, %[t]\n\t"
        "v_perm_b32 %[t], v127, v123, %[slo]\n\t"
        "v_perm_b32 v127, v127, v123, %[shi]\n\t"
        "v_mov_b32 v123, %[t]\n\t"
        "v_perm_b32 %[t], v132, v128, %[slo]\n\t"
        "v_perm_b32 v132, v132, v128, %[shi]\n\t"
        "v_mov_b32 v128, %[t]\n\t"
        "v_perm_b32 %[t], v133, v129, %[slo]\n\t"
        "v_perm_b32 v133, v133, v129, %[shi]\n\t"
        "v_mov_b32 v129, %[t]\n\t"
        "v_perm_b32 %[t], v134, v130, %[slo]\n\t"
        "v_perm_b32 v134, v134, v130, %[shi]\n\t"
        "v_mov_b32 v130, %[t]\n\t"
        "v_perm_b32 %[t], v135, v131, %[slo]\n\t"
        "v_perm_b32 v135, v135, v131, %[shi]\n\t"
        "v_mov_b32 v131, %[t]\n\t"
        "v_perm_b32 %[t], v140, v136, %[slo]\n\t"
        "v_perm_b32 v140, v140, v136, %[shi]\n\t"
        "v_mov_b32 v136, %[t]\n\t"
        "v_perm_b32 %[t], v141, v137, %[slo]\n\t"
        "v_perm_b32 v141, v141, v137, %[shi]\n\t"
        "v_mov_b32 v137, %[t]\n\t"
        "v_perm_b32 %[t], v142, v138, %[slo]\n\t"
        "v_perm_b32 v142, v142, v138, %[shi]\n\t"
        "v_mov_b32 v138, %[t]\n\t"
        "v_perm_b32 %[t], v143, v139, %[slo]\n\t"
        "v_perm_b32 v143, v143, v139, %[shi]\n\t"
        "v_mov_b32 v139, %[t]\n\t"
        "v_perm_b32 %[t], v148, v144, %[slo]\n\t"
        "v_perm_b32 v148, v148, v144, %[shi]\n\t"
        "v_mov_b32 v144, %[t]\n\t"
        "v_perm_b32 %[t], v149, v145, %[slo]\n\t"
        "v_perm_b32 v149, v149, v145, %[shi]\n\t"
        "v_mov_b32 v145, %[t]\n\t"
        "v_perm_b32 %[t], v150, v146, %[slo]\n\t"
        "v_perm_b32 v150, v150, v146, %[shi]\n\t"
        "v_mov_b32 v146, %[t]\n\t"
        "v_perm_b32 %[t], v151, v147, %[slo]\n\t"
        "v_perm_b32 v151, v151, v147, %[shi]\n\t"
        "v_mov_b32 v147, %[t]\n\t"
        "v_perm_b32 %[t], v156, v152, %[slo]\n\t"
        "v_perm_b32 v156, v156, v152, %[shi]\n\t"
        "v_mov_b32 v152, %[t]\n\t"
        "v_perm_b32 %[t], v157, v153, %[slo]\n\t"
        "v_perm_b32 v157, v157, v153, %[shi]\n\t"
        "v_mov_b32 v153, %[t]\n\t"
        "v_perm_b32 %[t], v158, v154, %[slo]\n\t"
        "v_perm_b32 v158, v158, v154, %[shi]\n\t"
        "v_mov_b32 v154, %[t]\n\t"
        "v_perm_b32 %[t], v159, v155, %[slo]\n\t"
        "v_perm_b32 v159, v159, v155, %[shi]\n\t"
        "v_mov_b32 v155, %[t]\n\t"
        "v_perm_b32 %[t], v164, v160, %[slo]\n\t"
        "v_perm_b32 v164, v164, v160, %[shi]\n\t"
        "v_mov_b32 v160, %[t]\n\t"
        "v_perm_b32 %[t], v165, v161, %[slo]\n\t"
        "v_perm_b32 v165, v165, v161, %[shi]\n\t"
        "v_mov_b32 v161, %[t]\n\t"
        "v_perm_b32 %[t], v166, v162, %[slo]\n\t"
        "v_perm_b32 v166, v166, v162, %[shi]\n\t"
        "v_mov_b32 v162, %[t]\n\t"
        "v_perm_b32 %[t], v167, v163, %[slo]\n\t"
        "v_perm_b32 v167, v167, v163, %[shi]\n\t"
        "v_mov_b32 v163, %[t]\n\t"
        "v_perm_b32 %[t], v172, v168, %[slo]\n\t"
        "v_perm_b32 v172, v172, v168, %[shi]\n\t"
        "v_mov_b32 v168, %[t]\n\t"
        "v_perm_b32 %[t], v173, v169, %[slo]\n\t"
        "v_perm_b32 v173, v173, v169, %[shi]\n\t"
        "v_mov_b32 v169, %[t]\n\t"
        "v_perm_b32 %[t], v174, v170, %[slo]\n\t"
        "v_perm_b32 v174, v174, v170, %[shi]\n\t"
        "v_mov_b32 v170, %[t]\n\t"
        "v_perm_b32 %[t], v175, v171, %[slo]\n\t"
        "v_perm_b32 v175, v175, v171, %[shi]\n\t"
        "v_mov_b32 v171, %[t]\n\t"
        "v_perm_b32 %[t], v180, v176, %[slo]\n\t"
        "v_perm_b32 v180, v180, v176, %[shi]\n\t"
        "v_mov_b32 v176, %[t]\n\t"
        "v_perm_b32 %[t], v181, v177, %[slo]\n\t"
        "v_perm_b32 v181, v181, v177, %[shi]\n\t"
        "v_mov_b32 v177, %[t]\n\t"
        "v_perm_b32 %[t], v182, v178, %[slo]\n\t"
        "v_perm_b32 v182, v182, v178, %[shi]\n\t"
        "v_mov_b32 v178, %[t]\n\t"
        "v_perm_b32 %[t], v183, v179, %[slo]\n\t"
        "v_perm_b32 v183, v183, v179, %[shi]\n\t"
        "v_mov_b32 v179, %[t]\n\t"
        "v_perm_b32 %[t], v188, v184, %[slo]\n\t"
        "v_perm_b32 v188, v188, v184, %[shi]\n\t"
        "v_mov_b32 v184, %[t]\n\t"
        "v_perm_b32 %[t], v189, v185, %[slo]\n\t"
        "v_perm_b32 v189, v189, v185, %[shi]\n\t"
        "v_mov_b32 v185, %[t]\n\t"
        "v_perm_b32 %[t], v190, v186, %[slo]\n\t"
        "v_perm_b32 v190, v190, v186, %[shi]\n\t"
        "v_mov_b32 v186, %[t]\n\t"
        "v_perm_b32 %[t], v191, v187, %[slo]\n\t"
        "v_perm_b32 v191, v191, v187, %[shi]\n\t"
        "v_mov_b32 v187, %[t]\n\t"
        "v_perm_b32 %[t], v196, v192, %[slo]\n\t"
        "v_perm_b32 v196, v196, v192, %[shi]\n\t"
        "v_mov_b32 v192, %[t]\n\t"
        "v_perm_b32 %[t], v197, v193, %[slo]\n\t"
        "v_perm_b32 v197, v197, v193, %[shi]\n\t"
        "v_mov_b32 v193, %[t]\n\t"
        "v_perm_b32 %[t], v198, v194, %[slo]\n\t"
        "v_perm_b32 v198, v198, v194, %[shi]\n\t"
        "v_mov_b32 v194, %[t]\n\t"
        "v_perm_b32 %[t], v199, v195, %[slo]\n\t"
        "v_perm_b32 v199, v199, v195, %[shi]\n\t"
        "v_mov_b32 v195, %[t]\n\t"
        "v_perm_b32 %[t], v204, v200, %[slo]\n\t"
        "v_perm_b32 v204, v204, v200, %[shi]\n\t"
        "v_mov_b32 v200, %[t]\n\t"
        "v_perm_b32 %[t], v205, v201, %[slo]\n\t"
        "v_perm_b32 v205, v205, v201, %[shi]\n\t"
        "v_mov_b32 v201, %[t]\n\t"
        "v_perm_b32 %[t], v206, v202, %[slo]\n\t"
        "v_perm_b32 v206, v206, v202, %[shi]\n\t"
        "v_mov_b32 v202, %[t]\n\t"
        "v_perm_b32 %[t], v207, v203, %[slo]\n\t"
        "v_perm_b32 v207, v207, v203, %[shi]\n\t"
        "v_mov_b32 v203, %[t]\n\t"
        : [t] "=&v"(t) : [slo] "s"(0x05040100u), [shi] "s"(0x07060302u) : "memory");
}

DEV void xstats(int xa, float (&sx)[2], float (&sq)[2]) {
    int xb; u32 sc;
    asm volatile(
        "v_mov_b32 v24, 0x3f803f80\n\t"
        "v_mov_b32 v25, 0x3f803f80\n\t"
        "v_mov_b32 v26, 0x3f803f80\n\t"
        "v_mov_b32 v27, 0x3f803f80\n\t"
        "v_mov_b32 v48, 0\n\t"
        "v_mov_b32 v49, 0\n\t"
        "v_mov_b32 v50, 0\n\t"
        "v_mov_b32 v51, 0\n\t"
        "v_mov_b32 v52, 0\n\t"
        "v_mov_b32 v53, 0\n\t"
        "v_mov_b32 v54, 0\n\t"
        "v_mov_b32 v55, 0\n\t"
        "v_mov_b32 v56, 0\n\t"
        "v_mov_b32 v57, 0\n\t"
        "v_mov_b32 v58, 0\n\t"
        "v_mov_b32 v59, 0\n\t"
        "v_mov_b32 v60, 0\n\t"
        "v_mov_b32 v61, 0\n\t"
        "v_mov_b32 v62, 0\n\t"
        "v_mov_b32 v63, 0\n\t"
        "v_xor_b32 %[xb], 64, %[xa]\n\t"
        "s_mov_b32 %[sc], 3\n\t"
        "Ll_%=:\n\t"
        "ds_read_b128 v[208:211], %[xa] offset:0\n\t"
        "ds_read_b128 v[212:215], %[xa] offset:6144\n\t"
        "ds_read_b128 v[216:219], %[xb] offset:0\n\t"
        "ds_read_b128 v[220:223], %[xb] offset:6144\n\t"
        "ds_read_b128 v[224:227], %[xa] offset:10240\n\t"
        "ds_read_b128 v[228:231], %[xa] offset:16384\n\t"
        "ds_read_b128 v[232:235], %[xb] offset:10240\n\t"
        "ds_read_b128 v[236:239], %[xb] offset:16384\n\t"
        "ds_read_b128 v[240:243], %[xa] offset:20480\n\t"
        "ds_read_b128 v[244:247], %[xa] offset:26624\n\t"
        "ds_read_b128 v[248:251], %[xb] offset:20480\n\t"
        "ds_read_b128 v[252:255], %[xb] offset:26624\n\t"
        "v_add_u32 %[xa], 30720, %[xa]\n\t"
        "v_add_u32 %[xb], 30720, %[xb]\n\t"
        "s_waitcnt lgkmcnt(11)\n\t"
        "v_mfma_f32_16x16x32_bf16 v[48:51], v[24:27], v[208:211], v[48:51]\n\t"
        "v_mfma_f32_16x16x32_bf16 v[56:59], v[208:211], v[208:211], v[56:59]\n\t"
        "s_waitcnt lgkmcnt(10)\n\t"
        "v_mfma_f32_16x16x32_bf16 v[52:55], v[24:27], v[212:215], v[52:55]\n\t"
        "v_mfma_f32_16x16x32_bf16 v[60:63], v[212:215], v[212:215], v[60:63]\n\t"
        "s_waitcnt lgkmcnt(9)\n\t"
        "v_mfma_f32_16x16x32_bf16 v[48:51], v[24:27], v[216:219], v[48:51]\n\t"
        "v_mfma_f32_16x16x32_bf16 v[56:59], v[216:219], v[216:219], v[56:59]\n\t"
        "s_waitcnt lgkmcnt(8)\n\t"
        "v_mfma_f32_16x16x32_bf16 v[52:55], v[24:27], v[220:223], v[52:55]\n\t"
        "v_mfma_f32_16x16x32_bf16 v[60:63], v[220:223], v[220:223], v[60:63]\n\t"
        "s_waitcnt lgkmcnt(7)\n\t"
        "v_mfma_f32_16x16x32_bf16 v[48:51], v[24:27], v[224:227], v[48:51]\n\t"
        "v_mfma_f32_16x16x32_bf16 v[56:59], v[224:227], v[224:227], v[56:59]\n\t"
        "s_waitcnt lgkmcnt(6)\n\t"
        "v_mfma_f32_16x16x32_bf16 v[52:55], v[24:27], v[228:231], v[52:55]\n\t"
        "v_mfma_f32_16x16x32_bf16 v[60:63], v[228:231], v[228:231], v[60:63]\n\t"
        "s_waitcnt lgkmcnt(5)\n\t"
        "v_mfma_f32_16x16x32_bf16 v[48:51], v[24:27], v[232:235], v[48:51]\n\t"
        "v_mfma_f32_16x16x32_bf16 v[56:59], v[232:235], v[232:235], v[56:59]\n\t"
        "s_waitcnt lgkmcnt(4)\n\t"
        "v_mfma_f32_16x16x32_bf16 v[52:55], v[24:27], v[236:239], v[52:55]\n\t"
        "v_mfma_f32_16x16x32_bf16 v[60:63], v[236:239], v[236:239], v[60:63]\n\t"
        "s_waitcnt lgkmcnt(3)\n\t"
        "v_mfma_f32_16x16x32_bf16 v[48:51], v[24:27], v[240:243], v[48:51]\n\t"
        "v_mfma_f32_16x16x32_bf16 v[56:59], v[240:243], v[240:243], v[56:59]\n\t"
        "s_waitcnt lgkmcnt(2)\n\t"
        "v_mfma_f32_16x16x32_bf16 v[52:55], v[24:27], v[244:247], v[52:55]\n\t"
        "v_mfma_f32_16x16x32_bf16 v[60:63], v[244:247], v[244:247], v[60:63]\n\t"
        "s_waitcnt lgkmcnt(1)\n\t"
        "v_mfma_f32_16x16x32_bf16 v[48:51], v[24:27], v[248:251], v[48:51]\n\t"
        "v_mfma_f32_16x16x32_bf16 v[56:59], v[248:251], v[248:251], v[56:59]\n\t"
        "s_waitcnt lgkmcnt(0)\n\t"
        "v_mfma_f32_16x16x32_bf16 v[52:55], v[24:27], v[252:255], v[52:55]\n\t"
        "v_mfma_f32_16x16x32_bf16 v[60:63], v[252:255], v[252:255], v[60:63]\n\t"
        "s_sub_u32 %[sc], %[sc], 1\n\t"
        "s_cmp_lg_u32 %[sc], 0\n\t"
        "s_cbranch_scc1 Ll_%=\n\t"
        "s_nop 7\n\t"
        "v_mov_b32 %[x0], v48\n\t"
        "v_cndmask_b32 v56, v56, v57, %[spar]\n\t"
        "v_cndmask_b32 v58, v58, v59, %[spar]\n\t"
        "v_cndmask_b32 %[q0], v56, v58, %[sl2]\n\t"
        "v_mov_b32 %[x1], v52\n\t"
        "v_cndmask_b32 v60, v60, v61, %[spar]\n\t"
        "v_cndmask_b32 v62, v62, v63, %[spar]\n\t"
        "v_cndmask_b32 %[q1], v60, v62, %[sl2]\n\t"
        : [xa] "+v"(xa), [xb] "=&v"(xb), [sc] "=&s"(sc), [x0] "=&v"(sx[0]), [q0] "=&v"(sq[0]), [x1] "=&v"(sx[1]), [q1] "=&v"(sq[1])
        : [spar] "s"(0xAAAAAAAAAAAAAAAAull), [sl2] "s"(0xCCCCCCCCCCCCCCCCull)
        : "memory", "scc", "v24", "v25", "v26", "v27");
}

DEV void xgather(u32x4 srd, u32 xo, u32 xp, u32 xq, int th, int hh, u32 ld, u32 lh, u32& cn) {
    u32 cr, sa, sb, g0, g1, g2;
    asm volatile(
        "s_nop 4\n\t"
        "s_sleep 5\n\t"
        "s_add_u32 m0, %[sld], 0\n\t"
        "s_add_u32 %[sb], %[sxo], m0\n\t"
        "s_sub_u32 %[sa], %[sb], 8192\n\t"
        "s_cmp_gt_i32 %[sth], 0\n\t"
        "s_cselect_b32 %[scr], %[sa], %[sb]\n\t"
        "buffer_load_dwordx4 v107, %[ssrd], %[scr] offen offset:0 sc1 lds\n\t"
        "s_cmp_gt_i32 %[sth], 1\n\t"
        "s_cselect_b32 %[scr], %[sa], %[sb]\n\t"
        "buffer_load_dwordx4 v107, %[ssrd], %[scr] offen offset:1024 sc1 lds\n\t"
        "s_cmp_gt_i32 %[sth], 2\n\t"
        "s_cselect_b32 %[scr], %[sa], %[sb]\n\t"
        "buffer_load_dwordx4 v107, %[ssrd], %[scr] offen offset:2048 sc1 lds\n\t"
        "s_cmp_gt_i32 %[sth], 3\n\t"
        "s_cselect_b32 %[scr], %[sa], %[sb]\n\t"
        "buffer_load_dwordx4 v107, %[ssrd], %[scr] offen offset:3072 sc1 lds\n\t"
        "s_add_u32 m0, %[sld], 4096\n\t"
        "s_add_u32 %[sb], %[sxo], m0\n\t"
        "s_sub_u32 %[sa], %[sb], 8192\n\t"
        "s_cmp_gt_i32 %[sth], 4\n\t"
        "s_cselect_b32 %[scr], %[sa], %[sb]\n\t"
        "buffer_load_dwordx4 v107, %[ssrd], %[scr] offen offset:0 sc1 lds\n\t"
        "s_cmp_gt_i32 %[sth], 5\n\t"
        "s_cselect_b32 %[scr], %[sa], %[sb]\n\t"
        "buffer_load_dwordx4 v107, %[ssrd], %[scr] offen offset:1024 sc1 lds\n\t"
        "s_cmp_gt_i32 %[sth], 6\n\t"
        "s_cselect_b32 %[scr], %[sa], %[sb]\n\t"
        "buffer_load_dwordx4 v107, %[ssrd], %[scr] offen offset:2048 sc1 lds\n\t"
        "s_cmp_gt_i32 %[sth], 7\n\t"
        "s_cselect_b32 %[scr], %[sa], %[sb]\n\t"
        "buffer_load_dwordx4 v107, %[ssrd], %[scr] offen offset:3072 sc1 lds\n\t"
        "v_and_b32 %[g0], 0x300, v107\n\t"
        "v_and_b32 %[g1], 0xf0, v107\n\t"
        "v_lshrrev_b32 v96, 8, v107\n\t"
        "v_lshl_add_u32 %[g0], %[g0], 2, %[g1]\n\t"
        "v_add_u32 %[g0], 768, %[g0]\n\t"
        "v_sub_u32 v96, %[shh], v96\n\t"
        "v_add_u32 v97, 0x2000, %[g0]\n\t"
        "v_add_u32 %[g1], %[sld], v107\n\t"
        "v_mov_b32 %[g2], 153984\n\t"
        "s_sleep 4\n\t"
        "s_add_u32 m0, %[sld], 16384\n\t"
        "s_add_u32 %[sb], %[sxo], m0\n\t"
        "s_sub_u32 %[sa], %[sb], 8192\n\t"
        "s_cmp_gt_i32 %[sth], 16\n\t"
        "s_cselect_b32 %[scr], %[sa], %[sb]\n\t"
        "buffer_load_dwordx4 v107, %[ssrd], %[scr] offen offset:0 sc1 lds\n\t"
        "s_cmp_gt_i32 %[sth], 17\n\t"
        "s_cselect_b32 %[scr], %[sa], %[sb]\n\t"
        "buffer_load_dwordx4 v107, %[ssrd], %[scr] offen offset:1024 sc1 lds\n\t"
        "s_cmp_gt_i32 %[sth], 18\n\t"
        "s_cselect_b32 %[scr], %[sa], %[sb]\n\t"
        "buffer_load_dwordx4 v107, %[ssrd], %[scr] offen offset:2048 sc1 lds\n\t"
        "s_cmp_gt_i32 %[sth], 19\n\t"
        "s_cselect_b32 %[scr], %[sa], %[sb]\n\t"
        "buffer_load_dwordx4 v107, %[ssrd], %[scr] offen offset:3072 sc1 lds\n\t"
        "s_add_u32 m0, %[sld], 20480\n\t"
        "s_add_u32 %[sb], %[sxo], m0\n\t"
        "s_sub_u32 %[sa], %[sb], 8192\n\t"
        "s_cmp_gt_i32 %[sth], 20\n\t"
        "s_cselect_b32 %[scr], %[sa], %[sb]\n\t"
        "buffer_load_dwordx4 v107, %[ssrd], %[scr] offen offset:0 sc1 lds\n\t"
        "s_cmp_gt_i32 %[sth], 21\n\t"
        "s_cselect_b32 %[scr], %[sa], %[sb]\n\t"
        "buffer_load_dwordx4 v107, %[ssrd], %[scr] offen offset:1024 sc1 lds\n\t"
        "s_cmp_gt_i32 %[sth], 22\n\t"
        "s_cselect_b32 %[scr], %[sa], %[sb]\n\t"
        "buffer_load_dwordx4 v107, %[ssrd], %[scr] offen offset:2048 sc1 lds\n\t"
        "s_cmp_gt_i32 %[sth], 23\n\t"
        "s_cselect_b32 %[scr], %[sa], %[sb]\n\t"
        "buffer_load_dwordx4 v107, %[ssrd], %[scr] offen offset:3072 sc1 lds\n\t"
        "s_waitcnt vmcnt(8)\n\t"
        "ds_read_b128 v[24:27], %[g1] offset:0\n\t"
        "ds_read_b128 v[28:31], %[g1] offset:1024\n\t"
        "ds_read_b128 v[32:35], %[g1] offset:2048\n\t"
        "ds_read_b128 v[36:39], %[g1] offset:3072\n\t"
        "ds_read_b128 v[40:43], %[g1] offset:4096\n\t"
        "ds_read_b128 v[44:47], %[g1] offset:5120\n\t"
        "ds_read_b128 v[88:91], %[g1] offset:6144\n\t"
        "ds_read_b128 v[92:95], %[g1] offset:7168\n\t"
        "Lh0_%=:\n\t"
        "s_waitcnt lgkmcnt(0)\n\t"
        "v_max3_u32 v24, v24, v25, v26\n\t"
        "v_max3_u32 v24, v24, v27, v28\n\t"
        "v_max3_u32 v24, v24, v29, v30\n\t"
        "v_max3_u32 v24, v24, v31, v32\n\t"
        "v_max3_u32 v24, v24, v33, v34\n\t"
        "v_max3_u32 v24, v24, v35, v36\n\t"
        "v_max3_u32 v24, v24, v37, v38\n\t"
        "v_max3_u32 v24, v24, v39, v40\n\t"
        "v_max3_u32 v24, v24, v41, v42\n\t"
        "v_max3_u32 v24, v24, v43, v44\n\t"
        "v_max3_u32 v24, v24, v45, v46\n\t"
        "v_max3_u32 v24, v24, v47, v88\n\t"
        "v_max3_u32 v24, v24, v89, v90\n\t"
        "v_max3_u32 v24, v24, v91, v92\n\t"
        "v_max3_u32 v24, v24, v93, v94\n\t"
        "v_max_u32 v24, v24, v95\n\t"
        "v_cmp_eq_u32 vcc, -1, v24\n\t"
        "v_mov_b32 v25, 1\n\t"
        "s_cbranch_vccnz Lx0_%=\n\t"
        "Ld0_%=:\n\t"
        "s_mov_b64 exec, 1\n\t"
        "ds_add_u32 %[g2], v25 offset:%[og0]+0\n\t"
        "s_mov_b64 exec, -1\n\t"
        "s_add_u32 m0, %[sld], 32768\n\t"
        "s_add_u32 %[sb], %[sxo], m0\n\t"
        "s_sub_u32 %[sa], %[sb], 8192\n\t"
        "s_cmp_gt_i32 %[sth], 32\n\t"
        "s_cselect_b32 %[scr], %[sa], %[sb]\n\t"
        "buffer_load_dwordx4 v107, %[ssrd], %[scr] offen offset:0 sc1 lds\n\t"
        "s_cmp_gt_i32 %[sth], 33\n\t"
        "s_cselect_b32 %[scr], %[sa], %[sb]\n\t"
        "buffer_load_dwordx4 v107, %[ssrd], %[scr] offen offset:1024 sc1 lds\n\t"
        "s_cmp_gt_i32 %[sth], 34\n\t"
        "s_cselect_b32 %[scr], %[sa], %[sb]\n\t"
        "buffer_load_dwordx4 v107, %[ssrd], %[scr] offen offset:2048 sc1 lds\n\t"
        "s_cmp_gt_i32 %[sth], 35\n\t"
        "s_cselect_b32 %[scr], %[sa], %[sb]\n\t"
        "buffer_load_dwordx4 v107, %[ssrd], %[scr] offen offset:3072 sc1 lds\n\t"
        "s_add_u32 m0, %[sld], 36864\n\t"
        "s_add_u32 %[sb], %[sxo], m0\n\t"
        "s_sub_u32 %[sa], %[sb], 8192\n\t"
        "s_cmp_gt_i32 %[sth], 36\n\t"
        "s_cselect_b32 %[scr], %[sa], %[sb]\n\t"
        "buffer_load_dwordx4 v107, %[ssrd], %[scr] offen offset:0 sc1 lds\n\t"
        "s_cmp_gt_i32 %[sth], 37\n\t"
        "s_cselect_b32 %[scr], %[sa], %[sb]\n\t"
        "buffer_load_dwordx4 v107, %[ssrd], %[scr] offen offset:1024 sc1 lds\n\t"
        "s_cmp_gt_i32 %[sth], 38\n\t"
        "s_cselect_b32 %[scr], %[sa], %[sb]\n\t"
        "buffer_load_dwordx4 v107, %[ssrd], %[scr] offen offset:2048 sc1 lds\n\t"
        "s_cmp_gt_i32 %[sth], 39\n\t"
        "s_cselect_b32 %[scr], %[sa], %[sb]\n\t"
        "buffer_load_dwordx4 v107, %[ssrd], %[scr] offen offset:3072 sc1 lds\n\t"
        "s_waitcnt vmcnt(8)\n\t"
        "ds_read_b128 v[24:27], %[g1] offset:16384\n\t"
        "ds_read_b128 v[28:31], %[g1] offset:17408\n\t"
        "ds_read_b128 v[32:35], %[g1] offset:18432\n\t"
        "ds_read_b128 v[36:39], %[g1] offset:19456\n\t"
        "ds_read_b128 v[40:43], %[g1] offset:20480\n\t"
        "ds_read_b128 v[44:47], %[g1] offset:21504\n\t"
        "ds_read_b128 v[88:91], %[g1] offset:22528\n\t"
        "ds_read_b128 v[92:95], %[g1] offset:23552\n\t"
        "s_add_u32 m0, %[sld], 49152\n\t"
        "s_add_u32 %[sb], %[sxo], m0\n\t"
        "s_sub_u32 %[sa], %[sb], 8192\n\t"
        "s_cmp_gt_i32 %[sth], 48\n\t"
        "s_cselect_b32 %[scr], %[sa], %[sb]\n\t"
        "buffer_load_dwordx4 v107, %[ssrd], %[scr] offen offset:0 sc1 lds\n\t"
        "s_cmp_gt_i32 %[sth], 49\n\t"
        "s_cselect_b32 %[scr], %[sa], %[sb]\n\t"
        "buffer_load_dwordx4 v107, %[ssrd], %[scr] offen offset:1024 sc1 lds\n\t"
        "s_cmp_gt_i32 %[sth], 50\n\t"
        "s_cselect_b32 %[scr], %[sa], %[sb]\n\t"
        "buffer_load_dwordx4 v107, %[ssrd], %[scr] offen offset:2048 sc1 lds\n\t"
        "s_cmp_gt_i32 %[sth], 51\n\t"
        "s_cselect_b32 %[scr], %[sa], %[sb]\n\t"
        "buffer_load_dwordx4 v107, %[ssrd], %[scr] offen offset:3072 sc1 lds\n\t"
        "s_add_u32 m0, %[sld], 53248\n\t"
        "s_add_u32 %[sb], %[sxo], m0\n\t"
        "s_sub_u32 %[sa], %[sb], 8192\n\t"
        "s_cmp_gt_i32 %[sth], 52\n\t"
        "s_cselect_b32 %[scr], %[sa], %[sb]\n\t"
        "buffer_load_dwordx4 v107, %[ssrd], %[scr] offen offset:0 sc1 lds\n\t"
        "s_cmp_gt_i32 %[sth], 53\n\t"
        "s_cselect_b32 %[scr], %[sa], %[sb]\n\t"
        "buffer_load_dwordx4 v107, %[ssrd], %[scr] offen offset:1024 sc1 lds\n\t"
        "s_cmp_gt_i32 %[sth], 54\n\t"
        "s_cselect_b32 %[scr], %[sa], %[sb]\n\t"
        "buffer_load_dwordx4 v107, %[ssrd], %[scr] offen offset:2048 sc1 lds\n\t"
        "s_cmp_gt_i32 %[sth], 55\n\t"
        "s_cselect_b32 %[scr], %[sa], %[sb]\n\t"
        "buffer_load_dwordx4 v107, %[ssrd], %[scr] offen offset:3072 sc1 lds\n\t"
        "Lh1_%=:\n\t"
        "s_waitcnt lgkmcnt(0)\n\t"
        "v_max3_u32 v24, v24, v25, v26\n\t"
        "v_max3_u32 v24, v24, v27, v28\n\t"
        "v_max3_u32 v24, v24, v29, v30\n\t"
        "v_max3_u32 v24, v24, v31, v32\n\t"
        "v_max3_u32 v24, v24, v33, v34\n\t"
        "v_max3_u32 v24, v24, v35, v36\n\t"
        "v_max3_u32 v24, v24, v37, v38\n\t"
        "v_max3_u32 v24, v24, v39, v40\n\t"
        "v_max3_u32 v24, v24, v41, v42\n\t"
        "v_max3_u32 v24, v24, v43, v44\n\t"
        "v_max3_u32 v24, v24, v45, v46\n\t"
        "v_max3_u32 v24, v24, v47, v88\n\t"
        "v_max3_u32 v24, v24, v89, v90\n\t"
        "v_max3_u32 v24, v24, v91, v92\n\t"
        "v_max3_u32 v24, v24, v93, v94\n\t"
        "v_max_u32 v24, v24, v95\n\t"
        "v_cmp_eq_u32 vcc, -1, v24\n\t"
        "v_mov_b32 v25, 1\n\t"
        "s_cbranch_vccnz Lx1_%=\n\t"
        "Ld1_%=:\n\t"
        "s_mov_b64 exec, 1\n\t"
        "ds_add_u32 %[g2], v25 offset:%[og0]+4\n\t"
        "s_mov_b64 exec, -1\n\t"
        "s_waitcnt vmcnt(8)\n\t"
        "ds_read_b128 v[24:27], %[g1] offset:32768\n\t"
        "ds_read_b128 v[28:31], %[g1] offset:33792\n\t"
        "ds_read_b128 v[32:35], %[g1] offset:34816\n\t"
        "ds_read_b128 v[36:39], %[g1] offset:35840\n\t"
        "ds_read_b128 v[40:43], %[g1] offset:36864\n\t"
        "ds_read_b128 v[44:47], %[g1] offset:37888\n\t"
        "ds_read_b128 v[88:91], %[g1] offset:38912\n\t"
        "ds_read_b128 v[92:95], %[g1] offset:39936\n\t"
        "v_cmp_lt_i32 vcc, 0, v96\n\t"
        "s_add_u32 m0, %[slh], 0\n\t"
        "s_add_u32 %[scr], %[sxq], 0\n\t"
        "v_cndmask_b32 v98, v97, %[g0], vcc\n\t"
        "buffer_load_dwordx4 v98, %[ssrd], %[scr] offen sc1 lds\n\t"
        "v_cmp_lt_i32 vcc, 4, v96\n\t"
        "s_add_u32 m0, %[slh], 1024\n\t"
        "s_add_u32 %[scr], %[sxq], 4096\n\t"
        "v_cndmask_b32 v99, v97, %[g0], vcc\n\t"
        "buffer_load_dwordx4 v99, %[ssrd], %[scr] offen sc1 lds\n\t"
        "v_cmp_lt_i32 vcc, 8, v96\n\t"
        "s_add_u32 m0, %[slh], 2048\n\t"
        "s_add_u32 %[scr], %[sxp], 8192\n\t"
        "v_cndmask_b32 v98, v97, %[g0], vcc\n\t"
        "buffer_load_dwordx4 v98, %[ssrd], %[scr] offen sc1 lds\n\t"
        "v_cmp_lt_i32 vcc, 12, v96\n\t"
        "s_add_u32 m0, %[slh], 3072\n\t"
        "s_add_u32 %[scr], %[sxp], 12288\n\t"
        "v_cndmask_b32 v99, v97, %[g0], vcc\n\t"
        "buffer_load_dwordx4 v99, %[ssrd], %[scr] offen sc1 lds\n\t"
        "v_cmp_lt_i32 vcc, 16, v96\n\t"
        "s_add_u32 m0, %[slh], 4096\n\t"
        "s_add_u32 %[scr], %[sxp], 16384\n\t"
        "v_cndmask_b32 v98, v97, %[g0], vcc\n\t"
        "buffer_load_dwordx4 v98, %[ssrd], %[scr] offen sc1 lds\n\t"
        "Lh2_%=:\n\t"
        "s_waitcnt lgkmcnt(0)\n\t"
        "v_max3_u32 v24, v24, v25, v26\n\t"
        "v_max3_u32 v24, v24, v27, v28\n\t"
        "v_max3_u32 v24, v24, v29, v30\n\t"
        "v_max3_u32 v24, v24, v31, v32\n\t"
        "v_max3_u32 v24, v24, v33, v34\n\t"
        "v_max3_u32 v24, v24, v35, v36\n\t"
        "v_max3_u32 v24, v24, v37, v38\n\t"
        "v_max3_u32 v24, v24, v39, v40\n\t"
        "v_max3_u32 v24, v24, v41, v42\n\t"
        "v_max3_u32 v24, v24, v43, v44\n\t"
        "v_max3_u32 v24, v24, v45, v46\n\t"
        "v_max3_u32 v24, v24, v47, v88\n\t"
        "v_max3_u32 v24, v24, v89, v90\n\t"
        "v_max3_u32 v24, v24, v91, v92\n\t"
        "v_max3_u32 v24, v24, v93, v94\n\t"
        "v_max_u32 v24, v24, v95\n\t"
        "v_cmp_eq_u32 vcc, -1, v24\n\t"
        "v_mov_b32 v25, 1\n\t"
        "s_cbranch_vccnz Lx2_%=\n\t"
        "Ld2_%=:\n\t"
        "s_mov_b64 exec, 1\n\t"
        "ds_add_u32 %[g2], v25 offset:%[og0]+8\n\t"
        "s_mov_b64 exec, -1\n\t"
        "s_waitcnt vmcnt(5)\n\t"
        "ds_read_b128 v[24:27], %[g1] offset:49152\n\t"
        "ds_read_b128 v[28:31], %[g1] offset:50176\n\t"
        "ds_read_b128 v[32:35], %[g1] offset:51200\n\t"
        "ds_read_b128 v[36:39], %[g1] offset:52224\n\t"
        "ds_read_b128 v[40:43], %[g1] offset:53248\n\t"
        "ds_read_b128 v[44:47], %[g1] offset:54272\n\t"
        "ds_read_b128 v[88:91], %[g1] offset:55296\n\t"
        "ds_read_b128 v[92:95], %[g1] offset:56320\n\t"
        "v_cmp_lt_i32 vcc, 20, v96\n\t"
        "s_add_u32 m0, %[slh], 5120\n\t"
        "s_add_u32 %[scr], %[sxp], 20480\n\t"
        "v_cndmask_b32 v99, v97, %[g0], vcc\n\t"
        "buffer_load_dwordx4 v99, %[ssrd], %[scr] offen sc1 lds\n\t"
        "v_cmp_lt_i32 vcc, 24, v96\n\t"
        "s_add_u32 m0, %[slh], 6144\n\t"
        "s_add_u32 %[scr], %[sxp], 24576\n\t"
        "v_cndmask_b32 v98, v97, %[g0], vcc\n\t"
        "buffer_load_dwordx4 v98, %[ssrd], %[scr] offen sc1 lds\n\t"
        "v_cmp_lt_i32 vcc, 28, v96\n\t"
        "s_add_u32 m0, %[slh], 7168\n\t"
        "s_add_u32 %[scr], %[sxp], 28672\n\t"
        "v_cndmask_b32 v99, v97, %[g0], vcc\n\t"
        "buffer_load_dwordx4 v99, %[ssrd], %[scr] offen sc1 lds\n\t"
        "v_cmp_lt_i32 vcc, 32, v96\n\t"
        "s_add_u32 m0, %[slh], 8192\n\t"
        "s_add_u32 %[scr], %[sxp], 32768\n\t"
        "v_cndmask_b32 v98, v97, %[g0], vcc\n\t"
        "buffer_load_dwordx4 v98, %[ssrd], %[scr] offen sc1 lds\n\t"
        "Lh3_%=:\n\t"
        "s_waitcnt lgkmcnt(0)\n\t"
        "v_max3_u32 v24, v24, v25, v26\n\t"
        "v_max3_u32 v24, v24, v27, v28\n\t"
        "v_max3_u32 v24, v24, v29, v30\n\t"
        "v_max3_u32 v24, v24, v31, v32\n\t"
        "v_max3_u32 v24, v24, v33, v34\n\t"
        "v_max3_u32 v24, v24, v35, v36\n\t"
        "v_max3_u32 v24, v24, v37, v38\n\t"
        "v_max3_u32 v24, v24, v39, v40\n\t"
        "v_max3_u32 v24, v24, v41, v42\n\t"
        "v_max3_u32 v24, v24, v43, v44\n\t"
        "v_max3_u32 v24, v24, v45, v46\n\t"
        "v_max3_u32 v24, v24, v47, v88\n\t"
        "v_max3_u32 v24, v24, v89, v90\n\t"
        "v_max3_u32 v24, v24, v91, v92\n\t"
        "v_max3_u32 v24, v24, v93, v94\n\t"
        "v_max_u32 v24, v24, v95\n\t"
        "v_cmp_eq_u32 vcc, -1, v24\n\t"
        "v_mov_b32 v25, 2\n\t"
        "s_cbranch_vccnz Lx3_%=\n\t"
        "Ld3_%=:\n\t"
        "s_mov_b64 exec, 1\n\t"
        "ds_add_u32 %[g2], v25 offset:%[og0]+12\n\t"
        "s_mov_b64 exec, -1\n\t"
        "s_waitcnt vmcnt(4)\n\t"
        "v_add_u32 %[g1], %[slh], v107\n\t"
        "ds_read_b128 v[24:27], %[g1] offset:0\n\t"
        "ds_read_b128 v[28:31], %[g1] offset:1024\n\t"
        "ds_read_b128 v[32:35], %[g1] offset:2048\n\t"
        "ds_read_b128 v[36:39], %[g1] offset:3072\n\t"
        "ds_read_b128 v[40:43], %[g1] offset:4096\n\t"
        "Lh4_%=:\n\t"
        "s_waitcnt lgkmcnt(0)\n\t"
        "v_max3_u32 v24, v24, v25, v26\n\t"
        "v_max3_u32 v24, v24, v27, v28\n\t"
        "v_max3_u32 v24, v24, v29, v30\n\t"
        "v_max3_u32 v24, v24, v31, v32\n\t"
        "v_max3_u32 v24, v24, v33, v34\n\t"
        "v_max3_u32 v24, v24, v35, v36\n\t"
        "v_max3_u32 v24, v24, v37, v38\n\t"
        "v_max3_u32 v24, v24, v39, v40\n\t"
        "v_max3_u32 v24, v24, v41, v42\n\t"
        "v_max_u32 v24, v24, v43\n\t"
        "v_cmp_eq_u32 vcc, -1, v24\n\t"
        "v_mov_b32 v25, 1\n\t"
        "s_cbranch_vccnz Lx4_%=\n\t"
        "Ld4_%=:\n\t"
        "s_waitcnt vmcnt(0)\n\t"
        "ds_read_b128 v[24:27], %[g1] offset:5120\n\t"
        "ds_read_b128 v[28:31], %[g1] offset:6144\n\t"
        "ds_read_b128 v[32:35], %[g1] offset:7168\n\t"
        "ds_read_b128 v[36:39], %[g1] offset:8192\n\t"
        "Lh5_%=:\n\t"
        "s_waitcnt lgkmcnt(0)\n\t"
        "v_max3_u32 v24, v24, v25, v26\n\t"
        "v_max3_u32 v24, v24, v27, v28\n\t"
        "v_max3_u32 v24, v24, v29, v30\n\t"
        "v_max3_u32 v24, v24, v31, v32\n\t"
        "v_max3_u32 v24, v24, v33, v34\n\t"
        "v_max3_u32 v24, v24, v35, v36\n\t"
        "v_max3_u32 v24, v24, v37, v38\n\t"
        "v_max_u32 v24, v24, v39\n\t"
        "v_cmp_eq_u32 vcc, -1, v24\n\t"
        "v_mov_b32 v25, 1\n\t"
        "s_cbranch_vccnz Lx5_%=\n\t"
        "Ld5_%=:\n\t"
        "s_mov_b64 exec, 1\n\t"
        "ds_add_u32 %[g2], v25 offset:%[og0]+16\n\t"
        "s_mov_b64 exec, -1\n\t"
        "s_waitcnt lgkmcnt(0)\n\t"
        "s_branch Le_%=\n\t"
        "Lx0_%=:\n\t"
        "s_cmp_lt_i32 %[scn], 1\n\t"
        "s_cbranch_scc1 Ld0_%=\n\t"
        "s_sub_u32 %[scn], %[scn], 1\n\t"
        "s_add_u32 m0, %[sld], 0\n\t"
        "s_add_u32 %[sb], %[sxo], m0\n\t"
        "s_sub_u32 %[sa], %[sb], 8192\n\t"
        "s_cmp_gt_i32 %[sth], 0\n\t"
        "s_cselect_b32 %[scr], %[sa], %[sb]\n\t"
        "buffer_load_dwordx4 v107, %[ssrd], %[scr] offen offset:0 sc1 lds\n\t"
        "s_cmp_gt_i32 %[sth], 1\n\t"
        "s_cselect_b32 %[scr], %[sa], %[sb]\n\t"
        "buffer_load_dwordx4 v107, %[ssrd], %[scr] offen offset:1024 sc1 lds\n\t"
        "s_cmp_gt_i32 %[sth], 2\n\t"
        "s_cselect_b32 %[scr], %[sa], %[sb]\n\t"
        "buffer_load_dwordx4 v107, %[ssrd], %[scr] offen offset:2048 sc1 lds\n\t"
        "s_cmp_gt_i32 %[sth], 3\n\t"
        "s_cselect_b32 %[scr], %[sa], %[sb]\n\t"
        "buffer_load_dwordx4 v107, %[ssrd], %[scr] offen offset:3072 sc1 lds\n\t"
        "s_add_u32 m0, %[sld], 4096\n\t"
        "s_add_u32 %[sb], %[sxo], m0\n\t"
        "s_sub_u32 %[sa], %[sb], 8192\n\t"
        "s_cmp_gt_i32 %[sth], 4\n\t"
        "s_cselect_b32 %[scr], %[sa], %[sb]\n\t"
        "buffer_load_dwordx4 v107, %[ssrd], %[scr] offen offset:0 sc1 lds\n\t"
        "s_cmp_gt_i32 %[sth], 5\n\t"
        "s_cselect_b32 %[scr], %[sa], %[sb]\n\t"
        "buffer_load_dwordx4 v107, %[ssrd], %[scr] offen offset:1024 sc1 lds\n\t"
        "s_cmp_gt_i32 %[sth], 6\n\t"
        "s_cselect_b32 %[scr], %[sa], %[sb]\n\t"
        "buffer_load_dwordx4 v107, %[ssrd], %[scr] offen offset:2048 sc1 lds\n\t"
        "s_cmp_gt_i32 %[sth], 7\n\t"
        "s_cselect_b32 %[scr], %[sa], %[sb]\n\t"
        "buffer_load_dwordx4 v107, %[ssrd], %[scr] offen offset:3072 sc1 lds\n\t"
        "s_waitcnt vmcnt(0)\n\t"
        "ds_read_b128 v[24:27], %[g1] offset:0\n\t"
        "ds_read_b128 v[28:31], %[g1] offset:1024\n\t"
        "ds_read_b128 v[32:35], %[g1] offset:2048\n\t"
        "ds_read_b128 v[36:39], %[g1] offset:3072\n\t"
        "ds_read_b128 v[40:43], %[g1] offset:4096\n\t"
        "ds_read_b128 v[44:47], %[g1] offset:5120\n\t"
        "ds_read_b128 v[88:91], %[g1] offset:6144\n\t"
        "ds_read_b128 v[92:95], %[g1] offset:7168\n\t"
        "s_branch Lh0_%=\n\t"
        "Lx1_%=:\n\t"
        "s_cmp_lt_i32 %[scn], 1\n\t"
        "s_cbranch_scc1 Ld1_%=\n\t"
        "s_sub_u32 %[scn], %[scn], 1\n\t"
        "s_add_u32 m0, %[sld], 16384\n\t"
        "s_add_u32 %[sb], %[sxo], m0\n\t"
        "s_sub_u32 %[sa], %[sb], 8192\n\t"
        "s_cmp_gt_i32 %[sth], 16\n\t"
        "s_cselect_b32 %[scr], %[sa], %[sb]\n\t"
        "buffer_load_dwordx4 v107, %[ssrd], %[scr] offen offset:0 sc1 lds\n\t"
        "s_cmp_gt_i32 %[sth], 17\n\t"
        "s_cselect_b32 %[scr], %[sa], %[sb]\n\t"
        "buffer_load_dwordx4 v107, %[ssrd], %[scr] offen offset:1024 sc1 lds\n\t"
        "s_cmp_gt_i32 %[sth], 18\n\t"
        "s_cselect_b32 %[scr], %[sa], %[sb]\n\t"
        "buffer_load_dwordx4 v107, %[ssrd], %[scr] offen offset:2048 sc1 lds\n\t"
        "s_cmp_gt_i32 %[sth], 19\n\t"
        "s_cselect_b32 %[scr], %[sa], %[sb]\n\t"
        "buffer_load_dwordx4 v107, %[ssrd], %[scr] offen offset:3072 sc1 lds\n\t"
        "s_add_u32 m0, %[sld], 20480\n\t"
        "s_add_u32 %[sb], %[sxo], m0\n\t"
        "s_sub_u32 %[sa], %[sb], 8192\n\t"
        "s_cmp_gt_i32 %[sth], 20\n\t"
        "s_cselect_b32 %[scr], %[sa], %[sb]\n\t"
        "buffer_load_dwordx4 v107, %[ssrd], %[scr] offen offset:0 sc1 lds\n\t"
        "s_cmp_gt_i32 %[sth], 21\n\t"
        "s_cselect_b32 %[scr], %[sa], %[sb]\n\t"
        "buffer_load_dwordx4 v107, %[ssrd], %[scr] offen offset:1024 sc1 lds\n\t"
        "s_cmp_gt_i32 %[sth], 22\n\t"
        "s_cselect_b32 %[scr], %[sa], %[sb]\n\t"
        "buffer_load_dwordx4 v107, %[ssrd], %[scr] offen offset:2048 sc1 lds\n\t"
        "s_cmp_gt_i32 %[sth], 23\n\t"
        "s_cselect_b32 %[scr], %[sa], %[sb]\n\t"
        "buffer_load_dwordx4 v107, %[ssrd], %[scr] offen offset:3072 sc1 lds\n\t"
        "s_waitcnt vmcnt(0)\n\t"
        "ds_read_b128 v[24:27], %[g1] offset:16384\n\t"
        "ds_read_b128 v[28:31], %[g1] offset:17408\n\t"
        "ds_read_b128 v[32:35], %[g1] offset:18432\n\t"
        "ds_read_b128 v[36:39], %[g1] offset:19456\n\t"
        "ds_read_b128 v[40:43], %[g1] offset:20480\n\t"
        "ds_read_b128 v[44:47], %[g1] offset:21504\n\t"
        "ds_read_b128 v[88:91], %[g1] offset:22528\n\t"
        "ds_read_b128 v[92:95], %[g1] offset:23552\n\t"
        "s_branch Lh1_%=\n\t"
        "Lx2_%=:\n\t"
        "s_cmp_lt_i32 %[scn], 1\n\t"
        "s_cbranch_scc1 Ld2_%=\n\t"
        "s_sub_u32 %[scn], %[scn], 1\n\t"
        "s_add_u32 m0, %[sld], 32768\n\t"
        "s_add_u32 %[sb], %[sxo], m0\n\t"
        "s_sub_u32 %[sa], %[sb], 8192\n\t"
        "s_cmp_gt_i32 %[sth], 32\n\t"
        "s_cselect_b32 %[scr], %[sa], %[sb]\n\t"
        "buffer_load_dwordx4 v107, %[ssrd], %[scr] offen offset:0 sc1 lds\n\t"
        "s_cmp_gt_i32 %[sth], 33\n\t"
        "s_cselect_b32 %[scr], %[sa], %[sb]\n\t"
        "buffer_load_dwordx4 v107, %[ssrd], %[scr] offen offset:1024 sc1 lds\n\t"
        "s_cmp_gt_i32 %[sth], 34\n\t"
        "s_cselect_b32 %[scr], %[sa], %[sb]\n\t"
        "buffer_load_dwordx4 v107, %[ssrd], %[scr] offen offset:2048 sc1 lds\n\t"
        "s_cmp_gt_i32 %[sth], 35\n\t"
        "s_cselect_b32 %[scr], %[sa], %[sb]\n\t"
        "buffer_load_dwordx4 v107, %[ssrd], %[scr] offen offset:3072 sc1 lds\n\t"
        "s_add_u32 m0, %[sld], 36864\n\t"
        "s_add_u32 %[sb], %[sxo], m0\n\t"
        "s_sub_u32 %[sa], %[sb], 8192\n\t"
        "s_cmp_gt_i32 %[sth], 36\n\t"
        "s_cselect_b32 %[scr], %[sa], %[sb]\n\t"
        "buffer_load_dwordx4 v107, %[ssrd], %[scr] offen offset:0 sc1 lds\n\t"
        "s_cmp_gt_i32 %[sth], 37\n\t"
        "s_cselect_b32 %[scr], %[sa], %[sb]\n\t"
        "buffer_load_dwordx4 v107, %[ssrd], %[scr] offen offset:1024 sc1 lds\n\t"
        "s_cmp_gt_i32 %[sth], 38\n\t"
        "s_cselect_b32 %[scr], %[sa], %[sb]\n\t"
        "buffer_load_dwordx4 v107, %[ssrd], %[scr] offen offset:2048 sc1 lds\n\t"
        "s_cmp_gt_i32 %[sth], 39\n\t"
        "s_cselect_b32 %[scr], %[sa], %[sb]\n\t"
        "buffer_load_dwordx4 v107, %[ssrd], %[scr] offen offset:3072 sc1 lds\n\t"
        "s_waitcnt vmcnt(0)\n\t"
        "ds_read_b128 v[24:27], %[g1] offset:32768\n\t"
        "ds_read_b128 v[28:31], %[g1] offset:33792\n\t"
        "ds_read_b128 v[32:35], %[g1] offset:34816\n\t"
        "ds_read_b128 v[36:39], %[g1] offset:35840\n\t"
        "ds_read_b128 v[40:43], %[g1] offset:36864\n\t"
        "ds_read_b128 v[44:47], %[g1] offset:37888\n\t"
        "ds_read_b128 v[88:91], %[g1] offset:38912\n\t"
        "ds_read_b128 v[92:95], %[g1] offset:39936\n\t"
        "s_branch Lh2_%=\n\t"
        "Lx3_%=:\n\t"
        "s_cmp_lt_i32 %[scn], 1\n\t"
        "s_cbranch_scc1 Ld3_%=\n\t"
        "s_sub_u32 %[scn], %[scn], 1\n\t"
        "s_add_u32 m0, %[sld], 49152\n\t"
        "s_add_u32 %[sb], %[sxo], m0\n\t"
        "s_sub_u32 %[sa], %[sb], 8192\n\t"
        "s_cmp_gt_i32 %[sth], 48\n\t"
        "s_cselect_b32 %[scr], %[sa], %[sb]\n\t"
        "buffer_load_dwordx4 v107, %[ssrd], %[scr] offen offset:0 sc1 lds\n\t"
        "s_cmp_gt_i32 %[sth], 49\n\t"
        "s_cselect_b32 %[scr], %[sa], %[sb]\n\t"
        "buffer_load_dwordx4 v107, %[ssrd], %[scr] offen offset:1024 sc1 lds\n\t"
        "s_cmp_gt_i32 %[sth], 50\n\t"
        "s_cselect_b32 %[scr], %[sa], %[sb]\n\t"
        "buffer_load_dwordx4 v107, %[ssrd], %[scr] offen offset:2048 sc1 lds\n\t"
        "s_cmp_gt_i32 %[sth], 51\n\t"
        "s_cselect_b32 %[scr], %[sa], %[sb]\n\t"
        "buffer_load_dwordx4 v107, %[ssrd], %[scr] offen offset:3072 sc1 lds\n\t"
        "s_add_u32 m0, %[sld], 53248\n\t"
        "s_add_u32 %[sb], %[sxo], m0\n\t"
        "s_sub_u32 %[sa], %[sb], 8192\n\t"
        "s_cmp_gt_i32 %[sth], 52\n\t"
        "s_cselect_b32 %[scr], %[sa], %[sb]\n\t"
        "buffer_load_dwordx4 v107, %[ssrd], %[scr] offen offset:0 sc1 lds\n\t"
        "s_cmp_gt_i32 %[sth], 53\n\t"
        "s_cselect_b32 %[scr], %[sa], %[sb]\n\t"
        "buffer_load_dwordx4 v107, %[ssrd], %[scr] offen offset:1024 sc1 lds\n\t"
        "s_cmp_gt_i32 %[sth], 54\n\t"
        "s_cselect_b32 %[scr], %[sa], %[sb]\n\t"
        "buffer_load_dwordx4 v107, %[ssrd], %[scr] offen offset:2048 sc1 lds\n\t"
        "s_cmp_gt_i32 %[sth], 55\n\t"
        "s_cselect_b32 %[scr], %[sa], %[sb]\n\t"
        "buffer_load_dwordx4 v107, %[ssrd], %[scr] offen offset:3072 sc1 lds\n\t"
        "s_waitcnt vmcnt(0)\n\t"
        "ds_read_b128 v[24:27], %[g1] offset:49152\n\t"
        "ds_read_b128 v[28:31], %[g1] offset:50176\n\t"
        "ds_read_b128 v[32:35], %[g1] offset:51200\n\t"
        "ds_read_b128 v[36:39], %[g1] offset:52224\n\t"
        "ds_read_b128 v[40:43], %[g1] offset:53248\n\t"
        "ds_read_b128 v[44:47], %[g1] offset:54272\n\t"
        "ds_read_b128 v[88:91], %[g1] offset:55296\n\t"
        "ds_read_b128 v[92:95], %[g1] offset:56320\n\t"
        "s_branch Lh3_%=\n\t"
        "Lx4_%=:\n\t"
        "s_cmp_lt_i32 %[scn], 1\n\t"
        "s_cbranch_scc1 Ld4_%=\n\t"
        "s_sub_u32 %[scn], %[scn], 1\n\t"
        "v_cmp_lt_i32 vcc, 0, v96\n\t"
        "s_add_u32 m0, %[slh], 0\n\t"
        "s_add_u32 %[scr], %[sxq], 0\n\t"
        "v_cndmask_b32 v98, v97, %[g0], vcc\n\t"
        "buffer_load_dwordx4 v98, %[ssrd], %[scr] offen sc1 lds\n\t"
        "v_cmp_lt_i32 vcc, 4, v96\n\t"
        "s_add_u32 m0, %[slh], 1024\n\t"
        "s_add_u32 %[scr], %[sxq], 4096\n\t"
        "v_cndmask_b32 v99, v97, %[g0], vcc\n\t"
        "buffer_load_dwordx4 v99, %[ssrd], %[scr] offen sc1 lds\n\t"
        "v_cmp_lt_i32 vcc, 8, v96\n\t"
        "s_add_u32 m0, %[slh], 2048\n\t"
        "s_add_u32 %[scr], %[sxp], 8192\n\t"
        "v_cndmask_b32 v98, v97, %[g0], vcc\n\t"
        "buffer_load_dwordx4 v98, %[ssrd], %[scr] offen sc1 lds\n\t"
        "v_cmp_lt_i32 vcc, 12, v96\n\t"
        "s_add_u32 m0, %[slh], 3072\n\t"
        "s_add_u32 %[scr], %[sxp], 12288\n\t"
        "v_cndmask_b32 v99, v97, %[g0], vcc\n\t"
        "buffer_load_dwordx4 v99, %[ssrd], %[scr] offen sc1 lds\n\t"
        "v_cmp_lt_i32 vcc, 16, v96\n\t"
        "s_add_u32 m0, %[slh], 4096\n\t"
        "s_add_u32 %[scr], %[sxp], 16384\n\t"
        "v_cndmask_b32 v98, v97, %[g0], vcc\n\t"
        "buffer_load_dwordx4 v98, %[ssrd], %[scr] offen sc1 lds\n\t"
        "s_waitcnt vmcnt(0)\n\t"
        "v_add_u32 %[g1], %[slh], v107\n\t"
        "ds_read_b128 v[24:27], %[g1] offset:0\n\t"
        "ds_read_b128 v[28:31], %[g1] offset:1024\n\t"
        "ds_read_b128 v[32:35], %[g1] offset:2048\n\t"
        "ds_read_b128 v[36:39], %[g1] offset:3072\n\t"
        "ds_read_b128 v[40:43], %[g1] offset:4096\n\t"
        "s_branch Lh4_%=\n\t"
        "Lx5_%=:\n\t"
        "s_cmp_lt_i32 %[scn], 1\n\t"
        "s_cbranch_scc1 Ld5_%=\n\t"
        "s_sub_u32 %[scn], %[scn], 1\n\t"
        "v_cmp_lt_i32 vcc, 20, v96\n\t"
        "s_add_u32 m0, %[slh], 5120\n\t"
        "s_add_u32 %[scr], %[sxp], 20480\n\t"
        "v_cndmask_b32 v99, v97, %[g0], vcc\n\t"
        "buffer_load_dwordx4 v99, %[ssrd], %[scr] offen sc1 lds\n\t"
        "v_cmp_lt_i32 vcc, 24, v96\n\t"
        "s_add_u32 m0, %[slh], 6144\n\t"
        "s_add_u32 %[scr], %[sxp], 24576\n\t"
        "v_cndmask_b32 v98, v97, %[g0], vcc\n\t"
        "buffer_load_dwordx4 v98, %[ssrd], %[scr] offen sc1 lds\n\t"
        "v_cmp_lt_i32 vcc, 28, v96\n\t"
        "s_add_u32 m0, %[slh], 7168\n\t"
        "s_add_u32 %[scr], %[sxp], 28672\n\t"
        "v_cndmask_b32 v99, v97, %[g0], vcc\n\t"
        "buffer_load_dwordx4 v99, %[ssrd], %[scr] offen sc1 lds\n\t"
        "v_cmp_lt_i32 vcc, 32, v96\n\t"
        "s_add_u32 m0, %[slh], 8192\n\t"
        "s_add_u32 %[scr], %[sxp], 32768\n\t"
        "v_cndmask_b32 v98, v97, %[g0], vcc\n\t"
        "buffer_load_dwordx4 v98, %[ssrd], %[scr] offen sc1 lds\n\t"
        "s_waitcnt vmcnt(0)\n\t"
        "ds_read_b128 v[24:27], %[g1] offset:5120\n\t"
        "ds_read_b128 v[28:31], %[g1] offset:6144\n\t"
        "ds_read_b128 v[32:35], %[g1] offset:7168\n\t"
        "ds_read_b128 v[36:39], %[g1] offset:8192\n\t"
        "s_branch Lh5_%=\n\t"
        "Le_%=:\n\t"
        : [scr] "=&s"(cr), [sa] "=&s"(sa), [sb] "=&s"(sb), [scn] "+s"(cn), [g0] "=&v"(g0), [g1] "=&v"(g1), [g2] "=&v"(g2)
        : [ssrd] "s"(srd), [sxo] "s"(xo), [sxp] "s"(xp), [sxq] "s"(xq), [sth] "s"(th), [shh] "s"(hh), [sld] "s"(ld), [slh] "s"(lh), [og0] "n"(BC_G0)
        : "memory", "scc", "vcc", "v24", "v25", "v26", "v27", "v28", "v29", "v30", "v31", "v32", "v33", "v34", "v35", "v36", "v37", "v38", "v39", "v40", "v41", "v42", "v43", "v44", "v45", "v46", "v47");
}

DEV void gu_set() {
    asm volatile(
        "v_lshrrev_b32 v110, 5, v107\n\t"
        "v_bfe_u32 v111, v107, 6, 3\n\t"
        "v_lshrrev_b32 v96, 1, v107\n\t"
        "v_and_or_b32 v110, v110, 16, v111\n\t"
        "v_and_b32 v96, 24, v96\n\t"
        "v_mad_u32_u24 v110, v110, 48, v96\n\t"
        "v_add_u32 v110, 98048, v110\n\t"
        "v_add_u32 v111, 27632, v110\n\t"
        "v_cndmask_b32 v111, v110, v111, %[sl2]\n\t"
        :: [sl2] "s"(0xCCCCCCCCCCCCCCCCull) : "memory");
}
DEV void gu_pre() {
    asm volatile(
        "ds_read_b64_tr_b16 v[48:49], v110 offset:0\n\t"
        "ds_read_b64_tr_b16 v[50:51], v110 offset:384\n\t"
        "ds_read_b64_tr_b16 v[52:53], v110 offset:1536\n\t"
        "ds_read_b64_tr_b16 v[54:55], v110 offset:1920\n\t"
        "ds_read_b64_tr_b16 v[56:57], v110 offset:27648\n\t"
        "ds_read_b64_tr_b16 v[58:59], v110 offset:28032\n\t"
        "ds_read_b64_tr_b16 v[60:61], v110 offset:29184\n\t"
        "ds_read_b64_tr_b16 v[62:63], v110 offset:29568\n\t"
        "ds_read_b64_tr_b16 v[64:65], v111 offset:32\n\t"
        "ds_read_b64_tr_b16 v[66:67], v111 offset:416\n\t"
        "ds_read_b64_tr_b16 v[68:69], v111 offset:1568\n\t"
        "ds_read_b64_tr_b16 v[70:71], v111 offset:1952\n\t"
        "ds_read_b64_tr_b16 v[72:73], v110 offset:24576\n\t"
        "ds_read_b64_tr_b16 v[74:75], v110 offset:24960\n\t"
        "ds_read_b64_tr_b16 v[76:77], v110 offset:26112\n\t"
        "ds_read_b64_tr_b16 v[78:79], v110 offset:26496\n\t"
        "ds_read_b64_tr_b16 v[80:81], v110 offset:52224\n\t"
        "ds_read_b64_tr_b16 v[82:83], v110 offset:52608\n\t"
        "ds_read_b64_tr_b16 v[84:85], v110 offset:53760\n\t"
        "ds_read_b64_tr_b16 v[86:87], v110 offset:54144\n\t"
        "ds_read_b64_tr_b16 v[88:89], v111 offset:24608\n\t"
        "ds_read_b64_tr_b16 v[90:91], v111 offset:24992\n\t"
        "ds_read_b64_tr_b16 v[92:93], v111 offset:26144\n\t"
        "ds_read_b64_tr_b16 v[94:95], v111 offset:26528\n\t"
        "ds_read_b64_tr_b16 v[220:221], v110 offset:3072\n\t"
        "ds_read_b64_tr_b16 v[222:223], v110 offset:3456\n\t"
        "ds_read_b64_tr_b16 v[224:225], v110 offset:4608\n\t"
        "ds_read_b64_tr_b16 v[226:227], v110 offset:4992\n\t"
        "ds_read_b64_tr_b16 v[228:229], v110 offset:30720\n\t"
        "ds_read_b64_tr_b16 v[230:231], v110 offset:31104\n\t"
        "ds_read_b64_tr_b16 v[232:233], v110 offset:32256\n\t"
        "ds_read_b64_tr_b16 v[234:235], v110 offset:32640\n\t"
        "ds_read_b64_tr_b16 v[236:237], v111 offset:3104\n\t"
        "ds_read_b64_tr_b16 v[238:239], v111 offset:3488\n\t"
        "ds_read_b64_tr_b16 v[240:241], v111 offset:4640\n\t"
        "ds_read_b64_tr_b16 v[242:243], v111 offset:5024\n\t"
        ::: "memory");
}

DEV void gu_tr(u32 o) {
    asm volatile(
        "v_lshrrev_b32 v32, 5, v107\n\t"
        "v_bfe_u32 v33, v107, 6, 3\n\t"
        "v_lshrrev_b32 v34, 1, v107\n\t"
        "v_and_or_b32 v32, v32, 16, v33\n\t"
        "v_and_b32 v34, 24, v34\n\t"
        "v_mad_u32_u24 v32, v32, 48, v34\n\t"
        "v_add_u32 v32, %[so], v32\n\t"
        "v_add_u32 v33, 27632, v32\n\t"
        "v_cndmask_b32 v33, v32, v33, %[sl2]\n\t"
        "v_add_u32 v34, %[so], v107\n\t"
        "ds_read_b64_tr_b16 v[48:49], v32 offset:0\n\t"
        "ds_read_b64_tr_b16 v[50:51], v32 offset:384\n\t"
        "ds_read_b64_tr_b16 v[52:53], v32 offset:1536\n\t"
        "ds_read_b64_tr_b16 v[54:55], v32 offset:1920\n\t"
        "ds_read_b64_tr_b16 v[56:57], v32 offset:27648\n\t"
        "ds_read_b64_tr_b16 v[58:59], v32 offset:28032\n\t"
        "ds_read_b64_tr_b16 v[60:61], v32 offset:29184\n\t"
        "ds_read_b64_tr_b16 v[62:63], v32 offset:29568\n\t"
        "ds_read_b64_tr_b16 v[64:65], v33 offset:32\n\t"
        "ds_read_b64_tr_b16 v[66:67], v33 offset:416\n\t"
        "ds_read_b64_tr_b16 v[68:69], v33 offset:1568\n\t"
        "ds_read_b64_tr_b16 v[70:71], v33 offset:1952\n\t"
        "ds_read_b64_tr_b16 v[72:73], v32 offset:3072\n\t"
        "ds_read_b64_tr_b16 v[74:75], v32 offset:3456\n\t"
        "ds_read_b64_tr_b16 v[76:77], v32 offset:4608\n\t"
        "ds_read_b64_tr_b16 v[78:79], v32 offset:4992\n\t"
        "ds_read_b64_tr_b16 v[80:81], v32 offset:30720\n\t"
        "ds_read_b64_tr_b16 v[82:83], v32 offset:31104\n\t"
        "ds_read_b64_tr_b16 v[84:85], v32 offset:32256\n\t"
        "ds_read_b64_tr_b16 v[86:87], v32 offset:32640\n\t"
        "ds_read_b64_tr_b16 v[24:25], v33 offset:3104\n\t"
        "ds_read_b64_tr_b16 v[26:27], v33 offset:3488\n\t"
        "ds_read_b64_tr_b16 v[28:29], v33 offset:4640\n\t"
        "ds_read_b64_tr_b16 v[30:31], v33 offset:5024\n\t"
        "s_waitcnt lgkmcnt(12)\n\t"
        "ds_write_b128 v34, v[48:51] offset:0\n\t"
        "ds_write_b128 v34, v[52:55] offset:1024\n\t"
        "ds_write_b128 v34, v[56:59] offset:27648\n\t"
        "ds_write_b128 v34, v[60:63] offset:28672\n\t"
        "ds_write_b128 v34, v[64:67] offset:2048\n\t"
        "ds_write_b128 v34, v[68:71] offset:29696\n\t"
        "s_waitcnt lgkmcnt(6)\n\t"
        "ds_write_b128 v34, v[72:75] offset:3072\n\t"
        "ds_write_b128 v34, v[76:79] offset:4096\n\t"
        "ds_write_b128 v34, v[80:83] offset:30720\n\t"
        "ds_write_b128 v34, v[84:87] offset:31744\n\t"
        "ds_write_b128 v34, v[24:27] offset:5120\n\t"
        "ds_write_b128 v34, v[28:31] offset:32768\n\t"
        :: [so] "s"(o), [sl2] "s"(0xCCCCCCCCCCCCCCCCull) : "memory", "v24", "v25", "v26", "v27", "v28", "v29", "v30", "v31", "v32", "v33", "v34");
}

DEV void ffn(u32x4 srd, u32 cr, u32 c0, u32 c1, u32 ya, u32 ye, u32 yb, u32 re, float& m, float& rs, u32& cn) {
    u32 ap[12];
    asm volatile(
        "s_nop 4\n\t"
        "Ls_%=:\n\t"
        "buffer_load_dwordx4 v[24:27], %[c0], %[ssrd], %[scr] offen sc1\n\t"
        "buffer_load_dwordx4 v[28:31], %[c1], %[ssrd], %[scr] offen sc1\n\t"
        "v_mov_b32 v32, 0\n\t"
        "v_mov_b32 v33, 0\n\t"
        "v_mov_b32 v34, 0\n\t"
        "v_mov_b32 v35, 0\n\t"
        "v_mov_b32 v36, 0\n\t"
        "v_mov_b32 v37, 0\n\t"
        "v_mov_b32 v38, 0\n\t"
        "v_mov_b32 v39, 0\n\t"
        "v_mov_b32 v40, 0\n\t"
        "v_mov_b32 v41, 0\n\t"
        "v_mov_b32 v42, 0\n\t"
        "v_mov_b32 v43, 0\n\t"
        "v_mov_b32 v96, 0x3f803f80\n\t"
        "v_mov_b32 v97, 0x3f803f80\n\t"
        "v_mov_b32 v98, 0x3f803f80\n\t"
        "v_mov_b32 v99, 0x3f803f80\n\t"
        "v_mov_b32 v252, 0x3f803f80\n\t"
        "v_mov_b32 v253, 0x3f803f80\n\t"
        "v_mov_b32 v254, 0x3f803f80\n\t"
        "v_mov_b32 v255, 0x3f803f80\n\t"
        "v_mov_b32 v44, 0x4444\n\t"
        "v_mov_b32 v45, 0xeeee\n\t"
        "v_cndmask_b32 v44, v44, v45, %[spar]\n\t"
        "v_add_u32 v45, 98048, v107\n\t"
        "s_cmp_lg_u32 %[sre], 0\n\t"
        "s_cbranch_scc1 Lr_%=\n\t"
        "s_waitcnt vmcnt(0)\n\t"
        "Lb_%=:\n\t"
        "v_mov_b32_dpp v96, v24 quad_perm:[0,1,2,3] row_mask:0xf bank_mask:0x1 bound_ctrl:1\n\t"
        "v_mov_b32_dpp v97, v25 quad_perm:[0,1,2,3] row_mask:0xf bank_mask:0x1 bound_ctrl:1\n\t"
        "v_mov_b32_dpp v98, v26 quad_perm:[0,1,2,3] row_mask:0xf bank_mask:0x1 bound_ctrl:1\n\t"
        "v_mov_b32_dpp v99, v27 quad_perm:[0,1,2,3] row_mask:0xf bank_mask:0x1 bound_ctrl:1\n\t"
        "v_max3_u32 v46, v24, v25, v26\n\t"
        "v_max3_u32 v46, v46, v27, v28\n\t"
        "v_smfmac_f32_16x16x64_bf16 v[32:35], v[96:99], v[48:55], v44\n\t"
        "v_mov_b32_dpp v252, v28 quad_perm:[0,1,2,3] row_mask:0xf bank_mask:0x1 bound_ctrl:1\n\t"
        "v_mov_b32_dpp v253, v29 quad_perm:[0,1,2,3] row_mask:0xf bank_mask:0x1 bound_ctrl:1\n\t"
        "ds_read_b128 v[48:51], v45 offset:33792\n\t"
        "ds_read_b128 v[52:55], v45 offset:34816\n\t"
        "v_smfmac_f32_16x16x64_bf16 v[36:39], v[96:99], v[56:63], v44\n\t"
        "v_mov_b32_dpp v254, v30 quad_perm:[0,1,2,3] row_mask:0xf bank_mask:0x1 bound_ctrl:1\n\t"
        "v_mov_b32_dpp v255, v31 quad_perm:[0,1,2,3] row_mask:0xf bank_mask:0x1 bound_ctrl:1\n\t"
        "ds_read_b128 v[56:59], v45 offset:8192\n\t"
        "ds_read_b128 v[60:63], v45 offset:35840\n\t"
        "v_smfmac_f32_16x16x64_bf16 v[40:43], v[96:99], v[64:71], v44\n\t"
        "v_max3_u32 v46, v46, v29, v30\n\t"
        "v_max_u32 v46, v46, v31\n\t"
        "ds_read_b128 v[64:67], v45 offset:9216\n\t"
        "ds_read_b128 v[68:71], v45 offset:10240\n\t"
        "v_smfmac_f32_16x16x64_bf16 v[32:35], v[252:255], v[72:79], v44\n\t"
        "v_mov_b32_dpp v96, v24 row_shl:2 row_mask:0xf bank_mask:0x1 bound_ctrl:1\n\t"
        "v_mov_b32_dpp v97, v25 row_shl:2 row_mask:0xf bank_mask:0x1 bound_ctrl:1\n\t"
        "ds_read_b128 v[72:75], v45 offset:36864\n\t"
        "ds_read_b128 v[76:79], v45 offset:37888\n\t"
        "v_smfmac_f32_16x16x64_bf16 v[36:39], v[252:255], v[80:87], v44\n\t"
        "v_mov_b32_dpp v98, v26 row_shl:2 row_mask:0xf bank_mask:0x1 bound_ctrl:1\n\t"
        "v_mov_b32_dpp v99, v27 row_shl:2 row_mask:0xf bank_mask:0x1 bound_ctrl:1\n\t"
        "ds_read_b128 v[80:83], v45 offset:11264\n\t"
        "ds_read_b128 v[84:87], v45 offset:38912\n\t"
        "v_smfmac_f32_16x16x64_bf16 v[40:43], v[252:255], v[88:95], v44\n\t"
        "v_cmp_eq_u32 vcc, -1, v46\n\t"
        "ds_read_b128 v[88:91], v45 offset:12288\n\t"
        "ds_read_b128 v[92:95], v45 offset:13312\n\t"
        "v_smfmac_f32_16x16x64_bf16 v[32:35], v[96:99], v[220:227], v44\n\t"
        "v_mov_b32_dpp v252, v24 row_shl:4 row_mask:0xf bank_mask:0x1 bound_ctrl:1\n\t"
        "v_mov_b32_dpp v253, v25 row_shl:4 row_mask:0xf bank_mask:0x1 bound_ctrl:1\n\t"
        "ds_read_b128 v[220:223], v45 offset:39936\n\t"
        "ds_read_b128 v[224:227], v45 offset:40960\n\t"
        "v_smfmac_f32_16x16x64_bf16 v[36:39], v[96:99], v[228:235], v44\n\t"
        "v_mov_b32_dpp v254, v26 row_shl:4 row_mask:0xf bank_mask:0x1 bound_ctrl:1\n\t"
        "ds_read_b128 v[228:231], v45 offset:14336\n\t"
        "ds_read_b128 v[232:235], v45 offset:41984\n\t"
        "s_cbranch_vccnz Ly_%=\n\t"
        "Lz_%=:\n\t"
        "v_smfmac_f32_16x16x64_bf16 v[40:43], v[96:99], v[236:243], v44\n\t"
        "v_mov_b32_dpp v255, v27 row_shl:4 row_mask:0xf bank_mask:0x1 bound_ctrl:1\n\t"
        "ds_read_b128 v[236:239], v45 offset:15360\n\t"
        "ds_read_b128 v[240:243], v45 offset:16384\n\t"
        "v_smfmac_f32_16x16x64_bf16 v[32:35], v[252:255], v[244:251], v44\n\t"
        "v_mov_b32_dpp v96, v24 row_shl:6 row_mask:0xf bank_mask:0x1 bound_ctrl:1\n\t"
        "v_mov_b32_dpp v97, v25 row_shl:6 row_mask:0xf bank_mask:0x1 bound_ctrl:1\n\t"
        "ds_read_b128 v[244:247], v45 offset:43008\n\t"
        "ds_read_b128 v[248:251], v45 offset:44032\n\t"
        "s_waitcnt lgkmcnt(15)\n\t"
        "v_smfmac_f32_16x16x64_bf16 v[36:39], v[252:255], v[48:55], v44\n\t"
        "v_mov_b32_dpp v98, v26 row_shl:6 row_mask:0xf bank_mask:0x1 bound_ctrl:1\n\t"
        "ds_read_b128 v[48:51], v45 offset:17408\n\t"
        "ds_read_b128 v[52:55], v45 offset:45056\n\t"
        "v_smfmac_f32_16x16x64_bf16 v[40:43], v[252:255], v[56:63], v44\n\t"
        "v_mov_b32_dpp v99, v27 row_shl:6 row_mask:0xf bank_mask:0x1 bound_ctrl:1\n\t"
        "ds_read_b128 v[56:59], v45 offset:18432\n\t"
        "ds_read_b128 v[60:63], v45 offset:19456\n\t"
        "s_waitcnt lgkmcnt(15)\n\t"
        "v_smfmac_f32_16x16x64_bf16 v[32:35], v[96:99], v[64:71], v44\n\t"
        "v_mov_b32_dpp v252, v24 row_shl:8 row_mask:0xf bank_mask:0x1 bound_ctrl:1\n\t"
        "v_mov_b32_dpp v253, v25 row_shl:8 row_mask:0xf bank_mask:0x1 bound_ctrl:1\n\t"
        "ds_read_b128 v[64:67], v45 offset:46080\n\t"
        "ds_read_b128 v[68:71], v45 offset:47104\n\t"
        "v_smfmac_f32_16x16x64_bf16 v[36:39], v[96:99], v[72:79], v44\n\t"
        "v_mov_b32_dpp v254, v26 row_shl:8 row_mask:0xf bank_mask:0x1 bound_ctrl:1\n\t"
        "ds_read_b128 v[72:75], v45 offset:20480\n\t"
        "ds_read_b128 v[76:79], v45 offset:48128\n\t"
        "s_waitcnt lgkmcnt(15)\n\t"
        "v_smfmac_f32_16x16x64_bf16 v[40:43], v[96:99], v[80:87], v44\n\t"
        "v_mov_b32_dpp v255, v27 row_shl:8 row_mask:0xf bank_mask:0x1 bound_ctrl:1\n\t"
        "ds_read_b128 v[80:83], v45 offset:21504\n\t"
        "ds_read_b128 v[84:87], v45 offset:22528\n\t"
        "v_smfmac_f32_16x16x64_bf16 v[32:35], v[252:255], v[88:95], v44\n\t"
        "v_mov_b32_dpp v96, v24 row_shl:10 row_mask:0xf bank_mask:0x1 bound_ctrl:1\n\t"
        "v_mov_b32_dpp v97, v25 row_shl:10 row_mask:0xf bank_mask:0x1 bound_ctrl:1\n\t"
        "ds_read_b128 v[88:91], v45 offset:49152\n\t"
        "ds_read_b128 v[92:95], v45 offset:50176\n\t"
        "s_waitcnt lgkmcnt(15)\n\t"
        "v_smfmac_f32_16x16x64_bf16 v[36:39], v[252:255], v[220:227], v44\n\t"
        "v_mov_b32_dpp v98, v26 row_shl:10 row_mask:0xf bank_mask:0x1 bound_ctrl:1\n\t"
        "ds_read_b128 v[220:223], v45 offset:23552\n\t"
        "ds_read_b128 v[224:227], v45 offset:51200\n\t"
        "v_smfmac_f32_16x16x64_bf16 v[40:43], v[252:255], v[228:235], v44\n\t"
        "v_mov_b32_dpp v99, v27 row_shl:10 row_mask:0xf bank_mask:0x1 bound_ctrl:1\n\t"
        "s_waitcnt lgkmcnt(15)\n\t"
        "s_nop 0\n\t"
        "v_smfmac_f32_16x16x64_bf16 v[32:35], v[96:99], v[236:243], v44\n\t"
        "v_mov_b32_dpp v252, v24 row_shl:12 row_mask:0xf bank_mask:0x1 bound_ctrl:1\n\t"
        "v_mov_b32_dpp v253, v25 row_shl:12 row_mask:0xf bank_mask:0x1 bound_ctrl:1\n\t"
        "s_waitcnt lgkmcnt(14)\n\t"
        "v_smfmac_f32_16x16x64_bf16 v[36:39], v[96:99], v[244:251], v44\n\t"
        "v_mov_b32_dpp v254, v26 row_shl:12 row_mask:0xf bank_mask:0x1 bound_ctrl:1\n\t"
        "s_waitcnt lgkmcnt(12)\n\t"
        "v_smfmac_f32_16x16x64_bf16 v[40:43], v[96:99], v[48:55], v44\n\t"
        "v_mov_b32_dpp v255, v27 row_shl:12 row_mask:0xf bank_mask:0x1 bound_ctrl:1\n\t"
        "s_waitcnt lgkmcnt(10)\n\t"
        "s_nop 0\n\t"
        "v_smfmac_f32_16x16x64_bf16 v[32:35], v[252:255], v[56:63], v44\n\t"
        "v_mov_b32_dpp v96, v24 row_shl:14 row_mask:0xf bank_mask:0x1 bound_ctrl:1\n\t"
        "v_mov_b32_dpp v97, v25 row_shl:14 row_mask:0xf bank_mask:0x1 bound_ctrl:1\n\t"
        "s_waitcnt lgkmcnt(8)\n\t"
        "v_smfmac_f32_16x16x64_bf16 v[36:39], v[252:255], v[64:71], v44\n\t"
        "v_mov_b32_dpp v98, v26 row_shl:14 row_mask:0xf bank_mask:0x1 bound_ctrl:1\n\t"
        "s_waitcnt lgkmcnt(6)\n\t"
        "v_smfmac_f32_16x16x64_bf16 v[40:43], v[252:255], v[72:79], v44\n\t"
        "v_mov_b32_dpp v99, v27 row_shl:14 row_mask:0xf bank_mask:0x1 bound_ctrl:1\n\t"
        "s_waitcnt lgkmcnt(0)\n\t"
        "s_nop 0\n\t"
        "v_smfmac_f32_16x16x64_bf16 v[40:43], v[96:99], v[220:227], v44\n\t"
        "v_readlane_b32 %[sa0], v28, 2\n\t"
        "v_smfmac_f32_16x16x64_bf16 v[32:35], v[96:99], v[80:87], v44\n\t"
        "v_readlane_b32 %[sa1], v29, 2\n\t"
        "v_smfmac_f32_16x16x64_bf16 v[36:39], v[96:99], v[88:95], v44\n\t"
        "s_nop 3\n\t"
        "v_add_f32 v40, v40, v41\n\t"
        "s_nop 0\n\t"
        "v_add_f32 v46, v32, v33\n\t"
        "v_mov_b32_dpp v41, v40 row_shl:8 row_mask:0xf bank_mask:0xf bound_ctrl:1\n\t"
        "v_add_f32 v47, v36, v37\n\t"
        "v_permlane16_swap_b32 v46, v40\n\t"
        "s_nop 0\n\t"
        "v_permlane16_swap_b32 v47, v41\n\t"
        "v_fma_f32 v46, -%[sa0], v40, v46\n\t"
        "v_fma_f32 v47, -%[sa0], v41, v47\n\t"
        "v_mul_f32 v46, %[sa1], v46\n\t"
        "v_mul_f32 v47, %[sa1], v47\n\t"
        "v_mul_f32 v44, 0xbfb8aa3b, v46\n\t"
        "v_exp_f32 v44, v44\n\t"
        "v_mul_f32 v46, v46, v47\n\t"
        "v_add_f32 v44, 1.0, v44\n\t"
        "v_rcp_f32 v44, v44\n\t"
        "s_mov_b32 %[sm], %[sa0]\n\t"
        "s_mov_b32 %[srs], %[sa1]\n\t"
        "v_mul_f32 v46, v46, v44\n\t"
        "v_mov_b32 v60, 0\n\t"
        "v_mov_b32 v61, 0\n\t"
        "v_mov_b32_dpp v47, v46 row_shl:1 row_mask:0xf bank_mask:0xf bound_ctrl:1\n\t"
        "v_cvt_pk_bf16_f32 v46, v46, v47\n\t"
        "v_mov_b32 v62, 0\n\t"
        "v_readlane_b32 %[sa0], v46, 0\n\t"
        "v_mov_b32 v63, 0\n\t"
        "v_readlane_b32 %[sa1], v46, 2\n\t"
        "v_mov_b32 v64, 0\n\t"
        "v_readlane_b32 %[sa2], v46, 4\n\t"
        "v_mov_b32 v65, 0\n\t"
        "v_readlane_b32 %[sa3], v46, 6\n\t"
        "v_mov_b32 v66, 0\n\t"
        "v_readlane_b32 %[sa4], v46, 8\n\t"
        "v_mov_b32 v67, 0\n\t"
        "v_readlane_b32 %[sa5], v46, 10\n\t"
        "v_mov_b32 v68, 0\n\t"
        "v_readlane_b32 %[sa6], v46, 12\n\t"
        "v_readlane_b32 %[sa7], v46, 14\n\t"
        "v_readlane_b32 %[sa8], v46, 16\n\t"
        "v_readlane_b32 %[sa9], v46, 18\n\t"
        "v_readlane_b32 %[sa10], v46, 20\n\t"
        "v_readlane_b32 %[sa11], v46, 22\n\t"
        "v_dot2c_f32_bf16 v60, %[sa0], v112\n\t"
        "v_dot2c_f32_bf16 v61, %[sa0], v116\n\t"
        "v_dot2c_f32_bf16 v62, %[sa0], v113\n\t"
        "v_dot2c_f32_bf16 v63, %[sa0], v117\n\t"
        "v_dot2c_f32_bf16 v60, %[sa1], v120\n\t"
        "v_dot2c_f32_bf16 v61, %[sa1], v124\n\t"
        "v_dot2c_f32_bf16 v62, %[sa1], v121\n\t"
        "v_dot2c_f32_bf16 v63, %[sa1], v125\n\t"
        "v_dot2c_f32_bf16 v60, %[sa2], v128\n\t"
        "v_dot2c_f32_bf16 v61, %[sa2], v132\n\t"
        "v_dot2c_f32_bf16 v62, %[sa2], v129\n\t"
        "v_dot2c_f32_bf16 v63, %[sa2], v133\n\t"
        "v_dot2c_f32_bf16 v60, %[sa3], v136\n\t"
        "v_dot2c_f32_bf16 v61, %[sa3], v140\n\t"
        "v_dot2c_f32_bf16 v62, %[sa3], v137\n\t"
        "v_dot2c_f32_bf16 v63, %[sa3], v141\n\t"
        "v_dot2c_f32_bf16 v60, %[sa4], v144\n\t"
        "v_dot2c_f32_bf16 v61, %[sa4], v148\n\t"
        "v_dot2c_f32_bf16 v62, %[sa4], v145\n\t"
        "v_dot2c_f32_bf16 v63, %[sa4], v149\n\t"
        "v_dot2c_f32_bf16 v60, %[sa5], v152\n\t"
        "v_dot2c_f32_bf16 v61, %[sa5], v156\n\t"
        "v_dot2c_f32_bf16 v62, %[sa5], v153\n\t"
        "v_dot2c_f32_bf16 v63, %[sa5], v157\n\t"
        "v_dot2c_f32_bf16 v60, %[sa6], v160\n\t"
        "v_dot2c_f32_bf16 v61, %[sa6], v164\n\t"
        "v_dot2c_f32_bf16 v62, %[sa6], v161\n\t"
        "v_dot2c_f32_bf16 v63, %[sa6], v165\n\t"
        "v_dot2c_f32_bf16 v60, %[sa7], v168\n\t"
        "v_dot2c_f32_bf16 v61, %[sa7], v172\n\t"
        "v_dot2c_f32_bf16 v62, %[sa7], v169\n\t"
        "v_dot2c_f32_bf16 v63, %[sa7], v173\n\t"
        "v_dot2c_f32_bf16 v60, %[sa8], v176\n\t"
        "v_dot2c_f32_bf16 v61, %[sa8], v180\n\t"
        "v_dot2c_f32_bf16 v62, %[sa8], v177\n\t"
        "v_dot2c_f32_bf16 v63, %[sa8], v181\n\t"
        "v_dot2c_f32_bf16 v60, %[sa9], v184\n\t"
        "v_dot2c_f32_bf16 v61, %[sa9], v188\n\t"
        "v_dot2c_f32_bf16 v62, %[sa9], v185\n\t"
        "v_dot2c_f32_bf16 v63, %[sa9], v189\n\t"
        "v_dot2c_f32_bf16 v60, %[sa10], v192\n\t"
        "v_dot2c_f32_bf16 v61, %[sa10], v196\n\t"
        "v_dot2c_f32_bf16 v62, %[sa10], v193\n\t"
        "v_dot2c_f32_bf16 v63, %[sa10], v197\n\t"
        "v_dot2c_f32_bf16 v60, %[sa11], v200\n\t"
        "v_dot2c_f32_bf16 v61, %[sa11], v204\n\t"
        "v_dot2c_f32_bf16 v62, %[sa11], v201\n\t"
        "v_dot2c_f32_bf16 v63, %[sa11], v205\n\t"
        "v_dot2c_f32_bf16 v64, %[sa0], v114\n\t"
        "v_dot2c_f32_bf16 v65, %[sa0], v118\n\t"
        "v_dot2c_f32_bf16 v66, %[sa0], v115\n\t"
        "v_dot2c_f32_bf16 v67, %[sa0], v119\n\t"
        "v_dot2c_f32_bf16 v68, %[sa0], v208\n\t"
        "v_min_u32 v60, -2, v60\n\t"
        "v_min_u32 v61, -2, v61\n\t"
        "v_min_u32 v62, -2, v62\n\t"
        "v_min_u32 v63, -2, v63\n\t"
        "buffer_store_dwordx4 v[60:63], %[ya], %[ssrd], %[syb] offen sc1\n\t"
        "v_dot2c_f32_bf16 v64, %[sa1], v122\n\t"
        "v_dot2c_f32_bf16 v65, %[sa1], v126\n\t"
        "v_dot2c_f32_bf16 v66, %[sa1], v123\n\t"
        "v_dot2c_f32_bf16 v67, %[sa1], v127\n\t"
        "v_dot2c_f32_bf16 v68, %[sa1], v209\n\t"
        "v_dot2c_f32_bf16 v64, %[sa2], v130\n\t"
        "v_dot2c_f32_bf16 v65, %[sa2], v134\n\t"
        "v_dot2c_f32_bf16 v66, %[sa2], v131\n\t"
        "v_dot2c_f32_bf16 v67, %[sa2], v135\n\t"
        "v_dot2c_f32_bf16 v68, %[sa2], v210\n\t"
        "v_dot2c_f32_bf16 v64, %[sa3], v138\n\t"
        "v_dot2c_f32_bf16 v65, %[sa3], v142\n\t"
        "v_dot2c_f32_bf16 v66, %[sa3], v139\n\t"
        "v_dot2c_f32_bf16 v67, %[sa3], v143\n\t"
        "v_dot2c_f32_bf16 v68, %[sa3], v211\n\t"
        "v_dot2c_f32_bf16 v64, %[sa4], v146\n\t"
        "v_dot2c_f32_bf16 v65, %[sa4], v150\n\t"
        "v_dot2c_f32_bf16 v66, %[sa4], v147\n\t"
        "v_dot2c_f32_bf16 v67, %[sa4], v151\n\t"
        "v_dot2c_f32_bf16 v68, %[sa4], v212\n\t"
        "v_dot2c_f32_bf16 v64, %[sa5], v154\n\t"
        "v_dot2c_f32_bf16 v65, %[sa5], v158\n\t"
        "v_dot2c_f32_bf16 v66, %[sa5], v155\n\t"
        "v_dot2c_f32_bf16 v67, %[sa5], v159\n\t"
        "v_dot2c_f32_bf16 v68, %[sa5], v213\n\t"
        "v_dot2c_f32_bf16 v64, %[sa6], v162\n\t"
        "v_dot2c_f32_bf16 v65, %[sa6], v166\n\t"
        "v_dot2c_f32_bf16 v66, %[sa6], v163\n\t"
        "v_dot2c_f32_bf16 v67, %[sa6], v167\n\t"
        "v_dot2c_f32_bf16 v68, %[sa6], v214\n\t"
        "v_dot2c_f32_bf16 v64, %[sa7], v170\n\t"
        "v_dot2c_f32_bf16 v65, %[sa7], v174\n\t"
        "v_dot2c_f32_bf16 v66, %[sa7], v171\n\t"
        "v_dot2c_f32_bf16 v67, %[sa7], v175\n\t"
        "v_dot2c_f32_bf16 v68, %[sa7], v215\n\t"
        "v_dot2c_f32_bf16 v64, %[sa8], v178\n\t"
        "v_dot2c_f32_bf16 v65, %[sa8], v182\n\t"
        "v_dot2c_f32_bf16 v66, %[sa8], v179\n\t"
        "v_dot2c_f32_bf16 v67, %[sa8], v183\n\t"
        "v_dot2c_f32_bf16 v68, %[sa8], v216\n\t"
        "v_dot2c_f32_bf16 v64, %[sa9], v186\n\t"
        "v_dot2c_f32_bf16 v65, %[sa9], v190\n\t"
        "v_dot2c_f32_bf16 v66, %[sa9], v187\n\t"
        "v_dot2c_f32_bf16 v67, %[sa9], v191\n\t"
        "v_dot2c_f32_bf16 v68, %[sa9], v217\n\t"
        "v_dot2c_f32_bf16 v64, %[sa10], v194\n\t"
        "v_dot2c_f32_bf16 v65, %[sa10], v198\n\t"
        "v_dot2c_f32_bf16 v66, %[sa10], v195\n\t"
        "v_dot2c_f32_bf16 v67, %[sa10], v199\n\t"
        "v_dot2c_f32_bf16 v68, %[sa10], v218\n\t"
        "v_dot2c_f32_bf16 v64, %[sa11], v202\n\t"
        "v_dot2c_f32_bf16 v65, %[sa11], v206\n\t"
        "v_dot2c_f32_bf16 v66, %[sa11], v203\n\t"
        "v_dot2c_f32_bf16 v67, %[sa11], v207\n\t"
        "v_dot2c_f32_bf16 v68, %[sa11], v219\n\t"
        "v_min_u32 v64, -2, v64\n\t"
        "v_min_u32 v65, -2, v65\n\t"
        "v_min_u32 v66, -2, v66\n\t"
        "v_min_u32 v67, -2, v67\n\t"
        "v_min_u32 v68, -2, v68\n\t"
        "buffer_store_dwordx4 v[64:67], %[ya], %[ssrd], %[syc] offen sc1\n\t"
        "buffer_store_dword v68, %[ye], %[ssrd], %[syb] offen sc1\n\t"
        "v_add_u32 v45, 157472, %[c0]\n\t"
        "ds_write_b128 v45, v[24:27]\n\t"
        "s_mov_b32 exec_lo, 0x30003\n\t"
        "s_mov_b32 exec_hi, 0x30003\n\t"
        "ds_write_b128 v45, v[28:31] offset:1024\n\t"
        "v_mov_b32 v44, 153984\n\t"
        "v_mov_b32 v46, 1\n\t"
        "s_mov_b64 exec, 1\n\t"
        "ds_add_u32 v44, v46 offset:108\n\t"
        "s_mov_b64 exec, -1\n\t"
        "s_branch Le_%=\n\t"
        "Ly_%=:\n\t"
        "s_cmp_lt_i32 %[scn], 1\n\t"
        "s_cbranch_scc1 Lz_%=\n\t"
        "s_sub_u32 %[scn], %[scn], 16\n\t"
        "s_mov_b32 %[sre], 1\n\t"
        "s_nop 7\n\t"
        "s_nop 7\n\t"
        "s_waitcnt lgkmcnt(0)\n\t"
        "s_branch Ls_%=\n\t"
        "Lr_%=:\n\t"
        "ds_read_b64_tr_b16 v[64:65], v111 offset:32\n\t"
        "ds_read_b64_tr_b16 v[66:67], v111 offset:416\n\t"
        "ds_read_b64_tr_b16 v[68:69], v111 offset:1568\n\t"
        "ds_read_b64_tr_b16 v[70:71], v111 offset:1952\n\t"
        "ds_read_b64_tr_b16 v[72:73], v110 offset:24576\n\t"
        "ds_read_b64_tr_b16 v[74:75], v110 offset:24960\n\t"
        "ds_read_b64_tr_b16 v[76:77], v110 offset:26112\n\t"
        "ds_read_b64_tr_b16 v[78:79], v110 offset:26496\n\t"
        "ds_read_b64_tr_b16 v[80:81], v110 offset:52224\n\t"
        "ds_read_b64_tr_b16 v[82:83], v110 offset:52608\n\t"
        "ds_read_b64_tr_b16 v[84:85], v110 offset:53760\n\t"
        "ds_read_b64_tr_b16 v[86:87], v110 offset:54144\n\t"
        "ds_read_b64_tr_b16 v[88:89], v111 offset:24608\n\t"
        "ds_read_b64_tr_b16 v[90:91], v111 offset:24992\n\t"
        "ds_read_b64_tr_b16 v[92:93], v111 offset:26144\n\t"
        "ds_read_b64_tr_b16 v[94:95], v111 offset:26528\n\t"
        "ds_read_b64_tr_b16 v[220:221], v110 offset:3072\n\t"
        "ds_read_b64_tr_b16 v[222:223], v110 offset:3456\n\t"
        "ds_read_b64_tr_b16 v[224:225], v110 offset:4608\n\t"
        "ds_read_b64_tr_b16 v[226:227], v110 offset:4992\n\t"
        "ds_read_b64_tr_b16 v[228:229], v110 offset:30720\n\t"
        "ds_read_b64_tr_b16 v[230:231], v110 offset:31104\n\t"
        "ds_read_b64_tr_b16 v[232:233], v110 offset:32256\n\t"
        "ds_read_b64_tr_b16 v[234:235], v110 offset:32640\n\t"
        "ds_read_b64_tr_b16 v[236:237], v111 offset:3104\n\t"
        "ds_read_b64_tr_b16 v[238:239], v111 offset:3488\n\t"
        "ds_read_b64_tr_b16 v[240:241], v111 offset:4640\n\t"
        "ds_read_b64_tr_b16 v[242:243], v111 offset:5024\n\t"
        "ds_read_b128 v[244:247], v45 offset:6144\n\t"
        "ds_read_b128 v[248:251], v45 offset:7168\n\t"
        "s_waitcnt vmcnt(0)\n\t"
        "ds_read_b64_tr_b16 v[48:49], v110 offset:0\n\t"
        "ds_read_b64_tr_b16 v[50:51], v110 offset:384\n\t"
        "ds_read_b64_tr_b16 v[52:53], v110 offset:1536\n\t"
        "ds_read_b64_tr_b16 v[54:55], v110 offset:1920\n\t"
        "ds_read_b64_tr_b16 v[56:57], v110 offset:27648\n\t"
        "ds_read_b64_tr_b16 v[58:59], v110 offset:28032\n\t"
        "ds_read_b64_tr_b16 v[60:61], v110 offset:29184\n\t"
        "ds_read_b64_tr_b16 v[62:63], v110 offset:29568\n\t"
        "s_waitcnt lgkmcnt(0)\n\t"
        "s_branch Lb_%=\n\t"
        "Le_%=:\n\t"
        : [sa0] "=&s"(ap[0]), [sa1] "=&s"(ap[1]), [sa2] "=&s"(ap[2]), [sa3] "=&s"(ap[3]), [sa4] "=&s"(ap[4]), [sa5] "=&s"(ap[5]), [sa6] "=&s"(ap[6]), [sa7] "=&s"(ap[7]), [sa8] "=&s"(ap[8]), [sa9] "=&s"(ap[9]), [sa10] "=&s"(ap[10]), [sa11] "=&s"(ap[11]), [sm] "=&s"(m), [srs] "=&s"(rs), [sre] "+s"(re), [scn] "+s"(cn)
        : [ssrd] "s"(srd), [scr] "s"(cr), [c0] "v"(c0), [c1] "v"(c1), [ya] "v"(ya), [ye] "v"(ye),
          [syb] "s"(yb), [syc] "s"(yb + 262144), [spar] "s"(0xAAAAAAAAAAAAAAAAull)
        : "memory", "scc", "vcc", "v24", "v25", "v26", "v27", "v28", "v29", "v30", "v31", "v32", "v33", "v34", "v35", "v36", "v37", "v38", "v39", "v40", "v41", "v42", "v43", "v44", "v45", "v46", "v47");
}

DEV void poll_stats(u32x4 srd, u32 va, u32 so, float (&o)[3], u32& cn) {
    u32 t;
    asm volatile(
        "s_nop 4\n\t"
        "buffer_load_dwordx4 v[24:27], %[va], %[ssrd], %[so] offen offset:0 sc1\n\t"
        "buffer_load_dwordx4 v[28:31], %[va], %[ssrd], %[so] offen offset:1024 sc1\n\t"
        "buffer_load_dwordx4 v[32:35], %[va], %[ssrd], %[so] offen offset:256 sc1\n\t"
        "buffer_load_dwordx4 v[36:39], %[va], %[ssrd], %[so] offen offset:1280 sc1\n\t"
        "buffer_load_dwordx4 v[40:43], %[va], %[ssrd], %[so] offen offset:512 sc1\n\t"
        "buffer_load_dwordx4 v[44:47], %[va], %[ssrd], %[so] offen offset:1536 sc1\n\t"
        "buffer_load_dwordx4 v[48:51], %[va], %[ssrd], %[so] offen offset:768 sc1\n\t"
        "buffer_load_dwordx4 v[68:71], %[va], %[ssrd], %[so] offen offset:1792 sc1\n\t"
        "Lp_%=:\n\t"
        "s_waitcnt vmcnt(0)\n\t"
        "v_max3_u32 v27, v24, v25, v26\n\t"
        "v_max3_u32 v31, v28, v29, v30\n\t"
        "v_max3_u32 v35, v32, v33, v34\n\t"
        "v_max3_u32 v39, v36, v37, v38\n\t"
        "v_max3_u32 v43, v40, v41, v42\n\t"
        "v_max3_u32 v47, v44, v45, v46\n\t"
        "v_max3_u32 v51, v48, v49, v50\n\t"
        "v_max3_u32 v71, v68, v69, v70\n\t"
        "v_add_f32 v24, v24, v28\n\t"
        "v_add_f32 v32, v32, v36\n\t"
        "v_add_f32 v40, v40, v44\n\t"
        "v_add_f32 v48, v48, v68\n\t"
        "v_add_f32 v25, v25, v29\n\t"
        "v_add_f32 v33, v33, v37\n\t"
        "v_add_f32 v41, v41, v45\n\t"
        "v_add_f32 v49, v49, v69\n\t"
        "v_add_f32 v26, v26, v30\n\t"
        "v_add_f32 v34, v34, v38\n\t"
        "v_add_f32 v42, v42, v46\n\t"
        "v_add_f32 v50, v50, v70\n\t"
        "v_max3_u32 %[t], v27, v31, v35\n\t"
        "v_max3_u32 v39, v39, v43, v47\n\t"
        "v_max3_u32 %[t], %[t], v51, v71\n\t"
        "v_max_u32 %[t], %[t], v39\n\t"
        "v_cmp_eq_u32 vcc, -1, %[t]\n\t"
        "v_add_f32 v24, v24, v32\n\t"
        "v_add_f32 v40, v40, v48\n\t"
        "v_add_f32 v25, v25, v33\n\t"
        "v_add_f32 v41, v41, v49\n\t"
        "v_add_f32 v26, v26, v34\n\t"
        "v_add_f32 v42, v42, v50\n\t"
        "s_cbranch_vccz Lf0_%=\n\t"
        "buffer_load_dwordx4 v[24:27], %[va], %[ssrd], %[so] offen offset:0 sc1\n\t"
        "buffer_load_dwordx4 v[28:31], %[va], %[ssrd], %[so] offen offset:1024 sc1\n\t"
        "buffer_load_dwordx4 v[32:35], %[va], %[ssrd], %[so] offen offset:256 sc1\n\t"
        "buffer_load_dwordx4 v[36:39], %[va], %[ssrd], %[so] offen offset:1280 sc1\n\t"
        "buffer_load_dwordx4 v[40:43], %[va], %[ssrd], %[so] offen offset:512 sc1\n\t"
        "buffer_load_dwordx4 v[44:47], %[va], %[ssrd], %[so] offen offset:1536 sc1\n\t"
        "buffer_load_dwordx4 v[48:51], %[va], %[ssrd], %[so] offen offset:768 sc1\n\t"
        "buffer_load_dwordx4 v[68:71], %[va], %[ssrd], %[so] offen offset:1792 sc1\n\t"
        "s_sub_u32 %[scn], %[scn], 1\n\t"
        "s_cmp_lg_u32 %[scn], 0\n\t"
        "s_cbranch_scc1 Lp_%=\n\t"
        "s_waitcnt vmcnt(0)\n\t"
        "Lf0_%=:\n\t"
        "v_add_f32 %[o0], v24, v40\n\t"
        "v_add_f32 %[o1], v25, v41\n\t"
        "v_add_f32 %[o2], v26, v42\n\t"
        "Le_%=:\n\t"
        : [o0] "=&v"(o[0]), [o1] "=&v"(o[1]), [o2] "=&v"(o[2]), [t] "=&v"(t), [scn] "+s"(cn)
        : [ssrd] "s"(srd), [va] "v"(va), [so] "s"(so)
        : "memory", "scc", "vcc", "v24", "v25", "v26", "v27", "v28", "v29", "v30", "v31", "v32", "v33", "v34", "v35", "v36", "v37", "v38", "v39", "v40", "v41", "v42", "v43", "v44", "v45", "v46", "v47");
}
DEV void poll_stats1(u32x4 srd, u32 so, float (&o)[3], u32& cn) {
    u32 t;
    asm volatile(
        "s_nop 4\n\t"
        "buffer_load_dwordx4 v[24:27], v107, %[ssrd], %[so] offen sc1\n\t"
        "buffer_load_dwordx4 v[28:31], v107, %[ssrd], %[so] offen offset:1024 sc1\n\t"
        "Lp_%=:\n\t"
        "s_waitcnt vmcnt(0)\n\t"
        "v_max3_u32 %[t], v24, v25, v26\n\t"
        "v_max3_u32 %[t], %[t], v27, v28\n\t"
        "v_max3_u32 %[t], %[t], v29, v30\n\t"
        "v_max_u32 %[t], %[t], v31\n\t"
        "v_cmp_eq_u32 vcc, -1, %[t]\n\t"
        "s_cbranch_vccz Lf0_%=\n\t"
        "buffer_load_dwordx4 v[24:27], v107, %[ssrd], %[so] offen sc1\n\t"
        "buffer_load_dwordx4 v[28:31], v107, %[ssrd], %[so] offen offset:1024 sc1\n\t"
        "s_sub_u32 %[scn], %[scn], 1\n\t"
        "s_cmp_lg_u32 %[scn], 0\n\t"
        "s_cbranch_scc1 Lp_%=\n\t"
        "s_waitcnt vmcnt(0)\n\t"
        "Lf0_%=:\n\t"
        "v_add_f32 %[o0], v24, v28\n\t"
        "v_add_f32 %[o1], v25, v29\n\t"
        "v_add_f32 %[o2], v26, v30\n\t"
        "Le_%=:\n\t"
        : [o0] "=&v"(o[0]), [o1] "=&v"(o[1]), [o2] "=&v"(o[2]), [t] "=&v"(t), [scn] "+s"(cn)
        : [ssrd] "s"(srd), [so] "s"(so)
        : "memory", "scc", "vcc", "v24", "v25", "v26", "v27", "v28", "v29", "v30", "v31");
}
DEV void poll_keys2(u32x4 srd, u32 so, u32 tag, u32& o, u32& cn) {
    u32 t;
    asm volatile(
        "s_nop 4\n\t"
        "buffer_load_dwordx4 v[48:51], v107, %[ssrd], %[so] offen sc1\n\t"
        "Lp_%=:\n\t"
        "s_waitcnt vmcnt(0)\n\t"
        "v_xor_b32 %[t], %[stag], v49\n\t"
        "v_xor_b32 %[o0], %[stag], v51\n\t"
        "v_or_b32 %[t], %[t], %[o0]\n\t"
        "v_cmp_ne_u32 vcc, 0, %[t]\n\t"
        "s_cbranch_vccz Lf0_%=\n\t"
        "buffer_load_dwordx4 v[48:51], v107, %[ssrd], %[so] offen sc1\n\t"
        "s_sub_u32 %[scn], %[scn], 1\n\t"
        "s_cmp_lg_u32 %[scn], 0\n\t"
        "s_cbranch_scc1 Lp_%=\n\t"
        "s_waitcnt vmcnt(0)\n\t"
        "Lf0_%=:\n\t"
        "v_max_u32 %[o0], v48, v50\n\t"
        "Le_%=:\n\t"
        : [o0] "=&v"(o), [t] "=&v"(t), [scn] "+s"(cn)
        : [ssrd] "s"(srd), [so] "s"(so), [stag] "s"(tag)
        : "memory", "scc", "vcc");
}
DEV void poll_keys(u32x4 srd, u32 so, u32 tag, u64 lm, u32& o, u32& cn) {
    u32 t;
    asm volatile(
        "s_nop 4\n\t"
        "buffer_load_dwordx4 v[96:99], v107, %[ssrd], %[so] offen sc1\n\t"
        "v_add_u32 %[t], 98048, v107\n\t"
        "ds_read_b128 v[244:247], %[t] offset:6144\n\t"
        "ds_read_b128 v[248:251], %[t] offset:7168\n\t"
        "s_waitcnt lgkmcnt(0)\n\t"
        "Lp_%=:\n\t"
        "s_waitcnt vmcnt(0)\n\t"
        "v_xor_b32 %[t], %[stag], v97\n\t"
        "v_xor_b32 %[o0], %[stag], v99\n\t"
        "v_or_b32 %[t], %[t], %[o0]\n\t"
        "v_max_u32 %[o0], v96, v98\n\t"
        "v_cndmask_b32 %[t], %[t], 0, %[slm]\n\t"
        "v_cmp_ne_u32 vcc, 0, %[t]\n\t"
        "s_cbranch_vccz Lf0_%=\n\t"
        "buffer_load_dwordx4 v[96:99], v107, %[ssrd], %[so] offen sc1\n\t"
        "s_sub_u32 %[scn], %[scn], 1\n\t"
        "s_cmp_lg_u32 %[scn], 0\n\t"
        "s_cbranch_scc1 Lp_%=\n\t"
        "s_waitcnt vmcnt(0)\n\t"
        "Lf0_%=:\n\t"
        "v_cndmask_b32 %[o0], %[o0], 0, %[slm]\n\t"
        "Le_%=:\n\t"
        : [o0] "=&v"(o), [t] "=&v"(t), [scn] "+s"(cn)
        : [ssrd] "s"(srd), [so] "s"(so), [stag] "s"(tag), [slm] "s"(lm)
        : "memory", "scc", "vcc");
}
DEV void poll_flags(u32x4 srd, u32 fo, u32 tag, u32& cn) {
    u32 t, d0;
    asm volatile(
        "s_nop 4\n\t"
        "s_sleep 5\n\t"
        "buffer_load_dword v48, %[fo], %[ssrd], 0 offen sc1\n\t"
        "Lp_%=:\n\t"
        "s_waitcnt vmcnt(0)\n\t"
        "v_cmp_ne_u32 vcc, %[stag], v48\n\t"
        "s_cbranch_vccz Lf0_%=\n\t"
        "buffer_load_dword v48, %[fo], %[ssrd], 0 offen sc1\n\t"
        "s_sub_u32 %[scn], %[scn], 1\n\t"
        "s_cmp_lg_u32 %[scn], 0\n\t"
        "s_cbranch_scc1 Lp_%=\n\t"
        "s_waitcnt vmcnt(0)\n\t"
        "Lf0_%=:\n\t"
        "s_nop 0\n\t"
        "Le_%=:\n\t"
        : [d0] "=&s"(d0), [t] "=&v"(t), [scn] "+s"(cn)
        : [ssrd] "s"(srd), [fo] "v"(fo), [stag] "s"(tag)
        : "memory", "scc", "vcc");
}
DEV void poll_flagh(u32x4 srd, u32 fo, u32 tag, u32& cn) {
    u32 t, d0;
    asm volatile(
        "s_nop 4\n\t"
        "buffer_load_dword v48, %[fo], %[ssrd], 0 offen sc1\n\t"
        "Lp_%=:\n\t"
        "s_waitcnt vmcnt(0)\n\t"
        "v_cmp_ne_u32 vcc, %[stag], v48\n\t"
        "s_cbranch_vccz Lf0_%=\n\t"
        "buffer_load_dword v48, %[fo], %[ssrd], 0 offen sc1\n\t"
        "s_sub_u32 %[scn], %[scn], 1\n\t"
        "s_cmp_lg_u32 %[scn], 0\n\t"
        "s_cbranch_scc1 Lp_%=\n\t"
        "s_waitcnt vmcnt(0)\n\t"
        "Lf0_%=:\n\t"
        "s_nop 0\n\t"
        "Le_%=:\n\t"
        : [d0] "=&s"(d0), [t] "=&v"(t), [scn] "+s"(cn)
        : [ssrd] "s"(srd), [fo] "v"(fo), [stag] "s"(tag)
        : "memory", "scc", "vcc");
}
DEV void reduce(u32x4 srd, u32x4 out, u32 va, u32 so, int xa, float m, float rs, float sg, u32 xo, u32 oo, int fa, u32& ab, u32& cn) {
    u32 t, n;
    asm volatile(
        "ds_read_b32 v78, %[xa]\n\t"
        "v_mov_b32 v79, %[sm]\n\t"
        "v_mov_b32 v76, %[sg]\n\t"
        "v_mov_b32 v77, %[sg]\n\t"
        "v_mul_f32 v79, %[srs], -v79\n\t"
        "s_cmp_eq_u32 %[sab], 2\n\t"
        "s_cselect_b32 %[sn], 2, 1\n\t"
        "Lw_%=:\n\t"
        "ds_read_b32 %[t], %[fa]\n\t"
        "s_sleep 1\n\t"
        "s_sub_u32 %[sn], %[sn], 1\n\t"
        "s_waitcnt lgkmcnt(0)\n\t"
        "v_cmp_eq_u32 vcc, %[sab], %[t]\n\t"
        "s_cbranch_vccnz Le_%=\n\t"
        "s_cbranch_scc0 Lw_%=\n\t"
        "Lp_%=:\n\t"
        "buffer_load_dwordx4 v[48:51], %[va], %[ssrd], %[so0] offen sc1\n\t"
        "buffer_load_dwordx4 v[52:55], %[va], %[ssrd], %[so1] offen sc1\n\t"
        "buffer_load_dwordx4 v[56:59], %[va], %[ssrd], %[so2] offen sc1\n\t"
        "buffer_load_dwordx4 v[60:63], %[va], %[ssrd], %[so3] offen sc1\n\t"
        "v_lshlrev_b32 v72, 16, v78\n\t"
        "v_and_b32 v73, 0xffff0000, v78\n\t"
        "v_fma_f32 v74, v72, %[srs], v79\n\t"
        "v_fma_f32 v75, v73, %[srs], v79\n\t"
        "v_cvt_pk_bf16_f32 v75, v74, v75\n\t"
        "v_lshlrev_b32 v74, 16, v75\n\t"
        "v_and_b32 v75, 0xffff0000, v75\n\t"
        "s_cmp_eq_u32 %[sab], 2\n\t"
        "s_cselect_b32 %[sn], 3, 0\n\t"
        "Lx_%=:\n\t"
        "ds_read_b32 %[t], %[fa]\n\t"
        "s_sleep 1\n\t"
        "s_sub_u32 %[sn], %[sn], 1\n\t"
        "s_waitcnt lgkmcnt(0)\n\t"
        "v_cmp_eq_u32 vcc, %[sab], %[t]\n\t"
        "s_cbranch_vccnz Le_%=\n\t"
        "s_cbranch_scc0 Lx_%=\n\t"
        "s_waitcnt vmcnt(0)\n\t"
        "ds_read_b32 %[t], %[fa]\n\t"
        ".long 0xd3b24040, 0x18026930 ; v_pk_add_f32 v(64:65), v(48:49), v(52:53)\n\t"
        ".long 0xd3b24042, 0x18026d32 ; v_pk_add_f32 v(66:67), v(50:51), v(54:55)\n\t"
        ".long 0xd3b24044, 0x18027938 ; v_pk_add_f32 v(68:69), v(56:57), v(60:61)\n\t"
        ".long 0xd3b24046, 0x18027d3a ; v_pk_add_f32 v(70:71), v(58:59), v(62:63)\n\t"
        ".long 0xd3b24040, 0x18028940 ; v_pk_add_f32 v(64:65), v(64:65), v(68:69)\n\t"
        ".long 0xd3b24042, 0x18028d42 ; v_pk_add_f32 v(66:67), v(66:67), v(70:71)\n\t"
        "s_nop 1\n\t"
        "v_permlane32_swap_b32 v64, v66\n\t"
        "v_permlane32_swap_b32 v65, v67\n\t"
        ".long 0xd3b24040, 0x18028540 ; v_pk_add_f32 v(64:65), v(64:65), v(66:67)\n\t"
        "v_cmp_u_f32 vcc, v64, v65\n\t"
        "s_nop 0\n\t"
        "v_add_f32_dpp v64, v64, v64 row_ror:8 row_mask:0xf bank_mask:0xf\n\t"
        "v_add_f32_dpp v65, v65, v65 row_ror:8 row_mask:0xf bank_mask:0xf\n\t"
        "s_cbranch_vccnz Ln_%=\n\t"
        "Lf_%=:\n\t"
        "v_add_f32_dpp v64, v64, v64 row_ror:4 row_mask:0xf bank_mask:0xf\n\t"
        "v_add_f32_dpp v65, v65, v65 row_ror:4 row_mask:0xf bank_mask:0xf\n\t"
        "s_nop 0\n\t"
        "v_add_f32_dpp v64, v64, v64 row_ror:2 row_mask:0xf bank_mask:0xf\n\t"
        "v_add_f32_dpp v65, v65, v65 row_ror:2 row_mask:0xf bank_mask:0xf\n\t"
        "s_waitcnt lgkmcnt(0)\n\t"
        "v_add_f32_dpp v64, v64, v64 row_ror:1 row_mask:0xf bank_mask:0xf\n\t"
        "v_add_f32_dpp v65, v65, v65 row_ror:1 row_mask:0xf bank_mask:0xf\n\t"
        "v_readfirstlane_b32 %[sn], %[t]\n\t"
        "v_add_f32_dpp v64, v64, v64 row_bcast:15 row_mask:0xa bank_mask:0xf\n\t"
        "v_add_f32_dpp v65, v65, v65 row_bcast:15 row_mask:0xa bank_mask:0xf\n\t"
        "v_cvt_pk_bf16_f32 v66, v64, v65\n\t"
        "v_lshlrev_b32 v64, 16, v66\n\t"
        "v_and_b32 v65, 0xffff0000, v66\n\t"
        ".long 0xd3b14040, 0x18029940 ; v_pk_mul_f32 v(64:65), v(64:65), v(76:77)\n\t"
        "v_cvt_pk_bf16_f32 v66, v64, v65\n\t"
        "v_lshlrev_b32 v64, 16, v66\n\t"
        "v_and_b32 v65, 0xffff0000, v66\n\t"
        ".long 0xd3b24040, 0x18029540 ; v_pk_add_f32 v(64:65), v(64:65), v(74:75)\n\t"
        "s_or_b32 %[sn], %[sn], %[sab]\n\t"
        "v_cvt_pk_bf16_f32 v66, v64, v65\n\t"
        "v_lshlrev_b32 v64, 16, v66\n\t"
        "v_and_b32 v65, 0xffff0000, v66\n\t"
        ".long 0xd3b24040, 0x18029140 ; v_pk_add_f32 v(64:65), v(64:65), v(72:73)\n\t"
        "s_bitcmp1_b32 %[sn], 0\n\t"
        "v_cvt_pk_bf16_f32 v66, v64, v65\n\t"
        "s_cbranch_scc0 Lg_%=\n\t"
        "Lh_%=:\n\t"
        "buffer_store_dword v66, %[oo], %[sout], 0 offen\n\t"
        "v_min_u32 v67, -2, v66\n\t"
        "buffer_store_dword v67, %[xo], %[ssrd], 0 offen sc1\n\t"
        "s_mov_b32 %[sab], 0\n\t"
        "s_branch Le_%=\n\t"
        "Ln_%=:\n\t"
        "v_max3_u32 v48, v48, v49, v50\n\t"
        "v_max3_u32 v48, v48, v51, v52\n\t"
        "v_max3_u32 v48, v48, v53, v54\n\t"
        "v_max3_u32 v48, v48, v55, v56\n\t"
        "v_max3_u32 v48, v48, v57, v58\n\t"
        "v_max3_u32 v48, v48, v59, v60\n\t"
        "v_max3_u32 v48, v48, v61, v62\n\t"
        "v_max_u32 v48, v48, v63\n\t"
        "v_cmp_eq_u32 vcc, -1, v48\n\t"
        "s_cbranch_vccz Lf_%=\n\t"
        "s_cmp_lt_i32 %[scn], 1\n\t"
        "s_cbranch_scc1 Lf_%=\n\t"
        "s_sub_u32 %[scn], %[scn], 16\n\t"
        "s_branch Lp_%=\n\t"
        "Lg_%=:\n\t"
        "ds_read_b32 %[t], %[fa]\n\t"
        "s_cmp_lt_i32 %[scn], 1\n\t"
        "s_cbranch_scc1 Lh_%=\n\t"
        "s_sub_u32 %[scn], %[scn], 1\n\t"
        "s_waitcnt lgkmcnt(0)\n\t"
        "v_readfirstlane_b32 %[sn], %[t]\n\t"
        "s_cmp_eq_u32 %[sn], 1\n\t"
        "s_cbranch_scc1 Lh_%=\n\t"
        "s_cmp_eq_u32 %[sn], 0\n\t"
        "s_cbranch_scc1 Lg_%=\n\t"
        "Le_%=:\n\t"
        "s_waitcnt lgkmcnt(0)\n\t"
        : [t] "=&v"(t), [sn] "=&s"(n), [sab] "+s"(ab), [scn] "+s"(cn)
        : [ssrd] "s"(srd), [sout] "s"(out), [va] "v"(va), [xa] "v"(xa), [sm] "s"(m), [srs] "s"(rs), [sg] "v"(sg), [xo] "v"(xo), [oo] "v"(oo), [fa] "v"(fa), [so0] "s"(so + 0), [so1] "s"(so + 4096), [so2] "s"(so + 8192), [so3] "s"(so + 12288)
        : "memory", "scc", "vcc");
}
DEV void poll_row(u32x4 srd, u32 v1, u32 v2, u32 (&o)[6], u32& cn) {
    u32 t;
    asm volatile(
        "s_nop 4\n\t"
        "buffer_load_dwordx2 v[48:49], %[v1], %[ssrd], 0 offen sc1\n\t"
        "buffer_load_dwordx2 v[50:51], %[v1], %[ssrd], 0 offen offset:2048 sc1\n\t"
        "buffer_load_dwordx2 v[52:53], %[v2], %[ssrd], 0 offen sc1\n\t"
        "Lp_%=:\n\t"
        "s_waitcnt vmcnt(0)\n\t"
        "v_max3_u32 %[t], v48, v49, v50\n\t"
        "v_max3_u32 %[t], %[t], v51, v52\n\t"
        "v_max_u32 %[t], %[t], v53\n\t"
        "v_cmp_eq_u32 vcc, -1, %[t]\n\t"
        "s_cbranch_vccz Lf0_%=\n\t"
        "buffer_load_dwordx2 v[48:49], %[v1], %[ssrd], 0 offen sc1\n\t"
        "buffer_load_dwordx2 v[50:51], %[v1], %[ssrd], 0 offen offset:2048 sc1\n\t"
        "buffer_load_dwordx2 v[52:53], %[v2], %[ssrd], 0 offen sc1\n\t"
        "s_sub_u32 %[scn], %[scn], 1\n\t"
        "s_cmp_lg_u32 %[scn], 0\n\t"
        "s_cbranch_scc1 Lp_%=\n\t"
        "s_waitcnt vmcnt(0)\n\t"
        "Lf0_%=:\n\t"
        "v_mov_b32 %[o0], v48\n\t"
        "v_mov_b32 %[o1], v49\n\t"
        "v_mov_b32 %[o2], v50\n\t"
        "v_mov_b32 %[o3], v51\n\t"
        "v_mov_b32 %[o4], v52\n\t"
        "v_mov_b32 %[o5], v53\n\t"
        "Le_%=:\n\t"
        : [o0] "=&v"(o[0]), [o1] "=&v"(o[1]), [o2] "=&v"(o[2]), [o3] "=&v"(o[3]), [o4] "=&v"(o[4]), [o5] "=&v"(o[5]), [t] "=&v"(t), [scn] "+s"(cn)
        : [ssrd] "s"(srd), [v1] "v"(v1), [v2] "v"(v2)
        : "memory", "scc", "vcc");
}

DEV int ffn_slice(int b, int& nu) {
    const int j = b >> 3, t = j - 20, t3 = (t * 43) >> 7, r = t - 3 * t3;
    nu = (j >= 20 && r < 2) ? 24 : 16;
    return (b & 7) * 576 + (j < 20 ? j * 16 : 320 + t3 * 64 + r * 24);
}
DEV void gu_dma(const u16* wg, const u16* wu, int lane, int b, int i0, int i1) {
    int nu;
    const int f0 = ffn_slice(b, nu);
    u32 gv[3];
#pragma unroll
    for (int m = 0; m < 3; ++m) {
        const int q = 64 * m + lane, sg = (q * 171) >> 9, part = q - 3 * sg;
        const int kin = 32 * ((sg >> 1) & 1) + 8 * (sg >> 4) + (sg & 5) + 2 * ((sg >> 3) & 1);
        gv[m] = (u32)kin * 9216u + f0 * 2u + ((part == 2 && nu == 16) ? 16u : part * 16u);
    }
#pragma nounroll
    for (int i = i0; i < i1; ++i) {
        int gi = i + 2; if (i0 == 0) { gi += (0xC840 >> (((b >> 3) & 3) * 4)) & 15; if (gi >= 17) gi -= 15; } else if (gi >= 18) gi -= 18;
        const u32x4 sd = mk_srd(gi < 9 ? wg : wu, 5308416);
        asm volatile("" : "+s"(gi));
        int g9 = gi - 9 * ((gi * 57) >> 9);
        asm volatile("" : "+s"(g9));
        const u32 so = g9 * (64 * 9216), ld = L_G + gi * 3072;
#pragma unroll
        for (int m = 0; m < 3; ++m) dma16g(sd, gv[m], so, ld + m * 1024);
    }
}

DEV void xt_upd(u32 (&XT)[10], float rm, float rrs) {
#pragma unroll
    for (int i = 0; i < 10; ++i) {
        const float a = lo16(XT[i]), c = hi16(XT[i]);
        const u32 h = pk2((a - rm) * rrs, (c - rm) * rrs);
        XT[i] = pk2(a + lo16(h), c + hi16(h));
    }
}

DEV void gu_pf(const u16* wg, const u16* wu, int lane, int b, int i) {
    const int j = b >> 3;
    const u32x4 sg = mk_srd(j < 16 ? wg : wu, 5308416);
    const int q = i * 54 + lane, r = (q * 57) >> 9;
    const u32 o = lane < 54 ? (u32)((j & 15) * 36 + r) * 9216u + (b & 7) * 1152u + (q - 9 * r) * 128u : (u32)OOB;
    asm volatile("s_nop 4\n\tbuffer_load_dword v98, %0, %1, 0 offen" :: "v"(o), "s"(sg) : "memory");
}

DEV int k_pos(int p, int cb) { return p >= 9 * cb + 8 ? p : (p >= 9 * cb ? p - 9 * cb : p + 8); }
DEV u32 w_voff(int wp, int r16, int p, int cb) {
    int ch = 16 * wp + r16; if (ch > 107) ch = 107;
    const int sec = (ch * 57) >> 11, c = ch - 36 * sec;
    return (u32)(8 * p) * 3456u + (sec == 2 ? 0u : (u32)(sec + 1) * 1152u) + cb * 144u + c * 4u;
}

__global__ void NOPK __attribute__((amdgpu_num_vgpr(24))) __launch_bounds__(512, 2)
mega(const u16* __restrict__ x0, const u16* __restrict__ wqkv, const u16* __restrict__ wo, const u16* __restrict__ wr,
     const u16* __restrict__ wg, const u16* __restrict__ wu, const u16* __restrict__ wd, u16* __restrict__ out, char* __restrict__ ws) {
    const int w_ = __builtin_amdgcn_readfirstlane(threadIdx.x >> 6);
    const int b_ = blockIdx.x;
    {
        u64 pc; u32 off;
        asm volatile("s_getpc_b64 %0\n"
                     "Lcode0:\n\t"
                     "v_lshlrev_b32 %1, 7, %2\n\t"
                     "v_and_b32 v99, 3, %2\n\t"
                     "v_cmp_gt_u32 vcc, Lcode1-Lcode0, %1\n\t"
                     "s_mov_b64 exec, vcc\n\t"
                     "v_cmp_eq_u32 vcc, %3, v99\n\t"
                     "s_and_b64 exec, exec, vcc\n\t"
                     "s_cbranch_execz Lcode2\n\t"
                     "global_load_dword v99, %1, %0\n"
                     "Lcode2:\n\t"
                     "s_mov_b64 exec, -1"
                     : "=&s"(pc), "=&v"(off) : "v"(threadIdx.x), "s"((b_ >> 3) & 3) : "vcc", "scc", "memory");
    }
    const __amdgpu_buffer_rsrc_t rs = __builtin_amdgcn_make_buffer_rsrc((void*)ws, 0, WS_BYTES, 0x00020000);
    u32 tag_, tmo = 0;
    {
        IDS
        int u = rg * 4 + (w & 3);
        const bool uok = u < 126;
        if (u > 125) u = 125;
        const int su = (u * 147) >> 10, wp = u - 7 * su;
        u32 wv = w_voff(wp, r16, g, cb), so = (u32)su * 110592u;
        sts32(L_BC + lane * 4, 0u);
        asm volatile("" : "+v"(wv), "+s"(so));
        asm volatile("" :: "s"(x0), "s"(wqkv), "s"(wo), "s"(ws));
        wld8p(mk_srd(wqkv, 1990656), wv, so, w);
        bar();
        { const char* ep_ = ws; asm volatile("" : "+s"(ep_)); tag_ = *(const __attribute__((address_space(4))) u32*)(ep_ + OFF_EPOCH) + 1u; }
        const int kp = k_pos(4 * su + g, cb);
        wunit_st(mk_srd(ws, WS_BYTES), (uok && !(wp == 6 && r16 >= 12)) ? OFF_WI + ((cb * 7 + wp) * 18 + (kp >> 2)) * 2048 + ((kp & 3) * (wp == 6 ? 12 : 16) + r16) * 16 : OOB, w);
        x_dma(mk_srd(x0 - 9216, 2359296 + 18432), (u32)((rg * 64 + (lane >> 3)) * 1152) + (u32)(((lane ^ (lane >> 4)) & 7) << 4), rg == 0 ? OOB : 0,
              ((XCI >> (4 * w)) & 15) + cb, (XCN >> (4 * w)) & 15);
        if (w < 4) {
            const int fa = lane < WREP ? OFF_FLAG + ((lane * 8 + cb) * 32 + rg) * FST : OOB;
            if (w > 0) { VMWAIT(10); } else { VMWAIT(0); }
            BST32(tag_, lds_inc_rtn(L_BC + BC_FC) == 3u ? fa : OOB, 0);
            __builtin_amdgcn_s_setprio(0);
        }
#define PFI(w) ((w) >= 4 ? (w) - 4 : (w) + 2)
#define PFON(w) ((w) > 0 && (w) < 7 && PFI(w) < PFA)
        if (PFON(w)) gu_pf(wg, wu, lane, b, PFI(w));
        if (w == 6) {
#pragma unroll
            for (int i = 0; i < 3; ++i) {
                const int q = lane + 64 * i;
                if (q < 144) sts128((q >> 4) * XCH + (q & 15) * 16, (u32x4){ONES2, ONES2, ONES2, ONES2});
            }
        }
        lc_set(lane_consts(w_, b_));
    }
    MidS a;
    {
        IDS
        const int rt4 = w & 3, kh = w >> 2, hp = 2 * cb + kh;
        const u32 obase = OFF_OT + (rg * 4 + rt4) * 18432;
        const u32 wl = OFF_WOI + cb * 82944;
        const u32 lo = w < 4 ? 16384 + w * 6144 : (w < 7 ? 64512 + (w - 4) * 6144 : 57344), eo = kh * 41472 + rt4 * 4096;
        a.n7 = rt4 == 3 ? 1024 : 0;
        a.ob = obase + kh * 9216; a.st = obase + hp * 1024; a.tt = obase + 16384 + hp * 128;
        a.c0 = (rg == 0 && rt4 == 0) ? 0xff61b1e6u : 0u;
        a.l1d = kh ? (u32)-(L_WOE + 9 * 4608) : 5 * 4608; a.tvd = kh ? 128 : 8;
        a.wi = wl + lo; a.ld = lo >= 64512 ? lo - 64512 : L_WOE + lo; a.wx = wl + eo; a.ldx = L_WOE + eo;
        a.msk = (u32)(kh - 1); a.scr = rt4 == 2 ? L_SCR3 : L_SCR + (rt4 == 3 ? 2 : rt4) * 4608; a.pk = L_PK(rt4);
        a.stat = OFF_STATS + (rg * 4 + rt4) * 2048 + cb * 256;
        a.x1 = OFF_X1G + rg * 73728 + 9 * cb * 1024 + rt4 * 256;
        asm volatile("" : "+s"(a.ob), "+s"(a.st), "+s"(a.tt), "+s"(a.c0), "+s"(a.l1d), "+s"(a.tvd), "+s"(a.wi), "+s"(a.ld), "+s"(a.wx), "+s"(a.ldx), "+s"(a.msk),
                          "+s"(a.scr), "+s"(a.stat), "+s"(a.x1), "+s"(a.pk), "+s"(a.n7));
    }
    asm volatile("" : "+s"(tag_));
#define VA_MASK (CSL(w) < 6 ? ~0ull : 0x0000FFFFFFFFFFFFull)
#define V0_MASK (CSL(w) < 4 ? ~0ull : (CSL(w) == 4 ? 0xFFFFFFFFull : 0ull))
    a.lb = L_WOE + (w_ >> 2) * 9 * 4608;
    a.skip = 0;
    a.wc = L_BC + BC_WC + (w_ >> 2) * 4;
    u64 lm0 = 0;
#pragma nounroll
    for (int layer = 0; layer < 2; ++layer) {
    u32 tag = tag_; asm volatile("" : "+s"(tag));
    if (layer == 0) {
    if (w_ < 7) {
        IDS
        const u32x4 sws = mk_srd(ws, WS_BYTES);
        const u32 fo = w > 0 ? OOB : OFF_GFLAG + (lane < 18 ? lane * 8 + cb : (((lane & 8) && rg > 0) ? rg - 1 : rg) * 8 + (lane & 7)) * 16;
        int xt[3];
        {
            const int row = 16 + 16 * (w & 3) + r16;
#pragma unroll
            for (int p = 0; p < 3; ++p) {
                const int pc = 9 * cb + 4 * p + (p < 2 ? g : 0);
                xt[p] = (pc >> 3) * XCH + row * 128 + (((pc ^ (row >> 1)) & 7) << 4) + (p < 2 ? 0 : (g & 1) * 8);
            }
            asm volatile("" : "+v"(xt[0]), "+v"(xt[1]), "+v"(xt[2]));
        }
        if (w >= 1) {
            if (PFON(w)) { VMWAIT(1); } else { VMWAIT(0); }
            lds_inc(L_BC + BC_XC, lane);
        }
        if (w >= 4) {
            LWAIT0(L_BC + BC_XC, 6, 22);
            float xs[2], xq[2];
            xstats((w - 4) * 2048 + r16 * 128 + (((g ^ (r16 >> 1)) & 7) << 4), xs, xq);
#pragma unroll
            for (int t = 0; t < 2; ++t) {
                const int row = 16 * (w - 4 + 3 * t) + r16;
                float m = xs[t] * (1.f / 576.f);
                float rsd = ln_rs(fmaxf(xq[t] * (1.f / 576.f) - m * m, 0.f));
                m = ln_m(m);
                if (row < 2) { m = 0.f; rsd = 0.f; }
                if (g == (r16 >> 2) && row < 80) sts64(L_MR + row * 8, (u32x2){__float_as_uint(m), __float_as_uint(rsd)});
            }
            lds_inc(L_BC + BC_MC, lane);
        }
        if (w == 0) {
            u32 cn = SPIN_MAX;
            poll_flags(sws, OFF_FLAG + ((rg & (WREP - 1)) * 256 + cb * 32 + (lane & 31)) * FST, tag, cn);
            sts32v(L_BC + BC_GC, 1u);
            if (cn == 0) TMO(3);
        } else {
            LWAIT0(L_BC + BC_GC, 1, 23);
        }
        LWAIT0(L_BC + BC_XC, 6, 22);
        {
            const u32 ib = OFF_WI + (cb * 7 + CSL(w)) * 36864;
            u32 cn = SPIN_MAX;
            gemm0(sws, r16 * 128 + (((g ^ (r16 >> 1)) & 7) << 4), (CSL(w) == 6 ? g * 12 + (r16 < 12 ? r16 : 11) : lane) * 16, ib, xt, 2048 + 2048 * (w & 3), 3u << cb, g >= cb ? XCH : 0, g + 4 >= cb ? XCH : 0, fo, w > 0 ? 0u : tag, L_CS + w * 128, VA_MASK, V0_MASK, cn);
            if ((int)cn < 1) TMO(25);
        }
    } else {
        IDS
        __builtin_amdgcn_s_setprio(3);
        {
            u32 U[3][8];
            const int ks = rg < 18 ? rg : 0;
            const u32 rb = rg < 18 ? (u32)(ks < 16 ? 36 * ks + 4 * g : 36 * (8 * (ks - 16) + 2 * g) + 16) * 1152u + cb * 144u : (u32)OOB;
#pragma unroll
            for (int p = 0; p < 3; ++p) old8(U[p], mk_srd(wo, 663552), rb + p * 64u + (p < 2 ? r16 * 4u : ((r16 & 7) >> 1) * 4u), (ks < 16 ? 20 : 36) * 1152);
            u32 sw_ = SENT; asm volatile("" : "+v"(sw_));
            const u32x4 sv = {sw_, sw_, sw_, sw_};
            const int ob = OFF_OT + rg * 4 * 18432;
            LWAIT(L_BC + BC_FC, 4, 43);
#pragma unroll
            for (int i = 0; i < 8; ++i) BST128(sv, lane * 16 + (i & 1) * 1024, ob + (i >> 1) * 18432 + cb * 2048);
            BST128(sv, g * 18432 + r16 * 16, ob + 16384 + cb * 256);
            BST128(sv, g * 2048 + r16 * 16, OFF_STATS + rg * 4 * 2048 + cb * 256);
            const int wb = rg < 18 ? OFF_WOI + cb * 82944 + ks * 4608 : OOB;
#pragma unroll
            for (int p = 0; p < 3; ++p) {
                vmwait8n(U[p], 10);
                u32x4 f0, f1;
                wfrag(U[p], f0, f1);
                if (p < 2) { BST128(f0, wb + p * 2048 + lane * 16, 0); BST128(f1, wb + p * 2048 + 1024 + lane * 16, 0); }
                else BST128((r16 & 1) ? f1 : f0, r16 < 8 ? wb + 4096 + (g * 8 + r16) * 16 : OOB, 0);
            }
        }
        u32 wq[6];
        {
            const u32x4 sr = mk_srd(wr, 1152);
#pragma unroll
            for (int i = 0; i < 5; ++i) bld32(wq[i], sr, lane * 4u, 0u, 256 * i);
            bld32(wq[5], sr, lane < 32 ? cb * 144u + lane * 4u : ((lane < 40 && !(lane & 2)) ? cb * 144u + 128u + ((lane >> 2) & 1) * 8u + (lane & 1) * 4u : (u32)OOB), 0u, 0);
        }
        { int nu; const int f0 = ffn_slice(b, nu); dload(mk_srd(wd, (f0 + nu) * 1152), lane * 2u, f0 * 1152u); }
        asm volatile("s_waitcnt vmcnt(%6)" : "+v"(wq[0]), "+v"(wq[1]), "+v"(wq[2]), "+v"(wq[3]), "+v"(wq[4]), "+v"(wq[5]) : "n"(48) : "memory");
        if (lane == 0) BST32(tag, OFF_GFLAG + (rg * 8 + cb) * 16, 0);
        {
            float sacc = 0.f;
#pragma unroll
            for (int q = 0; q < 5; ++q) sacc = dot2(wq[q], ONES2, sacc);
            sacc = gsum(rsum(sacc));
            if (lane < 48) sts32(L_WR + lane * 4, wq[5]);
            if (lane < 36) sts32(L_WR + 192 + lane * 4, ONES2);
            if (lane == 0) sts32(L_BC + BC_WRS, __float_as_uint(sacc));
        }
        asm volatile("s_nop 4\n\tbuffer_load_dword v98, %0, %1, 0 offen sc1" :: "v"(lane < 2 ? OFF_KEY + b * 256 + lane * 128 : (lane == 2 ? OFF_XFLAG + b * 128 : OOB)), "s"(mk_srd(ws, WS_BYTES)) : "memory");
        LWAIT(L_BC + BC_GC, 1, 31);
        __builtin_amdgcn_s_sleep(30);
        __builtin_amdgcn_s_setprio(3);
#pragma unroll
        for (int i = PFA; i < 6; ++i) gu_pf(wg, wu, lane, b, i);
        vmwaitn(6 - PFA);
        dpermx(); dperm(); gu_set();
        VMWAIT(0);
        __builtin_amdgcn_s_setprio(3);
        gu_dma(wg, wu, lane, b, 0, 15);
        {
            u32 sw_ = SENT; asm volatile("" : "+v"(sw_));
            const u32x4 sv = {sw_, sw_, sw_, sw_};
            const int l16 = lane * 16;
#pragma unroll
            for (int l = 0; l < 2; ++l) {
                BST128(sv, (r16 < 9 ? g * 1280 + cb * 144 + r16 * 16 : ((r16 == 9 && cb == 0) ? g * 1280 + 1152 : OOB)), OFF_CROW + (l * 128 + rg * 4) * 1280);
            }
            if (b < 144) {
#pragma unroll
                for (int i = 0; i < 4; ++i) BST128(sv, l16 + i * 1024, OFF_YP + b * 4096);
            }
            if (b >= 251) BST128(sv, l16, OFF_X2R + (b - 251) * 1024);
#pragma unroll
            for (int i = 0; i < 9; ++i) BST128(sv, l16 + (i & 3) * 1024, OFF_X1G + rg * 73728 + cb * 9216 + (i >> 2) * 4096);
            BST128(sv, g * 2048 + r16 * 16, OFF_STATS + STATS_B + rg * 4 * 2048 + cb * 256);
        }
        __builtin_amdgcn_s_setprio(0);
    }
    }
#define WO_PRE() do { const u32x4 sws = mk_srd(ws, WS_BYTES); dma16x4_sc1(sws, lane * 16u, a.wx, a.ldx); if (2 > 1) { dma16x4_sc1(sws, lane * 16u, a.wi, a.ld); \
    dma16x2_sc1(sws, lane * 16u, a.wi + 4096, a.ld + 4096); } a.skip = 3; } while (0)
        {
            IDS
            u32 cn = LSPIN_MAX / 4;
            a.srd = mk_srd(ws, WS_BYTES); a.layer = layer;
            att(a);
            if (layer == 0 && w == 7) BST32(tag, lane == 0 ? OFF_XFLAG + (rg * 8 + cb) * XFS : OOB, 0);
            mid(a, cn);
            a.stat += STATS_B;
            if (cn == 0) TMO(4);
        }
        if (layer == 0 && w_ == 7) { IDS gu_dma(wg, wu, lane, b, 15, 18); }
        if (layer == 0) {
            IDS
#pragma nounroll
            for (u32 m = (0x21004000u >> (4 * w)) & 7; m; m &= m - 1) { gu_tr(L_G + 6144 + __builtin_ctz(m) * 6144); lds_inc(L_BC + BC_PP, lane); }
        }
        if (w_ == (layer == 0 ? 6 : 3)) {
            IDS
            const int tile = rg * 4 + g;
            __builtin_amdgcn_s_setprio(3);
            float wrsum = __uint_as_float(lds32(L_BC + BC_WRS));
            const int cr = OFF_CROW + (layer * 128 + tile) * 1280;
            int ka = r16 < 4 ? OFF_KEY + (layer * 32 + cb * 4 + r16) * 1024 + tile * 8 : OOB;
            int pa_ = L_PK(g) + (r16 < 9 ? r16 : 0) * 256 + 240, ca_ = r16 < 9 ? cr + cb * 144 + r16 * 16 : OOB, sa_ = cb == 0 ? cr + 1152 : OOB;
            u32 idx = 2047 - (rg * 64 + lane), tv = tag, sv = g * 2048 + r16 * 16;
            asm volatile("" : "+v"(wrsum), "+v"(ka), "+v"(pa_), "+v"(ca_), "+v"(sa_), "+v"(idx), "+v"(tv), "+v"(sv));
            float t[3];
            u32 cn = SPIN_MAX;
            LWAIT0(L_BC + BC_PK, 4 * (layer + 1), 27);
            if (layer == 0) { gemm1p(); __builtin_amdgcn_s_sleep(3); }
            if (layer == 1) __builtin_amdgcn_s_sleep(8);
            poll_stats(mk_srd(ws, WS_BYTES), sv, OFF_STATS + layer * STATS_B + rg * 8192, t, cn);
            const float rm0 = t[0] * (1.f / 576.f);
            const float var = fmaxf(t[1] * (1.f / 576.f) - rm0 * rm0, 0.f);
            const float rm = ln_m(rm0), rrs = ln_rs(var);
            const float score = rrs * (t[2] - rm * wrsum);
            const u32 sb = pk2(0.f, score);
            const u32 key = ((sb ^ ((u32)((int)sb >> 31) | 0x80000000u)) & 0xffff0000u) | idx;
            const u32 best = rmax(key);
            BST64(((u32x2){best, tv}), ka, 0);
            CFENCE();
            {
                const u32x4 v = lds128(pa_ - (int)(best & 15u) * 16);
                sts64(L_RS + lane * 8, (u32x2){__float_as_uint(rm), __float_as_uint(rrs)});
                sts32(L_BC + BC_TB + g * 4, best);
                CFENCE();
                sts32v(L_BC + BC_KC, layer + 1);
                BST128(((u32x4){san(v.x), san(v.y), san(v.z), san(v.w)}), ca_, 0);
                BST128(((u32x4){san(__float_as_uint(rm)), san(__float_as_uint(rrs)), san(__float_as_uint(rm)), san(__float_as_uint(rrs))}), key == best ? sa_ : OOB, 0);
            }
            if (cn == 0) TMO(5);
            if (layer == 0) {
                sts64(L_MR + (16 + lane) * 8, ln_next(rm0, var, rm, rrs));
                lds_inc(L_BC + BC_MC, lane);
            }
            __builtin_amdgcn_s_setprio(0);
        }
        if (layer == 0 && w_ == 3) {
            IDS
            float t[3];
            u32 cn = SPIN_MAX;
            LWAIT0(L_BC + BC_PK, 4, 27);
            gemm1p();
            poll_stats1(mk_srd(ws, WS_BYTES), OFF_STATS + (rg > 0 ? rg * 4 - 1 : 3) * 2048, t, cn);
            if (cn == 0) TMO(15);
            const float hm = gsum(t[0]) * (1.f / 576.f);
            const float hv = fmaxf(gsum(t[1]) * (1.f / 576.f) - hm * hm, 0.f);
            if (r16 > 1) sts64(L_MR + r16 * 8, ln_next(hm, hv, ln_m(hm), ln_rs(hv)));
            lds_inc(L_BC + BC_MC, lane);
        }
        if (layer == 0 && (w_ == 4 || w_ == 5)) {
            IDS
            u32 cn = SPIN_MAX;
            const int h = w - 4;
            poll_flagh(mk_srd(ws, WS_BYTES), OFF_XFLAG + ((((lane & 8) && rg > 0) ? rg - 1 : rg) * 8 + (lane & 7)) * XFS, tag, cn);
            if (cn == 0) TMO(6);
            u32 own = OFF_X1G + rg * 73728, xp = own - (rg > 0 ? 73728 : 0) + h * 36864 - 8192, xq = xp + (h ? 0 : cb * 9216 + 8192);
            int th = 9 * cb - 8 * h, hh = 9 * cb + 8 - 36 * h;
            asm volatile("" : "+s"(own), "+s"(xp), "+s"(xq), "+s"(th), "+s"(hh));
            LWAIT0(L_BC + BC_PK, 4, 27);
            __builtin_amdgcn_s_setprio(3);
            gemm1p();
            cn = SPIN_MAX;
            xgather(mk_srd(ws, WS_BYTES), own, xp, xq, th, hh, 8192 + h * 8192, L_XH + h * 9216, cn);
            if ((int)cn < 1) TMO(39);
            __builtin_amdgcn_s_setprio(0);
        }
        if (w_ < 4) {
            if (layer == 1) {
                IDS
                const int grow = rg * 64 + w * 16 + r16;
                LWAIT(L_BC + BC_KC, 2, 24);
                const u32 best = lds32(L_BC + BC_TB + w * 4);
                const bool isb = (best & 0x7ffu) == (u32)(2047 - grow);
                const u32x2 mrs = lds64(L_RS + (16 * w + r16) * 8);
                u32 XT[10];
                xt_get(XT); xt_upd(XT, __uint_as_float(mrs.x), __uint_as_float(mrs.y));
                const __amdgpu_buffer_rsrc_t ro = __builtin_amdgcn_make_buffer_rsrc((void*)out, 0, 2359296, 0x00020000);
                const int ob = grow * 1152 + cb * 144;
                const u32x4 v0 = {XT[0], XT[1], XT[2], XT[3]}, v1 = {XT[4], XT[5], XT[6], XT[7]};
                const u32x2 v2 = {XT[8], XT[9]};
                if (!isb) {
                    __builtin_amdgcn_raw_buffer_store_b128(v0, ro, ob + g * 16, 0, 0);
                    __builtin_amdgcn_raw_buffer_store_b128(v1, ro, ob + 64 + g * 16, 0, 0);
                    if (g < 2) __builtin_amdgcn_raw_buffer_store_b64(v2, ro, ob + 128 + g * 8, 0, 0);
                }
                LWAIT(L_BC + BC_FIN, 1, 13);
                if (isb && best != lds32v(L_BC + BC_BEST1)) {
                    __builtin_amdgcn_raw_buffer_store_b128(v0, ro, ob + g * 16, 0, 0);
                    __builtin_amdgcn_raw_buffer_store_b128(v1, ro, ob + 64 + g * 16, 0, 0);
                    if (g < 2) __builtin_amdgcn_raw_buffer_store_b64(v2, ro, ob + 128 + g * 8, 0, 0);
                }
            }
        } else if (w_ == 4 && layer == 1) {
            IDS
            __builtin_amdgcn_s_setprio(3);
            u32 k, cn = SPIN_MAX;
            LWAIT(L_BC + BC_SP, 1, 17);
            poll_keys2(mk_srd(ws, WS_BYTES), OFF_KEY + (32 + (b & 31)) * 1024, tag, k, cn);
            if (cn == 0) TMO(9);
            k = rmax(k);
            const u32 best = umax2(umax2(__builtin_amdgcn_readlane(k, 0), __builtin_amdgcn_readlane(k, 16)), umax2(__builtin_amdgcn_readlane(k, 32), __builtin_amdgcn_readlane(k, 48)));
            sts32v(L_BC + BC_BEST1, best);
            sts32v(L_BC + BC_FIN, best == lds32v(L_BC + BC_SP) ? 1u : 2u);
        }
        if (w_ < 7) {
            if (layer == 0) {
                u32 cn = LSPIN_MAX, best, lc;
                {
                    IDS
                    int fb = L_BC;
                    lc = 2061 - 64 * rg;
                    asm volatile("" : "+v"(fb), "+s"(lc));
                    if (w < 4 && w != 3) { LWAIT0(L_BC + BC_PK, 4, 27); gemm1p(); }
                    LWAIT0(L_BC + BC_G0, 2, 16);
                    gemm1a(g * 1024 + r16 * 16, fb, L_CS + w * 128, VA_MASK, V0_MASK, w == 3 ? 5 : 4, best, cn);
                }
                if (w_ < 4 || best == 0u || lc - (best & 0x7ffu) < 78u) {
                IDS
                if (w < 4) __builtin_amdgcn_s_setprio(0);
                if (best == 0u) {
                    LWAIT(L_BC + BC_BEST, 1, 28);
                    best = __builtin_amdgcn_readfirstlane(lds32v(L_BC + BC_BEST));
                }
                if (w == 0) {
                    const u64 lm = LATE_MASK(2047 - (int)(best & 0x7ffu));
                    if (b == 255) BST32(tag, lane == 0 ? OFF_EPOCH : OOB, 0);
                    u32 sw_ = SENT; asm volatile("" : "+v"(sw_));
                    const u32x4 sv = {sw_, sw_, sw_, sw_};
#pragma nounroll
                    for (int t = 0; t < 3; ++t) {
                        const bool on = t == 2 ? b >= 112 : (b < 144 && ((lm >> (2 * (rg ^ (16 * t)))) & 1) == (u64)t);
                        const int o = t == 2 ? OFF_YP2 + (b - 112) * 4096 : OFF_YP + YP_B + (b ^ (4 * t)) * 4096;
                        if (on) {
#pragma unroll
                            for (int i = 0; i < 4; ++i) BST128(sv, lane * 16 + i * 1024, o);
                        }
                    }
                }
                if (w < 3) {
                    LWAIT(L_BC + BC_C1, 7, 48);
                    WO_PRE();
                }
                if (lc - (best & 0x7ffu) < 78u)
                    gemm1b(L_BC, L_XR + cb * 144 + g * 16, L_XR + cb * 144 + 128 + (g & 1) * 8, w + 1, L_CS + w * 128, VA_MASK, V0_MASK, cn);
                rs_get(L_RS + (16 * (w & 3) + r16) * 8);
                }
                if ((int)cn < 1) TMO(16);
            }
        } else {
            IDS
            if (layer == 1) __builtin_amdgcn_s_setprio(3);
            int ridx;
#pragma nounroll
            for (int rd = layer; ; ++rd) {
                IDS
                u32 c0 = r16 * 64 + g * 16, c1 = r16 < 2 ? c0 + 1024 : 1152;
                u32 ya = (lane >> 2) * 16384u + (lane & 3) * 16u, ye = (32 + g) * 16384u + r16 * 4u;
                asm volatile("" : "+v"(c0), "+v"(c1), "+v"(ya), "+v"(ye));
                const u32x4 sws = mk_srd(ws, WS_BYTES);
                u32 yp = rd == 2 ? OFF_YP2 : OFF_YP + rd * YP_B;
                asm volatile("" : "+s"(yp));
                u32 best;
                if (rd == 2) best = __builtin_amdgcn_readfirstlane(lds32v(L_BC + BC_BEST1));
                else {
                    u32 k, cn = SPIN_MAX;
                    const bool late = (lm0 >> (2 * rg)) & 1;
                    u32 ko = OFF_KEY + (layer * 32 + (b & 31)) * 1024;
                    asm volatile("" : "+s"(ko));
                    VMWAIT(0);
                    __builtin_amdgcn_s_setprio(3);
                    gu_pre();
                    if (!late) {
                        LWAIT(L_BC + BC_KC, layer + 1, 29);
                        if (layer == 0) __builtin_amdgcn_s_sleep(5);
                        if (layer == 1) __builtin_amdgcn_s_sleep(9);
                    }
                    if (layer == 0) LWAIT0(L_BC + BC_PP, 3, 33);
                    poll_keys(sws, ko, tag, lm0, k, cn);
                    if (cn == 0) TMO(7);
                    k = rmax(k);
                    best = umax2(umax2(__builtin_amdgcn_readlane(k, 0), __builtin_amdgcn_readlane(k, 16)), umax2(__builtin_amdgcn_readlane(k, 32), __builtin_amdgcn_readlane(k, 48)));
                }
                ridx = 2047 - (int)(best & 0x7ffu);
                sts32v(L_BC + BC_BEST + rd * 8, best);
                float fm, frs;
                {
                    u32 cn = LSPIN_MAX;
                    ffn(sws, OFF_CROW + (layer * 128 + (ridx >> 4)) * 1280, c0, c1, ya, ye, yp + b * 64, rd >> 1, fm, frs, cn);
                    if ((int)cn < 1) TMO(14);
                }
                u32 ab = 3 - (rd & 1);
                if (b < 144) {
                    const int q = (b & 7) * 18 + (b >> 3), c0 = q < 128 ? 8 * (q & 63) + 4 * (q >> 6) : 4 * q;
                    const u32 sbits = best >> 16;
                    const u32 sraw = (sbits & 0x8000u) ? (sbits & 0x7fffu) : (~sbits & 0xffffu);
                    const float sc = __uint_as_float(sraw << 16);
                    const float sg = rbf(__builtin_amdgcn_rcpf(1.f + __builtin_amdgcn_exp2f(-sc * 1.4426950408889634f)));
                    const u32 so = (lane & 31) == 16 ? 2 * (g & 2) : OOB;
                    u32 cn = LSPIN_MAX;
                    reduce(mk_srd(ws, WS_BYTES), mk_srd(out, 2359296), lane * 64u, yp + (q >> 2) * 16384 + (q & 3) * 16, L_XR + 2 * c0 + 2 * (g & 2), fm, frs, sg,
                           so + (layer == 0 ? OFF_X2R + 32 * q : OOB), so + (layer == 1 ? ridx * 1152 + 2 * c0 : OOB), L_BC + BC_FIN, ab, cn);
                    if ((int)cn < 1) TMO(8);
                } else if (rd == 1) {
                    LWAIT0(L_BC + BC_FIN, 1, 18);
                    ab = __builtin_amdgcn_readfirstlane(lds32v(L_BC + BC_FIN));
                }
                if (ab != 2u) break;
            }
            lm0 = LATE_MASK(ridx);
            const int prow = ridx - (rg * 64 - 16);
            const bool affected = (lm0 >> (2 * rg)) & 1;
            if (layer == 0 && affected) {
                LWAIT0(L_BC + BC_G0 + 16, 2, 41);
                if (prow >= 16) sts32v(L_RS + (prow - 16) * 8 + 4, 0u);
                u32 o[6], cn = SPIN_MAX;
                int h0 = L_XH + k_pos(lane, cb) * 256, h1 = L_XH + k_pos(64 + (r16 >> 1), cb) * 256 + (r16 & 1) * 8;
                u32 gw = 1u | (u32)prow << 8;
                asm volatile("" : "+v"(h0), "+v"(h1), "+v"(gw));
                poll_row(mk_srd(ws, WS_BYTES), OFF_X2R + lane * 32, OFF_X2R + 4096 + r16 * 32, o, cn);
                if (cn == 0) TMO(2);
                u32x4 c0 = {o[0], o[1], o[2], o[3]}; u32x2 c1 = {o[4], o[5]};
                sts128(h0, c0);
                sts64(h1, c1);
                CFENCE();
                sts32v(L_BC + BC_GO, gw);
                CFENCE();
                sts128(L_XR + lane * 16, c0);
                sts64(L_XR + 1024 + r16 * 8, c1);
                if (lane >= 16) c1 = (u32x2){0u, 0u};
                float psx = 0.f, psq = 0.f;
#pragma unroll
                for (int i = 0; i < 4; ++i) {
                    psx = dot2(c0[i], ONES2, psx); psq = dot2(c0[i], c0[i], psq);
                    if (i < 2) { psx = dot2(c1[i], ONES2, psx); psq = dot2(c1[i], c1[i], psq); }
                }
                psx = gsum(rsum(psx)); psq = gsum(rsum(psq));
                CFENCE();
                const float pm0 = psx * (1.f / 576.f);
                const float prs = ln_rs(fmaxf(psq * (1.f / 576.f) - pm0 * pm0, 0.f)), pm = ln_m(pm0);
                sts32v(L_MR, __float_as_uint(pm)); sts32v(L_MR + 4, __float_as_uint(prs)); sts32v(L_BC + BC_PST, 255u);
            }
            __builtin_amdgcn_s_setprio(0);
        }
    }
    if (tmo != 0u) BST32(tmo, OFF_TMO, 0);
    asm volatile("Lcode1:");
}

extern "C" void kernel_launch(void* const* d_in, const int* in_sizes, int n_in, void* d_out, int out_size, void* d_ws, size_t ws_size,
                              hipStream_t stream) {
    (void)in_sizes; (void)n_in; (void)out_size; (void)ws_size;
    hipFuncSetAttribute((const void*)mega, hipFuncAttributeMaxDynamicSharedMemorySize, LDS_BYTES);
    mega<<<256, 512, LDS_BYTES, stream>>>((const u16*)d_in[0], (const u16*)d_in[1], (const u16*)d_in[2], (const u16*)d_in[4],
                                          (const u16*)d_in[5], (const u16*)d_in[6], (const u16*)d_in[7], (u16*)d_out, (char*)d_ws);
}
```

```cpp
#include <hip/hip_runtime.h>
#include <stdint.h>

#define WREP 8
#define FST 32
#define NTD " nt"
#define XFS 32
#define XCI 0x04208760u
#define XCN 0x02221110u
#define PFA 3

#if defined(__HIP_DEVICE_COMPILE__)
#define NOPK __attribute__((target("no-packed-fp32-ops")))
#else
#define NOPK
#endif
#define DEV __device__ __forceinline__ NOPK
typedef unsigned short u16;
typedef unsigned int u32;
typedef unsigned long long u64;
#define __uint_as_float(x) __builtin_bit_cast(float, (u32)(x))
#define __float_as_uint(x) __builtin_bit_cast(u32, (float)(x))
#define fmaxf(a, b) __builtin_fmaxf((a), (b))
typedef __attribute__((ext_vector_type(2))) float f32x2;
typedef __attribute__((ext_vector_type(2))) __bf16 bf16x2;
typedef __attribute__((ext_vector_type(2))) u32 u32x2;
typedef __attribute__((ext_vector_type(4))) u32 u32x4;

extern __shared__ __attribute__((aligned(16))) char smem[];

#define SPIN_MAX 20000
#define LSPIN_MAX 400000

#define WS_BYTES 0x1000000
#define OFF_EPOCH 0
#define OFF_TMO 256
#define OFF_FLAG 0xF0000
#define OFF_GFLAG 4096
#define OFF_XFLAG 0x18000
#define OFF_KEY 16384
#define OFF_X2R 81920
#define OFF_STATS 0x20000
#define STATS_B 0x40000
#define OFF_CROW 0xA0000
#define OFF_YP 0x100000
#define YP_B 589824
#define OFF_OT 0x240000
#define OFF_X1G 0x6C0000
#define OFF_WI 0x900000
#define OFF_WOI 0xB00000
#define OFF_YP2 0xD00000
#define OOB 0x7FFFFF00

#define LDS_BYTES 163840
#define XCH 10240
#define L_XH 73728
#define L_KV 0
#define KVS 304
#define L_Q 24320
#define L_WOE 33536
#define L_SCR 18432
#define L_G 98048
#define L_U 125696
#define L_MISC 153344
#define L_MR (L_MISC)
#define L_BC (L_MISC + 640)
#define BC_BEST 0
#define BC_SP 8
#define BC_WRS 4
#define BC_GC 16
#define BC_C1 20
#define BC_GO 28
#define BC_PST 32
#define BC_FIN 36
#define BC_BEST1 40
#define BC_XC 44
#define BC_ZERO 48
#define BC_G0 96
#define BC_TB 168
#define BC_KC 64
#define BC_MC 160
#define BC_FC 76
#define BC_PP 56
#define BC_PK 84
#define BC_PC 152
#define BC_WC 144
#define L_RS 92160
#define L_PK(t) ((t) < 2 ? 92672 + (t) * 2304 : ((t) == 2 ? L_SCR3 : L_HROW))
#define L_WR (L_MISC + 9888)
#define L_CS (L_MISC + 928)
#define L_HROW (L_MISC + 1824)
#define L_XR (L_MISC + 4128)
#define L_SCR3 (L_MISC + 5280)

#define SENT 0xFFFFFFFFu

DEV void bar() {
    asm volatile("s_waitcnt lgkmcnt(0)" ::: "memory");
    __builtin_amdgcn_s_barrier();
    asm volatile("" ::: "memory");
}
#define VMWAIT(n) asm volatile("s_waitcnt vmcnt(" #n ")" ::: "memory")
DEV void vmwaitn(const int n) { asm volatile("s_waitcnt vmcnt(%0)" :: "n"(n) : "memory"); }
DEV void vmwait8n(u32 (&d)[8], const int n) {
    asm volatile("s_waitcnt vmcnt(%8)" : "+v"(d[0]), "+v"(d[1]), "+v"(d[2]), "+v"(d[3]), "+v"(d[4]), "+v"(d[5]), "+v"(d[6]), "+v"(d[7]) : "n"(n) : "memory");
}
DEV u32x4 mk_srd(const void* p, u32 bytes) {
    u64 a = (u64)p;
    asm volatile("" : "+s"(a));
    return (u32x4){(u32)a, (u32)(a >> 32) & 0xffffu, bytes, 0x00020000u};
}
DEV void dma16g(u32x4 srd, u32 voff, u32 soff, u32 lds_dst) {
    asm volatile("s_mov_b32 m0, %0\n\ts_nop 3\n\tbuffer_load_dwordx4 %1, %2, %3 offen lds" :: "s"(lds_dst), "v"(voff), "s"(srd), "s"(soff) : "memory");
}
DEV void dma16x4_sc1(u32x4 srd, u32 voff, u32 soff, u32 lds_dst) {
    asm volatile("s_mov_b32 m0, %0\n\ts_nop 3\n\tbuffer_load_dwordx4 %1, %2, %3 offen sc1 lds\n\tbuffer_load_dwordx4 %1, %2, %3 offen offset:1024 sc1 lds\n\t"
                 "buffer_load_dwordx4 %1, %2, %3 offen offset:2048 sc1 lds\n\tbuffer_load_dwordx4 %1, %2, %3 offen offset:3072 sc1 lds"
                 :: "s"(lds_dst), "v"(voff), "s"(srd), "s"(soff) : "memory");
}
DEV void dma16x2_sc1(u32x4 srd, u32 voff, u32 soff, u32 lds_dst) {
    asm volatile("s_mov_b32 m0, %0\n\ts_nop 3\n\tbuffer_load_dwordx4 %1, %2, %3 offen sc1 lds\n\tbuffer_load_dwordx4 %1, %2, %3 offen offset:1024 sc1 lds"
                 :: "s"(lds_dst), "v"(voff), "s"(srd), "s"(soff) : "memory");
}
DEV void bld32(u32& d, u32x4 srd, u32 voff, u32 soff, const int imm) {
    asm volatile("s_nop 4\n\tbuffer_load_dword %0, %1, %2, %3 offen offset:%4" : "=v"(d) : "v"(voff), "s"(srd), "s"(soff), "n"(imm));
}
DEV void old8(u32 (&d)[8], u32x4 srd, u32 voff, u32 s2) {
    asm volatile("s_nop 4\n\tbuffer_load_dword %0, %8, %9, 0 offen nt\n\tbuffer_load_dword %1, %8, %9, 0 offen offset:1152 nt\n\t"
                 "buffer_load_dword %2, %8, %9, 0 offen offset:2304 nt\n\tbuffer_load_dword %3, %8, %9, 0 offen offset:3456 nt\n\t"
                 "buffer_load_dword %4, %8, %9, %10 offen nt\n\tbuffer_load_dword %5, %8, %9, %10 offen offset:1152 nt\n\t"
                 "buffer_load_dword %6, %8, %9, %10 offen offset:2304 nt\n\tbuffer_load_dword %7, %8, %9, %10 offen offset:3456 nt"
                 : "=&v"(d[0]), "=&v"(d[1]), "=&v"(d[2]), "=&v"(d[3]), "=&v"(d[4]), "=&v"(d[5]), "=&v"(d[6]), "=&v"(d[7])
                 : "v"(voff), "s"(srd), "s"(s2));
}
DEV void wld8p(u32x4 srd, u32 voff, u32 soff, int w) {
    u32 t;
    asm volatile("s_cmp_gt_u32 %4, 3\n\ts_cbranch_scc1 Lw8_%=\n\ts_setprio 3\n\ts_nop 1\n\t"
                 "buffer_load_dword v48, %1, %2, %3 offen nt\n\tbuffer_load_dword v49, %1, %2, %3 offen offset:3456 nt\n\ts_add_u32 %0, %3, 6912\n\t"
                 "buffer_load_dword v50, %1, %2, %0 offen nt\n\tbuffer_load_dword v51, %1, %2, %0 offen offset:3456 nt\n\ts_add_u32 %0, %3, 13824\n\t"
                 "buffer_load_dword v52, %1, %2, %0 offen nt\n\tbuffer_load_dword v53, %1, %2, %0 offen offset:3456 nt\n\ts_add_u32 %0, %3, 20736\n\t"
                 "buffer_load_dword v54, %1, %2, %0 offen nt\n\tbuffer_load_dword v55, %1, %2, %0 offen offset:3456 nt\n"
                 "Lw8_%=:"
                 : "=&s"(t) : "v"(voff), "s"(srd), "s"(soff), "s"(w) : "scc", "memory");
}
DEV void x_dma(u32x4 srd, u32 xv, u32 orm, u32 c, u32 n) {
    u32 z1, z2, z3, y0, y1, so, t;
    asm volatile("s_cmp_eq_u32 %8, 0\n\ts_cbranch_scc1 Lx1_%=\n\t"
                 "v_xor_b32 %2, 64, %9\n\tv_or_b32 %3, %11, %9\n\tv_add_u32 %0, 0x2000, %2\n\tv_add_u32 %1, 0x4000, %9\n\tv_or_b32 %4, %11, %0\n\tv_add_u32 %2, 0x6000, %2\n"
                 "Lx0_%=:\n\t"
                 "s_cmp_gt_u32 %7, 8\n\ts_cselect_b32 %6, 9, 0\n\ts_sub_u32 %7, %7, %6\n\ts_mul_i32 m0, %7, 10240\n\ts_lshl_b32 %5, %7, 7\n\t"
                 "s_mov_b32 exec_lo, 0xffff0000\n\tbuffer_load_dwordx4 %3, %10, %5 offen lds\n\ts_mov_b32 exec_lo, -1\n\t"
                 "buffer_load_dwordx4 %4, %10, %5 offen offset:1024 lds\n\tbuffer_load_dwordx4 %1, %10, %5 offen offset:2048 lds\n\tbuffer_load_dwordx4 %2, %10, %5 offen offset:3072 lds\n\t"
                 "s_add_u32 m0, m0, 4096\n\ts_add_u32 %5, %5, 36864\n\t"
                 "buffer_load_dwordx4 %9, %10, %5 offen lds\n\tbuffer_load_dwordx4 %0, %10, %5 offen offset:1024 lds\n\t"
                 "buffer_load_dwordx4 %1, %10, %5 offen offset:2048 lds\n\tbuffer_load_dwordx4 %2, %10, %5 offen offset:3072 lds\n\t"
                 "s_add_u32 m0, m0, 4096\n\ts_add_u32 %5, %5, 36864\n\t"
                 "buffer_load_dwordx4 %9, %10, %5 offen lds\n\tbuffer_load_dwordx4 %0, %10, %5 offen offset:1024 lds\n\t"
                 "s_add_u32 %7, %7, 1\n\ts_sub_u32 %8, %8, 1\n\ts_cmp_lg_u32 %8, 0\n\ts_cbranch_scc1 Lx0_%=\n"
                 "Lx1_%=:"
                 : "=&v"(z1), "=&v"(z2), "=&v"(z3), "=&v"(y0), "=&v"(y1), "=&s"(so), "=&s"(t), "+s"(c), "+s"(n) : "v"(xv), "s"(srd), "s"(orm) : "scc", "memory");
}
DEV void wunit_st(u32x4 srd, u32 ib, int w) {
    asm volatile("s_cmp_gt_u32 %2, 3\n\ts_cbranch_scc1 Lu_%=\n\ts_waitcnt vmcnt(0)\n\t"
                 "v_perm_b32 v56, v49, v48, %3\n\tv_perm_b32 v57, v51, v50, %3\n\tv_perm_b32 v58, v53, v52, %3\n\tv_perm_b32 v59, v55, v54, %3\n\t"
                 "buffer_store_dwordx4 v[56:59], %0, %1, 0 offen sc1\n\t"
                 "v_perm_b32 v60, v49, v48, %4\n\tv_perm_b32 v61, v51, v50, %4\n\tv_perm_b32 v62, v53, v52, %4\n\tv_perm_b32 v63, v55, v54, %4\n\t"
                 "buffer_store_dwordx4 v[60:63], %0, %1, 0 offen offset:1024 sc1\n"
                 "Lu_%=:" :: "v"(ib), "s"(srd), "s"(w), "s"(0x05040100u), "s"(0x07060302u) : "scc", "memory");
}
DEV u32 plo(u32 hi, u32 lo) { return __builtin_amdgcn_perm(hi, lo, 0x05040100u); }
DEV u32 phi(u32 hi, u32 lo) { return __builtin_amdgcn_perm(hi, lo, 0x07060302u); }
DEV void wfrag(const u32 (&d)[8], u32x4& f0, u32x4& f1) {
    f0 = (u32x4){plo(d[1], d[0]), plo(d[3], d[2]), plo(d[5], d[4]), plo(d[7], d[6])};
    f1 = (u32x4){phi(d[1], d[0]), phi(d[3], d[2]), phi(d[5], d[4]), phi(d[7], d[6])};
}
DEV u32 pk2(float a, float b) { f32x2 v = {a, b}; return __builtin_bit_cast(u32, __builtin_convertvector(v, bf16x2)); }
DEV float rbf(float a) { return (float)(__bf16)a; }
DEV float ln_rs(float var) { return __builtin_amdgcn_rcpf(rbf(__builtin_amdgcn_sqrtf(rbf(rbf(var) + 1e-5f)))); }
DEV float ln_m(float m) { return rbf(m); }
DEV u32x2 ln_next(float m, float var, float mb, float rs) {
    const float c = 1.f + rs;
    const float mb2 = ln_m(c * m - mb * rs);
    return (u32x2){__float_as_uint((mb * rs + mb2) * __builtin_amdgcn_rcpf(c)), __float_as_uint(c * ln_rs(c * c * var))};
}
DEV float lo16(u32 v) { return __uint_as_float(v << 16); }
DEV float hi16(u32 v) { return __uint_as_float(v & 0xffff0000u); }
DEV u32 san(u32 v) { return v < 0xFFFFFFFEu ? v : 0xFFFFFFFEu; }
DEV u32 umax2(u32 a, u32 b) { return a > b ? a : b; }
DEV float dot2(u32 a, u32 b, float c) {
    return __builtin_amdgcn_fdot2_f32_bf16(__builtin_bit_cast(bf16x2, a), __builtin_bit_cast(bf16x2, b), c, false);
}
DEV float gsum(float v) {
    u32 u = __float_as_uint(v);
    auto a = __builtin_amdgcn_permlane16_swap(u, u, false, false);
    v = __uint_as_float(a[0]) + __uint_as_float(a[1]);
    u = __float_as_uint(v);
    auto c = __builtin_amdgcn_permlane32_swap(u, u, false, false);
    return __uint_as_float(c[0]) + __uint_as_float(c[1]);
}
#define DPP(v, ctrl) ((u32)__builtin_amdgcn_update_dpp(0, (int)(v), (ctrl), 0xf, 0xf, false))
DEV u32 rmax(u32 v) {
    v = umax2(v, DPP(v, 0x128)); v = umax2(v, DPP(v, 0x124)); v = umax2(v, DPP(v, 0x122)); v = umax2(v, DPP(v, 0x121));
    return v;
}
DEV float rsum(float v) {
    v += __uint_as_float(DPP(__float_as_uint(v), 0x128)); v += __uint_as_float(DPP(__float_as_uint(v), 0x124));
    v += __uint_as_float(DPP(__float_as_uint(v), 0x122)); v += __uint_as_float(DPP(__float_as_uint(v), 0x121));
    return v;
}
DEV u32 lds32(int off) { return *(const u32*)(smem + off); }
typedef __attribute__((address_space(3))) volatile u32* lds_vu32;
DEV u32 lds32v(int off) { return *(lds_vu32)(smem + off); }
DEV u32x2 lds64(int off) { return *(const u32x2*)(smem + off); }
DEV u32x4 lds128(int off) { return *(const u32x4*)(smem + off); }
DEV void sts32(int off, u32 v) { *(u32*)(smem + off) = v; }
DEV void sts32v(int off, u32 v) { *(lds_vu32)(smem + off) = v; }
DEV void sts64(int off, u32x2 v) { *(u32x2*)(smem + off) = v; }
DEV void sts128(int off, u32x4 v) { *(u32x4*)(smem + off) = v; }
DEV void lds_inc(int off, int lane) {
    (void)lane;
    asm volatile("s_mov_b64 exec, 1\n\tds_add_u32 %0, %1\n\ts_mov_b64 exec, -1" :: "v"(off), "v"(1u) : "memory");
}
DEV u32 lds_inc_rtn(int off) {
    u32 old;
    asm volatile("s_mov_b64 exec, 1\n\tds_add_rtn_u32 %0, %1, %2\n\ts_mov_b64 exec, -1\n\ts_waitcnt lgkmcnt(0)" : "=&v"(old) : "v"(off), "v"(1u) : "memory");
    return __builtin_amdgcn_readfirstlane(old);
}
#define BST128(v, off, soff) __builtin_amdgcn_raw_buffer_store_b128((v), rs, (off) + (soff), 0, 16)
#define BST64(v, off, soff) __builtin_amdgcn_raw_buffer_store_b64((v), rs, (off), (soff), 16)
#define BST32(v, off, soff) __builtin_amdgcn_raw_buffer_store_b32((v), rs, (off), (soff), 16)
#define CFENCE() asm volatile("" ::: "memory")
#define SB() __builtin_amdgcn_sched_barrier(0)
#define TMO(code) (tmo = (code))
#define LWAIT(off, target, code) do { int it_ = 0; _Pragma("nounroll") for (; it_ < LSPIN_MAX; ++it_) { if (lds32v(off) >= (u32)(target)) break; __builtin_amdgcn_s_sleep(1); } \
    if (it_ == LSPIN_MAX) TMO(code); CFENCE(); } while (0)

#define LWAIT0(off, target, code) do { int it_ = 0; _Pragma("nounroll") for (; it_ < LSPIN_MAX; ++it_) { if (lds32v(off) >= (u32)(target)) break; } \
    if (it_ == LSPIN_MAX) TMO(code); CFENCE(); } while (0)

#define ONES2 0x3F803F80u
#define LATE_MASK(r) ((((r) & 63) >= 50 ? 15ull : 3ull) << (((r) >> 6) * 2))
#define RG_OF(b) (((b) & 7) * 4 + (((b) >> 3) & 3))
#define CB_OF(b) ((b) >> 5)
#define IDS \
    int lane; asm volatile("v_mbcnt_lo_u32_b32 %0, -1, 0\n\tv_mbcnt_hi_u32_b32 %0, -1, %0" : "=v"(lane)); \
    int w = w_; asm volatile("" : "+s"(w)); \
    int b = b_; asm volatile("" : "+s"(b)); \
    const int rg = RG_OF(b), cb = CB_OF(b); \
    const int r16 = lane & 15, g = lane >> 4; \
    (void)rg; (void)cb; (void)r16; (void)g; (void)w;

#define CSL(w) ((0x4653210 >> (4 * (w))) & 15)
struct LaneC { int ka, kt, qa, qt, va, ta, l0; u32 av, tv0, ot; int sd, sstr; };
DEV LaneC lane_consts(int w_, int b_) {
    IDS
    LaneC c;
    const int rt4 = w & 3, pp = w >> 2;
    const int kvb = L_KV + 16 * rt4 * KVS + 72 * pp;
    const int qb = L_Q + (16 * rt4 + r16) * 144 + 72 * pp;
    const int trr = kvb + (4 * g + (r16 >> 2)) * KVS + 144;
    c.kt = kvb + r16 * KVS + 32; c.ka = kvb + r16 * KVS + g * 8;
    c.qa = qb + g * 8; c.qt = g == 2 ? qb + 32 : L_BC + BC_ZERO;
    c.va = trr + (r16 & 3) * 8; c.ta = trr + 32;
    c.l0 = 4096 + (g >> 1) * 4608 + (lane & 31) * 16;
    c.av = lane * 16;
    c.tv0 = pp ? 7168 + g * 256 + r16 * 8 : 7168 + lane * 16;
    c.ot = 0;
    const int P = 4 * CSL(w) + g;
    c.sstr = P >= 18 ? 144 : KVS;
    c.sd = (P >= 18 ? (L_Q - 16 * 144 + (P - 18) * 16) : (L_KV + P * 16)) + r16 * c.sstr;
    c.sstr *= 16;
    return c;
}

struct MidS { u32x4 srd; u32 ob, st, tt, c0, l1d, tvd, wi, ld, wx, ldx, msk, scr, stat, x1, layer, pk, n7, skip, wc, lb; };

DEV void lc_set(const LaneC& c) {
    asm volatile("v_mov_b32 v100, %0\n\t" "v_mov_b32 v101, %1\n\t" "v_mov_b32 v102, %2\n\t" "v_mov_b32 v103, %3\n\t" "v_mov_b32 v104, %4\n\t" "v_mov_b32 v105, %5\n\t" "v_mov_b32 v106, %6\n\t" "v_mov_b32 v107, %7\n\t" "v_mov_b32 v108, %8\n\t" "v_mov_b32 v110, %10\n\t" "v_mov_b32 v111, %11\n\t"
        :: "v"(c.ka), "v"(c.kt), "v"(c.qa), "v"(c.qt), "v"(c.va), "v"(c.ta), "v"(c.l0), "v"(c.av), "v"(c.tv0), "v"(c.ot), "v"(c.sd), "v"(c.sstr));
}
DEV void xt_get(u32 (&x)[10]) {
    asm volatile("v_mov_b32 %0, v88\n\t" "v_mov_b32 %1, v89\n\t" "v_mov_b32 %2, v90\n\t" "v_mov_b32 %3, v91\n\t" "v_mov_b32 %4, v92\n\t" "v_mov_b32 %5, v93\n\t" "v_mov_b32 %6, v94\n\t" "v_mov_b32 %7, v95\n\t" "v_mov_b32 %8, v96\n\t" "v_mov_b32 %9, v97\n\t"
        : "=v"(x[0]), "=v"(x[1]), "=v"(x[2]), "=v"(x[3]), "=v"(x[4]), "=v"(x[5]), "=v"(x[6]), "=v"(x[7]), "=v"(x[8]), "=v"(x[9]));
}
DEV void rs_get(int a) { asm volatile("ds_read_b64 v[98:99], %0" :: "v"(a) : "memory"); }

DEV void gemm0(u32x4 srd, int xa, u32 iv, u32 si, int (&xt)[3], u32 ro, u32 cm, u32 c0, u32 c1, u32 fo, u32 fexp, u32 cs, u64 va, u64 v0, u32& cn) {
    int xb; u32 fl, sf;
    asm volatile(
        "s_nop 4\n\t"
        "s_setprio 2\n\t"
        "buffer_load_dwordx4 v[112:115], %[iv], %[ssrd], %[si] offen offset:0 sc1\n\t"
        "buffer_load_dwordx4 v[116:119], %[iv], %[ssrd], %[si] offen offset:1024 sc1\n\t"
        "buffer_load_dwordx4 v[120:123], %[iv], %[ssrd], %[si] offen offset:2048 sc1\n\t"
        "buffer_load_dwordx4 v[124:127], %[iv], %[ssrd], %[si] offen offset:3072 sc1\n\t"
        "s_add_u32 %[si], %[si], 4096\n\t"
        "buffer_load_dwordx4 v[128:131], %[iv], %[ssrd], %[si] offen offset:0 sc1\n\t"
        "buffer_load_dwordx4 v[132:135], %[iv], %[ssrd], %[si] offen offset:1024 sc1\n\t"
        "buffer_load_dwordx4 v[136:139], %[iv], %[ssrd], %[si] offen offset:2048 sc1\n\t"
        "buffer_load_dwordx4 v[140:143], %[iv], %[ssrd], %[si] offen offset:3072 sc1\n\t"
        "s_add_u32 %[si], %[si], 4096\n\t"
        "buffer_load_dwordx4 v[144:147], %[iv], %[ssrd], %[si] offen offset:0 sc1\n\t"
        "buffer_load_dwordx4 v[148:151], %[iv], %[ssrd], %[si] offen offset:1024 sc1\n\t"
        "ds_read_b128 v[88:91], %[x0]\n\t"
        "ds_read_b128 v[92:95], %[x1]\n\t"
        "ds_read_b64 v[96:97], %[x2]\n\t"
        "v_xor_b32 %[xb], 64, %[xa]\n\t"
        "v_subrev_u32 %[x0], %[sro], %[x0]\n\t"
        "v_subrev_u32 %[x1], %[sro], %[x1]\n\t"
        "ds_read_b128 v[24:27], %[x0] offset:0\n\t"
        "ds_read_b128 v[28:31], %[x0] offset:2048\n\t"
        "ds_read_b128 v[32:35], %[x0] offset:4096\n\t"
        "ds_read_b128 v[36:39], %[x0] offset:6144\n\t"
        "ds_read_b128 v[40:43], %[x0] offset:8192\n\t"
        "ds_read_b128 v[44:47], %[x1] offset:0\n\t"
        "s_waitcnt vmcnt(8) lgkmcnt(5)\n\t"
        "v_mfma_f32_16x16x32_bf16 v[48:51], v[112:115], v[24:27], 0\n\t"
        "v_mfma_f32_16x16x32_bf16 v[68:71], v[116:119], v[24:27], 0\n\t"
        "ds_read_b128 v[24:27], %[x1] offset:2048\n\t"
        "s_waitcnt lgkmcnt(5)\n\t"
        "v_mfma_f32_16x16x32_bf16 v[52:55], v[112:115], v[28:31], 0\n\t"
        "v_mfma_f32_16x16x32_bf16 v[72:75], v[116:119], v[28:31], 0\n\t"
        "buffer_load_dwordx4 v[152:155], %[iv], %[ssrd], %[si] offen offset:2048 sc1\n\t"
        "buffer_load_dwordx4 v[156:159], %[iv], %[ssrd], %[si] offen offset:3072 sc1\n\t"
        "ds_read_b128 v[28:31], %[x1] offset:4096\n\t"
        "s_waitcnt lgkmcnt(5)\n\t"
        "v_mfma_f32_16x16x32_bf16 v[56:59], v[112:115], v[32:35], 0\n\t"
        "v_mfma_f32_16x16x32_bf16 v[76:79], v[116:119], v[32:35], 0\n\t"
        "ds_read_b128 v[32:35], %[x1] offset:6144\n\t"
        "s_waitcnt lgkmcnt(5)\n\t"
        "v_mfma_f32_16x16x32_bf16 v[60:63], v[112:115], v[36:39], 0\n\t"
        "v_mfma_f32_16x16x32_bf16 v[80:83], v[116:119], v[36:39], 0\n\t"
        "ds_read_b128 v[36:39], %[x1] offset:8192\n\t"
        "s_waitcnt lgkmcnt(5)\n\t"
        "v_mfma_f32_16x16x32_bf16 v[64:67], v[112:115], v[40:43], 0\n\t"
        "v_mfma_f32_16x16x32_bf16 v[84:87], v[116:119], v[40:43], 0\n\t"
        "s_bitcmp1_b32 %[scm], 0\n\t"
        "s_cbranch_scc1 Lc0_%=\n\t"
        "Ld0_%=:\n\t"
        "ds_read_b128 v[40:43], %[xa] offset:0\n\t"
        "s_waitcnt vmcnt(8) lgkmcnt(5)\n\t"
        "v_mfma_f32_16x16x32_bf16 v[48:51], v[120:123], v[44:47], v[48:51]\n\t"
        "v_mfma_f32_16x16x32_bf16 v[68:71], v[124:127], v[44:47], v[68:71]\n\t"
        "ds_read_b128 v[44:47], %[xa] offset:2048\n\t"
        "s_waitcnt lgkmcnt(5)\n\t"
        "v_mfma_f32_16x16x32_bf16 v[52:55], v[120:123], v[24:27], v[52:55]\n\t"
        "v_mfma_f32_16x16x32_bf16 v[72:75], v[124:127], v[24:27], v[72:75]\n\t"
        "s_add_u32 %[si], %[si], 4096\n\t"
        "buffer_load_dwordx4 v[160:163], %[iv], %[ssrd], %[si] offen offset:0 sc1\n\t"
        "buffer_load_dwordx4 v[164:167], %[iv], %[ssrd], %[si] offen offset:1024 sc1\n\t"
        "ds_read_b128 v[24:27], %[xa] offset:4096\n\t"
        "s_waitcnt lgkmcnt(5)\n\t"
        "v_mfma_f32_16x16x32_bf16 v[56:59], v[120:123], v[28:31], v[56:59]\n\t"
        "v_mfma_f32_16x16x32_bf16 v[76:79], v[124:127], v[28:31], v[76:79]\n\t"
        "ds_read_b128 v[28:31], %[xa] offset:6144\n\t"
        "s_waitcnt lgkmcnt(5)\n\t"
        "v_mfma_f32_16x16x32_bf16 v[60:63], v[120:123], v[32:35], v[60:63]\n\t"
        "v_mfma_f32_16x16x32_bf16 v[80:83], v[124:127], v[32:35], v[80:83]\n\t"
        "ds_read_b128 v[32:35], %[xa] offset:8192\n\t"
        "s_waitcnt lgkmcnt(5)\n\t"
        "v_mfma_f32_16x16x32_bf16 v[64:67], v[120:123], v[36:39], v[64:67]\n\t"
        "v_mfma_f32_16x16x32_bf16 v[84:87], v[124:127], v[36:39], v[84:87]\n\t"
        "ds_read_b128 v[36:39], %[xb] offset:0\n\t"
        "s_waitcnt vmcnt(8) lgkmcnt(5)\n\t"
        "v_mfma_f32_16x16x32_bf16 v[48:51], v[128:131], v[40:43], v[48:51]\n\t"
        "v_mfma_f32_16x16x32_bf16 v[68:71], v[132:135], v[40:43], v[68:71]\n\t"
        "ds_read_b128 v[40:43], %[xb] offset:2048\n\t"
        "s_waitcnt lgkmcnt(5)\n\t"
        "v_mfma_f32_16x16x32_bf16 v[52:55], v[128:131], v[44:47], v[52:55]\n\t"
        "v_mfma_f32_16x16x32_bf16 v[72:75], v[132:135], v[44:47], v[72:75]\n\t"
        "buffer_load_dwordx4 v[168:171], %[iv], %[ssrd], %[si] offen offset:2048 sc1\n\t"
        "buffer_load_dwordx4 v[172:175], %[iv], %[ssrd], %[si] offen offset:3072 sc1\n\t"
        "ds_read_b128 v[44:47], %[xb] offset:4096\n\t"
        "s_waitcnt lgkmcnt(5)\n\t"
        "v_mfma_f32_16x16x32_bf16 v[56:59], v[128:131], v[24:27], v[56:59]\n\t"
        "v_mfma_f32_16x16x32_bf16 v[76:79], v[132:135], v[24:27], v[76:79]\n\t"
        "ds_read_b128 v[24:27], %[xb] offset:6144\n\t"
        "s_waitcnt lgkmcnt(5)\n\t"
        "v_mfma_f32_16x16x32_bf16 v[60:63], v[128:131], v[28:31], v[60:63]\n\t"
        "v_mfma_f32_16x16x32_bf16 v[80:83], v[132:135], v[28:31], v[80:83]\n\t"
        "ds_read_b128 v[28:31], %[xb] offset:8192\n\t"
        "s_waitcnt lgkmcnt(5)\n\t"
        "v_mfma_f32_16x16x32_bf16 v[64:67], v[128:131], v[32:35], v[64:67]\n\t"
        "v_mfma_f32_16x16x32_bf16 v[84:87], v[132:135], v[32:35], v[84:87]\n\t"
        "s_bitcmp1_b32 %[scm], 1\n\t"
        "s_cbranch_scc1 Lc1_%=\n\t"
        "Ld1_%=:\n\t"
        "ds_read_b128 v[32:35], %[xa] offset:10240\n\t"
        "s_waitcnt vmcnt(8) lgkmcnt(5)\n\t"
        "v_mfma_f32_16x16x32_bf16 v[48:51], v[136:139], v[36:39], v[48:51]\n\t"
        "v_mfma_f32_16x16x32_bf16 v[68:71], v[140:143], v[36:39], v[68:71]\n\t"
        "ds_read_b128 v[36:39], %[xa] offset:12288\n\t"
        "s_waitcnt lgkmcnt(5)\n\t"
        "v_mfma_f32_16x16x32_bf16 v[52:55], v[136:139], v[40:43], v[52:55]\n\t"
        "v_mfma_f32_16x16x32_bf16 v[72:75], v[140:143], v[40:43], v[72:75]\n\t"
        "s_add_u32 %[si], %[si], 4096\n\t"
        "buffer_load_dwordx4 v[176:179], %[iv], %[ssrd], %[si] offen offset:0 sc1\n\t"
        "buffer_load_dwordx4 v[180:183], %[iv], %[ssrd], %[si] offen offset:1024 sc1\n\t"
        "ds_read_b128 v[40:43], %[xa] offset:14336\n\t"
        "s_waitcnt lgkmcnt(5)\n\t"
        "v_mfma_f32_16x16x32_bf16 v[56:59], v[136:139], v[44:47], v[56:59]\n\t"
        "v_mfma_f32_16x16x32_bf16 v[76:79], v[140:143], v[44:47], v[76:79]\n\t"
        "ds_read_b128 v[44:47], %[xa] offset:16384\n\t"
        "s_waitcnt lgkmcnt(5)\n\t"
        "v_mfma_f32_16x16x32_bf16 v[60:63], v[136:139], v[24:27], v[60:63]\n\t"
        "v_mfma_f32_16x16x32_bf16 v[80:83], v[140:143], v[24:27], v[80:83]\n\t"
        "ds_read_b128 v[24:27], %[xa] offset:18432\n\t"
        "s_waitcnt lgkmcnt(5)\n\t"
        "v_mfma_f32_16x16x32_bf16 v[64:67], v[136:139], v[28:31], v[64:67]\n\t"
        "v_mfma_f32_16x16x32_bf16 v[84:87], v[140:143], v[28:31], v[84:87]\n\t"
        "ds_read_b128 v[28:31], %[xb] offset:10240\n\t"
        "s_waitcnt vmcnt(8) lgkmcnt(5)\n\t"
        "v_mfma_f32_16x16x32_bf16 v[48:51], v[144:147], v[32:35], v[48:51]\n\t"
        "v_mfma_f32_16x16x32_bf16 v[68:71], v[148:151], v[32:35], v[68:71]\n\t"
        "ds_read_b128 v[32:35], %[xb] offset:12288\n\t"
        "s_waitcnt lgkmcnt(5)\n\t"
        "v_mfma_f32_16x16x32_bf16 v[52:55], v[144:147], v[36:39], v[52:55]\n\t"
        "v_mfma_f32_16x16x32_bf16 v[72:75], v[148:151], v[36:39], v[72:75]\n\t"
        "buffer_load_dwordx4 v[184:187], %[iv], %[ssrd], %[si] offen offset:2048 sc1\n\t"
        "buffer_load_dwordx4 v[188:191], %[iv], %[ssrd], %[si] offen offset:3072 sc1\n\t"
        "ds_read_b128 v[36:39], %[xb] offset:14336\n\t"
        "s_waitcnt lgkmcnt(5)\n\t"
        "v_mfma_f32_16x16x32_bf16 v[56:59], v[144:147], v[40:43], v[56:59]\n\t"
        "v_mfma_f32_16x16x32_bf16 v[76:79], v[148:151], v[40:43], v[76:79]\n\t"
        "ds_read_b128 v[40:43], %[xb] offset:16384\n\t"
        "s_waitcnt lgkmcnt(5)\n\t"
        "v_mfma_f32_16x16x32_bf16 v[60:63], v[144:147], v[44:47], v[60:63]\n\t"
        "v_mfma_f32_16x16x32_bf16 v[80:83], v[148:151], v[44:47], v[80:83]\n\t"
        "ds_read_b128 v[44:47], %[xb] offset:18432\n\t"
        "s_waitcnt lgkmcnt(5)\n\t"
        "v_mfma_f32_16x16x32_bf16 v[64:67], v[144:147], v[24:27], v[64:67]\n\t"
        "v_mfma_f32_16x16x32_bf16 v[84:87], v[148:151], v[24:27], v[84:87]\n\t"
        "s_bitcmp1_b32 %[scm], 2\n\t"
        "s_cbranch_scc1 Lc2_%=\n\t"
        "Ld2_%=:\n\t"
        "ds_read_b128 v[24:27], %[xa] offset:20480\n\t"
        "s_waitcnt vmcnt(8) lgkmcnt(5)\n\t"
        "v_mfma_f32_16x16x32_bf16 v[48:51], v[152:155], v[28:31], v[48:51]\n\t"
        "v_mfma_f32_16x16x32_bf16 v[68:71], v[156:159], v[28:31], v[68:71]\n\t"
        "ds_read_b128 v[28:31], %[xa] offset:22528\n\t"
        "s_waitcnt lgkmcnt(5)\n\t"
        "v_mfma_f32_16x16x32_bf16 v[52:55], v[152:155], v[32:35], v[52:55]\n\t"
        "v_mfma_f32_16x16x32_bf16 v[72:75], v[156:159], v[32:35], v[72:75]\n\t"
        "s_add_u32 %[si], %[si], 4096\n\t"
        "buffer_load_dwordx4 v[192:195], %[iv], %[ssrd], %[si] offen offset:0 sc1\n\t"
        "buffer_load_dwordx4 v[196:199], %[iv], %[ssrd], %[si] offen offset:1024 sc1\n\t"
        "ds_read_b128 v[32:35], %[xa] offset:24576\n\t"
        "s_waitcnt lgkmcnt(5)\n\t"
        "v_mfma_f32_16x16x32_bf16 v[56:59], v[152:155], v[36:39], v[56:59]\n\t"
        "v_mfma_f32_16x16x32_bf16 v[76:79], v[156:159], v[36:39], v[76:79]\n\t"
        "ds_read_b128 v[36:39], %[xa] offset:26624\n\t"
        "s_waitcnt lgkmcnt(5)\n\t"
        "v_mfma_f32_16x16x32_bf16 v[60:63], v[152:155], v[40:43], v[60:63]\n\t"
        "v_mfma_f32_16x16x32_bf16 v[80:83], v[156:159], v[40:43], v[80:83]\n\t"
        "ds_read_b128 v[40:43], %[xa] offset:28672\n\t"
        "s_waitcnt lgkmcnt(5)\n\t"
        "v_mfma_f32_16x16x32_bf16 v[64:67], v[152:155], v[44:47], v[64:67]\n\t"
        "v_mfma_f32_16x16x32_bf16 v[84:87], v[156:159], v[44:47], v[84:87]\n\t"
        "ds_read_b128 v[44:47], %[xb] offset:20480\n\t"
        "s_setprio 1\n\t"
        "s_waitcnt vmcnt(8) lgkmcnt(5)\n\t"
        "v_mfma_f32_16x16x32_bf16 v[48:51], v[160:163], v[24:27], v[48:51]\n\t"
        "v_mfma_f32_16x16x32_bf16 v[68:71], v[164:167], v[24:27], v[68:71]\n\t"
        "ds_read_b128 v[24:27], %[xb] offset:22528\n\t"
        "s_waitcnt lgkmcnt(5)\n\t"
        "v_mfma_f32_16x16x32_bf16 v[52:55], v[160:163], v[28:31], v[52:55]\n\t"
        "v_mfma_f32_16x16x32_bf16 v[72:75], v[164:167], v[28:31], v[72:75]\n\t"
        "buffer_load_dwordx4 v[200:203], %[iv], %[ssrd], %[si] offen offset:2048 sc1\n\t"
        "buffer_load_dwordx4 v[204:207], %[iv], %[ssrd], %[si] offen offset:3072 sc1\n\t"
        "ds_read_b128 v[28:31], %[xb] offset:24576\n\t"
        "s_waitcnt lgkmcnt(5)\n\t"
        "v_mfma_f32_16x16x32_bf16 v[56:59], v[160:163], v[32:35], v[56:59]\n\t"
        "v_mfma_f32_16x16x32_bf16 v[76:79], v[164:167], v[32:35], v[76:79]\n\t"
        "ds_read_b128 v[32:35], %[xb] offset:26624\n\t"
        "s_waitcnt lgkmcnt(5)\n\t"
        "v_mfma_f32_16x16x32_bf16 v[60:63], v[160:163], v[36:39], v[60:63]\n\t"
        "v_mfma_f32_16x16x32_bf16 v[80:83], v[164:167], v[36:39], v[80:83]\n\t"
        "ds_read_b128 v[36:39], %[xb] offset:28672\n\t"
        "s_waitcnt lgkmcnt(5)\n\t"
        "v_mfma_f32_16x16x32_bf16 v[64:67], v[160:163], v[40:43], v[64:67]\n\t"
        "v_mfma_f32_16x16x32_bf16 v[84:87], v[164:167], v[40:43], v[84:87]\n\t"
        "s_bitcmp1_b32 %[scm], 3\n\t"
        "s_cbranch_scc1 Lc3_%=\n\t"
        "Ld3_%=:\n\t"
        "ds_read_b128 v[40:43], %[xa] offset:30720\n\t"
        "s_waitcnt vmcnt(8) lgkmcnt(5)\n\t"
        "v_mfma_f32_16x16x32_bf16 v[48:51], v[168:171], v[44:47], v[48:51]\n\t"
        "v_mfma_f32_16x16x32_bf16 v[68:71], v[172:175], v[44:47], v[68:71]\n\t"
        "ds_read_b128 v[44:47], %[xa] offset:32768\n\t"
        "s_waitcnt lgkmcnt(5)\n\t"
        "v_mfma_f32_16x16x32_bf16 v[52:55], v[168:171], v[24:27], v[52:55]\n\t"
        "v_mfma_f32_16x16x32_bf16 v[72:75], v[172:175], v[24:27], v[72:75]\n\t"
        "s_add_u32 %[si], %[si], 4096\n\t"
        "buffer_load_dwordx4 v[208:211], %[iv], %[ssrd], %[si] offen offset:0 sc1\n\t"
        "buffer_load_dwordx4 v[212:215], %[iv], %[ssrd], %[si] offen offset:1024 sc1\n\t"
        "ds_read_b128 v[24:27], %[xa] offset:34816\n\t"
        "s_waitcnt lgkmcnt(5)\n\t"
        "v_mfma_f32_16x16x32_bf16 v[56:59], v[168:171], v[28:31], v[56:59]\n\t"
        "v_mfma_f32_16x16x32_bf16 v[76:79], v[172:175], v[28:31], v[76:79]\n\t"
        "ds_read_b128 v[28:31], %[xa] offset:36864\n\t"
        "s_waitcnt lgkmcnt(5)\n\t"
        "v_mfma_f32_16x16x32_bf16 v[60:63], v[168:171], v[32:35], v[60:63]\n\t"
        "v_mfma_f32_16x16x32_bf16 v[80:83], v[172:175], v[32:35], v[80:83]\n\t"
        "ds_read_b128 v[32:35], %[xa] offset:38912\n\t"
        "s_waitcnt lgkmcnt(5)\n\t"
        "v_mfma_f32_16x16x32_bf16 v[64:67], v[168:171], v[36:39], v[64:67]\n\t"
        "v_mfma_f32_16x16x32_bf16 v[84:87], v[172:175], v[36:39], v[84:87]\n\t"
        "ds_read_b128 v[36:39], %[xb] offset:30720\n\t"
        "s_waitcnt vmcnt(8) lgkmcnt(5)\n\t"
        "v_mfma_f32_16x16x32_bf16 v[48:51], v[176:179], v[40:43], v[48:51]\n\t"
        "v_mfma_f32_16x16x32_bf16 v[68:71], v[180:183], v[40:43], v[68:71]\n\t"
        "ds_read_b128 v[40:43], %[xb] offset:32768\n\t"
        "s_waitcnt lgkmcnt(5)\n\t"
        "v_mfma_f32_16x16x32_bf16 v[52:55], v[176:179], v[44:47], v[52:55]\n\t"
        "v_mfma_f32_16x16x32_bf16 v[72:75], v[180:183], v[44:47], v[72:75]\n\t"
        "buffer_load_dwordx4 v[216:219], %[iv], %[ssrd], %[si] offen offset:2048 sc1\n\t"
        "buffer_load_dwordx4 v[220:223], %[iv], %[ssrd], %[si] offen offset:3072 sc1\n\t"
        "ds_read_b128 v[44:47], %[xb] offset:34816\n\t"
        "s_waitcnt lgkmcnt(5)\n\t"
        "v_mfma_f32_16x16x32_bf16 v[56:59], v[176:179], v[24:27], v[56:59]\n\t"
        "v_mfma_f32_16x16x32_bf16 v[76:79], v[180:183], v[24:27], v[76:79]\n\t"
        "ds_read_b128 v[24:27], %[xb] offset:36864\n\t"
        "s_waitcnt lgkmcnt(5)\n\t"
        "v_mfma_f32_16x16x32_bf16 v[60:63], v[176:179], v[28:31], v[60:63]\n\t"
        "v_mfma_f32_16x16x32_bf16 v[80:83], v[180:183], v[28:31], v[80:83]\n\t"
        "ds_read_b128 v[28:31], %[xb] offset:38912\n\t"
        "s_waitcnt lgkmcnt(5)\n\t"
        "v_mfma_f32_16x16x32_bf16 v[64:67], v[176:179], v[32:35], v[64:67]\n\t"
        "v_mfma_f32_16x16x32_bf16 v[84:87], v[180:183], v[32:35], v[84:87]\n\t"
        "s_bitcmp1_b32 %[scm], 4\n\t"
        "s_cbranch_scc1 Lc4_%=\n\t"
        "Ld4_%=:\n\t"
        "ds_read_b128 v[32:35], %[xa] offset:40960\n\t"
        "v_mov_b32 %[x2], 153984\n\t"
        "v_mov_b32 %[x0], 1\n\t"
        "s_mov_b64 exec, 1\n\t"
        "ds_add_u32 %[x2], %[x0] offset:%[omc]+4\n\t"
        "s_mov_b64 exec, -1\n\t"
        "s_waitcnt vmcnt(8) lgkmcnt(6)\n\t"
        "v_mfma_f32_16x16x32_bf16 v[48:51], v[184:187], v[36:39], v[48:51]\n\t"
        "v_mfma_f32_16x16x32_bf16 v[68:71], v[188:191], v[36:39], v[68:71]\n\t"
        "ds_read_b128 v[36:39], %[xa] offset:43008\n\t"
        "s_waitcnt lgkmcnt(6)\n\t"
        "v_mfma_f32_16x16x32_bf16 v[52:55], v[184:187], v[40:43], v[52:55]\n\t"
        "v_mfma_f32_16x16x32_bf16 v[72:75], v[188:191], v[40:43], v[72:75]\n\t"
        "s_add_u32 %[si], %[si], 4096\n\t"
        "buffer_load_dwordx4 v[224:227], %[iv], %[ssrd], %[si] offen offset:0 sc1\n\t"
        "buffer_load_dwordx4 v[228:231], %[iv], %[ssrd], %[si] offen offset:1024 sc1\n\t"
        "ds_read_b128 v[40:43], %[xa] offset:45056\n\t"
        "s_waitcnt lgkmcnt(6)\n\t"
        "v_mfma_f32_16x16x32_bf16 v[56:59], v[184:187], v[44:47], v[56:59]\n\t"
        "v_mfma_f32_16x16x32_bf16 v[76:79], v[188:191], v[44:47], v[76:79]\n\t"
        "ds_read_b128 v[44:47], %[xa] offset:47104\n\t"
        "s_waitcnt lgkmcnt(6)\n\t"
        "v_mfma_f32_16x16x32_bf16 v[60:63], v[184:187], v[24:27], v[60:63]\n\t"
        "v_mfma_f32_16x16x32_bf16 v[80:83], v[188:191], v[24:27], v[80:83]\n\t"
        "ds_read_b128 v[24:27], %[xa] offset:49152\n\t"
        "s_waitcnt lgkmcnt(6)\n\t"
        "v_mfma_f32_16x16x32_bf16 v[64:67], v[184:187], v[28:31], v[64:67]\n\t"
        "v_mfma_f32_16x16x32_bf16 v[84:87], v[188:191], v[28:31], v[84:87]\n\t"
        "ds_read_b128 v[28:31], %[xb] offset:40960\n\t"
        "s_waitcnt vmcnt(8) lgkmcnt(6)\n\t"
        "v_mfma_f32_16x16x32_bf16 v[48:51], v[192:195], v[32:35], v[48:51]\n\t"
        "v_mfma_f32_16x16x32_bf16 v[68:71], v[196:199], v[32:35], v[68:71]\n\t"
        "ds_read_b128 v[32:35], %[xb] offset:43008\n\t"
        "s_waitcnt lgkmcnt(5)\n\t"
        "v_mfma_f32_16x16x32_bf16 v[52:55], v[192:195], v[36:39], v[52:55]\n\t"
        "v_mfma_f32_16x16x32_bf16 v[72:75], v[196:199], v[36:39], v[72:75]\n\t"
        "buffer_load_dwordx4 v[232:235], %[iv], %[ssrd], %[si] offen offset:2048 sc1\n\t"
        "buffer_load_dwordx4 v[236:239], %[iv], %[ssrd], %[si] offen offset:3072 sc1\n\t"
        "ds_read_b128 v[36:39], %[xb] offset:45056\n\t"
        "s_waitcnt lgkmcnt(5)\n\t"
        "v_mfma_f32_16x16x32_bf16 v[56:59], v[192:195], v[40:43], v[56:59]\n\t"
        "v_mfma_f32_16x16x32_bf16 v[76:79], v[196:199], v[40:43], v[76:79]\n\t"
        "ds_read_b128 v[40:43], %[xb] offset:47104\n\t"
        "s_waitcnt lgkmcnt(5)\n\t"
        "v_mfma_f32_16x16x32_bf16 v[60:63], v[192:195], v[44:47], v[60:63]\n\t"
        "v_mfma_f32_16x16x32_bf16 v[80:83], v[196:199], v[44:47], v[80:83]\n\t"
        "ds_read_b128 v[44:47], %[xb] offset:49152\n\t"
        "s_waitcnt lgkmcnt(5)\n\t"
        "v_mfma_f32_16x16x32_bf16 v[64:67], v[192:195], v[24:27], v[64:67]\n\t"
        "v_mfma_f32_16x16x32_bf16 v[84:87], v[196:199], v[24:27], v[84:87]\n\t"
        "s_bitcmp1_b32 %[scm], 5\n\t"
        "s_cbranch_scc1 Lc5_%=\n\t"
        "Ld5_%=:\n\t"
        "v_add_u32 %[xa], 51200, %[xa]\n\t"
        "ds_read_b128 v[24:27], %[xa] offset:0\n\t"
        "s_waitcnt vmcnt(8) lgkmcnt(5)\n\t"
        "v_mfma_f32_16x16x32_bf16 v[48:51], v[200:203], v[28:31], v[48:51]\n\t"
        "v_mfma_f32_16x16x32_bf16 v[68:71], v[204:207], v[28:31], v[68:71]\n\t"
        "ds_read_b128 v[28:31], %[xa] offset:2048\n\t"
        "s_waitcnt lgkmcnt(5)\n\t"
        "v_mfma_f32_16x16x32_bf16 v[52:55], v[200:203], v[32:35], v[52:55]\n\t"
        "v_mfma_f32_16x16x32_bf16 v[72:75], v[204:207], v[32:35], v[72:75]\n\t"
        "s_add_u32 %[si], %[si], 4096\n\t"
        "buffer_load_dwordx4 v[240:243], %[iv], %[ssrd], %[si] offen offset:0 sc1\n\t"
        "buffer_load_dwordx4 v[244:247], %[iv], %[ssrd], %[si] offen offset:1024 sc1\n\t"
        "ds_read_b128 v[32:35], %[xa] offset:4096\n\t"
        "s_waitcnt lgkmcnt(5)\n\t"
        "v_mfma_f32_16x16x32_bf16 v[56:59], v[200:203], v[36:39], v[56:59]\n\t"
        "v_mfma_f32_16x16x32_bf16 v[76:79], v[204:207], v[36:39], v[76:79]\n\t"
        "ds_read_b128 v[36:39], %[xa] offset:6144\n\t"
        "s_waitcnt lgkmcnt(5)\n\t"
        "v_mfma_f32_16x16x32_bf16 v[60:63], v[200:203], v[40:43], v[60:63]\n\t"
        "v_mfma_f32_16x16x32_bf16 v[80:83], v[204:207], v[40:43], v[80:83]\n\t"
        "ds_read_b128 v[40:43], %[xa] offset:8192\n\t"
        "s_waitcnt lgkmcnt(5)\n\t"
        "v_mfma_f32_16x16x32_bf16 v[64:67], v[200:203], v[44:47], v[64:67]\n\t"
        "v_mfma_f32_16x16x32_bf16 v[84:87], v[204:207], v[44:47], v[84:87]\n\t"
        "v_add_u32 %[xb], 51200, %[xb]\n\t"
        "ds_read_b128 v[44:47], %[xb] offset:0\n\t"
        "s_setprio 0\n\t"
        "s_waitcnt vmcnt(8) lgkmcnt(5)\n\t"
        "v_mfma_f32_16x16x32_bf16 v[48:51], v[208:211], v[24:27], v[48:51]\n\t"
        "v_mfma_f32_16x16x32_bf16 v[68:71], v[212:215], v[24:27], v[68:71]\n\t"
        "ds_read_b128 v[24:27], %[xb] offset:2048\n\t"
        "s_waitcnt lgkmcnt(5)\n\t"
        "v_mfma_f32_16x16x32_bf16 v[52:55], v[208:211], v[28:31], v[52:55]\n\t"
        "v_mfma_f32_16x16x32_bf16 v[72:75], v[212:215], v[28:31], v[72:75]\n\t"
        "buffer_load_dwordx4 v[248:251], %[iv], %[ssrd], %[si] offen offset:2048 sc1\n\t"
        "buffer_load_dwordx4 v[252:255], %[iv], %[ssrd], %[si] offen offset:3072 sc1\n\t"
        "ds_read_b128 v[28:31], %[xb] offset:4096\n\t"
        "s_waitcnt lgkmcnt(5)\n\t"
        "v_mfma_f32_16x16x32_bf16 v[56:59], v[208:211], v[32:35], v[56:59]\n\t"
        "v_mfma_f32_16x16x32_bf16 v[76:79], v[212:215], v[32:35], v[76:79]\n\t"
        "ds_read_b128 v[32:35], %[xb] offset:6144\n\t"
        "s_waitcnt lgkmcnt(5)\n\t"
        "v_mfma_f32_16x16x32_bf16 v[60:63], v[208:211], v[36:39], v[60:63]\n\t"
        "v_mfma_f32_16x16x32_bf16 v[80:83], v[212:215], v[36:39], v[80:83]\n\t"
        "ds_read_b128 v[36:39], %[xb] offset:8192\n\t"
        "s_waitcnt lgkmcnt(5)\n\t"
        "v_mfma_f32_16x16x32_bf16 v[64:67], v[208:211], v[40:43], v[64:67]\n\t"
        "v_mfma_f32_16x16x32_bf16 v[84:87], v[212:215], v[40:43], v[84:87]\n\t"
        "s_bitcmp1_b32 %[scm], 6\n\t"
        "s_cbranch_scc1 Lc6_%=\n\t"
        "Ld6_%=:\n\t"
        "ds_read_b128 v[40:43], %[xa] offset:10240\n\t"
        "s_waitcnt vmcnt(8) lgkmcnt(5)\n\t"
        "v_mfma_f32_16x16x32_bf16 v[48:51], v[216:219], v[44:47], v[48:51]\n\t"
        "v_mfma_f32_16x16x32_bf16 v[68:71], v[220:223], v[44:47], v[68:71]\n\t"
        "ds_read_b128 v[44:47], %[xa] offset:12288\n\t"
        "s_waitcnt lgkmcnt(5)\n\t"
        "v_mfma_f32_16x16x32_bf16 v[52:55], v[216:219], v[24:27], v[52:55]\n\t"
        "v_mfma_f32_16x16x32_bf16 v[72:75], v[220:223], v[24:27], v[72:75]\n\t"
        "buffer_load_dword %[fl], %[fo], %[ssrd], 0 offen sc1\n\t"
        "ds_read_b128 v[24:27], %[xa] offset:14336\n\t"
        "s_waitcnt lgkmcnt(5)\n\t"
        "v_mfma_f32_16x16x32_bf16 v[56:59], v[216:219], v[28:31], v[56:59]\n\t"
        "v_mfma_f32_16x16x32_bf16 v[76:79], v[220:223], v[28:31], v[76:79]\n\t"
        "ds_read_b128 v[28:31], %[xa] offset:16384\n\t"
        "s_waitcnt lgkmcnt(5)\n\t"
        "v_mfma_f32_16x16x32_bf16 v[60:63], v[216:219], v[32:35], v[60:63]\n\t"
        "v_mfma_f32_16x16x32_bf16 v[80:83], v[220:223], v[32:35], v[80:83]\n\t"
        "ds_read_b128 v[32:35], %[xa] offset:18432\n\t"
        "s_waitcnt lgkmcnt(5)\n\t"
        "v_mfma_f32_16x16x32_bf16 v[64:67], v[216:219], v[36:39], v[64:67]\n\t"
        "v_mfma_f32_16x16x32_bf16 v[84:87], v[220:223], v[36:39], v[84:87]\n\t"
        "ds_read_b128 v[36:39], %[xb] offset:10240\n\t"
        "s_waitcnt vmcnt(7) lgkmcnt(5)\n\t"
        "v_mfma_f32_16x16x32_bf16 v[48:51], v[224:227], v[40:43], v[48:51]\n\t"
        "v_mfma_f32_16x16x32_bf16 v[68:71], v[228:231], v[40:43], v[68:71]\n\t"
        "ds_read_b128 v[40:43], %[xb] offset:12288\n\t"
        "s_waitcnt lgkmcnt(5)\n\t"
        "v_mfma_f32_16x16x32_bf16 v[52:55], v[224:227], v[44:47], v[52:55]\n\t"
        "v_mfma_f32_16x16x32_bf16 v[72:75], v[228:231], v[44:47], v[72:75]\n\t"
        "ds_read_b128 v[44:47], %[xb] offset:14336\n\t"
        "s_waitcnt lgkmcnt(5)\n\t"
        "v_mfma_f32_16x16x32_bf16 v[56:59], v[224:227], v[24:27], v[56:59]\n\t"
        "v_mfma_f32_16x16x32_bf16 v[76:79], v[228:231], v[24:27], v[76:79]\n\t"
        "ds_read_b128 v[24:27], %[xb] offset:16384\n\t"
        "s_waitcnt lgkmcnt(5)\n\t"
        "v_mfma_f32_16x16x32_bf16 v[60:63], v[224:227], v[28:31], v[60:63]\n\t"
        "v_mfma_f32_16x16x32_bf16 v[80:83], v[228:231], v[28:31], v[80:83]\n\t"
        "ds_read_b128 v[28:31], %[xb] offset:18432\n\t"
        "s_waitcnt lgkmcnt(5)\n\t"
        "v_mfma_f32_16x16x32_bf16 v[64:67], v[224:227], v[32:35], v[64:67]\n\t"
        "v_mfma_f32_16x16x32_bf16 v[84:87], v[228:231], v[32:35], v[84:87]\n\t"
        "s_bitcmp1_b32 %[scm], 7\n\t"
        "s_cbranch_scc1 Lc7_%=\n\t"
        "Ld7_%=:\n\t"
        "ds_read_b128 v[32:35], %[xa] offset:20480\n\t"
        "s_waitcnt vmcnt(5) lgkmcnt(5)\n\t"
        "v_mfma_f32_16x16x32_bf16 v[48:51], v[232:235], v[36:39], v[48:51]\n\t"
        "v_mfma_f32_16x16x32_bf16 v[68:71], v[236:239], v[36:39], v[68:71]\n\t"
        "ds_read_b128 v[36:39], %[xa] offset:22528\n\t"
        "s_waitcnt lgkmcnt(5)\n\t"
        "v_mfma_f32_16x16x32_bf16 v[52:55], v[232:235], v[40:43], v[52:55]\n\t"
        "v_mfma_f32_16x16x32_bf16 v[72:75], v[236:239], v[40:43], v[72:75]\n\t"
        "ds_read_b128 v[40:43], %[xa] offset:24576\n\t"
        "s_waitcnt lgkmcnt(5)\n\t"
        "v_mfma_f32_16x16x32_bf16 v[56:59], v[232:235], v[44:47], v[56:59]\n\t"
        "v_mfma_f32_16x16x32_bf16 v[76:79], v[236:239], v[44:47], v[76:79]\n\t"
        "ds_read_b128 v[44:47], %[xa] offset:26624\n\t"
        "s_waitcnt lgkmcnt(5)\n\t"
        "v_mfma_f32_16x16x32_bf16 v[60:63], v[232:235], v[24:27], v[60:63]\n\t"
        "v_mfma_f32_16x16x32_bf16 v[80:83], v[236:239], v[24:27], v[80:83]\n\t"
        "ds_read_b128 v[24:27], %[xa] offset:28672\n\t"
        "s_waitcnt lgkmcnt(5)\n\t"
        "v_mfma_f32_16x16x32_bf16 v[64:67], v[232:235], v[28:31], v[64:67]\n\t"
        "v_mfma_f32_16x16x32_bf16 v[84:87], v[236:239], v[28:31], v[84:87]\n\t"
        "ds_read_b128 v[28:31], %[xb] offset:20480\n\t"
        "s_waitcnt vmcnt(3) lgkmcnt(5)\n\t"
        "v_mfma_f32_16x16x32_bf16 v[48:51], v[240:243], v[32:35], v[48:51]\n\t"
        "v_mfma_f32_16x16x32_bf16 v[68:71], v[244:247], v[32:35], v[68:71]\n\t"
        "ds_read_b128 v[32:35], %[xb] offset:22528\n\t"
        "s_waitcnt lgkmcnt(5)\n\t"
        "v_mfma_f32_16x16x32_bf16 v[52:55], v[240:243], v[36:39], v[52:55]\n\t"
        "v_mfma_f32_16x16x32_bf16 v[72:75], v[244:247], v[36:39], v[72:75]\n\t"
        "ds_read_b128 v[36:39], %[xb] offset:24576\n\t"
        "v_and_b32 %[x0], 0xf0, v107\n\t"
        "v_lshrrev_b32 %[x0], 1, %[x0]\n\t"
        "v_add_u32 %[x0], 153344, %[x0]\n\t"
        "v_lshrrev_b32 %[x1], 8, v107\n\t"
        "v_lshl_add_u32 %[x1], %[x1], 5, %[scs]\n\t"
        "s_waitcnt lgkmcnt(5)\n\t"
        "v_mfma_f32_16x16x32_bf16 v[56:59], v[240:243], v[40:43], v[56:59]\n\t"
        "v_mfma_f32_16x16x32_bf16 v[76:79], v[244:247], v[40:43], v[76:79]\n\t"
        "ds_read_b128 v[40:43], %[xb] offset:26624\n\t"
        "s_waitcnt lgkmcnt(5)\n\t"
        "v_mfma_f32_16x16x32_bf16 v[60:63], v[240:243], v[44:47], v[60:63]\n\t"
        "v_mfma_f32_16x16x32_bf16 v[80:83], v[244:247], v[44:47], v[80:83]\n\t"
        "ds_read_b128 v[44:47], %[xb] offset:28672\n\t"
        "s_waitcnt lgkmcnt(5)\n\t"
        "v_mfma_f32_16x16x32_bf16 v[64:67], v[240:243], v[24:27], v[64:67]\n\t"
        "v_mfma_f32_16x16x32_bf16 v[84:87], v[244:247], v[24:27], v[84:87]\n\t"
        "s_waitcnt vmcnt(1) lgkmcnt(4)\n\t"
        "v_mfma_f32_16x16x32_bf16 v[48:51], v[248:251], v[28:31], v[48:51]\n\t"
        "v_mfma_f32_16x16x32_bf16 v[68:71], v[252:255], v[28:31], v[68:71]\n\t"
        "s_waitcnt lgkmcnt(3)\n\t"
        "v_mfma_f32_16x16x32_bf16 v[52:55], v[248:251], v[32:35], v[52:55]\n\t"
        "v_mfma_f32_16x16x32_bf16 v[72:75], v[252:255], v[32:35], v[72:75]\n\t"
        "ds_read_b64 v[34:35], %[x2] offset:%[omc]\n\t"
        "ds_read_b64 v[24:25], %[x0] offset:0\n\t"
        "ds_read_b64 v[26:27], %[x0] offset:128\n\t"
        "ds_read_b64 v[28:29], %[x0] offset:256\n\t"
        "ds_read_b64 v[30:31], %[x0] offset:384\n\t"
        "ds_read_b64 v[32:33], %[x0] offset:512\n\t"
        "s_waitcnt lgkmcnt(8)\n\t"
        "v_mfma_f32_16x16x32_bf16 v[56:59], v[248:251], v[36:39], v[56:59]\n\t"
        "v_mfma_f32_16x16x32_bf16 v[76:79], v[252:255], v[36:39], v[76:79]\n\t"
        "s_waitcnt lgkmcnt(7)\n\t"
        "v_mfma_f32_16x16x32_bf16 v[60:63], v[248:251], v[40:43], v[60:63]\n\t"
        "v_mfma_f32_16x16x32_bf16 v[80:83], v[252:255], v[40:43], v[80:83]\n\t"
        "s_waitcnt lgkmcnt(6)\n\t"
        "v_mfma_f32_16x16x32_bf16 v[64:67], v[248:251], v[44:47], v[64:67]\n\t"
        "v_mfma_f32_16x16x32_bf16 v[84:87], v[252:255], v[44:47], v[84:87]\n\t"
        "v_mov_b32_dpp v36, v48 row_newbcast:0 row_mask:0xf bank_mask:0xf bound_ctrl:1\n\t"
        "v_mov_b32_dpp v37, v49 row_newbcast:0 row_mask:0xf bank_mask:0xf bound_ctrl:1\n\t"
        "v_mov_b32_dpp v38, v50 row_newbcast:0 row_mask:0xf bank_mask:0xf bound_ctrl:1\n\t"
        "v_mov_b32_dpp v39, v51 row_newbcast:0 row_mask:0xf bank_mask:0xf bound_ctrl:1\n\t"
        "v_mov_b32_dpp v40, v68 row_newbcast:0 row_mask:0xf bank_mask:0xf bound_ctrl:1\n\t"
        "v_mov_b32_dpp v41, v69 row_newbcast:0 row_mask:0xf bank_mask:0xf bound_ctrl:1\n\t"
        "v_mov_b32_dpp v42, v70 row_newbcast:0 row_mask:0xf bank_mask:0xf bound_ctrl:1\n\t"
        "v_mov_b32_dpp v43, v71 row_newbcast:0 row_mask:0xf bank_mask:0xf bound_ctrl:1\n\t"
        "Lm_%=:\n\t"
        "s_waitcnt vmcnt(0) lgkmcnt(0)\n\t"
        "v_readfirstlane_b32 %[sf], v34\n\t"
        "v_cmp_ne_u32 vcc, %[sexp], %[fl]\n\t"
        "s_cmp_lt_u32 %[sf], 3\n\t"
        "s_cbranch_scc1 Ln_%=\n\t"
        "s_cbranch_vccnz Ln_%=\n\t"
        "Lo_%=:\n\t"
        "s_mov_b64 exec, %[sm16]\n\t"
        "ds_write_b128 %[x1], v[36:39]\n\t"
        "ds_write_b128 %[x1], v[40:43] offset:16\n\t"
        "s_mov_b64 exec, -1\n\t"
        "v_fma_f32 v48, -v24, v36, v48\n\t"
        "v_fma_f32 v49, -v24, v37, v49\n\t"
        "v_fma_f32 v50, -v24, v38, v50\n\t"
        "v_fma_f32 v51, -v24, v39, v51\n\t"
        "v_fma_f32 v68, -v24, v40, v68\n\t"
        "v_fma_f32 v69, -v24, v41, v69\n\t"
        "v_fma_f32 v70, -v24, v42, v70\n\t"
        "v_fma_f32 v71, -v24, v43, v71\n\t"
        "v_mul_f32 v48, v25, v48\n\t"
        "v_mul_f32 v49, v25, v49\n\t"
        "v_mul_f32 v50, v25, v50\n\t"
        "v_mul_f32 v51, v25, v51\n\t"
        "v_mul_f32 v68, v25, v68\n\t"
        "v_mul_f32 v69, v25, v69\n\t"
        "v_mul_f32 v70, v25, v70\n\t"
        "v_mul_f32 v71, v25, v71\n\t"
        "v_cvt_pk_bf16_f32 v48, v48, v68\n\t"
        "v_cvt_pk_bf16_f32 v49, v49, v69\n\t"
        "v_cvt_pk_bf16_f32 v50, v50, v70\n\t"
        "v_cvt_pk_bf16_f32 v51, v51, v71\n\t"
        "v_add_u32 v68, v110, v111\n\t"
        "v_add_u32 v69, v68, v111\n\t"
        "v_add_u32 v70, v69, v111\n\t"
        "v_add_u32 v71, v70, v111\n\t"
        "v_fma_f32 v52, -v26, v36, v52\n\t"
        "v_fma_f32 v53, -v26, v37, v53\n\t"
        "v_fma_f32 v54, -v26, v38, v54\n\t"
        "v_fma_f32 v55, -v26, v39, v55\n\t"
        "v_fma_f32 v72, -v26, v40, v72\n\t"
        "v_fma_f32 v73, -v26, v41, v73\n\t"
        "v_fma_f32 v74, -v26, v42, v74\n\t"
        "v_fma_f32 v75, -v26, v43, v75\n\t"
        "v_mul_f32 v52, v27, v52\n\t"
        "v_mul_f32 v53, v27, v53\n\t"
        "v_mul_f32 v54, v27, v54\n\t"
        "v_mul_f32 v55, v27, v55\n\t"
        "v_mul_f32 v72, v27, v72\n\t"
        "v_mul_f32 v73, v27, v73\n\t"
        "v_mul_f32 v74, v27, v74\n\t"
        "v_mul_f32 v75, v27, v75\n\t"
        "v_cvt_pk_bf16_f32 v52, v52, v72\n\t"
        "v_cvt_pk_bf16_f32 v53, v53, v73\n\t"
        "v_cvt_pk_bf16_f32 v54, v54, v74\n\t"
        "v_cvt_pk_bf16_f32 v55, v55, v75\n\t"
        "v_fma_f32 v56, -v28, v36, v56\n\t"
        "v_fma_f32 v57, -v28, v37, v57\n\t"
        "v_fma_f32 v58, -v28, v38, v58\n\t"
        "v_fma_f32 v59, -v28, v39, v59\n\t"
        "v_fma_f32 v76, -v28, v40, v76\n\t"
        "v_fma_f32 v77, -v28, v41, v77\n\t"
        "v_fma_f32 v78, -v28, v42, v78\n\t"
        "v_fma_f32 v79, -v28, v43, v79\n\t"
        "v_mul_f32 v56, v29, v56\n\t"
        "v_mul_f32 v57, v29, v57\n\t"
        "v_mul_f32 v58, v29, v58\n\t"
        "v_mul_f32 v59, v29, v59\n\t"
        "v_mul_f32 v76, v29, v76\n\t"
        "v_mul_f32 v77, v29, v77\n\t"
        "v_mul_f32 v78, v29, v78\n\t"
        "v_mul_f32 v79, v29, v79\n\t"
        "v_cvt_pk_bf16_f32 v56, v56, v76\n\t"
        "v_cvt_pk_bf16_f32 v57, v57, v77\n\t"
        "v_cvt_pk_bf16_f32 v58, v58, v78\n\t"
        "v_cvt_pk_bf16_f32 v59, v59, v79\n\t"
        "v_fma_f32 v60, -v30, v36, v60\n\t"
        "v_fma_f32 v61, -v30, v37, v61\n\t"
        "v_fma_f32 v62, -v30, v38, v62\n\t"
        "v_fma_f32 v63, -v30, v39, v63\n\t"
        "v_fma_f32 v80, -v30, v40, v80\n\t"
        "v_fma_f32 v81, -v30, v41, v81\n\t"
        "v_fma_f32 v82, -v30, v42, v82\n\t"
        "v_fma_f32 v83, -v30, v43, v83\n\t"
        "v_mul_f32 v60, v31, v60\n\t"
        "v_mul_f32 v61, v31, v61\n\t"
        "v_mul_f32 v62, v31, v62\n\t"
        "v_mul_f32 v63, v31, v63\n\t"
        "v_mul_f32 v80, v31, v80\n\t"
        "v_mul_f32 v81, v31, v81\n\t"
        "v_mul_f32 v82, v31, v82\n\t"
        "v_mul_f32 v83, v31, v83\n\t"
        "v_cvt_pk_bf16_f32 v60, v60, v80\n\t"
        "v_cvt_pk_bf16_f32 v61, v61, v81\n\t"
        "v_cvt_pk_bf16_f32 v62, v62, v82\n\t"
        "v_cvt_pk_bf16_f32 v63, v63, v83\n\t"
        "v_fma_f32 v64, -v32, v36, v64\n\t"
        "v_fma_f32 v65, -v32, v37, v65\n\t"
        "v_fma_f32 v66, -v32, v38, v66\n\t"
        "v_fma_f32 v67, -v32, v39, v67\n\t"
        "v_fma_f32 v84, -v32, v40, v84\n\t"
        "v_fma_f32 v85, -v32, v41, v85\n\t"
        "v_fma_f32 v86, -v32, v42, v86\n\t"
        "v_fma_f32 v87, -v32, v43, v87\n\t"
        "v_mul_f32 v64, v33, v64\n\t"
        "v_mul_f32 v65, v33, v65\n\t"
        "v_mul_f32 v66, v33, v66\n\t"
        "v_mul_f32 v67, v33, v67\n\t"
        "v_mul_f32 v84, v33, v84\n\t"
        "v_mul_f32 v85, v33, v85\n\t"
        "v_mul_f32 v86, v33, v86\n\t"
        "v_mul_f32 v87, v33, v87\n\t"
        "v_cvt_pk_bf16_f32 v64, v64, v84\n\t"
        "v_cvt_pk_bf16_f32 v65, v65, v85\n\t"
        "v_cvt_pk_bf16_f32 v66, v66, v86\n\t"
        "v_cvt_pk_bf16_f32 v67, v67, v87\n\t"
        "Lu_%=:\n\t"
        "v_readfirstlane_b32 %[sf], v35\n\t"
        "s_cmp_ge_u32 %[sf], 7\n\t"
        "s_cbranch_scc1 Lv_%=\n\t"
        "s_cmp_lt_i32 %[scn], 1\n\t"
        "s_cbranch_scc1 Lv_%=\n\t"
        "s_sub_u32 %[scn], %[scn], 1\n\t"
        "s_sleep 4\n\t"
        "ds_read_b32 v35, %[x2] offset:%[omc]+4\n\t"
        "s_waitcnt lgkmcnt(0)\n\t"
        "s_branch Lu_%=\n\t"
        "Lv_%=:\n\t"
        "s_mov_b64 exec, %[sva]\n\t"
        "ds_write_b128 v68, v[52:55]\n\t"
        "ds_write_b128 v69, v[56:59]\n\t"
        "ds_write_b128 v70, v[60:63]\n\t"
        "ds_write_b128 v71, v[64:67]\n\t"
        "s_mov_b64 exec, %[sv0]\n\t"
        "ds_write_b128 v110, v[48:51]\n\t"
        "s_mov_b64 exec, -1\n\t"
        "s_branch Le_%=\n\t"
        "Ln_%=:\n\t"
        "s_cmp_lt_i32 %[scn], 1\n\t"
        "s_cbranch_scc1 Lo_%=\n\t"
        "s_sub_u32 %[scn], %[scn], 1\n\t"
        "buffer_load_dword %[fl], %[fo], %[ssrd], 0 offen sc1\n\t"
        "ds_read_b64 v[34:35], %[x2] offset:%[omc]\n\t"
        "ds_read_b64 v[24:25], %[x0] offset:0\n\t"
        "ds_read_b64 v[26:27], %[x0] offset:128\n\t"
        "ds_read_b64 v[28:29], %[x0] offset:256\n\t"
        "ds_read_b64 v[30:31], %[x0] offset:384\n\t"
        "ds_read_b64 v[32:33], %[x0] offset:512\n\t"
        "s_branch Lm_%=\n\t"
        "Lc0_%=:\n\t"
        "v_add_u32 %[xa], %[c0], %[xa]\n\t"
        "v_add_u32 %[xb], %[c1], %[xb]\n\t"
        "v_xor_b32 %[c0], 10240, %[c0]\n\t"
        "v_xor_b32 %[c1], 10240, %[c1]\n\t"
        "s_branch Ld0_%=\n\t"
        "Lc1_%=:\n\t"
        "v_add_u32 %[xa], %[c0], %[xa]\n\t"
        "v_add_u32 %[xb], %[c1], %[xb]\n\t"
        "v_xor_b32 %[c0], 10240, %[c0]\n\t"
        "v_xor_b32 %[c1], 10240, %[c1]\n\t"
        "s_branch Ld1_%=\n\t"
        "Lc2_%=:\n\t"
        "v_add_u32 %[xa], %[c0], %[xa]\n\t"
        "v_add_u32 %[xb], %[c1], %[xb]\n\t"
        "v_xor_b32 %[c0], 10240, %[c0]\n\t"
        "v_xor_b32 %[c1], 10240, %[c1]\n\t"
        "s_branch Ld2_%=\n\t"
        "Lc3_%=:\n\t"
        "v_add_u32 %[xa], %[c0], %[xa]\n\t"
        "v_add_u32 %[xb], %[c1], %[xb]\n\t"
        "v_xor_b32 %[c0], 10240, %[c0]\n\t"
        "v_xor_b32 %[c1], 10240, %[c1]\n\t"
        "s_branch Ld3_%=\n\t"
        "Lc4_%=:\n\t"
        "v_add_u32 %[xa], %[c0], %[xa]\n\t"
        "v_add_u32 %[xb], %[c1], %[xb]\n\t"
        "v_xor_b32 %[c0], 10240, %[c0]\n\t"
        "v_xor_b32 %[c1], 10240, %[c1]\n\t"
        "s_branch Ld4_%=\n\t"
        "Lc5_%=:\n\t"
        "v_add_u32 %[xa], %[c0], %[xa]\n\t"
        "v_add_u32 %[xb], %[c1], %[xb]\n\t"
        "v_xor_b32 %[c0], 10240, %[c0]\n\t"
        "v_xor_b32 %[c1], 10240, %[c1]\n\t"
        "s_branch Ld5_%=\n\t"
        "Lc6_%=:\n\t"
        "v_add_u32 %[xa], %[c0], %[xa]\n\t"
        "v_add_u32 %[xb], %[c1], %[xb]\n\t"
        "v_xor_b32 %[c0], 10240, %[c0]\n\t"
        "v_xor_b32 %[c1], 10240, %[c1]\n\t"
        "s_branch Ld6_%=\n\t"
        "Lc7_%=:\n\t"
        "v_add_u32 %[xa], %[c0], %[xa]\n\t"
        "v_add_u32 %[xb], %[c1], %[xb]\n\t"
        "v_xor_b32 %[c0], 10240, %[c0]\n\t"
        "v_xor_b32 %[c1], 10240, %[c1]\n\t"
        "s_branch Ld7_%=\n\t"
        "Le_%=:\n\t"
        : [xa] "+v"(xa), [xb] "=&v"(xb), [c0] "+v"(c0), [c1] "+v"(c1), [si] "+s"(si), [fl] "=&v"(fl), [sf] "=&s"(sf), [scn] "+s"(cn), [x0] "+v"(xt[0]), [x1] "+v"(xt[1]), [x2] "+v"(xt[2])
        : [ssrd] "s"(srd), [iv] "v"(iv), [sro] "s"(ro), [scm] "s"(cm), [fo] "v"(fo), [sexp] "s"(fexp), [scs] "s"(cs), [sva] "s"(va), [sv0] "s"(v0), [sm16] "s"(0x0001000100010001ull), [omc] "n"(BC_MC)
        : "memory", "scc", "vcc", "v255", "v24", "v25", "v26", "v27", "v28", "v29", "v30", "v31", "v32", "v33", "v34", "v35", "v36", "v37", "v38", "v39", "v40", "v41", "v42", "v43", "v44", "v45", "v46", "v47");
}

DEV void gemm1a(int xo0, int fb, u32 cs, u64 va, u64 v0, u32 thr, u32& sf, u32& cn) {
    int xa, xh; u32 one, fl;
    asm volatile(
        "v_add_u32 %[xa], 36864, %[xo0]\n\t"
        "v_mov_b32 %[one], 1\n\t"
        "s_setprio 2\n\t"
        "ds_read_b128 v[24:27], %[xo0] offset:8192\n\t"
        "ds_read_b128 v[28:31], %[xo0] offset:8448\n\t"
        "ds_read_b128 v[32:35], %[xo0] offset:8704\n\t"
        "ds_read_b128 v[36:39], %[xo0] offset:8960\n\t"
        "ds_read_b128 v[40:43], %[xo0] offset:12288\n\t"
        "ds_read_b128 v[44:47], %[xo0] offset:12544\n\t"
        "ds_read_b128 v[8:11], %[xo0] offset:12800\n\t"
        "ds_read_b128 v[12:15], %[xo0] offset:13056\n\t"
        "ds_read_b128 v[16:19], %[xo0] offset:16384\n\t"
        "ds_read_b128 v[20:23], %[xo0] offset:16640\n\t"
        "ds_read_b128 v[48:51], %[xo0] offset:16896\n\t"
        "ds_read_b128 v[68:71], %[xo0] offset:17152\n\t"
        "s_waitcnt lgkmcnt(11)\n\t"
        "v_mfma_f32_16x16x32_bf16 v[52:55], v[128:131], v[24:27], v[52:55]\n\t"
        "v_mfma_f32_16x16x32_bf16 v[72:75], v[132:135], v[24:27], v[72:75]\n\t"
        "s_waitcnt lgkmcnt(10)\n\t"
        "v_mfma_f32_16x16x32_bf16 v[56:59], v[128:131], v[28:31], v[56:59]\n\t"
        "ds_read_b128 v[24:27], %[xo0] offset:20480\n\t"
        "v_mfma_f32_16x16x32_bf16 v[76:79], v[132:135], v[28:31], v[76:79]\n\t"
        "s_waitcnt lgkmcnt(10)\n\t"
        "v_mfma_f32_16x16x32_bf16 v[60:63], v[128:131], v[32:35], v[60:63]\n\t"
        "ds_read_b128 v[28:31], %[xo0] offset:20736\n\t"
        "v_mfma_f32_16x16x32_bf16 v[80:83], v[132:135], v[32:35], v[80:83]\n\t"
        "s_waitcnt lgkmcnt(10)\n\t"
        "v_mfma_f32_16x16x32_bf16 v[64:67], v[128:131], v[36:39], v[64:67]\n\t"
        "ds_read_b128 v[32:35], %[xo0] offset:20992\n\t"
        "v_mfma_f32_16x16x32_bf16 v[84:87], v[132:135], v[36:39], v[84:87]\n\t"
        "s_waitcnt lgkmcnt(10)\n\t"
        "v_mfma_f32_16x16x32_bf16 v[52:55], v[136:139], v[40:43], v[52:55]\n\t"
        "ds_read_b128 v[36:39], %[xo0] offset:21248\n\t"
        "v_mfma_f32_16x16x32_bf16 v[72:75], v[140:143], v[40:43], v[72:75]\n\t"
        "s_waitcnt lgkmcnt(10)\n\t"
        "v_mfma_f32_16x16x32_bf16 v[56:59], v[136:139], v[44:47], v[56:59]\n\t"
        "ds_read_b32 %[fl], %[fb] offset:%[og0]+4\n\t"
        "v_mfma_f32_16x16x32_bf16 v[76:79], v[140:143], v[44:47], v[76:79]\n\t"
        "s_waitcnt lgkmcnt(10)\n\t"
        "v_mfma_f32_16x16x32_bf16 v[60:63], v[136:139], v[8:11], v[60:63]\n\t"
        "v_mfma_f32_16x16x32_bf16 v[80:83], v[140:143], v[8:11], v[80:83]\n\t"
        "s_waitcnt lgkmcnt(9)\n\t"
        "v_mfma_f32_16x16x32_bf16 v[64:67], v[136:139], v[12:15], v[64:67]\n\t"
        "v_mfma_f32_16x16x32_bf16 v[84:87], v[140:143], v[12:15], v[84:87]\n\t"
        "s_waitcnt lgkmcnt(8)\n\t"
        "v_mfma_f32_16x16x32_bf16 v[52:55], v[144:147], v[16:19], v[52:55]\n\t"
        "v_mfma_f32_16x16x32_bf16 v[72:75], v[148:151], v[16:19], v[72:75]\n\t"
        "s_waitcnt lgkmcnt(7)\n\t"
        "v_mfma_f32_16x16x32_bf16 v[56:59], v[144:147], v[20:23], v[56:59]\n\t"
        "v_mfma_f32_16x16x32_bf16 v[76:79], v[148:151], v[20:23], v[76:79]\n\t"
        "s_waitcnt lgkmcnt(6)\n\t"
        "v_mfma_f32_16x16x32_bf16 v[60:63], v[144:147], v[48:51], v[60:63]\n\t"
        "s_waitcnt lgkmcnt(0)\n\t"
        "v_cmp_gt_u32 vcc, 2, %[fl]\n\t"
        "v_mfma_f32_16x16x32_bf16 v[80:83], v[148:151], v[48:51], v[80:83]\n\t"
        "v_mfma_f32_16x16x32_bf16 v[64:67], v[144:147], v[68:71], v[64:67]\n\t"
        "s_cbranch_vccnz Ls16_%=\n\t"
        "Lg16_%=:\n\t"
        "ds_read_b128 v[40:43], %[xo0] offset:24576\n\t"
        "v_mfma_f32_16x16x32_bf16 v[84:87], v[148:151], v[68:71], v[84:87]\n\t"
        "ds_read_b128 v[44:47], %[xo0] offset:24832\n\t"
        "v_mfma_f32_16x16x32_bf16 v[52:55], v[152:155], v[24:27], v[52:55]\n\t"
        "ds_read_b128 v[8:11], %[xo0] offset:25088\n\t"
        "v_mfma_f32_16x16x32_bf16 v[72:75], v[156:159], v[24:27], v[72:75]\n\t"
        "ds_read_b128 v[12:15], %[xo0] offset:25344\n\t"
        "v_mfma_f32_16x16x32_bf16 v[56:59], v[152:155], v[28:31], v[56:59]\n\t"
        "ds_read_b128 v[16:19], %[xo0] offset:28672\n\t"
        "v_mfma_f32_16x16x32_bf16 v[76:79], v[156:159], v[28:31], v[76:79]\n\t"
        "ds_read_b128 v[20:23], %[xo0] offset:28928\n\t"
        "v_mfma_f32_16x16x32_bf16 v[60:63], v[152:155], v[32:35], v[60:63]\n\t"
        "ds_read_b128 v[48:51], %[xo0] offset:29184\n\t"
        "v_mfma_f32_16x16x32_bf16 v[80:83], v[156:159], v[32:35], v[80:83]\n\t"
        "ds_read_b128 v[68:71], %[xo0] offset:29440\n\t"
        "v_mfma_f32_16x16x32_bf16 v[64:67], v[152:155], v[36:39], v[64:67]\n\t"
        "ds_read_b128 v[24:27], %[xo0] offset:32768\n\t"
        "v_mfma_f32_16x16x32_bf16 v[84:87], v[156:159], v[36:39], v[84:87]\n\t"
        "ds_read_b128 v[28:31], %[xo0] offset:33024\n\t"
        "s_waitcnt lgkmcnt(9)\n\t"
        "v_mfma_f32_16x16x32_bf16 v[52:55], v[160:163], v[40:43], v[52:55]\n\t"
        "ds_read_b128 v[32:35], %[xo0] offset:33280\n\t"
        "v_mfma_f32_16x16x32_bf16 v[72:75], v[164:167], v[40:43], v[72:75]\n\t"
        "ds_read_b128 v[36:39], %[xo0] offset:33536\n\t"
        "s_mov_b64 exec, 1\n\t"
        "ds_add_u32 %[fb], %[one] offset:%[omc]+4\n\t"
        "s_mov_b64 exec, -1\n\t"
        "s_waitcnt lgkmcnt(11)\n\t"
        "v_and_b32 %[xo0], 0xf0, v107\n\t"
        "v_lshrrev_b32 %[xo0], 1, %[xo0]\n\t"
        "v_mfma_f32_16x16x32_bf16 v[56:59], v[160:163], v[44:47], v[56:59]\n\t"
        "ds_read_b128 v[40:43], %[xa] offset:0\n\t"
        "v_add_u32 %[xo0], 153344, %[xo0]\n\t"
        "v_lshrrev_b32 %[xh], 8, v107\n\t"
        "v_mfma_f32_16x16x32_bf16 v[76:79], v[164:167], v[44:47], v[76:79]\n\t"
        "s_waitcnt lgkmcnt(11)\n\t"
        "v_lshl_add_u32 %[xh], %[xh], 5, %[scs]\n\t"
        "v_mfma_f32_16x16x32_bf16 v[60:63], v[160:163], v[8:11], v[60:63]\n\t"
        "ds_read_b128 v[44:47], %[xa] offset:256\n\t"
        "v_mfma_f32_16x16x32_bf16 v[80:83], v[164:167], v[8:11], v[80:83]\n\t"
        "s_waitcnt lgkmcnt(11)\n\t"
        "v_mfma_f32_16x16x32_bf16 v[64:67], v[160:163], v[12:15], v[64:67]\n\t"
        "ds_read_b128 v[8:11], %[xa] offset:512\n\t"
        "v_mfma_f32_16x16x32_bf16 v[84:87], v[164:167], v[12:15], v[84:87]\n\t"
        "s_waitcnt lgkmcnt(11)\n\t"
        "v_mfma_f32_16x16x32_bf16 v[52:55], v[168:171], v[16:19], v[52:55]\n\t"
        "ds_read_b128 v[12:15], %[xa] offset:768\n\t"
        "v_mfma_f32_16x16x32_bf16 v[72:75], v[172:175], v[16:19], v[72:75]\n\t"
        "s_waitcnt lgkmcnt(11)\n\t"
        "v_mfma_f32_16x16x32_bf16 v[56:59], v[168:171], v[20:23], v[56:59]\n\t"
        "ds_read_b32 %[fl], %[fb] offset:%[og0]+8\n\t"
        "v_mfma_f32_16x16x32_bf16 v[76:79], v[172:175], v[20:23], v[76:79]\n\t"
        "s_waitcnt lgkmcnt(11)\n\t"
        "v_mfma_f32_16x16x32_bf16 v[60:63], v[168:171], v[48:51], v[60:63]\n\t"
        "v_mfma_f32_16x16x32_bf16 v[80:83], v[172:175], v[48:51], v[80:83]\n\t"
        "s_waitcnt lgkmcnt(10)\n\t"
        "v_mfma_f32_16x16x32_bf16 v[64:67], v[168:171], v[68:71], v[64:67]\n\t"
        "v_mfma_f32_16x16x32_bf16 v[84:87], v[172:175], v[68:71], v[84:87]\n\t"
        "s_waitcnt lgkmcnt(9)\n\t"
        "v_mfma_f32_16x16x32_bf16 v[52:55], v[176:179], v[24:27], v[52:55]\n\t"
        "v_mfma_f32_16x16x32_bf16 v[72:75], v[180:183], v[24:27], v[72:75]\n\t"
        "s_waitcnt lgkmcnt(8)\n\t"
        "v_mfma_f32_16x16x32_bf16 v[56:59], v[176:179], v[28:31], v[56:59]\n\t"
        "v_mfma_f32_16x16x32_bf16 v[76:79], v[180:183], v[28:31], v[76:79]\n\t"
        "s_waitcnt lgkmcnt(7)\n\t"
        "v_mfma_f32_16x16x32_bf16 v[60:63], v[176:179], v[32:35], v[60:63]\n\t"
        "s_waitcnt lgkmcnt(0)\n\t"
        "v_cmp_gt_u32 vcc, 2, %[fl]\n\t"
        "v_mfma_f32_16x16x32_bf16 v[80:83], v[180:183], v[32:35], v[80:83]\n\t"
        "v_mfma_f32_16x16x32_bf16 v[64:67], v[176:179], v[36:39], v[64:67]\n\t"
        "s_cbranch_vccnz Ls32_%=\n\t"
        "Lg32_%=:\n\t"
        "ds_read_b128 v[16:19], %[xa] offset:4096\n\t"
        "v_mfma_f32_16x16x32_bf16 v[84:87], v[180:183], v[36:39], v[84:87]\n\t"
        "ds_read_b128 v[20:23], %[xa] offset:4352\n\t"
        "v_mfma_f32_16x16x32_bf16 v[52:55], v[184:187], v[40:43], v[52:55]\n\t"
        "ds_read_b128 v[48:51], %[xa] offset:4608\n\t"
        "v_mfma_f32_16x16x32_bf16 v[72:75], v[188:191], v[40:43], v[72:75]\n\t"
        "ds_read_b128 v[68:71], %[xa] offset:4864\n\t"
        "v_mfma_f32_16x16x32_bf16 v[56:59], v[184:187], v[44:47], v[56:59]\n\t"
        "ds_read_b128 v[24:27], %[xa] offset:8192\n\t"
        "v_mfma_f32_16x16x32_bf16 v[76:79], v[188:191], v[44:47], v[76:79]\n\t"
        "ds_read_b128 v[28:31], %[xa] offset:8448\n\t"
        "v_mfma_f32_16x16x32_bf16 v[60:63], v[184:187], v[8:11], v[60:63]\n\t"
        "ds_read_b128 v[32:35], %[xa] offset:8704\n\t"
        "v_mfma_f32_16x16x32_bf16 v[80:83], v[188:191], v[8:11], v[80:83]\n\t"
        "ds_read_b128 v[36:39], %[xa] offset:8960\n\t"
        "v_mfma_f32_16x16x32_bf16 v[64:67], v[184:187], v[12:15], v[64:67]\n\t"
        "ds_read_b32 %[fl], %[fb] offset:%[og0]+12\n\t"
        "ds_read_b128 v[40:43], %[xa] offset:12288\n\t"
        "v_mfma_f32_16x16x32_bf16 v[84:87], v[188:191], v[12:15], v[84:87]\n\t"
        "ds_read_b128 v[44:47], %[xa] offset:16384\n\t"
        "s_waitcnt lgkmcnt(10)\n\t"
        "v_mfma_f32_16x16x32_bf16 v[52:55], v[192:195], v[16:19], v[52:55]\n\t"
        "v_mfma_f32_16x16x32_bf16 v[72:75], v[196:199], v[16:19], v[72:75]\n\t"
        "s_waitcnt lgkmcnt(9)\n\t"
        "v_mfma_f32_16x16x32_bf16 v[56:59], v[192:195], v[20:23], v[56:59]\n\t"
        "v_mfma_f32_16x16x32_bf16 v[76:79], v[196:199], v[20:23], v[76:79]\n\t"
        "s_waitcnt lgkmcnt(8)\n\t"
        "v_mfma_f32_16x16x32_bf16 v[60:63], v[192:195], v[48:51], v[60:63]\n\t"
        "ds_read_b128 v[8:11], %[xh] offset:0\n\t"
        "v_mfma_f32_16x16x32_bf16 v[80:83], v[196:199], v[48:51], v[80:83]\n\t"
        "ds_read_b128 v[12:15], %[xh] offset:16\n\t"
        "s_waitcnt lgkmcnt(9)\n\t"
        "v_mfma_f32_16x16x32_bf16 v[64:67], v[192:195], v[68:71], v[64:67]\n\t"
        "ds_read_b64 v[98:99], %[fb] offset:%[omc]\n\t"
        "v_mfma_f32_16x16x32_bf16 v[84:87], v[196:199], v[68:71], v[84:87]\n\t"
        "ds_read_b64 v[16:17], %[xo0] offset:128\n\t"
        "s_waitcnt lgkmcnt(10)\n\t"
        "v_mfma_f32_16x16x32_bf16 v[52:55], v[200:203], v[24:27], v[52:55]\n\t"
        "s_waitcnt lgkmcnt(6)\n\t"
        "v_cmp_gt_u32 vcc, %[sthr], %[fl]\n\t"
        "ds_read_b64 v[18:19], %[xo0] offset:256\n\t"
        "v_mfma_f32_16x16x32_bf16 v[72:75], v[204:207], v[24:27], v[72:75]\n\t"
        "ds_read_b64 v[20:21], %[xo0] offset:384\n\t"
        "v_mfma_f32_16x16x32_bf16 v[56:59], v[200:203], v[28:31], v[56:59]\n\t"
        "s_cbranch_vccnz Ls42_%=\n\t"
        "Lg42_%=:\n\t"
        "ds_read_b128 v[48:51], %[xa] offset:20480\n\t"
        "v_mfma_f32_16x16x32_bf16 v[76:79], v[204:207], v[28:31], v[76:79]\n\t"
        "ds_read_b64 v[22:23], %[xo0] offset:512\n\t"
        "v_mfma_f32_16x16x32_bf16 v[60:63], v[200:203], v[32:35], v[60:63]\n\t"
        "ds_read_b128 v[68:71], %[xa] offset:24576\n\t"
        "v_mfma_f32_16x16x32_bf16 v[80:83], v[204:207], v[32:35], v[80:83]\n\t"
        "ds_read_b128 v[24:27], %[xa] offset:28672\n\t"
        "v_mfma_f32_16x16x32_bf16 v[64:67], v[200:203], v[36:39], v[64:67]\n\t"
        "ds_read_b128 v[28:31], %[xa] offset:32768\n\t"
        "v_mfma_f32_16x16x32_bf16 v[84:87], v[204:207], v[36:39], v[84:87]\n\t"
        "ds_read_b128 v[32:35], %[xa] offset:12544\n\t"
        "s_waitcnt lgkmcnt(13)\n\t"
        "v_mfma_f32_16x16x32_bf16 v[52:55], v[208:211], v[40:43], v[52:55]\n\t"
        "ds_read_b128 v[36:39], %[xa] offset:16640\n\t"
        "v_mfma_f32_16x16x32_bf16 v[72:75], v[212:215], v[40:43], v[72:75]\n\t"
        "s_waitcnt lgkmcnt(13)\n\t"
        "v_mfma_f32_16x16x32_bf16 v[52:55], v[216:219], v[44:47], v[52:55]\n\t"
        "ds_read_b128 v[40:43], %[xa] offset:20736\n\t"
        "v_mfma_f32_16x16x32_bf16 v[72:75], v[220:223], v[44:47], v[72:75]\n\t"
        "s_waitcnt lgkmcnt(7)\n\t"
        "v_mfma_f32_16x16x32_bf16 v[52:55], v[224:227], v[48:51], v[52:55]\n\t"
        "ds_read_b128 v[44:47], %[xa] offset:24832\n\t"
        "v_add_u32 %[xh], 73728, v107\n\t"
        "v_mfma_f32_16x16x32_bf16 v[72:75], v[228:231], v[48:51], v[72:75]\n\t"
        "s_waitcnt lgkmcnt(6)\n\t"
        "v_mfma_f32_16x16x32_bf16 v[52:55], v[232:235], v[68:71], v[52:55]\n\t"
        "ds_read_b128 v[48:51], %[xa] offset:28928\n\t"
        "v_mfma_f32_16x16x32_bf16 v[72:75], v[236:239], v[68:71], v[72:75]\n\t"
        "s_waitcnt lgkmcnt(6)\n\t"
        "v_mfma_f32_16x16x32_bf16 v[52:55], v[240:243], v[24:27], v[52:55]\n\t"
        "ds_read_b128 v[68:71], %[xa] offset:33024\n\t"
        "v_mfma_f32_16x16x32_bf16 v[72:75], v[244:247], v[24:27], v[72:75]\n\t"
        "s_waitcnt lgkmcnt(6)\n\t"
        "v_mfma_f32_16x16x32_bf16 v[52:55], v[248:251], v[28:31], v[52:55]\n\t"
        "v_cmp_gt_u32 vcc, 5, v98\n\t"
        "ds_read_b128 v[24:27], %[xa] offset:12800\n\t"
        "v_mfma_f32_16x16x32_bf16 v[72:75], v[252:255], v[28:31], v[72:75]\n\t"
        "s_waitcnt lgkmcnt(6)\n\t"
        "v_mfma_f32_16x16x32_bf16 v[56:59], v[208:211], v[32:35], v[56:59]\n\t"
        "s_cbranch_vccnz Ln_%=\n\t"
        "Lo_%=:\n\t"
        "ds_read_b128 v[28:31], %[xa] offset:16896\n\t"
        "v_mfma_f32_16x16x32_bf16 v[76:79], v[212:215], v[32:35], v[76:79]\n\t"
        "s_waitcnt lgkmcnt(6)\n\t"
        "v_mfma_f32_16x16x32_bf16 v[56:59], v[216:219], v[36:39], v[56:59]\n\t"
        "ds_read_b128 v[32:35], %[xa] offset:20992\n\t"
        "v_fma_f32 v52, -v16, v8, v52\n\t"
        "v_fma_f32 v53, -v16, v9, v53\n\t"
        "v_mfma_f32_16x16x32_bf16 v[76:79], v[220:223], v[36:39], v[76:79]\n\t"
        "s_waitcnt lgkmcnt(6)\n\t"
        "v_fma_f32 v54, -v16, v10, v54\n\t"
        "v_fma_f32 v55, -v16, v11, v55\n\t"
        "v_mfma_f32_16x16x32_bf16 v[56:59], v[224:227], v[40:43], v[56:59]\n\t"
        "ds_read_b128 v[36:39], %[xa] offset:25088\n\t"
        "v_fma_f32 v72, -v16, v12, v72\n\t"
        "v_fma_f32 v73, -v16, v13, v73\n\t"
        "v_mfma_f32_16x16x32_bf16 v[76:79], v[228:231], v[40:43], v[76:79]\n\t"
        "s_waitcnt lgkmcnt(6)\n\t"
        "v_fma_f32 v74, -v16, v14, v74\n\t"
        "v_fma_f32 v75, -v16, v15, v75\n\t"
        "v_mfma_f32_16x16x32_bf16 v[56:59], v[232:235], v[44:47], v[56:59]\n\t"
        "ds_read_b128 v[40:43], %[xa] offset:29184\n\t"
        "v_mul_f32 v52, v17, v52\n\t"
        "v_mul_f32 v53, v17, v53\n\t"
        "v_mfma_f32_16x16x32_bf16 v[76:79], v[236:239], v[44:47], v[76:79]\n\t"
        "s_waitcnt lgkmcnt(6)\n\t"
        "v_mul_f32 v54, v17, v54\n\t"
        "v_mul_f32 v55, v17, v55\n\t"
        "v_mfma_f32_16x16x32_bf16 v[56:59], v[240:243], v[48:51], v[56:59]\n\t"
        "ds_read_b128 v[44:47], %[xa] offset:33280\n\t"
        "v_mul_f32 v72, v17, v72\n\t"
        "v_mul_f32 v73, v17, v73\n\t"
        "v_mfma_f32_16x16x32_bf16 v[76:79], v[244:247], v[48:51], v[76:79]\n\t"
        "s_waitcnt lgkmcnt(6)\n\t"
        "v_mul_f32 v74, v17, v74\n\t"
        "v_mul_f32 v75, v17, v75\n\t"
        "v_mfma_f32_16x16x32_bf16 v[56:59], v[248:251], v[68:71], v[56:59]\n\t"
        "ds_read_b128 v[48:51], %[xa] offset:13056\n\t"
        "v_cvt_pk_bf16_f32 v52, v52, v72\n\t"
        "v_cvt_pk_bf16_f32 v53, v53, v73\n\t"
        "v_mfma_f32_16x16x32_bf16 v[76:79], v[252:255], v[68:71], v[76:79]\n\t"
        "s_waitcnt lgkmcnt(6)\n\t"
        "v_cvt_pk_bf16_f32 v54, v54, v74\n\t"
        "v_cvt_pk_bf16_f32 v55, v55, v75\n\t"
        "v_mfma_f32_16x16x32_bf16 v[60:63], v[208:211], v[24:27], v[60:63]\n\t"
        "v_add_u32 v16, v110, v111\n\t"
        "v_mfma_f32_16x16x32_bf16 v[80:83], v[212:215], v[24:27], v[80:83]\n\t"
        "ds_read_b128 v[72:75], %[xa] offset:17152\n\t"
        "s_waitcnt lgkmcnt(6)\n\t"
        "s_cmp_eq_u64 %[sv0], 0\n\t"
        "s_cbranch_scc1 Lq_%=\n\t"
        "v_mfma_f32_16x16x32_bf16 v[60:63], v[216:219], v[28:31], v[60:63]\n\t"
        "ds_read_b32 %[fl], %[fb] offset:%[og0]+16\n\t"
        "ds_read_b128 v[24:27], %[xa] offset:21248\n\t"
        "v_fma_f32 v56, -v18, v8, v56\n\t"
        "v_fma_f32 v57, -v18, v9, v57\n\t"
        "v_mfma_f32_16x16x32_bf16 v[80:83], v[220:223], v[28:31], v[80:83]\n\t"
        "s_waitcnt lgkmcnt(7)\n\t"
        "v_fma_f32 v58, -v18, v10, v58\n\t"
        "v_fma_f32 v59, -v18, v11, v59\n\t"
        "v_mfma_f32_16x16x32_bf16 v[60:63], v[224:227], v[32:35], v[60:63]\n\t"
        "ds_read_b128 v[28:31], %[xa] offset:25344\n\t"
        "v_fma_f32 v76, -v18, v12, v76\n\t"
        "v_fma_f32 v77, -v18, v13, v77\n\t"
        "v_mfma_f32_16x16x32_bf16 v[80:83], v[228:231], v[32:35], v[80:83]\n\t"
        "s_waitcnt lgkmcnt(7)\n\t"
        "v_fma_f32 v78, -v18, v14, v78\n\t"
        "v_fma_f32 v79, -v18, v15, v79\n\t"
        "v_mfma_f32_16x16x32_bf16 v[60:63], v[232:235], v[36:39], v[60:63]\n\t"
        "ds_read_b128 v[32:35], %[xa] offset:29440\n\t"
        "v_mul_f32 v56, v19, v56\n\t"
        "v_mul_f32 v57, v19, v57\n\t"
        "v_mfma_f32_16x16x32_bf16 v[80:83], v[236:239], v[36:39], v[80:83]\n\t"
        "s_waitcnt lgkmcnt(7)\n\t"
        "v_mul_f32 v58, v19, v58\n\t"
        "v_mul_f32 v59, v19, v59\n\t"
        "v_mfma_f32_16x16x32_bf16 v[60:63], v[240:243], v[40:43], v[60:63]\n\t"
        "ds_read_b128 v[36:39], %[xa] offset:33536\n\t"
        "s_mov_b64 exec, 1\n\t"
        "ds_add_u32 %[fb], %[one] offset:%[oc1]\n\t"
        "s_mov_b64 exec, -1\n\t"
        "v_mul_f32 v76, v19, v76\n\t"
        "v_mul_f32 v77, v19, v77\n\t"
        "v_mfma_f32_16x16x32_bf16 v[80:83], v[244:247], v[40:43], v[80:83]\n\t"
        "s_waitcnt lgkmcnt(8)\n\t"
        "v_mul_f32 v78, v19, v78\n\t"
        "v_mul_f32 v79, v19, v79\n\t"
        "v_mfma_f32_16x16x32_bf16 v[60:63], v[248:251], v[44:47], v[60:63]\n\t"
        "v_cvt_pk_bf16_f32 v56, v56, v76\n\t"
        "v_cvt_pk_bf16_f32 v57, v57, v77\n\t"
        "v_mfma_f32_16x16x32_bf16 v[80:83], v[252:255], v[44:47], v[80:83]\n\t"
        "s_waitcnt lgkmcnt(7)\n\t"
        "v_cvt_pk_bf16_f32 v58, v58, v78\n\t"
        "v_cvt_pk_bf16_f32 v59, v59, v79\n\t"
        "v_mfma_f32_16x16x32_bf16 v[64:67], v[208:211], v[48:51], v[64:67]\n\t"
        "s_waitcnt lgkmcnt(5)\n\t"
        "v_cmp_gt_u32 vcc, 2, %[fl]\n\t"
        "v_add_u32 v18, v16, v111\n\t"
        "v_mfma_f32_16x16x32_bf16 v[84:87], v[212:215], v[48:51], v[84:87]\n\t"
        "v_mfma_f32_16x16x32_bf16 v[64:67], v[216:219], v[72:75], v[64:67]\n\t"
        "s_cbranch_vccnz Ls64_%=\n\t"
        "Lg64_%=:\n\t"
        "ds_read_b128 v[40:43], %[xh] offset:0\n\t"
        "v_fma_f32 v60, -v20, v8, v60\n\t"
        "v_fma_f32 v61, -v20, v9, v61\n\t"
        "v_mfma_f32_16x16x32_bf16 v[84:87], v[220:223], v[72:75], v[84:87]\n\t"
        "ds_read_b128 v[44:47], %[xh] offset:1024\n\t"
        "s_waitcnt lgkmcnt(6)\n\t"
        "v_fma_f32 v62, -v20, v10, v62\n\t"
        "v_fma_f32 v63, -v20, v11, v63\n\t"
        "v_mfma_f32_16x16x32_bf16 v[64:67], v[224:227], v[24:27], v[64:67]\n\t"
        "ds_read_b128 v[76:79], %[xh] offset:2048\n\t"
        "v_fma_f32 v80, -v20, v12, v80\n\t"
        "v_fma_f32 v81, -v20, v13, v81\n\t"
        "v_mfma_f32_16x16x32_bf16 v[84:87], v[228:231], v[24:27], v[84:87]\n\t"
        "ds_read_b128 v[72:75], %[xh] offset:3072\n\t"
        "s_waitcnt lgkmcnt(7)\n\t"
        "v_fma_f32 v82, -v20, v14, v82\n\t"
        "v_fma_f32 v83, -v20, v15, v83\n\t"
        "v_mfma_f32_16x16x32_bf16 v[64:67], v[232:235], v[28:31], v[64:67]\n\t"
        "ds_read_b128 v[24:27], %[xh] offset:4096\n\t"
        "v_mul_f32 v60, v21, v60\n\t"
        "v_mul_f32 v61, v21, v61\n\t"
        "v_mfma_f32_16x16x32_bf16 v[84:87], v[236:239], v[28:31], v[84:87]\n\t"
        "s_waitcnt lgkmcnt(7)\n\t"
        "v_mul_f32 v62, v21, v62\n\t"
        "v_mul_f32 v63, v21, v63\n\t"
        "v_mfma_f32_16x16x32_bf16 v[64:67], v[240:243], v[32:35], v[64:67]\n\t"
        "ds_read_b128 v[28:31], %[xh] offset:5120\n\t"
        "v_mul_f32 v80, v21, v80\n\t"
        "v_mul_f32 v81, v21, v81\n\t"
        "v_mfma_f32_16x16x32_bf16 v[84:87], v[244:247], v[32:35], v[84:87]\n\t"
        "s_waitcnt lgkmcnt(7)\n\t"
        "v_mul_f32 v82, v21, v82\n\t"
        "v_mul_f32 v83, v21, v83\n\t"
        "v_mfma_f32_16x16x32_bf16 v[64:67], v[248:251], v[36:39], v[64:67]\n\t"
        "ds_read_b128 v[32:35], %[xh] offset:6144\n\t"
        "v_cvt_pk_bf16_f32 v60, v60, v80\n\t"
        "v_cvt_pk_bf16_f32 v61, v61, v81\n\t"
        "v_mfma_f32_16x16x32_bf16 v[84:87], v[252:255], v[36:39], v[84:87]\n\t"
        "s_waitcnt lgkmcnt(6)\n\t"
        "v_cvt_pk_bf16_f32 v62, v62, v82\n\t"
        "v_cvt_pk_bf16_f32 v63, v63, v83\n\t"
        "v_mfma_f32_16x16x32_bf16 v[48:51], v[112:115], v[40:43], 0\n\t"
        "ds_read_b128 v[36:39], %[xh] offset:7168\n\t"
        "v_add_u32 v20, v18, v111\n\t"
        "v_mfma_f32_16x16x32_bf16 v[68:71], v[116:119], v[40:43], 0\n\t"
        "ds_read_b128 v[80:83], %[xh] offset:8192\n\t"
        "s_waitcnt lgkmcnt(7)\n\t"
        "v_mfma_f32_16x16x32_bf16 v[48:51], v[120:123], v[44:47], v[48:51]\n\t"
        "ds_read_b128 v[40:43], %[xh] offset:9216\n\t"
        "v_fma_f32 v64, -v22, v8, v64\n\t"
        "v_fma_f32 v65, -v22, v9, v65\n\t"
        "v_mfma_f32_16x16x32_bf16 v[68:71], v[124:127], v[44:47], v[68:71]\n\t"
        "s_waitcnt lgkmcnt(7)\n\t"
        "v_fma_f32 v66, -v22, v10, v66\n\t"
        "v_fma_f32 v67, -v22, v11, v67\n\t"
        "v_mfma_f32_16x16x32_bf16 v[48:51], v[128:131], v[76:79], v[48:51]\n\t"
        "ds_read_b128 v[44:47], %[xh] offset:10240\n\t"
        "v_fma_f32 v84, -v22, v12, v84\n\t"
        "v_fma_f32 v85, -v22, v13, v85\n\t"
        "v_mfma_f32_16x16x32_bf16 v[68:71], v[132:135], v[76:79], v[68:71]\n\t"
        "s_waitcnt lgkmcnt(7)\n\t"
        "v_fma_f32 v86, -v22, v14, v86\n\t"
        "v_fma_f32 v87, -v22, v15, v87\n\t"
        "v_mfma_f32_16x16x32_bf16 v[48:51], v[136:139], v[72:75], v[48:51]\n\t"
        "ds_read_b128 v[76:79], %[xh] offset:11264\n\t"
        "v_mul_f32 v64, v23, v64\n\t"
        "v_mul_f32 v65, v23, v65\n\t"
        "v_mfma_f32_16x16x32_bf16 v[68:71], v[140:143], v[72:75], v[68:71]\n\t"
        "s_waitcnt lgkmcnt(7)\n\t"
        "v_mul_f32 v66, v23, v66\n\t"
        "v_mul_f32 v67, v23, v67\n\t"
        "v_mfma_f32_16x16x32_bf16 v[48:51], v[144:147], v[24:27], v[48:51]\n\t"
        "ds_read_b128 v[72:75], %[xh] offset:12288\n\t"
        "v_mul_f32 v84, v23, v84\n\t"
        "v_mul_f32 v85, v23, v85\n\t"
        "v_mfma_f32_16x16x32_bf16 v[68:71], v[148:151], v[24:27], v[68:71]\n\t"
        "s_waitcnt lgkmcnt(7)\n\t"
        "v_mul_f32 v86, v23, v86\n\t"
        "v_mul_f32 v87, v23, v87\n\t"
        "v_mfma_f32_16x16x32_bf16 v[48:51], v[152:155], v[28:31], v[48:51]\n\t"
        "ds_read_b128 v[24:27], %[xh] offset:13312\n\t"
        "v_cvt_pk_bf16_f32 v64, v64, v84\n\t"
        "v_cvt_pk_bf16_f32 v65, v65, v85\n\t"
        "v_mfma_f32_16x16x32_bf16 v[68:71], v[156:159], v[28:31], v[68:71]\n\t"
        "s_waitcnt lgkmcnt(7)\n\t"
        "v_cvt_pk_bf16_f32 v66, v66, v86\n\t"
        "v_cvt_pk_bf16_f32 v67, v67, v87\n\t"
        "v_mfma_f32_16x16x32_bf16 v[48:51], v[160:163], v[32:35], v[48:51]\n\t"
        "ds_read_b128 v[28:31], %[xh] offset:14336\n\t"
        "v_add_u32 v22, v20, v111\n\t"
        "v_mfma_f32_16x16x32_bf16 v[68:71], v[164:167], v[32:35], v[68:71]\n\t"
        "s_waitcnt lgkmcnt(7)\n\t"
        "v_mfma_f32_16x16x32_bf16 v[48:51], v[168:171], v[36:39], v[48:51]\n\t"
        "ds_read_b128 v[32:35], %[xh] offset:15360\n\t"
        "v_mfma_f32_16x16x32_bf16 v[68:71], v[172:175], v[36:39], v[68:71]\n\t"
        "s_waitcnt lgkmcnt(7)\n\t"
        "v_mfma_f32_16x16x32_bf16 v[48:51], v[176:179], v[80:83], v[48:51]\n\t"
        "ds_read_b128 v[36:39], %[xh] offset:16384\n\t"
        "v_mfma_f32_16x16x32_bf16 v[68:71], v[180:183], v[80:83], v[68:71]\n\t"
        "s_waitcnt lgkmcnt(7)\n\t"
        "v_mfma_f32_16x16x32_bf16 v[48:51], v[184:187], v[40:43], v[48:51]\n\t"
        "ds_read_b128 v[80:83], %[xh] offset:17408\n\t"
        "v_mfma_f32_16x16x32_bf16 v[68:71], v[188:191], v[40:43], v[68:71]\n\t"
        "ds_read_b64 v[84:85], %[xo0]\n\t"
        "s_waitcnt lgkmcnt(8)\n\t"
        "v_mfma_f32_16x16x32_bf16 v[48:51], v[192:195], v[44:47], v[48:51]\n\t"
        "ds_read_b32 v99, %[fb] offset:%[omc]+4\n\t"
        "v_mfma_f32_16x16x32_bf16 v[68:71], v[196:199], v[44:47], v[68:71]\n\t"
        "ds_read_b32 v98, %[fb]\n\t"
        "s_waitcnt lgkmcnt(9)\n\t"
        "v_mfma_f32_16x16x32_bf16 v[48:51], v[200:203], v[76:79], v[48:51]\n\t"
        "v_mfma_f32_16x16x32_bf16 v[68:71], v[204:207], v[76:79], v[68:71]\n\t"
        "s_waitcnt lgkmcnt(8)\n\t"
        "v_mfma_f32_16x16x32_bf16 v[48:51], v[208:211], v[72:75], v[48:51]\n\t"
        "v_mfma_f32_16x16x32_bf16 v[68:71], v[212:215], v[72:75], v[68:71]\n\t"
        "s_waitcnt lgkmcnt(7)\n\t"
        "v_mfma_f32_16x16x32_bf16 v[48:51], v[216:219], v[24:27], v[48:51]\n\t"
        "v_mfma_f32_16x16x32_bf16 v[68:71], v[220:223], v[24:27], v[68:71]\n\t"
        "s_waitcnt lgkmcnt(6)\n\t"
        "v_mfma_f32_16x16x32_bf16 v[48:51], v[224:227], v[28:31], v[48:51]\n\t"
        "v_mfma_f32_16x16x32_bf16 v[68:71], v[228:231], v[28:31], v[68:71]\n\t"
        "s_waitcnt lgkmcnt(5)\n\t"
        "v_mfma_f32_16x16x32_bf16 v[48:51], v[232:235], v[32:35], v[48:51]\n\t"
        "v_mfma_f32_16x16x32_bf16 v[68:71], v[236:239], v[32:35], v[68:71]\n\t"
        "s_waitcnt lgkmcnt(4)\n\t"
        "v_mfma_f32_16x16x32_bf16 v[48:51], v[240:243], v[36:39], v[48:51]\n\t"
        "v_mfma_f32_16x16x32_bf16 v[68:71], v[244:247], v[36:39], v[68:71]\n\t"
        "s_waitcnt lgkmcnt(3)\n\t"
        "v_mfma_f32_16x16x32_bf16 v[48:51], v[248:251], v[80:83], v[48:51]\n\t"
        "v_mfma_f32_16x16x32_bf16 v[68:71], v[252:255], v[80:83], v[68:71]\n\t"
        "s_waitcnt lgkmcnt(0)\n\t"
        "s_nop 5\n\t"
        "v_fma_f32 v48, -v84, v8, v48\n\t"
        "v_fma_f32 v49, -v84, v9, v49\n\t"
        "v_fma_f32 v50, -v84, v10, v50\n\t"
        "v_fma_f32 v51, -v84, v11, v51\n\t"
        "v_fma_f32 v68, -v84, v12, v68\n\t"
        "v_fma_f32 v69, -v84, v13, v69\n\t"
        "v_fma_f32 v70, -v84, v14, v70\n\t"
        "v_fma_f32 v71, -v84, v15, v71\n\t"
        "v_mul_f32 v48, v85, v48\n\t"
        "v_mul_f32 v49, v85, v49\n\t"
        "v_mul_f32 v50, v85, v50\n\t"
        "v_mul_f32 v51, v85, v51\n\t"
        "v_mul_f32 v68, v85, v68\n\t"
        "v_mul_f32 v69, v85, v69\n\t"
        "v_mul_f32 v70, v85, v70\n\t"
        "v_mul_f32 v71, v85, v71\n\t"
        "v_cvt_pk_bf16_f32 v48, v48, v68\n\t"
        "v_cvt_pk_bf16_f32 v49, v49, v69\n\t"
        "v_cvt_pk_bf16_f32 v50, v50, v70\n\t"
        "v_cvt_pk_bf16_f32 v51, v51, v71\n\t"
        "s_branch Lu_%=\n\t"
        "Lq_%=:\n\t"
        "v_mfma_f32_16x16x32_bf16 v[60:63], v[216:219], v[28:31], v[60:63]\n\t"
        "ds_read_b128 v[24:27], %[xa] offset:21248\n\t"
        "v_fma_f32 v56, -v18, v8, v56\n\t"
        "v_fma_f32 v57, -v18, v9, v57\n\t"
        "v_mfma_f32_16x16x32_bf16 v[80:83], v[220:223], v[28:31], v[80:83]\n\t"
        "s_waitcnt lgkmcnt(6)\n\t"
        "v_fma_f32 v58, -v18, v10, v58\n\t"
        "v_fma_f32 v59, -v18, v11, v59\n\t"
        "v_mfma_f32_16x16x32_bf16 v[60:63], v[224:227], v[32:35], v[60:63]\n\t"
        "ds_read_b128 v[28:31], %[xa] offset:25344\n\t"
        "v_fma_f32 v76, -v18, v12, v76\n\t"
        "v_fma_f32 v77, -v18, v13, v77\n\t"
        "v_mfma_f32_16x16x32_bf16 v[80:83], v[228:231], v[32:35], v[80:83]\n\t"
        "s_waitcnt lgkmcnt(6)\n\t"
        "v_fma_f32 v78, -v18, v14, v78\n\t"
        "v_fma_f32 v79, -v18, v15, v79\n\t"
        "v_mfma_f32_16x16x32_bf16 v[60:63], v[232:235], v[36:39], v[60:63]\n\t"
        "ds_read_b128 v[32:35], %[xa] offset:29440\n\t"
        "v_mul_f32 v56, v19, v56\n\t"
        "v_mul_f32 v57, v19, v57\n\t"
        "v_mfma_f32_16x16x32_bf16 v[80:83], v[236:239], v[36:39], v[80:83]\n\t"
        "s_waitcnt lgkmcnt(6)\n\t"
        "v_mul_f32 v58, v19, v58\n\t"
        "v_mul_f32 v59, v19, v59\n\t"
        "v_mfma_f32_16x16x32_bf16 v[60:63], v[240:243], v[40:43], v[60:63]\n\t"
        "ds_read_b128 v[36:39], %[xa] offset:33536\n\t"
        "s_mov_b64 exec, 1\n\t"
        "ds_add_u32 %[fb], %[one] offset:%[oc1]\n\t"
        "s_mov_b64 exec, -1\n\t"
        "v_mul_f32 v76, v19, v76\n\t"
        "v_mul_f32 v77, v19, v77\n\t"
        "v_mfma_f32_16x16x32_bf16 v[80:83], v[244:247], v[40:43], v[80:83]\n\t"
        "s_waitcnt lgkmcnt(7)\n\t"
        "v_mul_f32 v78, v19, v78\n\t"
        "v_mul_f32 v79, v19, v79\n\t"
        "v_mfma_f32_16x16x32_bf16 v[60:63], v[248:251], v[44:47], v[60:63]\n\t"
        "v_cvt_pk_bf16_f32 v56, v56, v76\n\t"
        "v_cvt_pk_bf16_f32 v57, v57, v77\n\t"
        "v_mfma_f32_16x16x32_bf16 v[80:83], v[252:255], v[44:47], v[80:83]\n\t"
        "s_waitcnt lgkmcnt(6)\n\t"
        "v_cvt_pk_bf16_f32 v58, v58, v78\n\t"
        "v_cvt_pk_bf16_f32 v59, v59, v79\n\t"
        "v_mfma_f32_16x16x32_bf16 v[64:67], v[208:211], v[48:51], v[64:67]\n\t"
        "v_add_u32 v18, v16, v111\n\t"
        "v_mfma_f32_16x16x32_bf16 v[84:87], v[212:215], v[48:51], v[84:87]\n\t"
        "s_waitcnt lgkmcnt(5)\n\t"
        "v_mfma_f32_16x16x32_bf16 v[64:67], v[216:219], v[72:75], v[64:67]\n\t"
        "v_fma_f32 v60, -v20, v8, v60\n\t"
        "v_fma_f32 v61, -v20, v9, v61\n\t"
        "v_mfma_f32_16x16x32_bf16 v[84:87], v[220:223], v[72:75], v[84:87]\n\t"
        "s_waitcnt lgkmcnt(4)\n\t"
        "v_fma_f32 v62, -v20, v10, v62\n\t"
        "v_fma_f32 v63, -v20, v11, v63\n\t"
        "v_mfma_f32_16x16x32_bf16 v[64:67], v[224:227], v[24:27], v[64:67]\n\t"
        "v_fma_f32 v80, -v20, v12, v80\n\t"
        "v_fma_f32 v81, -v20, v13, v81\n\t"
        "v_mfma_f32_16x16x32_bf16 v[84:87], v[228:231], v[24:27], v[84:87]\n\t"
        "s_waitcnt lgkmcnt(3)\n\t"
        "v_fma_f32 v82, -v20, v14, v82\n\t"
        "v_fma_f32 v83, -v20, v15, v83\n\t"
        "v_mfma_f32_16x16x32_bf16 v[64:67], v[232:235], v[28:31], v[64:67]\n\t"
        "v_mul_f32 v60, v21, v60\n\t"
        "v_mul_f32 v61, v21, v61\n\t"
        "v_mfma_f32_16x16x32_bf16 v[84:87], v[236:239], v[28:31], v[84:87]\n\t"
        "s_waitcnt lgkmcnt(2)\n\t"
        "v_mul_f32 v62, v21, v62\n\t"
        "v_mul_f32 v63, v21, v63\n\t"
        "v_mfma_f32_16x16x32_bf16 v[64:67], v[240:243], v[32:35], v[64:67]\n\t"
        "v_mul_f32 v80, v21, v80\n\t"
        "v_mul_f32 v81, v21, v81\n\t"
        "v_mfma_f32_16x16x32_bf16 v[84:87], v[244:247], v[32:35], v[84:87]\n\t"
        "s_waitcnt lgkmcnt(1)\n\t"
        "v_mul_f32 v82, v21, v82\n\t"
        "v_mul_f32 v83, v21, v83\n\t"
        "v_mfma_f32_16x16x32_bf16 v[64:67], v[248:251], v[36:39], v[64:67]\n\t"
        "v_cvt_pk_bf16_f32 v60, v60, v80\n\t"
        "v_cvt_pk_bf16_f32 v61, v61, v81\n\t"
        "v_mfma_f32_16x16x32_bf16 v[84:87], v[252:255], v[36:39], v[84:87]\n\t"
        "v_cvt_pk_bf16_f32 v62, v62, v82\n\t"
        "v_cvt_pk_bf16_f32 v63, v63, v83\n\t"
        "ds_read_b32 v99, %[fb] offset:%[omc]+4\n\t"
        "ds_read_b32 v98, %[fb]\n\t"
        "v_add_u32 v20, v18, v111\n\t"
        "v_fma_f32 v64, -v22, v8, v64\n\t"
        "v_fma_f32 v65, -v22, v9, v65\n\t"
        "v_fma_f32 v66, -v22, v10, v66\n\t"
        "v_fma_f32 v67, -v22, v11, v67\n\t"
        "v_fma_f32 v84, -v22, v12, v84\n\t"
        "v_fma_f32 v85, -v22, v13, v85\n\t"
        "v_fma_f32 v86, -v22, v14, v86\n\t"
        "v_fma_f32 v87, -v22, v15, v87\n\t"
        "v_mul_f32 v64, v23, v64\n\t"
        "v_mul_f32 v65, v23, v65\n\t"
        "v_mul_f32 v66, v23, v66\n\t"
        "v_mul_f32 v67, v23, v67\n\t"
        "v_mul_f32 v84, v23, v84\n\t"
        "v_mul_f32 v85, v23, v85\n\t"
        "v_mul_f32 v86, v23, v86\n\t"
        "v_mul_f32 v87, v23, v87\n\t"
        "v_cvt_pk_bf16_f32 v64, v64, v84\n\t"
        "v_cvt_pk_bf16_f32 v65, v65, v85\n\t"
        "v_cvt_pk_bf16_f32 v66, v66, v86\n\t"
        "v_cvt_pk_bf16_f32 v67, v67, v87\n\t"
        "v_add_u32 v22, v20, v111\n\t"
        "s_waitcnt lgkmcnt(0)\n\t"
        "Lu_%=:\n\t"
        "v_readfirstlane_b32 %[sf], v99\n\t"
        "s_cmp_ge_u32 %[sf], 14\n\t"
        "s_cbranch_scc1 Lv_%=\n\t"
        "s_cmp_lt_i32 %[scn], 1\n\t"
        "s_cbranch_scc1 Lv_%=\n\t"
        "s_sub_u32 %[scn], %[scn], 1\n\t"
        "s_sleep 4\n\t"
        "ds_read_b32 v99, %[fb] offset:%[omc]+4\n\t"
        "s_waitcnt lgkmcnt(0)\n\t"
        "s_branch Lu_%=\n\t"
        "Lv_%=:\n\t"
        "s_mov_b64 exec, %[sva]\n\t"
        "ds_write_b128 v16, v[52:55]\n\t"
        "ds_write_b128 v18, v[56:59]\n\t"
        "ds_write_b128 v20, v[60:63]\n\t"
        "ds_write_b128 v22, v[64:67]\n\t"
        "s_mov_b64 exec, %[sv0]\n\t"
        "ds_write_b128 v110, v[48:51]\n\t"
        "s_mov_b64 exec, -1\n\t"
        "v_readfirstlane_b32 %[sf], v98\n\t"
        "s_branch Le_%=\n\t"
        "Ls16_%=:\n\t"
        "s_cmp_lt_i32 %[scn], 1\n\t"
        "s_cbranch_scc1 Lg16_%=\n\t"
        "s_sub_u32 %[scn], %[scn], 1\n\t"
        "ds_read_b32 %[fl], %[fb] offset:%[og0]+4\n\t"
        "s_waitcnt lgkmcnt(0)\n\t"
        "v_cmp_gt_u32 vcc, 2, %[fl]\n\t"
        "s_cbranch_vccnz Ls16_%=\n\t"
        "s_branch Lg16_%=\n\t"
        "Ls32_%=:\n\t"
        "s_cmp_lt_i32 %[scn], 1\n\t"
        "s_cbranch_scc1 Lg32_%=\n\t"
        "s_sub_u32 %[scn], %[scn], 1\n\t"
        "ds_read_b32 %[fl], %[fb] offset:%[og0]+8\n\t"
        "s_waitcnt lgkmcnt(0)\n\t"
        "v_cmp_gt_u32 vcc, 2, %[fl]\n\t"
        "s_cbranch_vccnz Ls32_%=\n\t"
        "s_branch Lg32_%=\n\t"
        "Ls42_%=:\n\t"
        "s_cmp_lt_i32 %[scn], 1\n\t"
        "s_cbranch_scc1 Lg42_%=\n\t"
        "s_sub_u32 %[scn], %[scn], 1\n\t"
        "ds_read_b32 %[fl], %[fb] offset:%[og0]+12\n\t"
        "s_waitcnt lgkmcnt(0)\n\t"
        "v_cmp_gt_u32 vcc, %[sthr], %[fl]\n\t"
        "s_cbranch_vccnz Ls42_%=\n\t"
        "s_branch Lg42_%=\n\t"
        "Ls64_%=:\n\t"
        "s_cmp_lt_i32 %[scn], 1\n\t"
        "s_cbranch_scc1 Lg64_%=\n\t"
        "s_sub_u32 %[scn], %[scn], 1\n\t"
        "ds_read_b32 %[fl], %[fb] offset:%[og0]+16\n\t"
        "s_waitcnt lgkmcnt(0)\n\t"
        "v_cmp_gt_u32 vcc, 2, %[fl]\n\t"
        "s_cbranch_vccnz Ls64_%=\n\t"
        "s_branch Lg64_%=\n\t"
        "Ln_%=:\n\t"
        "s_cmp_lt_i32 %[scn], 1\n\t"
        "s_cbranch_scc1 Lo_%=\n\t"
        "s_sub_u32 %[scn], %[scn], 1\n\t"
        "s_sleep 1\n\t"
        "ds_read_b64 v[98:99], %[fb] offset:%[omc]\n\t"
        "ds_read_b64 v[16:17], %[xo0] offset:128\n\t"
        "ds_read_b64 v[18:19], %[xo0] offset:256\n\t"
        "ds_read_b64 v[20:21], %[xo0] offset:384\n\t"
        "ds_read_b64 v[22:23], %[xo0] offset:512\n\t"
        "s_waitcnt lgkmcnt(0)\n\t"
        "v_cmp_gt_u32 vcc, 5, v98\n\t"
        "s_cbranch_vccnz Ln_%=\n\t"
        "s_branch Lo_%=\n\t"
        "Le_%=:\n\t"
        : [xa] "=&v"(xa), [xh] "=&v"(xh), [xo0] "+v"(xo0), [one] "=&v"(one), [fl] "=&v"(fl), [sf] "=&s"(sf), [scn] "+s"(cn)
        : [fb] "v"(fb), [scs] "s"(cs), [sva] "s"(va), [sv0] "s"(v0), [sthr] "s"(thr), [omc] "n"(BC_MC), [oc1] "n"(BC_C1), [og0] "n"(BC_G0)
        : "memory", "scc", "vcc", "v8", "v9", "v10", "v11", "v12", "v13", "v14", "v15", "v16", "v17", "v18", "v19", "v20", "v21", "v22", "v23", "v24", "v25", "v26", "v27", "v28", "v29", "v30", "v31", "v32", "v33", "v34", "v35", "v36", "v37", "v38", "v39", "v40", "v41", "v42", "v43", "v44", "v45", "v46", "v47");
}

DEV void gemm1p() {
    int a0, a1;
    asm volatile(
        "v_add_u32 %[a0], 92672, v107\n\t"
        "v_add_u32 %[a1], 153344, v107\n\t"
        "ds_read_b128 v[24:27], %[a0] offset:0\n\t"
        "ds_read_b128 v[28:31], %[a0] offset:2304\n\t"
        "ds_read_b128 v[32:35], %[a1] offset:5280\n\t"
        "ds_read_b128 v[36:39], %[a1] offset:1824\n\t"
        "ds_read_b128 v[40:43], %[a0] offset:1024\n\t"
        "ds_read_b128 v[44:47], %[a0] offset:3328\n\t"
        "ds_read_b128 v[48:51], %[a1] offset:6304\n\t"
        "ds_read_b128 v[68:71], %[a1] offset:2848\n\t"
        "s_waitcnt lgkmcnt(7)\n\t"
        "v_mfma_f32_16x16x32_bf16 v[52:55], v[112:115], v[24:27], 0\n\t"
        "v_mfma_f32_16x16x32_bf16 v[72:75], v[116:119], v[24:27], 0\n\t"
        "s_waitcnt lgkmcnt(6)\n\t"
        "v_mfma_f32_16x16x32_bf16 v[56:59], v[112:115], v[28:31], 0\n\t"
        "v_mfma_f32_16x16x32_bf16 v[76:79], v[116:119], v[28:31], 0\n\t"
        "s_waitcnt lgkmcnt(5)\n\t"
        "v_mfma_f32_16x16x32_bf16 v[60:63], v[112:115], v[32:35], 0\n\t"
        "v_mfma_f32_16x16x32_bf16 v[80:83], v[116:119], v[32:35], 0\n\t"
        "s_waitcnt lgkmcnt(4)\n\t"
        "v_mfma_f32_16x16x32_bf16 v[64:67], v[112:115], v[36:39], 0\n\t"
        "v_mfma_f32_16x16x32_bf16 v[84:87], v[116:119], v[36:39], 0\n\t"
        "s_waitcnt lgkmcnt(3)\n\t"
        "v_mfma_f32_16x16x32_bf16 v[52:55], v[120:123], v[40:43], v[52:55]\n\t"
        "v_mfma_f32_16x16x32_bf16 v[72:75], v[124:127], v[40:43], v[72:75]\n\t"
        "s_waitcnt lgkmcnt(2)\n\t"
        "v_mfma_f32_16x16x32_bf16 v[56:59], v[120:123], v[44:47], v[56:59]\n\t"
        "v_mfma_f32_16x16x32_bf16 v[76:79], v[124:127], v[44:47], v[76:79]\n\t"
        "s_waitcnt lgkmcnt(1)\n\t"
        "v_mfma_f32_16x16x32_bf16 v[60:63], v[120:123], v[48:51], v[60:63]\n\t"
        "v_mfma_f32_16x16x32_bf16 v[80:83], v[124:127], v[48:51], v[80:83]\n\t"
        "s_waitcnt lgkmcnt(0)\n\t"
        "v_mfma_f32_16x16x32_bf16 v[64:67], v[120:123], v[68:71], v[64:67]\n\t"
        "v_mfma_f32_16x16x32_bf16 v[84:87], v[124:127], v[68:71], v[84:87]\n\t"
        "s_nop 7\n\t"
        : [a0] "=&v"(a0), [a1] "=&v"(a1) :: "memory", "v24", "v25", "v26", "v27", "v28", "v29", "v30", "v31", "v32", "v33", "v34", "v35", "v36", "v37", "v38", "v39", "v40", "v41", "v42", "v43", "v44", "v45", "v46", "v47");
}

DEV void gemm1b(int fb, int xr, int xq, u32 w1, u32 cs, u64 va, u64 v0, u32& cn) {
    u32 sf, st; u64 mw, mx;
    asm volatile(
        "v_add_u32 v36, 73728, v107\n\t"
        "v_mov_b32 v35, 153344\n\t"
        "v_lshrrev_b32 v37, 8, v107\n\t"
        "v_lshl_add_u32 v37, v37, 5, %[scs]\n\t"
        "s_waitcnt lgkmcnt(0)\n\t"
        "ds_read_b128 v[48:51], v37 offset:0\n\t"
        "ds_read_b128 v[68:71], v37 offset:16\n\t"
        "v_lshrrev_b32 v37, 4, v111\n\t"
        "Lw_%=:\n\t"
        "ds_read_b32 v34, %[fb] offset:%[ogo]\n\t"
        "ds_read_b128 v[52:55], v36 offset:0\n\t"
        "ds_read_b128 v[56:59], v36 offset:1024\n\t"
        "ds_read_b128 v[60:63], v36 offset:2048\n\t"
        "ds_read_b128 v[64:67], v36 offset:3072\n\t"
        "s_sub_u32 %[scn], %[scn], 1\n\t"
        "s_waitcnt lgkmcnt(4)\n\t"
        "v_readfirstlane_b32 %[sf], v34\n\t"
        "s_cmp_lt_i32 %[scn], 1\n\t"
        "s_cbranch_scc1 Lg_%=\n\t"
        "s_cmp_lg_u32 %[sf], 0\n\t"
        "s_cbranch_scc1 Lg_%=\n\t"
        "s_sleep 1\n\t"
        "s_branch Lw_%=\n\t"
        "Lg_%=:\n\t"
        "ds_read_b128 v[72:75], v36 offset:4096\n\t"
        "ds_read_b128 v[76:79], v36 offset:5120\n\t"
        "ds_read_b128 v[80:83], v36 offset:6144\n\t"
        "ds_read_b128 v[84:87], v36 offset:7168\n\t"
        "s_lshr_b32 %[sf], %[sf], 8\n\t"
        "s_and_b32 %[st], %[sf], 15\n\t"
        "s_lshl_b64 %[smx], %[sm16], %[st]\n\t"
        "s_cmp_ge_u32 %[sf], 16\n\t"
        "s_cselect_b64 %[smw], %[sva], %[sv0]\n\t"
        "s_and_b64 %[smw], %[smw], %[sm16]\n\t"
        "s_lshr_b32 %[st], %[sf], 4\n\t"
        "s_cmp_eq_u32 %[st], %[sw1]\n\t"
        "s_cselect_b64 %[smx], %[smx], 0\n\t"
        "s_cmp_eq_u64 %[smw], 0\n\t"
        "s_cbranch_scc1 Le_%=\n\t"
        "s_waitcnt lgkmcnt(7)\n\t"
        "v_mfma_f32_16x16x32_bf16 v[24:27], v[112:115], v[52:55], 0\n\t"
        "v_mfma_f32_16x16x32_bf16 v[28:31], v[116:119], v[52:55], 0\n\t"
        "ds_read_b128 v[52:55], v36 offset:8192\n\t"
        "s_waitcnt lgkmcnt(7)\n\t"
        "v_mfma_f32_16x16x32_bf16 v[24:27], v[120:123], v[56:59], v[24:27]\n\t"
        "v_mfma_f32_16x16x32_bf16 v[28:31], v[124:127], v[56:59], v[28:31]\n\t"
        "ds_read_b128 v[56:59], v36 offset:9216\n\t"
        "v_mad_u32_u24 v37, v37, %[sf], v110\n\t"
        "s_waitcnt lgkmcnt(7)\n\t"
        "v_mfma_f32_16x16x32_bf16 v[24:27], v[128:131], v[60:63], v[24:27]\n\t"
        "v_mfma_f32_16x16x32_bf16 v[28:31], v[132:135], v[60:63], v[28:31]\n\t"
        "ds_read_b128 v[60:63], v36 offset:10240\n\t"
        "s_waitcnt lgkmcnt(7)\n\t"
        "v_mfma_f32_16x16x32_bf16 v[24:27], v[136:139], v[64:67], v[24:27]\n\t"
        "v_mfma_f32_16x16x32_bf16 v[28:31], v[140:143], v[64:67], v[28:31]\n\t"
        "ds_read_b128 v[64:67], v36 offset:11264\n\t"
        "s_waitcnt lgkmcnt(7)\n\t"
        "v_mfma_f32_16x16x32_bf16 v[24:27], v[144:147], v[72:75], v[24:27]\n\t"
        "v_mfma_f32_16x16x32_bf16 v[28:31], v[148:151], v[72:75], v[28:31]\n\t"
        "ds_read_b128 v[72:75], v36 offset:12288\n\t"
        "s_waitcnt lgkmcnt(7)\n\t"
        "v_mfma_f32_16x16x32_bf16 v[24:27], v[152:155], v[76:79], v[24:27]\n\t"
        "v_mfma_f32_16x16x32_bf16 v[28:31], v[156:159], v[76:79], v[28:31]\n\t"
        "ds_read_b128 v[76:79], v36 offset:13312\n\t"
        "s_waitcnt lgkmcnt(7)\n\t"
        "v_mfma_f32_16x16x32_bf16 v[24:27], v[160:163], v[80:83], v[24:27]\n\t"
        "v_mfma_f32_16x16x32_bf16 v[28:31], v[164:167], v[80:83], v[28:31]\n\t"
        "ds_read_b128 v[80:83], v36 offset:14336\n\t"
        "s_waitcnt lgkmcnt(7)\n\t"
        "v_mfma_f32_16x16x32_bf16 v[24:27], v[168:171], v[84:87], v[24:27]\n\t"
        "v_mfma_f32_16x16x32_bf16 v[28:31], v[172:175], v[84:87], v[28:31]\n\t"
        "ds_read_b128 v[84:87], v36 offset:15360\n\t"
        "s_waitcnt lgkmcnt(7)\n\t"
        "v_mfma_f32_16x16x32_bf16 v[24:27], v[176:179], v[52:55], v[24:27]\n\t"
        "v_mfma_f32_16x16x32_bf16 v[28:31], v[180:183], v[52:55], v[28:31]\n\t"
        "ds_read_b128 v[52:55], v36 offset:16384\n\t"
        "s_waitcnt lgkmcnt(7)\n\t"
        "v_mfma_f32_16x16x32_bf16 v[24:27], v[184:187], v[56:59], v[24:27]\n\t"
        "v_mfma_f32_16x16x32_bf16 v[28:31], v[188:191], v[56:59], v[28:31]\n\t"
        "ds_read_b128 v[56:59], v36 offset:17408\n\t"
        "s_waitcnt lgkmcnt(7)\n\t"
        "v_mfma_f32_16x16x32_bf16 v[24:27], v[192:195], v[60:63], v[24:27]\n\t"
        "v_mfma_f32_16x16x32_bf16 v[28:31], v[196:199], v[60:63], v[28:31]\n\t"
        "s_waitcnt lgkmcnt(6)\n\t"
        "v_mfma_f32_16x16x32_bf16 v[24:27], v[200:203], v[64:67], v[24:27]\n\t"
        "v_mfma_f32_16x16x32_bf16 v[28:31], v[204:207], v[64:67], v[28:31]\n\t"
        "s_waitcnt lgkmcnt(5)\n\t"
        "v_mfma_f32_16x16x32_bf16 v[24:27], v[208:211], v[72:75], v[24:27]\n\t"
        "v_mfma_f32_16x16x32_bf16 v[28:31], v[212:215], v[72:75], v[28:31]\n\t"
        "ds_read_b32 v34, %[fb] offset:%[ops]\n\t"
        "ds_read_b64 v[32:33], v35\n\t"
        "s_waitcnt lgkmcnt(6)\n\t"
        "v_mfma_f32_16x16x32_bf16 v[24:27], v[216:219], v[76:79], v[24:27]\n\t"
        "v_mfma_f32_16x16x32_bf16 v[28:31], v[220:223], v[76:79], v[28:31]\n\t"
        "s_waitcnt lgkmcnt(5)\n\t"
        "v_mfma_f32_16x16x32_bf16 v[24:27], v[224:227], v[80:83], v[24:27]\n\t"
        "v_mfma_f32_16x16x32_bf16 v[28:31], v[228:231], v[80:83], v[28:31]\n\t"
        "s_waitcnt lgkmcnt(4)\n\t"
        "v_mfma_f32_16x16x32_bf16 v[24:27], v[232:235], v[84:87], v[24:27]\n\t"
        "v_mfma_f32_16x16x32_bf16 v[28:31], v[236:239], v[84:87], v[28:31]\n\t"
        "s_waitcnt lgkmcnt(3)\n\t"
        "v_mfma_f32_16x16x32_bf16 v[24:27], v[240:243], v[52:55], v[24:27]\n\t"
        "v_mfma_f32_16x16x32_bf16 v[28:31], v[244:247], v[52:55], v[28:31]\n\t"
        "s_waitcnt lgkmcnt(2)\n\t"
        "v_mfma_f32_16x16x32_bf16 v[24:27], v[248:251], v[56:59], v[24:27]\n\t"
        "v_mfma_f32_16x16x32_bf16 v[28:31], v[252:255], v[56:59], v[28:31]\n\t"
        "Lm_%=:\n\t"
        "s_waitcnt lgkmcnt(0)\n\t"
        "v_readfirstlane_b32 %[st], v34\n\t"
        "s_cmp_ge_u32 %[st], %[sf]\n\t"
        "s_cbranch_scc1 Lo_%=\n\t"
        "s_cmp_lt_i32 %[scn], 1\n\t"
        "s_cbranch_scc1 Lo_%=\n\t"
        "s_sub_u32 %[scn], %[scn], 1\n\t"
        "ds_read_b32 v34, %[fb] offset:%[ops]\n\t"
        "ds_read_b64 v[32:33], v35\n\t"
        "s_branch Lm_%=\n\t"
        "Lo_%=:\n\t"
        "s_mov_b64 exec, %[smx]\n\t"
        "ds_read_b128 v[88:91], %[xr]\n\t"
        "ds_read_b128 v[92:95], %[xr] offset:64\n\t"
        "ds_read_b64 v[96:97], %[xq]\n\t"
        "s_mov_b64 exec, -1\n\t"
        "v_fma_f32 v24, -v32, v48, v24\n\t"
        "v_fma_f32 v25, -v32, v49, v25\n\t"
        "v_fma_f32 v26, -v32, v50, v26\n\t"
        "v_fma_f32 v27, -v32, v51, v27\n\t"
        "v_fma_f32 v28, -v32, v68, v28\n\t"
        "v_fma_f32 v29, -v32, v69, v29\n\t"
        "v_fma_f32 v30, -v32, v70, v30\n\t"
        "v_fma_f32 v31, -v32, v71, v31\n\t"
        "v_mul_f32 v24, v33, v24\n\t"
        "v_mul_f32 v25, v33, v25\n\t"
        "v_mul_f32 v26, v33, v26\n\t"
        "v_mul_f32 v27, v33, v27\n\t"
        "v_mul_f32 v28, v33, v28\n\t"
        "v_mul_f32 v29, v33, v29\n\t"
        "v_mul_f32 v30, v33, v30\n\t"
        "v_mul_f32 v31, v33, v31\n\t"
        "v_cvt_pk_bf16_f32 v24, v24, v28\n\t"
        "v_cvt_pk_bf16_f32 v25, v25, v29\n\t"
        "v_cvt_pk_bf16_f32 v26, v26, v30\n\t"
        "v_cvt_pk_bf16_f32 v27, v27, v31\n\t"
        "s_mov_b64 exec, %[smw]\n\t"
        "ds_write_b128 v37, v[24:27]\n\t"
        "s_mov_b64 exec, -1\n\t"
        "Le_%=:\n\t"
        "s_waitcnt lgkmcnt(0)\n\t"
        : [sf] "=&s"(sf), [st] "=&s"(st), [smw] "=&s"(mw), [smx] "=&s"(mx), [scn] "+s"(cn)
        : [fb] "v"(fb), [xr] "v"(xr), [xq] "v"(xq), [sw1] "s"(w1), [scs] "s"(cs), [sva] "s"(va), [sv0] "s"(v0), [sm16] "s"(0x0001000100010001ull), [ogo] "n"(BC_GO), [ops] "n"(BC_PST)
        : "memory", "scc", "v24", "v25", "v26", "v27", "v28", "v29", "v30", "v31", "v32", "v33", "v34", "v35", "v36", "v37");
}

DEV void att(const MidS& a) {
    u32 tm;
    asm volatile(
        "s_mov_b32 m0, %[sldx]\n\t"
        "s_nop 3\n\t"
        "s_waitcnt lgkmcnt(0)\n\t"
        "s_barrier\n\t"
        "s_setprio 0\n\t"
        "ds_read_b64 v[36:37], v102 offset:0\n\t"
        "ds_read_b32 v38, v103 offset:0\n\t"
        "ds_read_b64 v[20:21], v100 offset:0\n\t"
        "ds_read_b32 v22, v101 offset:0\n\t"
        "ds_read_b64 v[24:25], v100 offset:4864\n\t"
        "ds_read_b32 v26, v101 offset:4864\n\t"
        "ds_read_b64 v[40:41], v102 offset:40\n\t"
        "ds_read_b32 v42, v103 offset:4\n\t"
        "ds_read_b64 v[28:29], v100 offset:40\n\t"
        "ds_read_b32 v30, v101 offset:4\n\t"
        "ds_read_b64 v[32:33], v100 offset:4904\n\t"
        "ds_read_b32 v34, v101 offset:4868\n\t"
        "ds_read_b64_tr_b16 v[52:53], v105\n\t"
        "ds_read_b64_tr_b16 v[54:55], v105 offset:4864\n\t"
        "s_bitcmp1_b32 %[sskip], 0\n\t"
        "s_cbranch_scc1 Le0_%=\n\t"
        "buffer_load_dwordx4 v107, %[ssrd], %[swx] offen sc1 lds\n\t"
        "buffer_load_dwordx4 v107, %[ssrd], %[swx] offen offset:1024 sc1 lds\n\t"
        "Le0_%=:\n\t"
        "v_mov_b32 v23, 0\n\t"
        "v_mov_b32 v27, 0\n\t"
        "v_mov_b32 v31, 0\n\t"
        "v_mov_b32 v35, 0\n\t"
        "v_mov_b32 v39, 0\n\t"
        "v_mov_b32 v43, 0\n\t"
        "v_mov_b32 v60, %[sc0]\n\t"
        "v_mov_b32 v61, %[sc0]\n\t"
        "v_mov_b32 v62, %[sc0]\n\t"
        "v_mov_b32 v63, %[sc0]\n\t"
        "v_mov_b32 v68, %[sc0]\n\t"
        "v_mov_b32 v69, %[sc0]\n\t"
        "v_mov_b32 v70, %[sc0]\n\t"
        "v_mov_b32 v71, %[sc0]\n\t"
        "s_waitcnt lgkmcnt(10)\n\t"
        "v_mfma_f32_16x16x32_bf16 v[60:63], v[20:23], v[36:39], v[60:63]\n\t"
        "ds_read_b64_tr_b16 v[44:45], v104\n\t"
        "ds_read_b64_tr_b16 v[46:47], v104 offset:4864\n\t"
        "s_waitcnt lgkmcnt(10)\n\t"
        "v_mfma_f32_16x16x32_bf16 v[64:67], v[24:27], v[36:39], 0\n\t"
        "ds_read_b64_tr_b16 v[48:49], v104 offset:40\n\t"
        "ds_read_b64_tr_b16 v[50:51], v104 offset:4904\n\t"
        "s_waitcnt lgkmcnt(8)\n\t"
        "v_mfma_f32_16x16x32_bf16 v[68:71], v[28:31], v[40:43], v[68:71]\n\t"
        "s_waitcnt lgkmcnt(6)\n\t"
        "v_mfma_f32_16x16x32_bf16 v[72:75], v[32:35], v[40:43], 0\n\t"
        "s_cbranch_scc1 Le1_%=\n\t"
        "buffer_load_dwordx4 v107, %[ssrd], %[swx] offen offset:2048 sc1 lds\n\t"
        "buffer_load_dwordx4 v107, %[ssrd], %[swx] offen offset:3072 sc1 lds\n\t"
        "Le1_%=:\n\t"
        "v_mov_b32 v56, 0x0c0c0100\n\t"
        "v_mov_b32 v57, 0x01000c0c\n\t"
        "v_mov_b32 v58, 0x0c0c0c0c\n\t"
        "v_cndmask_b32 v56, v56, v57, %[spar]\n\t"
        "v_add_u32 v57, 0x0202, v56\n\t"
        "v_add_u32 v59, 0x02020000, v56\n\t"
        "v_cndmask_b32 v57, v57, v59, %[spar]\n\t"
        "v_cndmask_b32 v76, v56, v58, %[ssa]\n\t"
        "v_cndmask_b32 v78, v58, v56, %[ssa]\n\t"
        "v_cndmask_b32 v77, v57, v58, %[ssb]\n\t"
        "v_cndmask_b32 v79, v58, v57, %[ssb]\n\t"
        "v_mov_b32 v17, 0x3f803f80\n\t"
        "v_cndmask_b32 v28, v60, v61, %[spar]\n\t"
        "v_cndmask_b32 v36, v68, v69, %[spar]\n\t"
        "v_cndmask_b32 v29, v62, v63, %[spar]\n\t"
        "v_cndmask_b32 v37, v70, v71, %[spar]\n\t"
        "v_cndmask_b32 v30, v64, v65, %[spar]\n\t"
        "v_cndmask_b32 v38, v72, v73, %[spar]\n\t"
        "v_cndmask_b32 v31, v66, v67, %[spar]\n\t"
        "v_cndmask_b32 v39, v74, v75, %[spar]\n\t"
        "v_cndmask_b32 v28, v28, v30, %[ssa]\n\t"
        "v_cndmask_b32 v36, v36, v38, %[ssa]\n\t"
        "v_cndmask_b32 v29, v29, v31, %[ssb]\n\t"
        "v_cndmask_b32 v37, v37, v39, %[ssb]\n\t"
        "v_max_f32 v30, v28, v29\n\t"
        "v_max_f32 v38, v36, v37\n\t"
        "v_max_f32 v31, v28, v29\n\t"
        "v_max_f32 v39, v36, v37\n\t"
        "s_waitcnt lgkmcnt(4)\n\t"
        "v_permlane16_swap_b32 v30, v31\n\t"
        "v_permlane16_swap_b32 v38, v39\n\t"
        "v_max_f32 v32, v30, v31\n\t"
        "v_max_f32 v40, v38, v39\n\t"
        "v_max_f32 v33, v30, v31\n\t"
        "v_max_f32 v41, v38, v39\n\t"
        "v_cndmask_b32 v56, v17, v52, %[sl2]\n\t"
        "v_permlane32_swap_b32 v32, v33\n\t"
        "v_permlane32_swap_b32 v40, v41\n\t"
        "v_max_f32 v30, v32, v33\n\t"
        "v_max_f32 v38, v40, v41\n\t"
        "v_sub_f32 v28, v28, v30\n\t"
        "v_sub_f32 v36, v36, v38\n\t"
        "v_sub_f32 v29, v29, v30\n\t"
        "v_sub_f32 v37, v37, v38\n\t"
        "v_mul_f32 v28, 0x3eae1a93, v28\n\t"
        "v_mul_f32 v36, 0x3eae1a93, v36\n\t"
        "v_mul_f32 v29, 0x3eae1a93, v29\n\t"
        "v_mul_f32 v37, 0x3eae1a93, v37\n\t"
        "v_exp_f32 v28, v28\n\t"
        "v_exp_f32 v36, v36\n\t"
        "v_exp_f32 v29, v29\n\t"
        "v_exp_f32 v37, v37\n\t"
        "v_cndmask_b32 v57, v17, v53, %[sl2]\n\t"
        "v_cndmask_b32 v58, v17, v54, %[sl2]\n\t"
        "v_cndmask_b32 v59, v17, v55, %[sl2]\n\t"
        "v_cndmask_b32 v52, v52, v17, %[sl2]\n\t"
        "v_cndmask_b32 v53, v53, v17, %[sl2]\n\t"
        "v_cndmask_b32 v54, v54, v17, %[sl2]\n\t"
        "v_cndmask_b32 v55, v55, v17, %[sl2]\n\t"
        "v_cvt_pk_bf16_f32 v28, v28, v29\n\t"
        "v_cvt_pk_bf16_f32 v36, v36, v37\n\t"
        "v_perm_b32 v20, v28, v28, v76\n\t"
        "v_perm_b32 v21, v28, v28, v77\n\t"
        "v_perm_b32 v22, v28, v28, v78\n\t"
        "v_perm_b32 v23, v28, v28, v79\n\t"
        "v_perm_b32 v24, v36, v36, v76\n\t"
        "v_perm_b32 v25, v36, v36, v77\n\t"
        "v_perm_b32 v26, v36, v36, v78\n\t"
        "v_perm_b32 v27, v36, v36, v79\n\t"
        "s_waitcnt lgkmcnt(0)\n\t"
        "v_mfma_f32_16x16x32_bf16 v[64:67], v[52:55], v[20:23], 0\n\t"
        "v_mfma_f32_16x16x32_bf16 v[60:63], v[44:47], v[20:23], 0\n\t"
        "v_mfma_f32_16x16x32_bf16 v[72:75], v[56:59], v[24:27], 0\n\t"
        "v_mfma_f32_16x16x32_bf16 v[68:71], v[48:51], v[24:27], 0\n\t"
        "v_lshrrev_b32 v17, 1, v107\n\t"
        "s_nop 3\n\t"
        "v_rcp_f32 v36, v66\n\t"
        "s_nop 0\n\t"
        "v_mul_f32 v60, v60, v36\n\t"
        "v_mul_f32 v61, v61, v36\n\t"
        "v_mul_f32 v62, v62, v36\n\t"
        "v_mul_f32 v63, v63, v36\n\t"
        "v_rcp_f32 v37, v72\n\t"
        "v_cvt_pk_bf16_f32 v80, v60, v61\n\t"
        "v_cvt_pk_bf16_f32 v81, v62, v63\n\t"
        "v_mul_f32 v68, v68, v37\n\t"
        "v_mul_f32 v69, v69, v37\n\t"
        "v_mul_f32 v70, v70, v37\n\t"
        "v_mul_f32 v71, v71, v37\n\t"
        "v_bfi_b32 v80, 1, %[slay], v80\n\t"
        "v_cvt_pk_bf16_f32 v82, v68, v69\n\t"
        "v_cvt_pk_bf16_f32 v83, v70, v71\n\t"
        "v_mul_f32 v64, v64, v36\n\t"
        "v_mul_f32 v65, v65, v36\n\t"
        "v_bfi_b32 v82, 1, %[slay], v82\n\t"
        "buffer_store_dwordx4 v[80:83], v107, %[ssrd], %[sst] offen sc1\n\t"
        "v_mul_f32 v74, v74, v37\n\t"
        "v_mul_f32 v75, v75, v37\n\t"
        "v_cvt_pk_bf16_f32 v84, v64, v65\n\t"
        "v_cvt_pk_bf16_f32 v85, v74, v75\n\t"
        "v_bfi_b32 v84, 1, %[slay], v84\n\t"
        "s_mov_b64 exec, 0xffff\n\t"
        "buffer_store_dwordx2 v[84:85], v17, %[ssrd], %[stt] offen sc1\n\t"
        "s_mov_b64 exec, -1\n\t"
        "s_waitcnt vmcnt(2)\n\t"
        "s_barrier\n\t"
        "s_cmp_eq_u32 %[smsk], 0\n\t"
        "s_cbranch_scc0 Lpr_%=\n\t"
        "s_setprio 1\n\t"
        "Lpr_%=:\n\t"
        : [stm] "=&s"(tm)
        : [ssrd] "s"(a.srd), [sst] "s"(a.st), [stt] "s"(a.tt), [sc0] "s"(a.c0), [swx] "s"(a.wx), [sldx] "s"(a.ldx), [sskip] "s"(a.skip), [smsk] "s"(a.msk), [swi] "s"(a.wi), [sld] "s"(a.ld), [sn7] "s"(a.n7), [slay] "s"(a.layer),
          [spar] "s"(0xAAAAAAAAAAAAAAAAull), [ssa] "s"(0xf000ff00fff0ffffull), [ssb] "s"(0xc000fc00ffc0fffcull), [sl2] "s"(0xCCCCCCCCCCCCCCCCull)
        : "memory", "scc", "vcc", "v14", "v15", "v16", "v17", "v18", "v19", "v20", "v21", "v22", "v23", "v24", "v25", "v26", "v27", "v28", "v29", "v30", "v31", "v32", "v33", "v34", "v35", "v36", "v37", "v38", "v39", "v40", "v41", "v42", "v43", "v44", "v45", "v46", "v47");
}
DEV void mid(const MidS& a, u32& fo, u32& cn) {
    u32 ob4, ob4x;
    asm volatile(
        "s_nop 4\n\t"
        "s_add_u32 %[sob4], %[sob], 4096\n\t"
        "v_add_u32 v19, %[stvd], v108\n\t"
        "s_bitcmp1_b32 %[sskip], 1\n\t"
        "s_cbranch_scc1 L6_%=\n\t"
        "s_mov_b32 m0, %[sld]\n\t"
        "s_add_u32 %[sob4x], %[swi], 4096\n\t"
        "buffer_load_dwordx4 v107, %[ssrd], %[swi] offen offset:0 sc1 lds\n\t"
        "buffer_load_dwordx4 v107, %[ssrd], %[swi] offen offset:1024 sc1 lds\n\t"
        "buffer_load_dwordx4 v107, %[ssrd], %[swi] offen offset:2048 sc1 lds\n\t"
        "buffer_load_dwordx4 v107, %[ssrd], %[swi] offen offset:3072 sc1 lds\n\t"
        "s_add_u32 m0, m0, 4096\n\t"
        "s_cmp_eq_u32 %[sn7], 0\n\t"
        "buffer_load_dwordx4 v107, %[ssrd], %[sob4x] offen offset:0 sc1 lds\n\t"
        "buffer_load_dwordx4 v107, %[ssrd], %[sob4x] offen offset:1024 sc1 lds\n\t"
        "s_cbranch_scc1 L6_%=\n\t"
        "buffer_load_dwordx4 v107, %[ssrd], %[sob4x] offen offset:2048 sc1 lds\n\t"
        "L6_%=:\n\t"
        "s_and_b32 %[sob4x], %[smsk], 0xffff0000\n\t"
        "buffer_load_dwordx4 v[40:43], v107, %[ssrd], %[sob] offen offset:0 sc1\n\t"
        "v_lshlrev_b32 v20, 16, v88\n\t"
        "v_and_b32 v24, %[sob4x], v88\n\t"
        "v_and_b32 v20, %[smsk], v20\n\t"
        "buffer_load_dwordx4 v[44:47], v107, %[ssrd], %[sob] offen offset:1024 sc1\n\t"
        "v_lshlrev_b32 v21, 16, v89\n\t"
        "v_and_b32 v25, %[sob4x], v89\n\t"
        "v_and_b32 v21, %[smsk], v21\n\t"
        "buffer_load_dwordx4 v[48:51], v107, %[ssrd], %[sob] offen offset:2048 sc1\n\t"
        "v_lshlrev_b32 v22, 16, v90\n\t"
        "v_and_b32 v26, %[sob4x], v90\n\t"
        "v_and_b32 v22, %[smsk], v22\n\t"
        "v_lshlrev_b32 v23, 16, v91\n\t"
        "v_and_b32 v27, %[sob4x], v91\n\t"
        "v_and_b32 v23, %[smsk], v23\n\t"
        "v_lshlrev_b32 v28, 16, v92\n\t"
        "v_and_b32 v32, %[sob4x], v92\n\t"
        "v_and_b32 v28, %[smsk], v28\n\t"
        "v_lshlrev_b32 v29, 16, v93\n\t"
        "v_and_b32 v33, %[sob4x], v93\n\t"
        "v_and_b32 v29, %[smsk], v29\n\t"
        "v_lshlrev_b32 v30, 16, v94\n\t"
        "v_and_b32 v34, %[sob4x], v94\n\t"
        "v_and_b32 v30, %[smsk], v30\n\t"
        "v_lshlrev_b32 v31, 16, v95\n\t"
        "v_and_b32 v35, %[sob4x], v95\n\t"
        "v_and_b32 v31, %[smsk], v31\n\t"
        "v_lshlrev_b32 v36, 16, v96\n\t"
        "v_and_b32 v37, %[sob4x], v96\n\t"
        "v_and_b32 v36, %[smsk], v36\n\t"
        "v_lshlrev_b32 v38, 16, v97\n\t"
        "v_and_b32 v39, %[sob4x], v97\n\t"
        "v_and_b32 v38, %[smsk], v38\n\t"
        "s_and_b32 %[sob4x], %[smsk], %[slay]\n\t"
        "s_cbranch_scc0 Lu_%=\n\t"
        "v_sub_f32 v88, v20, v98\n\t"
        "v_sub_f32 v86, v24, v98\n\t"
        "v_sub_f32 v89, v21, v98\n\t"
        "v_sub_f32 v87, v25, v98\n\t"
        "v_mul_f32 v88, v99, v88\n\t"
        "v_mul_f32 v86, v99, v86\n\t"
        "v_mul_f32 v89, v99, v89\n\t"
        "v_mul_f32 v87, v99, v87\n\t"
        "v_cvt_pk_bf16_f32 v86, v88, v86\n\t"
        "v_cvt_pk_bf16_f32 v87, v89, v87\n\t"
        "v_lshlrev_b32 v88, 16, v86\n\t"
        "v_and_b32 v86, 0xffff0000, v86\n\t"
        "v_lshlrev_b32 v89, 16, v87\n\t"
        "v_and_b32 v87, 0xffff0000, v87\n\t"
        "v_add_f32 v20, v20, v88\n\t"
        "v_add_f32 v24, v24, v86\n\t"
        "v_add_f32 v21, v21, v89\n\t"
        "v_add_f32 v25, v25, v87\n\t"
        "v_cvt_pk_bf16_f32 v88, v20, v24\n\t"
        "v_cvt_pk_bf16_f32 v89, v21, v25\n\t"
        "v_lshlrev_b32 v20, 16, v88\n\t"
        "v_and_b32 v24, 0xffff0000, v88\n\t"
        "v_lshlrev_b32 v21, 16, v89\n\t"
        "v_and_b32 v25, 0xffff0000, v89\n\t"
        "v_sub_f32 v90, v22, v98\n\t"
        "v_sub_f32 v86, v26, v98\n\t"
        "v_sub_f32 v91, v23, v98\n\t"
        "v_sub_f32 v87, v27, v98\n\t"
        "v_mul_f32 v90, v99, v90\n\t"
        "v_mul_f32 v86, v99, v86\n\t"
        "v_mul_f32 v91, v99, v91\n\t"
        "v_mul_f32 v87, v99, v87\n\t"
        "v_cvt_pk_bf16_f32 v86, v90, v86\n\t"
        "v_cvt_pk_bf16_f32 v87, v91, v87\n\t"
        "v_lshlrev_b32 v90, 16, v86\n\t"
        "v_and_b32 v86, 0xffff0000, v86\n\t"
        "v_lshlrev_b32 v91, 16, v87\n\t"
        "v_and_b32 v87, 0xffff0000, v87\n\t"
        "v_add_f32 v22, v22, v90\n\t"
        "v_add_f32 v26, v26, v86\n\t"
        "v_add_f32 v23, v23, v91\n\t"
        "v_add_f32 v27, v27, v87\n\t"
        "v_cvt_pk_bf16_f32 v90, v22, v26\n\t"
        "v_cvt_pk_bf16_f32 v91, v23, v27\n\t"
        "v_lshlrev_b32 v22, 16, v90\n\t"
        "v_and_b32 v26, 0xffff0000, v90\n\t"
        "v_lshlrev_b32 v23, 16, v91\n\t"
        "v_and_b32 v27, 0xffff0000, v91\n\t"
        "v_sub_f32 v92, v28, v98\n\t"
        "v_sub_f32 v86, v32, v98\n\t"
        "v_sub_f32 v93, v29, v98\n\t"
        "v_sub_f32 v87, v33, v98\n\t"
        "v_mul_f32 v92, v99, v92\n\t"
        "v_mul_f32 v86, v99, v86\n\t"
        "v_mul_f32 v93, v99, v93\n\t"
        "v_mul_f32 v87, v99, v87\n\t"
        "v_cvt_pk_bf16_f32 v86, v92, v86\n\t"
        "v_cvt_pk_bf16_f32 v87, v93, v87\n\t"
        "v_lshlrev_b32 v92, 16, v86\n\t"
        "v_and_b32 v86, 0xffff0000, v86\n\t"
        "v_lshlrev_b32 v93, 16, v87\n\t"
        "v_and_b32 v87, 0xffff0000, v87\n\t"
        "v_add_f32 v28, v28, v92\n\t"
        "v_add_f32 v32, v32, v86\n\t"
        "v_add_f32 v29, v29, v93\n\t"
        "v_add_f32 v33, v33, v87\n\t"
        "v_cvt_pk_bf16_f32 v92, v28, v32\n\t"
        "v_cvt_pk_bf16_f32 v93, v29, v33\n\t"
        "v_lshlrev_b32 v28, 16, v92\n\t"
        "v_and_b32 v32, 0xffff0000, v92\n\t"
        "v_lshlrev_b32 v29, 16, v93\n\t"
        "v_and_b32 v33, 0xffff0000, v93\n\t"
        "v_sub_f32 v94, v30, v98\n\t"
        "v_sub_f32 v86, v34, v98\n\t"
        "v_sub_f32 v95, v31, v98\n\t"
        "v_sub_f32 v87, v35, v98\n\t"
        "v_mul_f32 v94, v99, v94\n\t"
        "v_mul_f32 v86, v99, v86\n\t"
        "v_mul_f32 v95, v99, v95\n\t"
        "v_mul_f32 v87, v99, v87\n\t"
        "v_cvt_pk_bf16_f32 v86, v94, v86\n\t"
        "v_cvt_pk_bf16_f32 v87, v95, v87\n\t"
        "v_lshlrev_b32 v94, 16, v86\n\t"
        "v_and_b32 v86, 0xffff0000, v86\n\t"
        "v_lshlrev_b32 v95, 16, v87\n\t"
        "v_and_b32 v87, 0xffff0000, v87\n\t"
        "v_add_f32 v30, v30, v94\n\t"
        "v_add_f32 v34, v34, v86\n\t"
        "v_add_f32 v31, v31, v95\n\t"
        "v_add_f32 v35, v35, v87\n\t"
        "v_cvt_pk_bf16_f32 v94, v30, v34\n\t"
        "v_cvt_pk_bf16_f32 v95, v31, v35\n\t"
        "v_lshlrev_b32 v30, 16, v94\n\t"
        "v_and_b32 v34, 0xffff0000, v94\n\t"
        "v_lshlrev_b32 v31, 16, v95\n\t"
        "v_and_b32 v35, 0xffff0000, v95\n\t"
        "v_sub_f32 v96, v36, v98\n\t"
        "v_sub_f32 v86, v37, v98\n\t"
        "v_sub_f32 v97, v38, v98\n\t"
        "v_sub_f32 v87, v39, v98\n\t"
        "v_mul_f32 v96, v99, v96\n\t"
        "v_mul_f32 v86, v99, v86\n\t"
        "v_mul_f32 v97, v99, v97\n\t"
        "v_mul_f32 v87, v99, v87\n\t"
        "v_cvt_pk_bf16_f32 v86, v96, v86\n\t"
        "v_cvt_pk_bf16_f32 v87, v97, v87\n\t"
        "v_lshlrev_b32 v96, 16, v86\n\t"
        "v_and_b32 v86, 0xffff0000, v86\n\t"
        "v_lshlrev_b32 v97, 16, v87\n\t"
        "v_and_b32 v87, 0xffff0000, v87\n\t"
        "v_add_f32 v36, v36, v96\n\t"
        "v_add_f32 v37, v37, v86\n\t"
        "v_add_f32 v38, v38, v97\n\t"
        "v_add_f32 v39, v39, v87\n\t"
        "v_cvt_pk_bf16_f32 v96, v36, v37\n\t"
        "v_cvt_pk_bf16_f32 v97, v38, v39\n\t"
        "v_lshlrev_b32 v36, 16, v96\n\t"
        "v_and_b32 v37, 0xffff0000, v96\n\t"
        "v_lshlrev_b32 v38, 16, v97\n\t"
        "v_and_b32 v39, 0xffff0000, v97\n\t"
        "Lu_%=:\n\t"
        "s_mov_b32 exec_lo, 0\n\t"
        "v_mov_b32 v36, 0\n\t"
        "v_mov_b32 v37, 0\n\t"
        "v_mov_b32 v38, 0\n\t"
        "v_mov_b32 v39, 0\n\t"
        "s_mov_b32 exec_lo, -1\n\t"
        "v_bfe_i32 v13, v107, 7, 1\n\t"
        "v_and_b32 v13, 0xaaaa, v13\n\t"
        "v_or_b32 v13, 0x4444, v13\n\t"
        "v_mov_b32 v15, %[swc]\n\t"
        "v_mov_b32 v16, 1\n\t"
        "v_add_u32 v18, %[slb], v107\n\t"
        "v_add_u32 v14, %[slb], v106\n\t"
        "ds_read_b128 v[76:79], v18 offset:0\n\t"
        "ds_read_b128 v[80:83], v18 offset:1024\n\t"
        "ds_read_b128 v[84:87], v18 offset:2048\n\t"
        "ds_read_b128 v[88:91], v18 offset:3072\n\t"
        "ds_read_b128 v[92:95], v18 offset:4608\n\t"
        "ds_read_b128 v[96:99], v18 offset:5632\n\t"
        "s_waitcnt vmcnt(3)\n\t"
        "s_mov_b64 exec, 1\n\t"
        "ds_add_u32 v15, v16\n\t"
        "s_mov_b64 exec, -1\n\t"
        "Lr0_%=:\n\t"
        "s_waitcnt vmcnt(2)\n\t"
        "v_bitop3_b32 v17, v40, v42, %[slay] bitop3:0x7e\n\t"
        "v_and_b32 v17, 1, v17\n\t"
        "v_cmp_eq_u32 vcc, 1, v17\n\t"
        "s_cbranch_vccnz Lx0_%=\n\t"
        "Ld0_%=:\n\t"
        "s_waitcnt lgkmcnt(6)\n\t"
        "v_mfma_f32_16x16x32_bf16 v[20:23], v[76:79], v[40:43], v[20:23]\n\t"
        "buffer_load_dwordx4 v[52:55], v107, %[ssrd], %[sob] offen offset:3072 sc1\n\t"
        "ds_read_b128 v[76:79], v18 offset:6656\n\t"
        "s_waitcnt lgkmcnt(6)\n\t"
        "v_mfma_f32_16x16x32_bf16 v[24:27], v[80:83], v[40:43], v[24:27]\n\t"
        "s_waitcnt vmcnt(2)\n\t"
        "v_bitop3_b32 v17, v44, v46, %[slay] bitop3:0x7e\n\t"
        "v_and_b32 v17, 1, v17\n\t"
        "v_cmp_eq_u32 vcc, 1, v17\n\t"
        "ds_read_b128 v[80:83], v18 offset:7680\n\t"
        "s_waitcnt lgkmcnt(6)\n\t"
        "v_mfma_f32_16x16x32_bf16 v[28:31], v[84:87], v[40:43], v[28:31]\n\t"
        "ds_read_b128 v[84:87], v14 offset:0\n\t"
        "s_waitcnt lgkmcnt(6)\n\t"
        "v_mfma_f32_16x16x32_bf16 v[32:35], v[88:91], v[40:43], v[32:35]\n\t"
        "s_cbranch_vccnz Lx1_%=\n\t"
        "Ld1_%=:\n\t"
        "ds_read_b32 v16, v15\n\t"
        "ds_read_b128 v[88:91], v18 offset:9216\n\t"
        "s_waitcnt lgkmcnt(7)\n\t"
        "v_mfma_f32_16x16x32_bf16 v[20:23], v[92:95], v[44:47], v[20:23]\n\t"
        "buffer_load_dwordx4 v[56:59], v107, %[ssrd], %[sob4] offen offset:0 sc1\n\t"
        "ds_read_b128 v[92:95], v18 offset:10240\n\t"
        "s_waitcnt lgkmcnt(7)\n\t"
        "v_mfma_f32_16x16x32_bf16 v[24:27], v[96:99], v[44:47], v[24:27]\n\t"
        "ds_read_b128 v[96:99], v18 offset:11264\n\t"
        "s_waitcnt lgkmcnt(6)\n\t"
        "v_mfma_f32_16x16x32_bf16 v[28:31], v[76:79], v[44:47], v[28:31]\n\t"
        "s_waitcnt vmcnt(2)\n\t"
        "v_bitop3_b32 v17, v48, v50, %[slay] bitop3:0x7e\n\t"
        "v_and_b32 v17, 1, v17\n\t"
        "v_cmp_eq_u32 vcc, 1, v17\n\t"
        "ds_read_b128 v[76:79], v18 offset:12288\n\t"
        "s_waitcnt lgkmcnt(6)\n\t"
        "v_mfma_f32_16x16x32_bf16 v[32:35], v[80:83], v[44:47], v[32:35]\n\t"
        "s_waitcnt lgkmcnt(4)\n\t"
        "Lwc_%=:\n\t"
        "v_readfirstlane_b32 %[sob4x], v16\n\t"
        "s_cmp_lt_u32 %[sob4x], %[swt]\n\t"
        "s_cbranch_scc1 Lws_%=\n\t"
        "Lwg_%=:\n\t"
        "ds_read_b128 v[80:83], v18 offset:13824\n\t"
        "v_smfmac_f32_16x16x64_bf16 v[36:39], v[84:87], v[40:47], v13\n\t"
        "s_cbranch_vccnz Lx2_%=\n\t"
        "Ld2_%=:\n\t"
        "ds_read_b128 v[84:87], v18 offset:14848\n\t"
        "ds_read_b128 v[40:43], v18 offset:15872\n\t"
        "s_waitcnt lgkmcnt(6)\n\t"
        "v_mfma_f32_16x16x32_bf16 v[20:23], v[88:91], v[48:51], v[20:23]\n\t"
        "buffer_load_dwordx4 v[60:63], v107, %[ssrd], %[sob4] offen offset:1024 sc1\n\t"
        "ds_read_b128 v[44:47], v18 offset:16896\n\t"
        "ds_read_b128 v[88:91], v14 offset:9216\n\t"
        "s_waitcnt lgkmcnt(7)\n\t"
        "v_mfma_f32_16x16x32_bf16 v[24:27], v[92:95], v[48:51], v[24:27]\n\t"
        "s_waitcnt vmcnt(2)\n\t"
        "v_bitop3_b32 v17, v52, v54, %[slay] bitop3:0x7e\n\t"
        "v_and_b32 v17, 1, v17\n\t"
        "v_cmp_eq_u32 vcc, 1, v17\n\t"
        "ds_read_b128 v[92:95], v18 offset:18432\n\t"
        "s_waitcnt lgkmcnt(7)\n\t"
        "v_mfma_f32_16x16x32_bf16 v[28:31], v[96:99], v[48:51], v[28:31]\n\t"
        "ds_read_b128 v[96:99], v18 offset:19456\n\t"
        "s_waitcnt lgkmcnt(7)\n\t"
        "v_mfma_f32_16x16x32_bf16 v[32:35], v[76:79], v[48:51], v[32:35]\n\t"
        "s_cbranch_vccnz Lx3_%=\n\t"
        "Ld3_%=:\n\t"
        "ds_read_b128 v[76:79], v18 offset:20480\n\t"
        "s_waitcnt lgkmcnt(7)\n\t"
        "v_mfma_f32_16x16x32_bf16 v[20:23], v[80:83], v[52:55], v[20:23]\n\t"
        "buffer_load_dwordx4 v[64:67], v107, %[ssrd], %[sob4] offen offset:2048 sc1\n\t"
        "ds_read_b128 v[80:83], v18 offset:21504\n\t"
        "s_waitcnt lgkmcnt(7)\n\t"
        "v_mfma_f32_16x16x32_bf16 v[24:27], v[84:87], v[52:55], v[24:27]\n\t"
        "v_mov_b32 v84, 0\n\t"
        "v_mov_b32 v85, 0\n\t"
        "v_mov_b32 v86, 0\n\t"
        "v_mov_b32 v87, 0\n\t"
        "s_mov_b32 exec_lo, 0\n\t"
        "ds_read_b128 v[84:87], v14 offset:13824\n\t"
        "s_mov_b32 exec_lo, -1\n\t"
        "s_waitcnt lgkmcnt(7)\n\t"
        "v_mfma_f32_16x16x32_bf16 v[28:31], v[40:43], v[52:55], v[28:31]\n\t"
        "s_waitcnt vmcnt(2)\n\t"
        "v_bitop3_b32 v17, v56, v58, %[slay] bitop3:0x7e\n\t"
        "v_and_b32 v17, 1, v17\n\t"
        "v_cmp_eq_u32 vcc, 1, v17\n\t"
        "v_add_u32 v18, %[sl1d], v18\n\t"
        "v_add_u32 v14, %[sl1d], v14\n\t"
        "ds_read_b128 v[40:43], v18 offset:0\n\t"
        "s_waitcnt lgkmcnt(7)\n\t"
        "v_mfma_f32_16x16x32_bf16 v[32:35], v[44:47], v[52:55], v[32:35]\n\t"
        "ds_read_b128 v[44:47], v18 offset:1024\n\t"
        "s_waitcnt lgkmcnt(7)\n\t"
        "v_smfmac_f32_16x16x64_bf16 v[36:39], v[88:91], v[48:55], v13\n\t"
        "s_cbranch_vccnz Lx4_%=\n\t"
        "Ld4_%=:\n\t"
        "ds_read_b128 v[88:91], v18 offset:2048\n\t"
        "ds_read_b128 v[48:51], v18 offset:3072\n\t"
        "s_waitcnt lgkmcnt(8)\n\t"
        "v_mfma_f32_16x16x32_bf16 v[20:23], v[92:95], v[56:59], v[20:23]\n\t"
        "buffer_load_dwordx2 v[68:69], v108, %[ssrd], %[sob] offen offset:0 sc1\n\t"
        "buffer_load_dwordx2 v[70:71], v19, %[ssrd], %[sob] offen offset:0 sc1\n\t"
        "ds_read_b128 v[92:95], v18 offset:4608\n\t"
        "s_waitcnt lgkmcnt(8)\n\t"
        "v_mfma_f32_16x16x32_bf16 v[24:27], v[96:99], v[56:59], v[24:27]\n\t"
        "ds_read_b128 v[96:99], v18 offset:5632\n\t"
        "s_waitcnt lgkmcnt(8)\n\t"
        "v_mfma_f32_16x16x32_bf16 v[28:31], v[76:79], v[56:59], v[28:31]\n\t"
        "s_waitcnt vmcnt(3)\n\t"
        "v_bitop3_b32 v17, v60, v62, %[slay] bitop3:0x7e\n\t"
        "v_and_b32 v17, 1, v17\n\t"
        "v_cmp_eq_u32 vcc, 1, v17\n\t"
        "ds_read_b128 v[76:79], v18 offset:6656\n\t"
        "s_waitcnt lgkmcnt(8)\n\t"
        "v_mfma_f32_16x16x32_bf16 v[32:35], v[80:83], v[56:59], v[32:35]\n\t"
        "ds_read_b128 v[80:83], v18 offset:7680\n\t"
        "s_waitcnt lgkmcnt(8)\n\t"
        "v_smfmac_f32_16x16x64_bf16 v[36:39], v[84:87], v[52:59], v13\n\t"
        "s_cbranch_vccnz Lx5_%=\n\t"
        "Ld5_%=:\n\t"
        "ds_read_b128 v[84:87], v14 offset:0\n\t"
        "s_waitcnt lgkmcnt(8)\n\t"
        "v_mfma_f32_16x16x32_bf16 v[20:23], v[40:43], v[60:63], v[20:23]\n\t"
        "buffer_load_dwordx2 v[72:73], v108, %[ssrd], %[sob] offen offset:1024 sc1\n\t"
        "buffer_load_dwordx2 v[74:75], v19, %[ssrd], %[sob] offen offset:1024 sc1\n\t"
        "ds_read_b128 v[52:55], v18 offset:9216\n\t"
        "s_waitcnt lgkmcnt(8)\n\t"
        "v_mfma_f32_16x16x32_bf16 v[24:27], v[44:47], v[60:63], v[24:27]\n\t"
        "s_waitcnt vmcnt(4)\n\t"
        "v_bitop3_b32 v17, v64, v66, %[slay] bitop3:0x7e\n\t"
        "v_and_b32 v17, 1, v17\n\t"
        "v_cmp_eq_u32 vcc, 1, v17\n\t"
        "ds_read_b128 v[56:59], v18 offset:10240\n\t"
        "s_waitcnt lgkmcnt(8)\n\t"
        "v_mfma_f32_16x16x32_bf16 v[28:31], v[88:91], v[60:63], v[28:31]\n\t"
        "ds_read_b128 v[40:43], v18 offset:11264\n\t"
        "s_waitcnt lgkmcnt(8)\n\t"
        "v_mfma_f32_16x16x32_bf16 v[32:35], v[48:51], v[60:63], v[32:35]\n\t"
        "s_cbranch_vccnz Lx6_%=\n\t"
        "Ld6_%=:\n\t"
        "ds_read_b128 v[44:47], v18 offset:12288\n\t"
        "s_waitcnt lgkmcnt(8)\n\t"
        "v_mfma_f32_16x16x32_bf16 v[20:23], v[92:95], v[64:67], v[20:23]\n\t"
        "ds_read_b128 v[88:91], v14 offset:9216\n\t"
        "s_waitcnt lgkmcnt(8)\n\t"
        "v_mfma_f32_16x16x32_bf16 v[24:27], v[96:99], v[64:67], v[24:27]\n\t"
        "ds_read_b128 v[48:51], v18 offset:13824\n\t"
        "s_waitcnt lgkmcnt(8)\n\t"
        "v_mfma_f32_16x16x32_bf16 v[28:31], v[76:79], v[64:67], v[28:31]\n\t"
        "s_waitcnt vmcnt(2)\n\t"
        "v_bitop3_b32 v17, v68, v70, %[slay] bitop3:0x7e\n\t"
        "v_and_b32 v17, 1, v17\n\t"
        "v_cmp_eq_u32 vcc, 1, v17\n\t"
        "ds_read_b128 v[92:95], v18 offset:14848\n\t"
        "s_waitcnt lgkmcnt(8)\n\t"
        "v_mfma_f32_16x16x32_bf16 v[32:35], v[80:83], v[64:67], v[32:35]\n\t"
        "ds_read_b128 v[96:99], v18 offset:15872\n\t"
        "s_waitcnt lgkmcnt(8)\n\t"
        "v_smfmac_f32_16x16x64_bf16 v[36:39], v[84:87], v[60:67], v13\n\t"
        "s_cbranch_vccnz Lx7_%=\n\t"
        "Ld7_%=:\n\t"
        "ds_read_b128 v[76:79], v18 offset:16896\n\t"
        "s_waitcnt lgkmcnt(8)\n\t"
        "v_mfma_f32_16x16x32_bf16 v[20:23], v[52:55], v[68:71], v[20:23]\n\t"
        "buffer_load_dword %[fo], %[fo], %[ssrd], 0 offen sc1\n\t"
        "s_waitcnt lgkmcnt(7)\n\t"
        "v_mfma_f32_16x16x32_bf16 v[24:27], v[56:59], v[68:71], v[24:27]\n\t"
        "s_waitcnt vmcnt(1)\n\t"
        "v_bitop3_b32 v17, v72, v74, %[slay] bitop3:0x7e\n\t"
        "v_and_b32 v17, 1, v17\n\t"
        "v_cmp_eq_u32 vcc, 1, v17\n\t"
        "s_waitcnt lgkmcnt(6)\n\t"
        "v_mfma_f32_16x16x32_bf16 v[28:31], v[40:43], v[68:71], v[28:31]\n\t"
        "s_waitcnt lgkmcnt(5)\n\t"
        "v_mfma_f32_16x16x32_bf16 v[32:35], v[44:47], v[68:71], v[32:35]\n\t"
        "s_cbranch_vccnz Lx8_%=\n\t"
        "Ld8_%=:\n\t"
        "s_waitcnt lgkmcnt(4)\n\t"
        "v_smfmac_f32_16x16x64_bf16 v[36:39], v[88:91], v[68:75], v13\n\t"
        "s_waitcnt lgkmcnt(3)\n\t"
        "v_mfma_f32_16x16x32_bf16 v[20:23], v[48:51], v[72:75], v[20:23]\n\t"
        "v_add_u32 v18, %[sscr], v107\n\t"
        "v_mov_b32 v15, 153984\n\t"
        "s_cmp_eq_u32 %[smsk], 0\n\t"
        "s_waitcnt lgkmcnt(2)\n\t"
        "v_mfma_f32_16x16x32_bf16 v[24:27], v[92:95], v[72:75], v[24:27]\n\t"
        "v_lshrrev_b32 v14, 1, v107\n\t"
        "v_add_u32 v14, %[sscr], v14\n\t"
        "s_waitcnt lgkmcnt(1)\n\t"
        "v_mfma_f32_16x16x32_bf16 v[28:31], v[96:99], v[72:75], v[28:31]\n\t"
        "v_permlane32_swap_b32 v36, v38\n\t"
        "v_permlane32_swap_b32 v37, v39\n\t"
        "s_waitcnt lgkmcnt(0)\n\t"
        "v_mfma_f32_16x16x32_bf16 v[32:35], v[76:79], v[72:75], v[32:35]\n\t"
        "v_add_f32 v36, v36, v38\n\t"
        "v_add_f32 v37, v37, v39\n\t"
        "s_cbranch_scc0 Lk0_%=\n\t"
        "v_mov_b32 v16, 1\n\t"
        "ds_write_b64 v14, v[36:37] offset:4096\n\t"
        "ds_write_b128 v18, v[20:23] offset:0\n\t"
        "ds_write_b128 v18, v[24:27] offset:1024\n\t"
        "ds_write_b128 v18, v[28:31] offset:2048\n\t"
        "ds_write_b128 v18, v[32:35] offset:3072\n\t"
        "s_mov_b64 exec, 1\n\t"
        "ds_add_u32 v15, v16 offset:%[opc]\n\t"
        "s_mov_b64 exec, -1\n\t"
        "s_waitcnt vmcnt(0)\n\t"
        "s_branch Lend_%=\n\t"
        "Lx0_%=:\n\t"
        "s_cmp_eq_u32 %[scn], 0\n\t"
        "s_cbranch_scc1 Ld0_%=\n\t"
        "s_sub_u32 %[scn], %[scn], 1\n\t"
        "buffer_load_dwordx4 v[40:43], v107, %[ssrd], %[sob] offen offset:0 sc1\n\t"
        "buffer_load_dwordx4 v[44:47], v107, %[ssrd], %[sob] offen offset:1024 sc1\n\t"
        "buffer_load_dwordx4 v[48:51], v107, %[ssrd], %[sob] offen offset:2048 sc1\n\t"
        "s_waitcnt vmcnt(2)\n\t"
        "v_bitop3_b32 v17, v40, v42, %[slay] bitop3:0x7e\n\t"
        "v_and_b32 v17, 1, v17\n\t"
        "v_cmp_eq_u32 vcc, 1, v17\n\t"
        "s_cbranch_vccnz Lx0_%=\n\t"
        "s_branch Ld0_%=\n\t"
        "Lx1_%=:\n\t"
        "s_cmp_eq_u32 %[scn], 0\n\t"
        "s_cbranch_scc1 Ld1_%=\n\t"
        "s_sub_u32 %[scn], %[scn], 1\n\t"
        "buffer_load_dwordx4 v[44:47], v107, %[ssrd], %[sob] offen offset:1024 sc1\n\t"
        "buffer_load_dwordx4 v[48:51], v107, %[ssrd], %[sob] offen offset:2048 sc1\n\t"
        "buffer_load_dwordx4 v[52:55], v107, %[ssrd], %[sob] offen offset:3072 sc1\n\t"
        "s_waitcnt vmcnt(2)\n\t"
        "v_bitop3_b32 v17, v44, v46, %[slay] bitop3:0x7e\n\t"
        "v_and_b32 v17, 1, v17\n\t"
        "v_cmp_eq_u32 vcc, 1, v17\n\t"
        "s_cbranch_vccnz Lx1_%=\n\t"
        "s_branch Ld1_%=\n\t"
        "Lx2_%=:\n\t"
        "s_cmp_eq_u32 %[scn], 0\n\t"
        "s_cbranch_scc1 Ld2_%=\n\t"
        "s_sub_u32 %[scn], %[scn], 1\n\t"
        "buffer_load_dwordx4 v[48:51], v107, %[ssrd], %[sob] offen offset:2048 sc1\n\t"
        "buffer_load_dwordx4 v[52:55], v107, %[ssrd], %[sob] offen offset:3072 sc1\n\t"
        "buffer_load_dwordx4 v[56:59], v107, %[ssrd], %[sob4] offen offset:0 sc1\n\t"
        "s_waitcnt vmcnt(2)\n\t"
        "v_bitop3_b32 v17, v48, v50, %[slay] bitop3:0x7e\n\t"
        "v_and_b32 v17, 1, v17\n\t"
        "v_cmp_eq_u32 vcc, 1, v17\n\t"
        "s_cbranch_vccnz Lx2_%=\n\t"
        "s_branch Ld2_%=\n\t"
        "Lx3_%=:\n\t"
        "s_cmp_eq_u32 %[scn], 0\n\t"
        "s_cbranch_scc1 Ld3_%=\n\t"
        "s_sub_u32 %[scn], %[scn], 1\n\t"
        "buffer_load_dwordx4 v[52:55], v107, %[ssrd], %[sob] offen offset:3072 sc1\n\t"
        "buffer_load_dwordx4 v[56:59], v107, %[ssrd], %[sob4] offen offset:0 sc1\n\t"
        "buffer_load_dwordx4 v[60:63], v107, %[ssrd], %[sob4] offen offset:1024 sc1\n\t"
        "s_waitcnt vmcnt(2)\n\t"
        "v_bitop3_b32 v17, v52, v54, %[slay] bitop3:0x7e\n\t"
        "v_and_b32 v17, 1, v17\n\t"
        "v_cmp_eq_u32 vcc, 1, v17\n\t"
        "s_cbranch_vccnz Lx3_%=\n\t"
        "s_branch Ld3_%=\n\t"
        "Lx4_%=:\n\t"
        "s_cmp_eq_u32 %[scn], 0\n\t"
        "s_cbranch_scc1 Ld4_%=\n\t"
        "s_sub_u32 %[scn], %[scn], 1\n\t"
        "buffer_load_dwordx4 v[56:59], v107, %[ssrd], %[sob4] offen offset:0 sc1\n\t"
        "buffer_load_dwordx4 v[60:63], v107, %[ssrd], %[sob4] offen offset:1024 sc1\n\t"
        "buffer_load_dwordx4 v[64:67], v107, %[ssrd], %[sob4] offen offset:2048 sc1\n\t"
        "s_waitcnt vmcnt(2)\n\t"
        "v_bitop3_b32 v17, v56, v58, %[slay] bitop3:0x7e\n\t"
        "v_and_b32 v17, 1, v17\n\t"
        "v_cmp_eq_u32 vcc, 1, v17\n\t"
        "s_cbranch_vccnz Lx4_%=\n\t"
        "s_branch Ld4_%=\n\t"
        "Lx5_%=:\n\t"
        "s_cmp_eq_u32 %[scn], 0\n\t"
        "s_cbranch_scc1 Ld5_%=\n\t"
        "s_sub_u32 %[scn], %[scn], 1\n\t"
        "buffer_load_dwordx4 v[60:63], v107, %[ssrd], %[sob4] offen offset:1024 sc1\n\t"
        "buffer_load_dwordx4 v[64:67], v107, %[ssrd], %[sob4] offen offset:2048 sc1\n\t"
        "buffer_load_dwordx2 v[68:69], v108, %[ssrd], %[sob] offen offset:0 sc1\n\t"
        "buffer_load_dwordx2 v[70:71], v19, %[ssrd], %[sob] offen offset:0 sc1\n\t"
        "s_waitcnt vmcnt(3)\n\t"
        "v_bitop3_b32 v17, v60, v62, %[slay] bitop3:0x7e\n\t"
        "v_and_b32 v17, 1, v17\n\t"
        "v_cmp_eq_u32 vcc, 1, v17\n\t"
        "s_cbranch_vccnz Lx5_%=\n\t"
        "s_branch Ld5_%=\n\t"
        "Lx6_%=:\n\t"
        "s_cmp_eq_u32 %[scn], 0\n\t"
        "s_cbranch_scc1 Ld6_%=\n\t"
        "s_sub_u32 %[scn], %[scn], 1\n\t"
        "buffer_load_dwordx4 v[64:67], v107, %[ssrd], %[sob4] offen offset:2048 sc1\n\t"
        "buffer_load_dwordx2 v[68:69], v108, %[ssrd], %[sob] offen offset:0 sc1\n\t"
        "buffer_load_dwordx2 v[70:71], v19, %[ssrd], %[sob] offen offset:0 sc1\n\t"
        "buffer_load_dwordx2 v[72:73], v108, %[ssrd], %[sob] offen offset:1024 sc1\n\t"
        "buffer_load_dwordx2 v[74:75], v19, %[ssrd], %[sob] offen offset:1024 sc1\n\t"
        "s_waitcnt vmcnt(4)\n\t"
        "v_bitop3_b32 v17, v64, v66, %[slay] bitop3:0x7e\n\t"
        "v_and_b32 v17, 1, v17\n\t"
        "v_cmp_eq_u32 vcc, 1, v17\n\t"
        "s_cbranch_vccnz Lx6_%=\n\t"
        "s_branch Ld6_%=\n\t"
        "Lx7_%=:\n\t"
        "s_cmp_eq_u32 %[scn], 0\n\t"
        "s_cbranch_scc1 Ld7_%=\n\t"
        "s_sub_u32 %[scn], %[scn], 1\n\t"
        "buffer_load_dwordx2 v[68:69], v108, %[ssrd], %[sob] offen offset:0 sc1\n\t"
        "buffer_load_dwordx2 v[70:71], v19, %[ssrd], %[sob] offen offset:0 sc1\n\t"
        "buffer_load_dwordx2 v[72:73], v108, %[ssrd], %[sob] offen offset:1024 sc1\n\t"
        "buffer_load_dwordx2 v[74:75], v19, %[ssrd], %[sob] offen offset:1024 sc1\n\t"
        "s_waitcnt vmcnt(2)\n\t"
        "v_bitop3_b32 v17, v68, v70, %[slay] bitop3:0x7e\n\t"
        "v_and_b32 v17, 1, v17\n\t"
        "v_cmp_eq_u32 vcc, 1, v17\n\t"
        "s_cbranch_vccnz Lx7_%=\n\t"
        "s_branch Ld7_%=\n\t"
        "Lx8_%=:\n\t"
        "s_cmp_eq_u32 %[scn], 0\n\t"
        "s_cbranch_scc1 Ld8_%=\n\t"
        "s_sub_u32 %[scn], %[scn], 1\n\t"
        "buffer_load_dwordx2 v[72:73], v108, %[ssrd], %[sob] offen offset:1024 sc1\n\t"
        "buffer_load_dwordx2 v[74:75], v19, %[ssrd], %[sob] offen offset:1024 sc1\n\t"
        "s_waitcnt vmcnt(0)\n\t"
        "v_bitop3_b32 v17, v72, v74, %[slay] bitop3:0x7e\n\t"
        "v_and_b32 v17, 1, v17\n\t"
        "v_cmp_eq_u32 vcc, 1, v17\n\t"
        "s_cbranch_vccnz Lx8_%=\n\t"
        "s_branch Ld8_%=\n\t"
        "Lws_%=:\n\t"
        "s_cmp_eq_u32 %[scn], 0\n\t"
        "s_cbranch_scc1 Lwg_%=\n\t"
        "s_sub_u32 %[scn], %[scn], 1\n\t"
        "s_sleep 1\n\t"
        "ds_read_b32 v16, v15\n\t"
        "s_waitcnt lgkmcnt(0)\n\t"
        "s_branch Lwc_%=\n\t"
        "Lk0_%=:\n\t"
        "s_waitcnt vmcnt(0)\n\t"
        "Lp_%=:\n\t"
        "ds_read_b32 v16, v15 offset:%[opc]\n\t"
        "ds_read_b128 v[52:55], v18 offset:0\n\t"
        "ds_read_b128 v[56:59], v18 offset:1024\n\t"
        "ds_read_b128 v[60:63], v18 offset:2048\n\t"
        "ds_read_b128 v[64:67], v18 offset:3072\n\t"
        "ds_read_b64 v[68:69], v14 offset:4096\n\t"
        "v_lshrrev_b32 v19, 8, v107\n\t"
        "v_lshl_add_u32 v19, v19, 4, %[swr]\n\t"
        "v_mov_b32 v17, 163424\n\t"
        "v_cndmask_b32 v19, v17, v19, %[sl1]\n\t"
        "ds_read_b128 v[40:43], v19 offset:0\n\t"
        "ds_read_b128 v[44:47], v19 offset:64\n\t"
        "ds_read_b128 v[48:51], v19 offset:128\n\t"
        "v_mov_b32 v97, 0\n\t"
        "v_mov_b32 v98, 0\n\t"
        "v_mov_b32 v99, 0\n\t"
        "v_and_b32 v84, 0xf0, v107\n\t"
        "v_mov_b32 v83, 0\n\t"
        "s_waitcnt lgkmcnt(8)\n\t"
        "v_readfirstlane_b32 %[sob4x], v16\n\t"
        "s_cmp_ge_u32 %[sob4x], %[swt]\n\t"
        "s_cbranch_scc1 Lq_%=\n\t"
        "s_cmp_eq_u32 %[scn], 0\n\t"
        "s_cbranch_scc1 Lq_%=\n\t"
        "s_sub_u32 %[scn], %[scn], 1\n\t"
        "s_waitcnt lgkmcnt(0)\n\t"
        "s_branch Lp_%=\n\t"
        "Lq_%=:\n\t"
        "s_waitcnt lgkmcnt(6)\n\t"
        "v_add_f32 v20, v20, v52\n\t"
        "v_add_f32 v21, v21, v53\n\t"
        "v_add_f32 v22, v22, v54\n\t"
        "v_add_f32 v23, v23, v55\n\t"
        "v_add_f32 v24, v24, v56\n\t"
        "v_add_f32 v25, v25, v57\n\t"
        "v_add_f32 v26, v26, v58\n\t"
        "v_add_f32 v27, v27, v59\n\t"
        "v_cvt_pk_bf16_f32 v88, v20, v24\n\t"
        "v_cvt_pk_bf16_f32 v89, v21, v25\n\t"
        "v_cvt_pk_bf16_f32 v90, v22, v26\n\t"
        "v_cvt_pk_bf16_f32 v91, v23, v27\n\t"
        "s_waitcnt lgkmcnt(4)\n\t"
        "v_add_f32 v28, v28, v60\n\t"
        "v_add_f32 v29, v29, v61\n\t"
        "v_add_f32 v30, v30, v62\n\t"
        "v_add_f32 v31, v31, v63\n\t"
        "v_add_f32 v32, v32, v64\n\t"
        "v_add_f32 v33, v33, v65\n\t"
        "v_add_f32 v34, v34, v66\n\t"
        "v_add_f32 v35, v35, v67\n\t"
        "v_cvt_pk_bf16_f32 v92, v28, v32\n\t"
        "v_cvt_pk_bf16_f32 v93, v29, v33\n\t"
        "v_cvt_pk_bf16_f32 v94, v30, v34\n\t"
        "v_cvt_pk_bf16_f32 v95, v31, v35\n\t"
        "s_waitcnt lgkmcnt(0)\n\t"
        "v_mfma_f32_16x16x32_bf16 v[72:75], v[40:43], v[88:91], 0\n\t"
        "v_mfma_f32_16x16x32_bf16 v[76:79], v[88:91], v[88:91], 0\n\t"
        "s_waitcnt lgkmcnt(3)\n\t"
        "v_add_f32 v36, v36, v68\n\t"
        "v_add_f32 v37, v37, v69\n\t"
        "v_cvt_pk_bf16_f32 v96, v36, v37\n\t"
        "s_nop 1\n\t"
        "v_permlane32_swap_b32 v96, v97\n\t"
        "v_mfma_f32_16x16x32_bf16 v[72:75], v[44:47], v[92:95], v[72:75]\n\t"
        "v_mfma_f32_16x16x32_bf16 v[76:79], v[92:95], v[92:95], v[76:79]\n\t"
        "v_mfma_f32_16x16x32_bf16 v[72:75], v[48:51], v[96:99], v[72:75]\n\t"
        "v_mfma_f32_16x16x32_bf16 v[76:79], v[96:99], v[96:99], v[76:79]\n\t"
        "v_and_b32 v17, 0x300, v107\n\t"
        "v_lshrrev_b32 v19, 5, v17\n\t"
        "v_lshl_add_u32 v17, v17, 2, v84\n\t"
        "v_add_u32 v19, v19, v84\n\t"
        "s_add_u32 %[sob4], %[sx1], 4096\n\t"
        "s_add_u32 %[sob4x], %[sx1], 8192\n\t"
        "s_cmp_lg_u32 %[slay], 0\n\t"
        "v_min_u32 v80, -2, v72\n\t"
        "v_min_u32 v82, -2, v73\n\t"
        "v_cndmask_b32 v76, v76, v77, %[spar]\n\t"
        "v_cndmask_b32 v78, v78, v79, %[spar]\n\t"
        "v_cndmask_b32 v76, v76, v78, %[sl2]\n\t"
        "v_min_u32 v81, -2, v76\n\t"
        "s_mov_b64 exec, %[sdiag]\n\t"
        "buffer_store_dwordx4 v[80:83], v84, %[ssrd], %[sstat] offen sc1\n\t"
        "s_mov_b64 exec, -1\n\t"
        "s_cbranch_scc1 Lpk_%=\n\t"
        "v_min_u32 v88, -2, v88\n\t"
        "v_min_u32 v89, -2, v89\n\t"
        "v_min_u32 v90, -2, v90\n\t"
        "v_min_u32 v91, -2, v91\n\t"
        "buffer_store_dwordx4 v[88:91], v17, %[ssrd], %[sx1] offen sc1\n\t"
        "v_min_u32 v92, -2, v92\n\t"
        "v_min_u32 v93, -2, v93\n\t"
        "v_min_u32 v94, -2, v94\n\t"
        "v_min_u32 v95, -2, v95\n\t"
        "v_min_u32 v96, -2, v96\n\t"
        "v_min_u32 v97, -2, v97\n\t"
        "buffer_store_dwordx4 v[92:95], v17, %[ssrd], %[sob4] offen sc1\n\t"
        "s_mov_b32 exec_hi, 0\n\t"
        "buffer_store_dwordx2 v[96:97], v19, %[ssrd], %[sob4x] offen sc1\n\t"
        "s_mov_b32 exec_hi, -1\n\t"
        "Lpk_%=:\n\t"
        "v_add_u32 v18, %[spk], v107\n\t"
        "v_add_u32 v19, %[spk], v19\n\t"
        "v_mov_b32 v16, 1\n\t"
        "ds_write_b128 v18, v[88:91]\n\t"
        "ds_write_b128 v18, v[92:95] offset:1024\n\t"
        "s_mov_b32 exec_hi, 0\n\t"
        "ds_write_b64 v19, v[96:97] offset:2048\n\t"
        "s_mov_b32 exec_lo, 1\n\t"
        "ds_add_u32 v15, v16 offset:%[opk]\n\t"
        "s_mov_b64 exec, -1\n\t"
        "Lend_%=:\n\t"
        "s_setprio 0\n\t"
        : [sob4] "=&s"(ob4), [sob4x] "=&s"(ob4x), [fo] "+v"(fo), [scn] "+s"(cn)
        : [ssrd] "s"(a.srd), [sob] "s"(a.ob), [sl1d] "s"(a.l1d), [stvd] "s"(a.tvd), [swi] "s"(a.wi), [sld] "s"(a.ld),
          [smsk] "s"(a.msk), [sscr] "s"(a.scr), [sstat] "s"(a.stat), [sx1] "s"(a.x1), [slay] "s"(a.layer), [spk] "s"(a.pk), [sn7] "s"(a.n7), [sskip] "s"(a.skip), [swc] "s"(a.wc), [swt] "s"(4 * a.layer + 4), [slb] "s"(a.lb), [opc] "n"(BC_PC), [opk] "n"(BC_PK), [swr] "s"(163232),
          [spar] "s"(0xAAAAAAAAAAAAAAAAull), [sl2] "s"(0xCCCCCCCCCCCCCCCCull), [sl1] "s"(0x2222222222222222ull), [sdiag] "s"(0xf0000f0000f0000full)
        : "memory", "scc", "vcc", "v13", "v14", "v15", "v16", "v17", "v18", "v19", "v20", "v21", "v22", "v23", "v24", "v25", "v26", "v27", "v28", "v29", "v30", "v31", "v32", "v33", "v34", "v35", "v36", "v37", "v38", "v39", "v40", "v41", "v42", "v43", "v44", "v45", "v46", "v47");
}

DEV void dload(u32x4 srd, u32 a2, u32 so) {
    asm volatile(
        "s_nop 4\n\t"
        "buffer_load_dwordx4 v[112:115], v107, %[ssrd], %[so] offen offset:0" NTD "\n\t"
        "buffer_load_dwordx4 v[116:119], v107, %[ssrd], %[so] offen offset:1152" NTD "\n\t"
        "buffer_load_dwordx4 v[120:123], v107, %[ssrd], %[so] offen offset:2304" NTD "\n\t"
        "buffer_load_ushort v220, %[a2], %[ssrd], %[so] offen offset:1024" NTD "\n\t"
        "buffer_load_ushort v221, %[a2], %[ssrd], %[so] offen offset:2176" NTD "\n\t"
        "buffer_load_ushort v222, %[a2], %[ssrd], %[so] offen offset:3328" NTD "\n\t"
        "s_add_u32 %[so], %[so], 3456\n\t"
        "buffer_load_dwordx4 v[124:127], v107, %[ssrd], %[so] offen offset:0" NTD "\n\t"
        "buffer_load_dwordx4 v[128:131], v107, %[ssrd], %[so] offen offset:1152" NTD "\n\t"
        "buffer_load_dwordx4 v[132:135], v107, %[ssrd], %[so] offen offset:2304" NTD "\n\t"
        "buffer_load_ushort v223, %[a2], %[ssrd], %[so] offen offset:1024" NTD "\n\t"
        "buffer_load_ushort v224, %[a2], %[ssrd], %[so] offen offset:2176" NTD "\n\t"
        "buffer_load_ushort v225, %[a2], %[ssrd], %[so] offen offset:3328" NTD "\n\t"
        "s_add_u32 %[so], %[so], 3456\n\t"
        "buffer_load_dwordx4 v[136:139], v107, %[ssrd], %[so] offen offset:0" NTD "\n\t"
        "buffer_load_dwordx4 v[140:143], v107, %[ssrd], %[so] offen offset:1152" NTD "\n\t"
        "buffer_load_dwordx4 v[144:147], v107, %[ssrd], %[so] offen offset:2304" NTD "\n\t"
        "buffer_load_ushort v226, %[a2], %[ssrd], %[so] offen offset:1024" NTD "\n\t"
        "buffer_load_ushort v227, %[a2], %[ssrd], %[so] offen offset:2176" NTD "\n\t"
        "buffer_load_ushort v228, %[a2], %[ssrd], %[so] offen offset:3328" NTD "\n\t"
        "s_add_u32 %[so], %[so], 3456\n\t"
        "buffer_load_dwordx4 v[148:151], v107, %[ssrd], %[so] offen offset:0" NTD "\n\t"
        "buffer_load_dwordx4 v[152:155], v107, %[ssrd], %[so] offen offset:1152" NTD "\n\t"
        "buffer_load_dwordx4 v[156:159], v107, %[ssrd], %[so] offen offset:2304" NTD "\n\t"
        "buffer_load_ushort v229, %[a2], %[ssrd], %[so] offen offset:1024" NTD "\n\t"
        "buffer_load_ushort v230, %[a2], %[ssrd], %[so] offen offset:2176" NTD "\n\t"
        "buffer_load_ushort v231, %[a2], %[ssrd], %[so] offen offset:3328" NTD "\n\t"
        "s_add_u32 %[so], %[so], 3456\n\t"
        "buffer_load_dwordx4 v[160:163], v107, %[ssrd], %[so] offen offset:0" NTD "\n\t"
        "buffer_load_dwordx4 v[164:167], v107, %[ssrd], %[so] offen offset:1152" NTD "\n\t"
        "buffer_load_dwordx4 v[168:171], v107, %[ssrd], %[so] offen offset:2304" NTD "\n\t"
        "buffer_load_ushort v232, %[a2], %[ssrd], %[so] offen offset:1024" NTD "\n\t"
        "buffer_load_ushort v233, %[a2], %[ssrd], %[so] offen offset:2176" NTD "\n\t"
        "buffer_load_ushort v234, %[a2], %[ssrd], %[so] offen offset:3328" NTD "\n\t"
        "s_add_u32 %[so], %[so], 3456\n\t"
        "buffer_load_dwordx4 v[172:175], v107, %[ssrd], %[so] offen offset:0" NTD "\n\t"
        "buffer_load_dwordx4 v[176:179], v107, %[ssrd], %[so] offen offset:1152" NTD "\n\t"
        "buffer_load_dwordx4 v[180:183], v107, %[ssrd], %[so] offen offset:2304" NTD "\n\t"
        "buffer_load_ushort v235, %[a2], %[ssrd], %[so] offen offset:1024" NTD "\n\t"
        "buffer_load_ushort v236, %[a2], %[ssrd], %[so] offen offset:2176" NTD "\n\t"
        "buffer_load_ushort v237, %[a2], %[ssrd], %[so] offen offset:3328" NTD "\n\t"
        "s_add_u32 %[so], %[so], 3456\n\t"
        "buffer_load_dwordx4 v[184:187], v107, %[ssrd], %[so] offen offset:0" NTD "\n\t"
        "buffer_load_dwordx4 v[188:191], v107, %[ssrd], %[so] offen offset:1152" NTD "\n\t"
        "buffer_load_dwordx4 v[192:195], v107, %[ssrd], %[so] offen offset:2304" NTD "\n\t"
        "buffer_load_ushort v238, %[a2], %[ssrd], %[so] offen offset:1024" NTD "\n\t"
        "buffer_load_ushort v239, %[a2], %[ssrd], %[so] offen offset:2176" NTD "\n\t"
        "buffer_load_ushort v240, %[a2], %[ssrd], %[so] offen offset:3328" NTD "\n\t"
        "s_add_u32 %[so], %[so], 3456\n\t"
        "buffer_load_dwordx4 v[196:199], v107, %[ssrd], %[so] offen offset:0" NTD "\n\t"
        "buffer_load_dwordx4 v[200:203], v107, %[ssrd], %[so] offen offset:1152" NTD "\n\t"
        "buffer_load_dwordx4 v[204:207], v107, %[ssrd], %[so] offen offset:2304" NTD "\n\t"
        "buffer_load_ushort v241, %[a2], %[ssrd], %[so] offen offset:1024" NTD "\n\t"
        "buffer_load_ushort v242, %[a2], %[ssrd], %[so] offen offset:2176" NTD "\n\t"
        "buffer_load_ushort v243, %[a2], %[ssrd], %[so] offen offset:3328" NTD "\n\t"
        "s_add_u32 %[so], %[so], 3456\n\t"
        : [so] "+s"(so) : [ssrd] "s"(srd), [a2] "v"(a2) : "memory", "scc");
}

DEV void dpermx() {
    asm volatile(
        "v_perm_b32 v208, v221, v220, %[slo]\n\t"
        "v_perm_b32 v209, v223, v222, %[slo]\n\t"
        "v_perm_b32 v210, v225, v224, %[slo]\n\t"
        "v_perm_b32 v211, v227, v226, %[slo]\n\t"
        "v_perm_b32 v212, v229, v228, %[slo]\n\t"
        "v_perm_b32 v213, v231, v230, %[slo]\n\t"
        "v_perm_b32 v214, v233, v232, %[slo]\n\t"
        "v_perm_b32 v215, v235, v234, %[slo]\n\t"
        "v_perm_b32 v216, v237, v236, %[slo]\n\t"
        "v_perm_b32 v217, v239, v238, %[slo]\n\t"
        "v_perm_b32 v218, v241, v240, %[slo]\n\t"
        "v_perm_b32 v219, v243, v242, %[slo]\n\t"
        :: [slo] "s"(0x05040100u) : "memory");
}
DEV void dperm() {
    u32 t;
    asm volatile(
        "v_perm_b32 %[t], v116, v112, %[slo]\n\t"
        "v_perm_b32 v116, v116, v112, %[shi]\n\t"
        "v_mov_b32 v112, %[t]\n\t"
        "v_perm_b32 %[t], v117, v113, %[slo]\n\t"
        "v_perm_b32 v117, v117, v113, %[shi]\n\t"
        "v_mov_b32 v113, %[t]\n\t"
        "v_perm_b32 %[t], v118, v114, %[slo]\n\t"
        "v_perm_b32 v118, v118, v114, %[shi]\n\t"
        "v_mov_b32 v114, %[t]\n\t"
        "v_perm_b32 %[t], v119, v115, %[slo]\n\t"
        "v_perm_b32 v119, v119, v115, %[shi]\n\t"
        "v_mov_b32 v115, %[t]\n\t"
        "v_perm_b32 %[t], v124, v120, %[slo]\n\t"
        "v_perm_b32 v124, v124, v120, %[shi]\n\t"
        "v_mov_b32 v120, %[t]\n\t"
        "v_perm_b32 %[t], v125, v121, %[slo]\n\t"
        "v_perm_b32 v125, v125, v121, %[shi]\n\t"
        "v_mov_b32 v121, %[t]\n\t"
        "v_perm_b32 %[t], v126, v122, %[slo]\n\t"
        "v_perm_b32 v126, v126, v122, %[shi]\n\t"
        "v_mov_b32 v122, %[t]\n\t"
        "v_perm_b32 %[t], v127, v123, %[slo]\n\t"
        "v_perm_b32 v127, v127, v123, %[shi]\n\t"
        "v_mov_b32 v123, %[t]\n\t"
        "v_perm_b32 %[t], v132, v128, %[slo]\n\t"
        "v_perm_b32 v132, v132, v128, %[shi]\n\t"
        "v_mov_b32 v128, %[t]\n\t"
        "v_perm_b32 %[t], v133, v129, %[slo]\n\t"
        "v_perm_b32 v133, v133, v129, %[shi]\n\t"
        "v_mov_b32 v129, %[t]\n\t"
        "v_perm_b32 %[t], v134, v130, %[slo]\n\t"
        "v_perm_b32 v134, v134, v130, %[shi]\n\t"
        "v_mov_b32 v130, %[t]\n\t"
        "v_perm_b32 %[t], v135, v131, %[slo]\n\t"
        "v_perm_b32 v135, v135, v131, %[shi]\n\t"
        "v_mov_b32 v131, %[t]\n\t"
        "v_perm_b32 %[t], v140, v136, %[slo]\n\t"
        "v_perm_b32 v140, v140, v136, %[shi]\n\t"
        "v_mov_b32 v136, %[t]\n\t"
        "v_perm_b32 %[t], v141, v137, %[slo]\n\t"
        "v_perm_b32 v141, v141, v137, %[shi]\n\t"
        "v_mov_b32 v137, %[t]\n\t"
        "v_perm_b32 %[t], v142, v138, %[slo]\n\t"
        "v_perm_b32 v142, v142, v138, %[shi]\n\t"
        "v_mov_b32 v138, %[t]\n\t"
        "v_perm_b32 %[t], v143, v139, %[slo]\n\t"
        "v_perm_b32 v143, v143, v139, %[shi]\n\t"
        "v_mov_b32 v139, %[t]\n\t"
        "v_perm_b32 %[t], v148, v144, %[slo]\n\t"
        "v_perm_b32 v148, v148, v144, %[shi]\n\t"
        "v_mov_b32 v144, %[t]\n\t"
        "v_perm_b32 %[t], v149, v145, %[slo]\n\t"
        "v_perm_b32 v149, v149, v145, %[shi]\n\t"
        "v_mov_b32 v145, %[t]\n\t"
        "v_perm_b32 %[t], v150, v146, %[slo]\n\t"
        "v_perm_b32 v150, v150, v146, %[shi]\n\t"
        "v_mov_b32 v146, %[t]\n\t"
        "v_perm_b32 %[t], v151, v147, %[slo]\n\t"
        "v_perm_b32 v151, v151, v147, %[shi]\n\t"
        "v_mov_b32 v147, %[t]\n\t"
        "v_perm_b32 %[t], v156, v152, %[slo]\n\t"
        "v_perm_b32 v156, v156, v152, %[shi]\n\t"
        "v_mov_b32 v152, %[t]\n\t"
        "v_perm_b32 %[t], v157, v153, %[slo]\n\t"
        "v_perm_b32 v157, v157, v153, %[shi]\n\t"
        "v_mov_b32 v153, %[t]\n\t"
        "v_perm_b32 %[t], v158, v154, %[slo]\n\t"
        "v_perm_b32 v158, v158, v154, %[shi]\n\t"
        "v_mov_b32 v154, %[t]\n\t"
        "v_perm_b32 %[t], v159, v155, %[slo]\n\t"
        "v_perm_b32 v159, v159, v155, %[shi]\n\t"
        "v_mov_b32 v155, %[t]\n\t"
        "v_perm_b32 %[t], v164, v160, %[slo]\n\t"
        "v_perm_b32 v164, v164, v160, %[shi]\n\t"
        "v_mov_b32 v160, %[t]\n\t"
        "v_perm_b32 %[t], v165, v161, %[slo]\n\t"
        "v_perm_b32 v165, v165, v161, %[shi]\n\t"
        "v_mov_b32 v161, %[t]\n\t"
        "v_perm_b32 %[t], v166, v162, %[slo]\n\t"
        "v_perm_b32 v166, v166, v162, %[shi]\n\t"
        "v_mov_b32 v162, %[t]\n\t"
        "v_perm_b32 %[t], v167, v163, %[slo]\n\t"
        "v_perm_b32 v167, v167, v163, %[shi]\n\t"
        "v_mov_b32 v163, %[t]\n\t"
        "v_perm_b32 %[t], v172, v168, %[slo]\n\t"
        "v_perm_b32 v172, v172, v168, %[shi]\n\t"
        "v_mov_b32 v168, %[t]\n\t"
        "v_perm_b32 %[t], v173, v169, %[slo]\n\t"
        "v_perm_b32 v173, v173, v169, %[shi]\n\t"
        "v_mov_b32 v169, %[t]\n\t"
        "v_perm_b32 %[t], v174, v170, %[slo]\n\t"
        "v_perm_b32 v174, v174, v170, %[shi]\n\t"
        "v_mov_b32 v170, %[t]\n\t"
        "v_perm_b32 %[t], v175, v171, %[slo]\n\t"
        "v_perm_b32 v175, v175, v171, %[shi]\n\t"
        "v_mov_b32 v171, %[t]\n\t"
        "v_perm_b32 %[t], v180, v176, %[slo]\n\t"
        "v_perm_b32 v180, v180, v176, %[shi]\n\t"
        "v_mov_b32 v176, %[t]\n\t"
        "v_perm_b32 %[t], v181, v177, %[slo]\n\t"
        "v_perm_b32 v181, v181, v177, %[shi]\n\t"
        "v_mov_b32 v177, %[t]\n\t"
        "v_perm_b32 %[t], v182, v178, %[slo]\n\t"
        "v_perm_b32 v182, v182, v178, %[shi]\n\t"
        "v_mov_b32 v178, %[t]\n\t"
        "v_perm_b32 %[t], v183, v179, %[slo]\n\t"
        "v_perm_b32 v183, v183, v179, %[shi]\n\t"
        "v_mov_b32 v179, %[t]\n\t"
        "v_perm_b32 %[t], v188, v184, %[slo]\n\t"
        "v_perm_b32 v188, v188, v184, %[shi]\n\t"
        "v_mov_b32 v184, %[t]\n\t"
        "v_perm_b32 %[t], v189, v185, %[slo]\n\t"
        "v_perm_b32 v189, v189, v185, %[shi]\n\t"
        "v_mov_b32 v185, %[t]\n\t"
        "v_perm_b32 %[t], v190, v186, %[slo]\n\t"
        "v_perm_b32 v190, v190, v186, %[shi]\n\t"
        "v_mov_b32 v186, %[t]\n\t"
        "v_perm_b32 %[t], v191, v187, %[slo]\n\t"
        "v_perm_b32 v191, v191, v187, %[shi]\n\t"
        "v_mov_b32 v187, %[t]\n\t"
        "v_perm_b32 %[t], v196, v192, %[slo]\n\t"
        "v_perm_b32 v196, v196, v192, %[shi]\n\t"
        "v_mov_b32 v192, %[t]\n\t"
        "v_perm_b32 %[t], v197, v193, %[slo]\n\t"
        "v_perm_b32 v197, v197, v193, %[shi]\n\t"
        "v_mov_b32 v193, %[t]\n\t"
        "v_perm_b32 %[t], v198, v194, %[slo]\n\t"
        "v_perm_b32 v198, v198, v194, %[shi]\n\t"
        "v_mov_b32 v194, %[t]\n\t"
        "v_perm_b32 %[t], v199, v195, %[slo]\n\t"
        "v_perm_b32 v199, v199, v195, %[shi]\n\t"
        "v_mov_b32 v195, %[t]\n\t"
        "v_perm_b32 %[t], v204, v200, %[slo]\n\t"
        "v_perm_b32 v204, v204, v200, %[shi]\n\t"
        "v_mov_b32 v200, %[t]\n\t"
        "v_perm_b32 %[t], v205, v201, %[slo]\n\t"
        "v_perm_b32 v205, v205, v201, %[shi]\n\t"
        "v_mov_b32 v201, %[t]\n\t"
        "v_perm_b32 %[t], v206, v202, %[slo]\n\t"
        "v_perm_b32 v206, v206, v202, %[shi]\n\t"
        "v_mov_b32 v202, %[t]\n\t"
        "v_perm_b32 %[t], v207, v203, %[slo]\n\t"
        "v_perm_b32 v207, v207, v203, %[shi]\n\t"
        "v_mov_b32 v203, %[t]\n\t"
        : [t] "=&v"(t) : [slo] "s"(0x05040100u), [shi] "s"(0x07060302u) : "memory");
}

DEV void xstats(int xa, float (&sx)[2], float (&sq)[2]) {
    int xb; u32 sc;
    asm volatile(
        "v_mov_b32 v24, 0x3f803f80\n\t"
        "v_mov_b32 v25, 0x3f803f80\n\t"
        "v_mov_b32 v26, 0x3f803f80\n\t"
        "v_mov_b32 v27, 0x3f803f80\n\t"
        "v_mov_b32 v48, 0\n\t"
        "v_mov_b32 v49, 0\n\t"
        "v_mov_b32 v50, 0\n\t"
        "v_mov_b32 v51, 0\n\t"
        "v_mov_b32 v52, 0\n\t"
        "v_mov_b32 v53, 0\n\t"
        "v_mov_b32 v54, 0\n\t"
        "v_mov_b32 v55, 0\n\t"
        "v_mov_b32 v56, 0\n\t"
        "v_mov_b32 v57, 0\n\t"
        "v_mov_b32 v58, 0\n\t"
        "v_mov_b32 v59, 0\n\t"
        "v_mov_b32 v60, 0\n\t"
        "v_mov_b32 v61, 0\n\t"
        "v_mov_b32 v62, 0\n\t"
        "v_mov_b32 v63, 0\n\t"
        "v_xor_b32 %[xb], 64, %[xa]\n\t"
        "s_mov_b32 %[sc], 3\n\t"
        "Ll_%=:\n\t"
        "ds_read_b128 v[208:211], %[xa] offset:0\n\t"
        "ds_read_b128 v[212:215], %[xa] offset:6144\n\t"
        "ds_read_b128 v[216:219], %[xb] offset:0\n\t"
        "ds_read_b128 v[220:223], %[xb] offset:6144\n\t"
        "ds_read_b128 v[224:227], %[xa] offset:10240\n\t"
        "ds_read_b128 v[228:231], %[xa] offset:16384\n\t"
        "ds_read_b128 v[232:235], %[xb] offset:10240\n\t"
        "ds_read_b128 v[236:239], %[xb] offset:16384\n\t"
        "ds_read_b128 v[240:243], %[xa] offset:20480\n\t"
        "ds_read_b128 v[244:247], %[xa] offset:26624\n\t"
        "ds_read_b128 v[248:251], %[xb] offset:20480\n\t"
        "ds_read_b128 v[252:255], %[xb] offset:26624\n\t"
        "v_add_u32 %[xa], 30720, %[xa]\n\t"
        "v_add_u32 %[xb], 30720, %[xb]\n\t"
        "s_waitcnt lgkmcnt(11)\n\t"
        "v_mfma_f32_16x16x32_bf16 v[48:51], v[24:27], v[208:211], v[48:51]\n\t"
        "v_mfma_f32_16x16x32_bf16 v[56:59], v[208:211], v[208:211], v[56:59]\n\t"
        "s_waitcnt lgkmcnt(10)\n\t"
        "v_mfma_f32_16x16x32_bf16 v[52:55], v[24:27], v[212:215], v[52:55]\n\t"
        "v_mfma_f32_16x16x32_bf16 v[60:63], v[212:215], v[212:215], v[60:63]\n\t"
        "s_waitcnt lgkmcnt(9)\n\t"
        "v_mfma_f32_16x16x32_bf16 v[48:51], v[24:27], v[216:219], v[48:51]\n\t"
        "v_mfma_f32_16x16x32_bf16 v[56:59], v[216:219], v[216:219], v[56:59]\n\t"
        "s_waitcnt lgkmcnt(8)\n\t"
        "v_mfma_f32_16x16x32_bf16 v[52:55], v[24:27], v[220:223], v[52:55]\n\t"
        "v_mfma_f32_16x16x32_bf16 v[60:63], v[220:223], v[220:223], v[60:63]\n\t"
        "s_waitcnt lgkmcnt(7)\n\t"
        "v_mfma_f32_16x16x32_bf16 v[48:51], v[24:27], v[224:227], v[48:51]\n\t"
        "v_mfma_f32_16x16x32_bf16 v[56:59], v[224:227], v[224:227], v[56:59]\n\t"
        "s_waitcnt lgkmcnt(6)\n\t"
        "v_mfma_f32_16x16x32_bf16 v[52:55], v[24:27], v[228:231], v[52:55]\n\t"
        "v_mfma_f32_16x16x32_bf16 v[60:63], v[228:231], v[228:231], v[60:63]\n\t"
        "s_waitcnt lgkmcnt(5)\n\t"
        "v_mfma_f32_16x16x32_bf16 v[48:51], v[24:27], v[232:235], v[48:51]\n\t"
        "v_mfma_f32_16x16x32_bf16 v[56:59], v[232:235], v[232:235], v[56:59]\n\t"
        "s_waitcnt lgkmcnt(4)\n\t"
        "v_mfma_f32_16x16x32_bf16 v[52:55], v[24:27], v[236:239], v[52:55]\n\t"
        "v_mfma_f32_16x16x32_bf16 v[60:63], v[236:239], v[236:239], v[60:63]\n\t"
        "s_waitcnt lgkmcnt(3)\n\t"
        "v_mfma_f32_16x16x32_bf16 v[48:51], v[24:27], v[240:243], v[48:51]\n\t"
        "v_mfma_f32_16x16x32_bf16 v[56:59], v[240:243], v[240:243], v[56:59]\n\t"
        "s_waitcnt lgkmcnt(2)\n\t"
        "v_mfma_f32_16x16x32_bf16 v[52:55], v[24:27], v[244:247], v[52:55]\n\t"
        "v_mfma_f32_16x16x32_bf16 v[60:63], v[244:247], v[244:247], v[60:63]\n\t"
        "s_waitcnt lgkmcnt(1)\n\t"
        "v_mfma_f32_16x16x32_bf16 v[48:51], v[24:27], v[248:251], v[48:51]\n\t"
        "v_mfma_f32_16x16x32_bf16 v[56:59], v[248:251], v[248:251], v[56:59]\n\t"
        "s_waitcnt lgkmcnt(0)\n\t"
        "v_mfma_f32_16x16x32_bf16 v[52:55], v[24:27], v[252:255], v[52:55]\n\t"
        "v_mfma_f32_16x16x32_bf16 v[60:63], v[252:255], v[252:255], v[60:63]\n\t"
        "s_sub_u32 %[sc], %[sc], 1\n\t"
        "s_cmp_lg_u32 %[sc], 0\n\t"
        "s_cbranch_scc1 Ll_%=\n\t"
        "s_nop 7\n\t"
        "v_mov_b32 %[x0], v48\n\t"
        "v_cndmask_b32 v56, v56, v57, %[spar]\n\t"
        "v_cndmask_b32 v58, v58, v59, %[spar]\n\t"
        "v_cndmask_b32 %[q0], v56, v58, %[sl2]\n\t"
        "v_mov_b32 %[x1], v52\n\t"
        "v_cndmask_b32 v60, v60, v61, %[spar]\n\t"
        "v_cndmask_b32 v62, v62, v63, %[spar]\n\t"
        "v_cndmask_b32 %[q1], v60, v62, %[sl2]\n\t"
        : [xa] "+v"(xa), [xb] "=&v"(xb), [sc] "=&s"(sc), [x0] "=&v"(sx[0]), [q0] "=&v"(sq[0]), [x1] "=&v"(sx[1]), [q1] "=&v"(sq[1])
        : [spar] "s"(0xAAAAAAAAAAAAAAAAull), [sl2] "s"(0xCCCCCCCCCCCCCCCCull)
        : "memory", "scc", "v24", "v25", "v26", "v27");
}

DEV void xgather(u32x4 srd, u32 xo, u32 xp, u32 xq, int th, int hh, u32 ld, u32 lh, u32& cn) {
    u32 cr, sa, sb, g0, g1, g2;
    asm volatile(
        "s_nop 4\n\t"
        "s_sleep 5\n\t"
        "s_add_u32 m0, %[sld], 0\n\t"
        "s_add_u32 %[sb], %[sxo], m0\n\t"
        "s_sub_u32 %[sa], %[sb], 8192\n\t"
        "s_cmp_gt_i32 %[sth], 0\n\t"
        "s_cselect_b32 %[scr], %[sa], %[sb]\n\t"
        "buffer_load_dwordx4 v107, %[ssrd], %[scr] offen offset:0 sc1 lds\n\t"
        "s_cmp_gt_i32 %[sth], 1\n\t"
        "s_cselect_b32 %[scr], %[sa], %[sb]\n\t"
        "buffer_load_dwordx4 v107, %[ssrd], %[scr] offen offset:1024 sc1 lds\n\t"
        "s_cmp_gt_i32 %[sth], 2\n\t"
        "s_cselect_b32 %[scr], %[sa], %[sb]\n\t"
        "buffer_load_dwordx4 v107, %[ssrd], %[scr] offen offset:2048 sc1 lds\n\t"
        "s_cmp_gt_i32 %[sth], 3\n\t"
        "s_cselect_b32 %[scr], %[sa], %[sb]\n\t"
        "buffer_load_dwordx4 v107, %[ssrd], %[scr] offen offset:3072 sc1 lds\n\t"
        "s_add_u32 m0, %[sld], 4096\n\t"
        "s_add_u32 %[sb], %[sxo], m0\n\t"
        "s_sub_u32 %[sa], %[sb], 8192\n\t"
        "s_cmp_gt_i32 %[sth], 4\n\t"
        "s_cselect_b32 %[scr], %[sa], %[sb]\n\t"
        "buffer_load_dwordx4 v107, %[ssrd], %[scr] offen offset:0 sc1 lds\n\t"
        "s_cmp_gt_i32 %[sth], 5\n\t"
        "s_cselect_b32 %[scr], %[sa], %[sb]\n\t"
        "buffer_load_dwordx4 v107, %[ssrd], %[scr] offen offset:1024 sc1 lds\n\t"
        "s_cmp_gt_i32 %[sth], 6\n\t"
        "s_cselect_b32 %[scr], %[sa], %[sb]\n\t"
        "buffer_load_dwordx4 v107, %[ssrd], %[scr] offen offset:2048 sc1 lds\n\t"
        "s_cmp_gt_i32 %[sth], 7\n\t"
        "s_cselect_b32 %[scr], %[sa], %[sb]\n\t"
        "buffer_load_dwordx4 v107, %[ssrd], %[scr] offen offset:3072 sc1 lds\n\t"
        "v_and_b32 %[g0], 0x300, v107\n\t"
        "v_and_b32 %[g1], 0xf0, v107\n\t"
        "v_lshrrev_b32 v96, 8, v107\n\t"
        "v_lshl_add_u32 %[g0], %[g0], 2, %[g1]\n\t"
        "v_add_u32 %[g0], 768, %[g0]\n\t"
        "v_sub_u32 v96, %[shh], v96\n\t"
        "v_add_u32 v97, 0x2000, %[g0]\n\t"
        "v_add_u32 %[g1], %[sld], v107\n\t"
        "v_mov_b32 %[g2], 153984\n\t"
        "s_sleep 4\n\t"
        "s_add_u32 m0, %[sld], 16384\n\t"
        "s_add_u32 %[sb], %[sxo], m0\n\t"
        "s_sub_u32 %[sa], %[sb], 8192\n\t"
        "s_cmp_gt_i32 %[sth], 16\n\t"
        "s_cselect_b32 %[scr], %[sa], %[sb]\n\t"
        "buffer_load_dwordx4 v107, %[ssrd], %[scr] offen offset:0 sc1 lds\n\t"
        "s_cmp_gt_i32 %[sth], 17\n\t"
        "s_cselect_b32 %[scr], %[sa], %[sb]\n\t"
        "buffer_load_dwordx4 v107, %[ssrd], %[scr] offen offset:1024 sc1 lds\n\t"
        "s_cmp_gt_i32 %[sth], 18\n\t"
        "s_cselect_b32 %[scr], %[sa], %[sb]\n\t"
        "buffer_load_dwordx4 v107, %[ssrd], %[scr] offen offset:2048 sc1 lds\n\t"
        "s_cmp_gt_i32 %[sth], 19\n\t"
        "s_cselect_b32 %[scr], %[sa], %[sb]\n\t"
        "buffer_load_dwordx4 v107, %[ssrd], %[scr] offen offset:3072 sc1 lds\n\t"
        "s_add_u32 m0, %[sld], 20480\n\t"
        "s_add_u32 %[sb], %[sxo], m0\n\t"
        "s_sub_u32 %[sa], %[sb], 8192\n\t"
        "s_cmp_gt_i32 %[sth], 20\n\t"
        "s_cselect_b32 %[scr], %[sa], %[sb]\n\t"
        "buffer_load_dwordx4 v107, %[ssrd], %[scr] offen offset:0 sc1 lds\n\t"
        "s_cmp_gt_i32 %[sth], 21\n\t"
        "s_cselect_b32 %[scr], %[sa], %[sb]\n\t"
        "buffer_load_dwordx4 v107, %[ssrd], %[scr] offen offset:1024 sc1 lds\n\t"
        "s_cmp_gt_i32 %[sth], 22\n\t"
        "s_cselect_b32 %[scr], %[sa], %[sb]\n\t"
        "buffer_load_dwordx4 v107, %[ssrd], %[scr] offen offset:2048 sc1 lds\n\t"
        "s_cmp_gt_i32 %[sth], 23\n\t"
        "s_cselect_b32 %[scr], %[sa], %[sb]\n\t"
        "buffer_load_dwordx4 v107, %[ssrd], %[scr] offen offset:3072 sc1 lds\n\t"
        "s_waitcnt vmcnt(8)\n\t"
        "ds_read_b128 v[24:27], %[g1] offset:0\n\t"
        "ds_read_b128 v[28:31], %[g1] offset:1024\n\t"
        "ds_read_b128 v[32:35], %[g1] offset:2048\n\t"
        "ds_read_b128 v[36:39], %[g1] offset:3072\n\t"
        "ds_read_b128 v[40:43], %[g1] offset:4096\n\t"
        "ds_read_b128 v[44:47], %[g1] offset:5120\n\t"
        "ds_read_b128 v[88:91], %[g1] offset:6144\n\t"
        "ds_read_b128 v[92:95], %[g1] offset:7168\n\t"
        "Lh0_%=:\n\t"
        "s_waitcnt lgkmcnt(0)\n\t"
        "v_max3_u32 v24, v24, v25, v26\n\t"
        "v_max3_u32 v24, v24, v27, v28\n\t"
        "v_max3_u32 v24, v24, v29, v30\n\t"
        "v_max3_u32 v24, v24, v31, v32\n\t"
        "v_max3_u32 v24, v24, v33, v34\n\t"
        "v_max3_u32 v24, v24, v35, v36\n\t"
        "v_max3_u32 v24, v24, v37, v38\n\t"
        "v_max3_u32 v24, v24, v39, v40\n\t"
        "v_max3_u32 v24, v24, v41, v42\n\t"
        "v_max3_u32 v24, v24, v43, v44\n\t"
        "v_max3_u32 v24, v24, v45, v46\n\t"
        "v_max3_u32 v24, v24, v47, v88\n\t"
        "v_max3_u32 v24, v24, v89, v90\n\t"
        "v_max3_u32 v24, v24, v91, v92\n\t"
        "v_max3_u32 v24, v24, v93, v94\n\t"
        "v_max_u32 v24, v24, v95\n\t"
        "v_cmp_eq_u32 vcc, -1, v24\n\t"
        "v_mov_b32 v25, 1\n\t"
        "s_cbranch_vccnz Lx0_%=\n\t"
        "Ld0_%=:\n\t"
        "s_mov_b64 exec, 1\n\t"
        "ds_add_u32 %[g2], v25 offset:%[og0]+0\n\t"
        "s_mov_b64 exec, -1\n\t"
        "s_add_u32 m0, %[sld], 32768\n\t"
        "s_add_u32 %[sb], %[sxo], m0\n\t"
        "s_sub_u32 %[sa], %[sb], 8192\n\t"
        "s_cmp_gt_i32 %[sth], 32\n\t"
        "s_cselect_b32 %[scr], %[sa], %[sb]\n\t"
        "buffer_load_dwordx4 v107, %[ssrd], %[scr] offen offset:0 sc1 lds\n\t"
        "s_cmp_gt_i32 %[sth], 33\n\t"
        "s_cselect_b32 %[scr], %[sa], %[sb]\n\t"
        "buffer_load_dwordx4 v107, %[ssrd], %[scr] offen offset:1024 sc1 lds\n\t"
        "s_cmp_gt_i32 %[sth], 34\n\t"
        "s_cselect_b32 %[scr], %[sa], %[sb]\n\t"
        "buffer_load_dwordx4 v107, %[ssrd], %[scr] offen offset:2048 sc1 lds\n\t"
        "s_cmp_gt_i32 %[sth], 35\n\t"
        "s_cselect_b32 %[scr], %[sa], %[sb]\n\t"
        "buffer_load_dwordx4 v107, %[ssrd], %[scr] offen offset:3072 sc1 lds\n\t"
        "s_add_u32 m0, %[sld], 36864\n\t"
        "s_add_u32 %[sb], %[sxo], m0\n\t"
        "s_sub_u32 %[sa], %[sb], 8192\n\t"
        "s_cmp_gt_i32 %[sth], 36\n\t"
        "s_cselect_b32 %[scr], %[sa], %[sb]\n\t"
        "buffer_load_dwordx4 v107, %[ssrd], %[scr] offen offset:0 sc1 lds\n\t"
        "s_cmp_gt_i32 %[sth], 37\n\t"
        "s_cselect_b32 %[scr], %[sa], %[sb]\n\t"
        "buffer_load_dwordx4 v107, %[ssrd], %[scr] offen offset:1024 sc1 lds\n\t"
        "s_cmp_gt_i32 %[sth], 38\n\t"
        "s_cselect_b32 %[scr], %[sa], %[sb]\n\t"
        "buffer_load_dwordx4 v107, %[ssrd], %[scr] offen offset:2048 sc1 lds\n\t"
        "s_cmp_gt_i32 %[sth], 39\n\t"
        "s_cselect_b32 %[scr], %[sa], %[sb]\n\t"
        "buffer_load_dwordx4 v107, %[ssrd], %[scr] offen offset:3072 sc1 lds\n\t"
        "s_waitcnt vmcnt(8)\n\t"
        "ds_read_b128 v[24:27], %[g1] offset:16384\n\t"
        "ds_read_b128 v[28:31], %[g1] offset:17408\n\t"
        "ds_read_b128 v[32:35], %[g1] offset:18432\n\t"
        "ds_read_b128 v[36:39], %[g1] offset:19456\n\t"
        "ds_read_b128 v[40:43], %[g1] offset:20480\n\t"
        "ds_read_b128 v[44:47], %[g1] offset:21504\n\t"
        "ds_read_b128 v[88:91], %[g1] offset:22528\n\t"
        "ds_read_b128 v[92:95], %[g1] offset:23552\n\t"
        "s_add_u32 m0, %[sld], 49152\n\t"
        "s_add_u32 %[sb], %[sxo], m0\n\t"
        "s_sub_u32 %[sa], %[sb], 8192\n\t"
        "s_cmp_gt_i32 %[sth], 48\n\t"
        "s_cselect_b32 %[scr], %[sa], %[sb]\n\t"
        "buffer_load_dwordx4 v107, %[ssrd], %[scr] offen offset:0 sc1 lds\n\t"
        "s_cmp_gt_i32 %[sth], 49\n\t"
        "s_cselect_b32 %[scr], %[sa], %[sb]\n\t"
        "buffer_load_dwordx4 v107, %[ssrd], %[scr] offen offset:1024 sc1 lds\n\t"
        "s_cmp_gt_i32 %[sth], 50\n\t"
        "s_cselect_b32 %[scr], %[sa], %[sb]\n\t"
        "buffer_load_dwordx4 v107, %[ssrd], %[scr] offen offset:2048 sc1 lds\n\t"
        "s_cmp_gt_i32 %[sth], 51\n\t"
        "s_cselect_b32 %[scr], %[sa], %[sb]\n\t"
        "buffer_load_dwordx4 v107, %[ssrd], %[scr] offen offset:3072 sc1 lds\n\t"
        "s_add_u32 m0, %[sld], 53248\n\t"
        "s_add_u32 %[sb], %[sxo], m0\n\t"
        "s_sub_u32 %[sa], %[sb], 8192\n\t"
        "s_cmp_gt_i32 %[sth], 52\n\t"
        "s_cselect_b32 %[scr], %[sa], %[sb]\n\t"
        "buffer_load_dwordx4 v107, %[ssrd], %[scr] offen offset:0 sc1 lds\n\t"
        "s_cmp_gt_i32 %[sth], 53\n\t"
        "s_cselect_b32 %[scr], %[sa], %[sb]\n\t"
        "buffer_load_dwordx4 v107, %[ssrd], %[scr] offen offset:1024 sc1 lds\n\t"
        "s_cmp_gt_i32 %[sth], 54\n\t"
        "s_cselect_b32 %[scr], %[sa], %[sb]\n\t"
        "buffer_load_dwordx4 v107, %[ssrd], %[scr] offen offset:2048 sc1 lds\n\t"
        "s_cmp_gt_i32 %[sth], 55\n\t"
        "s_cselect_b32 %[scr], %[sa], %[sb]\n\t"
        "buffer_load_dwordx4 v107, %[ssrd], %[scr] offen offset:3072 sc1 lds\n\t"
        "Lh1_%=:\n\t"
        "s_waitcnt lgkmcnt(0)\n\t"
        "v_max3_u32 v24, v24, v25, v26\n\t"
        "v_max3_u32 v24, v24, v27, v28\n\t"
        "v_max3_u32 v24, v24, v29, v30\n\t"
        "v_max3_u32 v24, v24, v31, v32\n\t"
        "v_max3_u32 v24, v24, v33, v34\n\t"
        "v_max3_u32 v24, v24, v35, v36\n\t"
        "v_max3_u32 v24, v24, v37, v38\n\t"
        "v_max3_u32 v24, v24, v39, v40\n\t"
        "v_max3_u32 v24, v24, v41, v42\n\t"
        "v_max3_u32 v24, v24, v43, v44\n\t"
        "v_max3_u32 v24, v24, v45, v46\n\t"
        "v_max3_u32 v24, v24, v47, v88\n\t"
        "v_max3_u32 v24, v24, v89, v90\n\t"
        "v_max3_u32 v24, v24, v91, v92\n\t"
        "v_max3_u32 v24, v24, v93, v94\n\t"
        "v_max_u32 v24, v24, v95\n\t"
        "v_cmp_eq_u32 vcc, -1, v24\n\t"
        "v_mov_b32 v25, 1\n\t"
        "s_cbranch_vccnz Lx1_%=\n\t"
        "Ld1_%=:\n\t"
        "s_mov_b64 exec, 1\n\t"
        "ds_add_u32 %[g2], v25 offset:%[og0]+4\n\t"
        "s_mov_b64 exec, -1\n\t"
        "s_waitcnt vmcnt(8)\n\t"
        "ds_read_b128 v[24:27], %[g1] offset:32768\n\t"
        "ds_read_b128 v[28:31], %[g1] offset:33792\n\t"
        "ds_read_b128 v[32:35], %[g1] offset:34816\n\t"
        "ds_read_b128 v[36:39], %[g1] offset:35840\n\t"
        "ds_read_b128 v[40:43], %[g1] offset:36864\n\t"
        "ds_read_b128 v[44:47], %[g1] offset:37888\n\t"
        "ds_read_b128 v[88:91], %[g1] offset:38912\n\t"
        "ds_read_b128 v[92:95], %[g1] offset:39936\n\t"
        "v_cmp_lt_i32 vcc, 0, v96\n\t"
        "s_add_u32 m0, %[slh], 0\n\t"
        "s_add_u32 %[scr], %[sxq], 0\n\t"
        "v_cndmask_b32 v98, v97, %[g0], vcc\n\t"
        "buffer_load_dwordx4 v98, %[ssrd], %[scr] offen sc1 lds\n\t"
        "v_cmp_lt_i32 vcc, 4, v96\n\t"
        "s_add_u32 m0, %[slh], 1024\n\t"
        "s_add_u32 %[scr], %[sxq], 4096\n\t"
        "v_cndmask_b32 v99, v97, %[g0], vcc\n\t"
        "buffer_load_dwordx4 v99, %[ssrd], %[scr] offen sc1 lds\n\t"
        "v_cmp_lt_i32 vcc, 8, v96\n\t"
        "s_add_u32 m0, %[slh], 2048\n\t"
        "s_add_u32 %[scr], %[sxp], 8192\n\t"
        "v_cndmask_b32 v98, v97, %[g0], vcc\n\t"
        "buffer_load_dwordx4 v98, %[ssrd], %[scr] offen sc1 lds\n\t"
        "v_cmp_lt_i32 vcc, 12, v96\n\t"
        "s_add_u32 m0, %[slh], 3072\n\t"
        "s_add_u32 %[scr], %[sxp], 12288\n\t"
        "v_cndmask_b32 v99, v97, %[g0], vcc\n\t"
        "buffer_load_dwordx4 v99, %[ssrd], %[scr] offen sc1 lds\n\t"
        "v_cmp_lt_i32 vcc, 16, v96\n\t"
        "s_add_u32 m0, %[slh], 4096\n\t"
        "s_add_u32 %[scr], %[sxp], 16384\n\t"
        "v_cndmask_b32 v98, v97, %[g0], vcc\n\t"
        "buffer_load_dwordx4 v98, %[ssrd], %[scr] offen sc1 lds\n\t"
        "Lh2_%=:\n\t"
        "s_waitcnt lgkmcnt(0)\n\t"
        "v_max3_u32 v24, v24, v25, v26\n\t"
        "v_max3_u32 v24, v24, v27, v28\n\t"
        "v_max3_u32 v24, v24, v29, v30\n\t"
        "v_max3_u32 v24, v24, v31, v32\n\t"
        "v_max3_u32 v24, v24, v33, v34\n\t"
        "v_max3_u32 v24, v24, v35, v36\n\t"
        "v_max3_u32 v24, v24, v37, v38\n\t"
        "v_max3_u32 v24, v24, v39, v40\n\t"
        "v_max3_u32 v24, v24, v41, v42\n\t"
        "v_max3_u32 v24, v24, v43, v44\n\t"
        "v_max3_u32 v24, v24, v45, v46\n\t"
        "v_max3_u32 v24, v24, v47, v88\n\t"
        "v_max3_u32 v24, v24, v89, v90\n\t"
        "v_max3_u32 v24, v24, v91, v92\n\t"
        "v_max3_u32 v24, v24, v93, v94\n\t"
        "v_max_u32 v24, v24, v95\n\t"
        "v_cmp_eq_u32 vcc, -1, v24\n\t"
        "v_mov_b32 v25, 1\n\t"
        "s_cbranch_vccnz Lx2_%=\n\t"
        "Ld2_%=:\n\t"
        "s_mov_b64 exec, 1\n\t"
        "ds_add_u32 %[g2], v25 offset:%[og0]+8\n\t"
        "s_mov_b64 exec, -1\n\t"
        "s_waitcnt vmcnt(5)\n\t"
        "ds_read_b128 v[24:27], %[g1] offset:49152\n\t"
        "ds_read_b128 v[28:31], %[g1] offset:50176\n\t"
        "ds_read_b128 v[32:35], %[g1] offset:51200\n\t"
        "ds_read_b128 v[36:39], %[g1] offset:52224\n\t"
        "ds_read_b128 v[40:43], %[g1] offset:53248\n\t"
        "ds_read_b128 v[44:47], %[g1] offset:54272\n\t"
        "ds_read_b128 v[88:91], %[g1] offset:55296\n\t"
        "ds_read_b128 v[92:95], %[g1] offset:56320\n\t"
        "v_cmp_lt_i32 vcc, 20, v96\n\t"
        "s_add_u32 m0, %[slh], 5120\n\t"
        "s_add_u32 %[scr], %[sxp], 20480\n\t"
        "v_cndmask_b32 v99, v97, %[g0], vcc\n\t"
        "buffer_load_dwordx4 v99, %[ssrd], %[scr] offen sc1 lds\n\t"
        "v_cmp_lt_i32 vcc, 24, v96\n\t"
        "s_add_u32 m0, %[slh], 6144\n\t"
        "s_add_u32 %[scr], %[sxp], 24576\n\t"
        "v_cndmask_b32 v98, v97, %[g0], vcc\n\t"
        "buffer_load_dwordx4 v98, %[ssrd], %[scr] offen sc1 lds\n\t"
        "v_cmp_lt_i32 vcc, 28, v96\n\t"
        "s_add_u32 m0, %[slh], 7168\n\t"
        "s_add_u32 %[scr], %[sxp], 28672\n\t"
        "v_cndmask_b32 v99, v97, %[g0], vcc\n\t"
        "buffer_load_dwordx4 v99, %[ssrd], %[scr] offen sc1 lds\n\t"
        "v_cmp_lt_i32 vcc, 32, v96\n\t"
        "s_add_u32 m0, %[slh], 8192\n\t"
        "s_add_u32 %[scr], %[sxp], 32768\n\t"
        "v_cndmask_b32 v98, v97, %[g0], vcc\n\t"
        "buffer_load_dwordx4 v98, %[ssrd], %[scr] offen sc1 lds\n\t"
        "Lh3_%=:\n\t"
        "s_waitcnt lgkmcnt(0)\n\t"
        "v_max3_u32 v24, v24, v25, v26\n\t"
        "v_max3_u32 v24, v24, v27, v28\n\t"
        "v_max3_u32 v24, v24, v29, v30\n\t"
        "v_max3_u32 v24, v24, v31, v32\n\t"
        "v_max3_u32 v24, v24, v33, v34\n\t"
        "v_max3_u32 v24, v24, v35, v36\n\t"
        "v_max3_u32 v24, v24, v37, v38\n\t"
        "v_max3_u32 v24, v24, v39, v40\n\t"
        "v_max3_u32 v24, v24, v41, v42\n\t"
        "v_max3_u32 v24, v24, v43, v44\n\t"
        "v_max3_u32 v24, v24, v45, v46\n\t"
        "v_max3_u32 v24, v24, v47, v88\n\t"
        "v_max3_u32 v24, v24, v89, v90\n\t"
        "v_max3_u32 v24, v24, v91, v92\n\t"
        "v_max3_u32 v24, v24, v93, v94\n\t"
        "v_max_u32 v24, v24, v95\n\t"
        "v_cmp_eq_u32 vcc, -1, v24\n\t"
        "v_mov_b32 v25, 2\n\t"
        "s_cbranch_vccnz Lx3_%=\n\t"
        "Ld3_%=:\n\t"
        "s_mov_b64 exec, 1\n\t"
        "ds_add_u32 %[g2], v25 offset:%[og0]+12\n\t"
        "s_mov_b64 exec, -1\n\t"
        "s_waitcnt vmcnt(4)\n\t"
        "v_add_u32 %[g1], %[slh], v107\n\t"
        "ds_read_b128 v[24:27], %[g1] offset:0\n\t"
        "ds_read_b128 v[28:31], %[g1] offset:1024\n\t"
        "ds_read_b128 v[32:35], %[g1] offset:2048\n\t"
        "ds_read_b128 v[36:39], %[g1] offset:3072\n\t"
        "ds_read_b128 v[40:43], %[g1] offset:4096\n\t"
        "Lh4_%=:\n\t"
        "s_waitcnt lgkmcnt(0)\n\t"
        "v_max3_u32 v24, v24, v25, v26\n\t"
        "v_max3_u32 v24, v24, v27, v28\n\t"
        "v_max3_u32 v24, v24, v29, v30\n\t"
        "v_max3_u32 v24, v24, v31, v32\n\t"
        "v_max3_u32 v24, v24, v33, v34\n\t"
        "v_max3_u32 v24, v24, v35, v36\n\t"
        "v_max3_u32 v24, v24, v37, v38\n\t"
        "v_max3_u32 v24, v24, v39, v40\n\t"
        "v_max3_u32 v24, v24, v41, v42\n\t"
        "v_max_u32 v24, v24, v43\n\t"
        "v_cmp_eq_u32 vcc, -1, v24\n\t"
        "v_mov_b32 v25, 1\n\t"
        "s_cbranch_vccnz Lx4_%=\n\t"
        "Ld4_%=:\n\t"
        "s_waitcnt vmcnt(0)\n\t"
        "ds_read_b128 v[24:27], %[g1] offset:5120\n\t"
        "ds_read_b128 v[28:31], %[g1] offset:6144\n\t"
        "ds_read_b128 v[32:35], %[g1] offset:7168\n\t"
        "ds_read_b128 v[36:39], %[g1] offset:8192\n\t"
        "Lh5_%=:\n\t"
        "s_waitcnt lgkmcnt(0)\n\t"
        "v_max3_u32 v24, v24, v25, v26\n\t"
        "v_max3_u32 v24, v24, v27, v28\n\t"
        "v_max3_u32 v24, v24, v29, v30\n\t"
        "v_max3_u32 v24, v24, v31, v32\n\t"
        "v_max3_u32 v24, v24, v33, v34\n\t"
        "v_max3_u32 v24, v24, v35, v36\n\t"
        "v_max3_u32 v24, v24, v37, v38\n\t"
        "v_max_u32 v24, v24, v39\n\t"
        "v_cmp_eq_u32 vcc, -1, v24\n\t"
        "v_mov_b32 v25, 1\n\t"
        "s_cbranch_vccnz Lx5_%=\n\t"
        "Ld5_%=:\n\t"
        "s_mov_b64 exec, 1\n\t"
        "ds_add_u32 %[g2], v25 offset:%[og0]+16\n\t"
        "s_mov_b64 exec, -1\n\t"
        "s_waitcnt lgkmcnt(0)\n\t"
        "s_branch Le_%=\n\t"
        "Lx0_%=:\n\t"
        "s_cmp_lt_i32 %[scn], 1\n\t"
        "s_cbranch_scc1 Ld0_%=\n\t"
        "s_sub_u32 %[scn], %[scn], 1\n\t"
        "s_add_u32 m0, %[sld], 0\n\t"
        "s_add_u32 %[sb], %[sxo], m0\n\t"
        "s_sub_u32 %[sa], %[sb], 8192\n\t"
        "s_cmp_gt_i32 %[sth], 0\n\t"
        "s_cselect_b32 %[scr], %[sa], %[sb]\n\t"
        "buffer_load_dwordx4 v107, %[ssrd], %[scr] offen offset:0 sc1 lds\n\t"
        "s_cmp_gt_i32 %[sth], 1\n\t"
        "s_cselect_b32 %[scr], %[sa], %[sb]\n\t"
        "buffer_load_dwordx4 v107, %[ssrd], %[scr] offen offset:1024 sc1 lds\n\t"
        "s_cmp_gt_i32 %[sth], 2\n\t"
        "s_cselect_b32 %[scr], %[sa], %[sb]\n\t"
        "buffer_load_dwordx4 v107, %[ssrd], %[scr] offen offset:2048 sc1 lds\n\t"
        "s_cmp_gt_i32 %[sth], 3\n\t"
        "s_cselect_b32 %[scr], %[sa], %[sb]\n\t"
        "buffer_load_dwordx4 v107, %[ssrd], %[scr] offen offset:3072 sc1 lds\n\t"
        "s_add_u32 m0, %[sld], 4096\n\t"
        "s_add_u32 %[sb], %[sxo], m0\n\t"
        "s_sub_u32 %[sa], %[sb], 8192\n\t"
        "s_cmp_gt_i32 %[sth], 4\n\t"
        "s_cselect_b32 %[scr], %[sa], %[sb]\n\t"
        "buffer_load_dwordx4 v107, %[ssrd], %[scr] offen offset:0 sc1 lds\n\t"
        "s_cmp_gt_i32 %[sth], 5\n\t"
        "s_cselect_b32 %[scr], %[sa], %[sb]\n\t"
        "buffer_load_dwordx4 v107, %[ssrd], %[scr] offen offset:1024 sc1 lds\n\t"
        "s_cmp_gt_i32 %[sth], 6\n\t"
        "s_cselect_b32 %[scr], %[sa], %[sb]\n\t"
        "buffer_load_dwordx4 v107, %[ssrd], %[scr] offen offset:2048 sc1 lds\n\t"
        "s_cmp_gt_i32 %[sth], 7\n\t"
        "s_cselect_b32 %[scr], %[sa], %[sb]\n\t"
        "buffer_load_dwordx4 v107, %[ssrd], %[scr] offen offset:3072 sc1 lds\n\t"
        "s_waitcnt vmcnt(0)\n\t"
        "ds_read_b128 v[24:27], %[g1] offset:0\n\t"
        "ds_read_b128 v[28:31], %[g1] offset:1024\n\t"
        "ds_read_b128 v[32:35], %[g1] offset:2048\n\t"
        "ds_read_b128 v[36:39], %[g1] offset:3072\n\t"
        "ds_read_b128 v[40:43], %[g1] offset:4096\n\t"
        "ds_read_b128 v[44:47], %[g1] offset:5120\n\t"
        "ds_read_b128 v[88:91], %[g1] offset:6144\n\t"
        "ds_read_b128 v[92:95], %[g1] offset:7168\n\t"
        "s_branch Lh0_%=\n\t"
        "Lx1_%=:\n\t"
        "s_cmp_lt_i32 %[scn], 1\n\t"
        "s_cbranch_scc1 Ld1_%=\n\t"
        "s_sub_u32 %[scn], %[scn], 1\n\t"
        "s_add_u32 m0, %[sld], 16384\n\t"
        "s_add_u32 %[sb], %[sxo], m0\n\t"
        "s_sub_u32 %[sa], %[sb], 8192\n\t"
        "s_cmp_gt_i32 %[sth], 16\n\t"
        "s_cselect_b32 %[scr], %[sa], %[sb]\n\t"
        "buffer_load_dwordx4 v107, %[ssrd], %[scr] offen offset:0 sc1 lds\n\t"
        "s_cmp_gt_i32 %[sth], 17\n\t"
        "s_cselect_b32 %[scr], %[sa], %[sb]\n\t"
        "buffer_load_dwordx4 v107, %[ssrd], %[scr] offen offset:1024 sc1 lds\n\t"
        "s_cmp_gt_i32 %[sth], 18\n\t"
        "s_cselect_b32 %[scr], %[sa], %[sb]\n\t"
        "buffer_load_dwordx4 v107, %[ssrd], %[scr] offen offset:2048 sc1 lds\n\t"
        "s_cmp_gt_i32 %[sth], 19\n\t"
        "s_cselect_b32 %[scr], %[sa], %[sb]\n\t"
        "buffer_load_dwordx4 v107, %[ssrd], %[scr] offen offset:3072 sc1 lds\n\t"
        "s_add_u32 m0, %[sld], 20480\n\t"
        "s_add_u32 %[sb], %[sxo], m0\n\t"
        "s_sub_u32 %[sa], %[sb], 8192\n\t"
        "s_cmp_gt_i32 %[sth], 20\n\t"
        "s_cselect_b32 %[scr], %[sa], %[sb]\n\t"
        "buffer_load_dwordx4 v107, %[ssrd], %[scr] offen offset:0 sc1 lds\n\t"
        "s_cmp_gt_i32 %[sth], 21\n\t"
        "s_cselect_b32 %[scr], %[sa], %[sb]\n\t"
        "buffer_load_dwordx4 v107, %[ssrd], %[scr] offen offset:1024 sc1 lds\n\t"
        "s_cmp_gt_i32 %[sth], 22\n\t"
        "s_cselect_b32 %[scr], %[sa], %[sb]\n\t"
        "buffer_load_dwordx4 v107, %[ssrd], %[scr] offen offset:2048 sc1 lds\n\t"
        "s_cmp_gt_i32 %[sth], 23\n\t"
        "s_cselect_b32 %[scr], %[sa], %[sb]\n\t"
        "buffer_load_dwordx4 v107, %[ssrd], %[scr] offen offset:3072 sc1 lds\n\t"
        "s_waitcnt vmcnt(0)\n\t"
        "ds_read_b128 v[24:27], %[g1] offset:16384\n\t"
        "ds_read_b128 v[28:31], %[g1] offset:17408\n\t"
        "ds_read_b128 v[32:35], %[g1] offset:18432\n\t"
        "ds_read_b128 v[36:39], %[g1] offset:19456\n\t"
        "ds_read_b128 v[40:43], %[g1] offset:20480\n\t"
        "ds_read_b128 v[44:47], %[g1] offset:21504\n\t"
        "ds_read_b128 v[88:91], %[g1] offset:22528\n\t"
        "ds_read_b128 v[92:95], %[g1] offset:23552\n\t"
        "s_branch Lh1_%=\n\t"
        "Lx2_%=:\n\t"
        "s_cmp_lt_i32 %[scn], 1\n\t"
        "s_cbranch_scc1 Ld2_%=\n\t"
        "s_sub_u32 %[scn], %[scn], 1\n\t"
        "s_add_u32 m0, %[sld], 32768\n\t"
        "s_add_u32 %[sb], %[sxo], m0\n\t"
        "s_sub_u32 %[sa], %[sb], 8192\n\t"
        "s_cmp_gt_i32 %[sth], 32\n\t"
        "s_cselect_b32 %[scr], %[sa], %[sb]\n\t"
        "buffer_load_dwordx4 v107, %[ssrd], %[scr] offen offset:0 sc1 lds\n\t"
        "s_cmp_gt_i32 %[sth], 33\n\t"
        "s_cselect_b32 %[scr], %[sa], %[sb]\n\t"
        "buffer_load_dwordx4 v107, %[ssrd], %[scr] offen offset:1024 sc1 lds\n\t"
        "s_cmp_gt_i32 %[sth], 34\n\t"
        "s_cselect_b32 %[scr], %[sa], %[sb]\n\t"
        "buffer_load_dwordx4 v107, %[ssrd], %[scr] offen offset:2048 sc1 lds\n\t"
        "s_cmp_gt_i32 %[sth], 35\n\t"
        "s_cselect_b32 %[scr], %[sa], %[sb]\n\t"
        "buffer_load_dwordx4 v107, %[ssrd], %[scr] offen offset:3072 sc1 lds\n\t"
        "s_add_u32 m0, %[sld], 36864\n\t"
        "s_add_u32 %[sb], %[sxo], m0\n\t"
        "s_sub_u32 %[sa], %[sb], 8192\n\t"
        "s_cmp_gt_i32 %[sth], 36\n\t"
        "s_cselect_b32 %[scr], %[sa], %[sb]\n\t"
        "buffer_load_dwordx4 v107, %[ssrd], %[scr] offen offset:0 sc1 lds\n\t"
        "s_cmp_gt_i32 %[sth], 37\n\t"
        "s_cselect_b32 %[scr], %[sa], %[sb]\n\t"
        "buffer_load_dwordx4 v107, %[ssrd], %[scr] offen offset:1024 sc1 lds\n\t"
        "s_cmp_gt_i32 %[sth], 38\n\t"
        "s_cselect_b32 %[scr], %[sa], %[sb]\n\t"
        "buffer_load_dwordx4 v107, %[ssrd], %[scr] offen offset:2048 sc1 lds\n\t"
        "s_cmp_gt_i32 %[sth], 39\n\t"
        "s_cselect_b32 %[scr], %[sa], %[sb]\n\t"
        "buffer_load_dwordx4 v107, %[ssrd], %[scr] offen offset:3072 sc1 lds\n\t"
        "s_waitcnt vmcnt(0)\n\t"
        "ds_read_b128 v[24:27], %[g1] offset:32768\n\t"
        "ds_read_b128 v[28:31], %[g1] offset:33792\n\t"
        "ds_read_b128 v[32:35], %[g1] offset:34816\n\t"
        "ds_read_b128 v[36:39], %[g1] offset:35840\n\t"
        "ds_read_b128 v[40:43], %[g1] offset:36864\n\t"
        "ds_read_b128 v[44:47], %[g1] offset:37888\n\t"
        "ds_read_b128 v[88:91], %[g1] offset:38912\n\t"
        "ds_read_b128 v[92:95], %[g1] offset:39936\n\t"
        "s_branch Lh2_%=\n\t"
        "Lx3_%=:\n\t"
        "s_cmp_lt_i32 %[scn], 1\n\t"
        "s_cbranch_scc1 Ld3_%=\n\t"
        "s_sub_u32 %[scn], %[scn], 1\n\t"
        "s_add_u32 m0, %[sld], 49152\n\t"
        "s_add_u32 %[sb], %[sxo], m0\n\t"
        "s_sub_u32 %[sa], %[sb], 8192\n\t"
        "s_cmp_gt_i32 %[sth], 48\n\t"
        "s_cselect_b32 %[scr], %[sa], %[sb]\n\t"
        "buffer_load_dwordx4 v107, %[ssrd], %[scr] offen offset:0 sc1 lds\n\t"
        "s_cmp_gt_i32 %[sth], 49\n\t"
        "s_cselect_b32 %[scr], %[sa], %[sb]\n\t"
        "buffer_load_dwordx4 v107, %[ssrd], %[scr] offen offset:1024 sc1 lds\n\t"
        "s_cmp_gt_i32 %[sth], 50\n\t"
        "s_cselect_b32 %[scr], %[sa], %[sb]\n\t"
        "buffer_load_dwordx4 v107, %[ssrd], %[scr] offen offset:2048 sc1 lds\n\t"
        "s_cmp_gt_i32 %[sth], 51\n\t"
        "s_cselect_b32 %[scr], %[sa], %[sb]\n\t"
        "buffer_load_dwordx4 v107, %[ssrd], %[scr] offen offset:3072 sc1 lds\n\t"
        "s_add_u32 m0, %[sld], 53248\n\t"
        "s_add_u32 %[sb], %[sxo], m0\n\t"
        "s_sub_u32 %[sa], %[sb], 8192\n\t"
        "s_cmp_gt_i32 %[sth], 52\n\t"
        "s_cselect_b32 %[scr], %[sa], %[sb]\n\t"
        "buffer_load_dwordx4 v107, %[ssrd], %[scr] offen offset:0 sc1 lds\n\t"
        "s_cmp_gt_i32 %[sth], 53\n\t"
        "s_cselect_b32 %[scr], %[sa], %[sb]\n\t"
        "buffer_load_dwordx4 v107, %[ssrd], %[scr] offen offset:1024 sc1 lds\n\t"
        "s_cmp_gt_i32 %[sth], 54\n\t"
        "s_cselect_b32 %[scr], %[sa], %[sb]\n\t"
        "buffer_load_dwordx4 v107, %[ssrd], %[scr] offen offset:2048 sc1 lds\n\t"
        "s_cmp_gt_i32 %[sth], 55\n\t"
        "s_cselect_b32 %[scr], %[sa], %[sb]\n\t"
        "buffer_load_dwordx4 v107, %[ssrd], %[scr] offen offset:3072 sc1 lds\n\t"
        "s_waitcnt vmcnt(0)\n\t"
        "ds_read_b128 v[24:27], %[g1] offset:49152\n\t"
        "ds_read_b128 v[28:31], %[g1] offset:50176\n\t"
        "ds_read_b128 v[32:35], %[g1] offset:51200\n\t"
        "ds_read_b128 v[36:39], %[g1] offset:52224\n\t"
        "ds_read_b128 v[40:43], %[g1] offset:53248\n\t"
        "ds_read_b128 v[44:47], %[g1] offset:54272\n\t"
        "ds_read_b128 v[88:91], %[g1] offset:55296\n\t"
        "ds_read_b128 v[92:95], %[g1] offset:56320\n\t"
        "s_branch Lh3_%=\n\t"
        "Lx4_%=:\n\t"
        "s_cmp_lt_i32 %[scn], 1\n\t"
        "s_cbranch_scc1 Ld4_%=\n\t"
        "s_sub_u32 %[scn], %[scn], 1\n\t"
        "v_cmp_lt_i32 vcc, 0, v96\n\t"
        "s_add_u32 m0, %[slh], 0\n\t"
        "s_add_u32 %[scr], %[sxq], 0\n\t"
        "v_cndmask_b32 v98, v97, %[g0], vcc\n\t"
        "buffer_load_dwordx4 v98, %[ssrd], %[scr] offen sc1 lds\n\t"
        "v_cmp_lt_i32 vcc, 4, v96\n\t"
        "s_add_u32 m0, %[slh], 1024\n\t"
        "s_add_u32 %[scr], %[sxq], 4096\n\t"
        "v_cndmask_b32 v99, v97, %[g0], vcc\n\t"
        "buffer_load_dwordx4 v99, %[ssrd], %[scr] offen sc1 lds\n\t"
        "v_cmp_lt_i32 vcc, 8, v96\n\t"
        "s_add_u32 m0, %[slh], 2048\n\t"
        "s_add_u32 %[scr], %[sxp], 8192\n\t"
        "v_cndmask_b32 v98, v97, %[g0], vcc\n\t"
        "buffer_load_dwordx4 v98, %[ssrd], %[scr] offen sc1 lds\n\t"
        "v_cmp_lt_i32 vcc, 12, v96\n\t"
        "s_add_u32 m0, %[slh], 3072\n\t"
        "s_add_u32 %[scr], %[sxp], 12288\n\t"
        "v_cndmask_b32 v99, v97, %[g0], vcc\n\t"
        "buffer_load_dwordx4 v99, %[ssrd], %[scr] offen sc1 lds\n\t"
        "v_cmp_lt_i32 vcc, 16, v96\n\t"
        "s_add_u32 m0, %[slh], 4096\n\t"
        "s_add_u32 %[scr], %[sxp], 16384\n\t"
        "v_cndmask_b32 v98, v97, %[g0], vcc\n\t"
        "buffer_load_dwordx4 v98, %[ssrd], %[scr] offen sc1 lds\n\t"
        "s_waitcnt vmcnt(0)\n\t"
        "v_add_u32 %[g1], %[slh], v107\n\t"
        "ds_read_b128 v[24:27], %[g1] offset:0\n\t"
        "ds_read_b128 v[28:31], %[g1] offset:1024\n\t"
        "ds_read_b128 v[32:35], %[g1] offset:2048\n\t"
        "ds_read_b128 v[36:39], %[g1] offset:3072\n\t"
        "ds_read_b128 v[40:43], %[g1] offset:4096\n\t"
        "s_branch Lh4_%=\n\t"
        "Lx5_%=:\n\t"
        "s_cmp_lt_i32 %[scn], 1\n\t"
        "s_cbranch_scc1 Ld5_%=\n\t"
        "s_sub_u32 %[scn], %[scn], 1\n\t"
        "v_cmp_lt_i32 vcc, 20, v96\n\t"
        "s_add_u32 m0, %[slh], 5120\n\t"
        "s_add_u32 %[scr], %[sxp], 20480\n\t"
        "v_cndmask_b32 v99, v97, %[g0], vcc\n\t"
        "buffer_load_dwordx4 v99, %[ssrd], %[scr] offen sc1 lds\n\t"
        "v_cmp_lt_i32 vcc, 24, v96\n\t"
        "s_add_u32 m0, %[slh], 6144\n\t"
        "s_add_u32 %[scr], %[sxp], 24576\n\t"
        "v_cndmask_b32 v98, v97, %[g0], vcc\n\t"
        "buffer_load_dwordx4 v98, %[ssrd], %[scr] offen sc1 lds\n\t"
        "v_cmp_lt_i32 vcc, 28, v96\n\t"
        "s_add_u32 m0, %[slh], 7168\n\t"
        "s_add_u32 %[scr], %[sxp], 28672\n\t"
        "v_cndmask_b32 v99, v97, %[g0], vcc\n\t"
        "buffer_load_dwordx4 v99, %[ssrd], %[scr] offen sc1 lds\n\t"
        "v_cmp_lt_i32 vcc, 32, v96\n\t"
        "s_add_u32 m0, %[slh], 8192\n\t"
        "s_add_u32 %[scr], %[sxp], 32768\n\t"
        "v_cndmask_b32 v98, v97, %[g0], vcc\n\t"
        "buffer_load_dwordx4 v98, %[ssrd], %[scr] offen sc1 lds\n\t"
        "s_waitcnt vmcnt(0)\n\t"
        "ds_read_b128 v[24:27], %[g1] offset:5120\n\t"
        "ds_read_b128 v[28:31], %[g1] offset:6144\n\t"
        "ds_read_b128 v[32:35], %[g1] offset:7168\n\t"
        "ds_read_b128 v[36:39], %[g1] offset:8192\n\t"
        "s_branch Lh5_%=\n\t"
        "Le_%=:\n\t"
        : [scr] "=&s"(cr), [sa] "=&s"(sa), [sb] "=&s"(sb), [scn] "+s"(cn), [g0] "=&v"(g0), [g1] "=&v"(g1), [g2] "=&v"(g2)
        : [ssrd] "s"(srd), [sxo] "s"(xo), [sxp] "s"(xp), [sxq] "s"(xq), [sth] "s"(th), [shh] "s"(hh), [sld] "s"(ld), [slh] "s"(lh), [og0] "n"(BC_G0)
        : "memory", "scc", "vcc", "v24", "v25", "v26", "v27", "v28", "v29", "v30", "v31", "v32", "v33", "v34", "v35", "v36", "v37", "v38", "v39", "v40", "v41", "v42", "v43", "v44", "v45", "v46", "v47");
}

DEV void gu_set() {
    asm volatile(
        "v_lshrrev_b32 v110, 5, v107\n\t"
        "v_bfe_u32 v111, v107, 6, 3\n\t"
        "v_lshrrev_b32 v96, 1, v107\n\t"
        "v_and_or_b32 v110, v110, 16, v111\n\t"
        "v_and_b32 v96, 24, v96\n\t"
        "v_mad_u32_u24 v110, v110, 48, v96\n\t"
        "v_add_u32 v110, 98048, v110\n\t"
        "v_add_u32 v111, 27632, v110\n\t"
        "v_cndmask_b32 v111, v110, v111, %[sl2]\n\t"
        :: [sl2] "s"(0xCCCCCCCCCCCCCCCCull) : "memory");
}
DEV void gu_pre() {
    asm volatile(
        "ds_read_b64_tr_b16 v[48:49], v110 offset:0\n\t"
        "ds_read_b64_tr_b16 v[50:51], v110 offset:384\n\t"
        "ds_read_b64_tr_b16 v[52:53], v110 offset:1536\n\t"
        "ds_read_b64_tr_b16 v[54:55], v110 offset:1920\n\t"
        "ds_read_b64_tr_b16 v[56:57], v110 offset:27648\n\t"
        "ds_read_b64_tr_b16 v[58:59], v110 offset:28032\n\t"
        "ds_read_b64_tr_b16 v[60:61], v110 offset:29184\n\t"
        "ds_read_b64_tr_b16 v[62:63], v110 offset:29568\n\t"
        "ds_read_b64_tr_b16 v[64:65], v111 offset:32\n\t"
        "ds_read_b64_tr_b16 v[66:67], v111 offset:416\n\t"
        "ds_read_b64_tr_b16 v[68:69], v111 offset:1568\n\t"
        "ds_read_b64_tr_b16 v[70:71], v111 offset:1952\n\t"
        "ds_read_b64_tr_b16 v[72:73], v110 offset:24576\n\t"
        "ds_read_b64_tr_b16 v[74:75], v110 offset:24960\n\t"
        "ds_read_b64_tr_b16 v[76:77], v110 offset:26112\n\t"
        "ds_read_b64_tr_b16 v[78:79], v110 offset:26496\n\t"
        "ds_read_b64_tr_b16 v[80:81], v110 offset:52224\n\t"
        "ds_read_b64_tr_b16 v[82:83], v110 offset:52608\n\t"
        "ds_read_b64_tr_b16 v[84:85], v110 offset:53760\n\t"
        "ds_read_b64_tr_b16 v[86:87], v110 offset:54144\n\t"
        "ds_read_b64_tr_b16 v[88:89], v111 offset:24608\n\t"
        "ds_read_b64_tr_b16 v[90:91], v111 offset:24992\n\t"
        "ds_read_b64_tr_b16 v[92:93], v111 offset:26144\n\t"
        "ds_read_b64_tr_b16 v[94:95], v111 offset:26528\n\t"
        "ds_read_b64_tr_b16 v[220:221], v110 offset:3072\n\t"
        "ds_read_b64_tr_b16 v[222:223], v110 offset:3456\n\t"
        "ds_read_b64_tr_b16 v[224:225], v110 offset:4608\n\t"
        "ds_read_b64_tr_b16 v[226:227], v110 offset:4992\n\t"
        "ds_read_b64_tr_b16 v[228:229], v110 offset:30720\n\t"
        "ds_read_b64_tr_b16 v[230:231], v110 offset:31104\n\t"
        "ds_read_b64_tr_b16 v[232:233], v110 offset:32256\n\t"
        "ds_read_b64_tr_b16 v[234:235], v110 offset:32640\n\t"
        "ds_read_b64_tr_b16 v[236:237], v111 offset:3104\n\t"
        "ds_read_b64_tr_b16 v[238:239], v111 offset:3488\n\t"
        "ds_read_b64_tr_b16 v[240:241], v111 offset:4640\n\t"
        "ds_read_b64_tr_b16 v[242:243], v111 offset:5024\n\t"
        ::: "memory");
}

DEV void gu_tr(u32 o) {
    asm volatile(
        "v_lshrrev_b32 v32, 5, v107\n\t"
        "v_bfe_u32 v33, v107, 6, 3\n\t"
        "v_lshrrev_b32 v34, 1, v107\n\t"
        "v_and_or_b32 v32, v32, 16, v33\n\t"
        "v_and_b32 v34, 24, v34\n\t"
        "v_mad_u32_u24 v32, v32, 48, v34\n\t"
        "v_add_u32 v32, %[so], v32\n\t"
        "v_add_u32 v33, 27632, v32\n\t"
        "v_cndmask_b32 v33, v32, v33, %[sl2]\n\t"
        "v_add_u32 v34, %[so], v107\n\t"
        "ds_read_b64_tr_b16 v[48:49], v32 offset:0\n\t"
        "ds_read_b64_tr_b16 v[50:51], v32 offset:384\n\t"
        "ds_read_b64_tr_b16 v[52:53], v32 offset:1536\n\t"
        "ds_read_b64_tr_b16 v[54:55], v32 offset:1920\n\t"
        "ds_read_b64_tr_b16 v[56:57], v32 offset:27648\n\t"
        "ds_read_b64_tr_b16 v[58:59], v32 offset:28032\n\t"
        "ds_read_b64_tr_b16 v[60:61], v32 offset:29184\n\t"
        "ds_read_b64_tr_b16 v[62:63], v32 offset:29568\n\t"
        "ds_read_b64_tr_b16 v[64:65], v33 offset:32\n\t"
        "ds_read_b64_tr_b16 v[66:67], v33 offset:416\n\t"
        "ds_read_b64_tr_b16 v[68:69], v33 offset:1568\n\t"
        "ds_read_b64_tr_b16 v[70:71], v33 offset:1952\n\t"
        "ds_read_b64_tr_b16 v[72:73], v32 offset:3072\n\t"
        "ds_read_b64_tr_b16 v[74:75], v32 offset:3456\n\t"
        "ds_read_b64_tr_b16 v[76:77], v32 offset:4608\n\t"
        "ds_read_b64_tr_b16 v[78:79], v32 offset:4992\n\t"
        "ds_read_b64_tr_b16 v[80:81], v32 offset:30720\n\t"
        "ds_read_b64_tr_b16 v[82:83], v32 offset:31104\n\t"
        "ds_read_b64_tr_b16 v[84:85], v32 offset:32256\n\t"
        "ds_read_b64_tr_b16 v[86:87], v32 offset:32640\n\t"
        "ds_read_b64_tr_b16 v[24:25], v33 offset:3104\n\t"
        "ds_read_b64_tr_b16 v[26:27], v33 offset:3488\n\t"
        "ds_read_b64_tr_b16 v[28:29], v33 offset:4640\n\t"
        "ds_read_b64_tr_b16 v[30:31], v33 offset:5024\n\t"
        "s_waitcnt lgkmcnt(12)\n\t"
        "ds_write_b128 v34, v[48:51] offset:0\n\t"
        "ds_write_b128 v34, v[52:55] offset:1024\n\t"
        "ds_write_b128 v34, v[56:59] offset:27648\n\t"
        "ds_write_b128 v34, v[60:63] offset:28672\n\t"
        "ds_write_b128 v34, v[64:67] offset:2048\n\t"
        "ds_write_b128 v34, v[68:71] offset:29696\n\t"
        "s_waitcnt lgkmcnt(6)\n\t"
        "ds_write_b128 v34, v[72:75] offset:3072\n\t"
        "ds_write_b128 v34, v[76:79] offset:4096\n\t"
        "ds_write_b128 v34, v[80:83] offset:30720\n\t"
        "ds_write_b128 v34, v[84:87] offset:31744\n\t"
        "ds_write_b128 v34, v[24:27] offset:5120\n\t"
        "ds_write_b128 v34, v[28:31] offset:32768\n\t"
        :: [so] "s"(o), [sl2] "s"(0xCCCCCCCCCCCCCCCCull) : "memory", "v24", "v25", "v26", "v27", "v28", "v29", "v30", "v31", "v32", "v33", "v34");
}

DEV void ffn(u32x4 srd, u32 cr, u32 c0, u32 c1, u32 ya, u32 ye, u32 yb, u32 re, float& m, float& rs, u32& cn) {
    u32 ap[12];
    asm volatile(
        "s_nop 4\n\t"
        "Ls_%=:\n\t"
        "buffer_load_dwordx4 v[24:27], %[c0], %[ssrd], %[scr] offen sc1\n\t"
        "buffer_load_dwordx4 v[28:31], %[c1], %[ssrd], %[scr] offen sc1\n\t"
        "v_mov_b32 v32, 0\n\t"
        "v_mov_b32 v33, 0\n\t"
        "v_mov_b32 v34, 0\n\t"
        "v_mov_b32 v35, 0\n\t"
        "v_mov_b32 v36, 0\n\t"
        "v_mov_b32 v37, 0\n\t"
        "v_mov_b32 v38, 0\n\t"
        "v_mov_b32 v39, 0\n\t"
        "v_mov_b32 v40, 0\n\t"
        "v_mov_b32 v41, 0\n\t"
        "v_mov_b32 v42, 0\n\t"
        "v_mov_b32 v43, 0\n\t"
        "v_mov_b32 v96, 0x3f803f80\n\t"
        "v_mov_b32 v97, 0x3f803f80\n\t"
        "v_mov_b32 v98, 0x3f803f80\n\t"
        "v_mov_b32 v99, 0x3f803f80\n\t"
        "v_mov_b32 v252, 0x3f803f80\n\t"
        "v_mov_b32 v253, 0x3f803f80\n\t"
        "v_mov_b32 v254, 0x3f803f80\n\t"
        "v_mov_b32 v255, 0x3f803f80\n\t"
        "v_mov_b32 v44, 0x4444\n\t"
        "v_mov_b32 v45, 0xeeee\n\t"
        "v_cndmask_b32 v44, v44, v45, %[spar]\n\t"
        "v_add_u32 v45, 98048, v107\n\t"
        "s_cmp_lg_u32 %[sre], 0\n\t"
        "s_cbranch_scc1 Lr_%=\n\t"
        "s_waitcnt vmcnt(0)\n\t"
        "Lb_%=:\n\t"
        "v_mov_b32_dpp v96, v24 quad_perm:[0,1,2,3] row_mask:0xf bank_mask:0x1 bound_ctrl:1\n\t"
        "v_mov_b32_dpp v97, v25 quad_perm:[0,1,2,3] row_mask:0xf bank_mask:0x1 bound_ctrl:1\n\t"
        "v_mov_b32_dpp v98, v26 quad_perm:[0,1,2,3] row_mask:0xf bank_mask:0x1 bound_ctrl:1\n\t"
        "v_mov_b32_dpp v99, v27 quad_perm:[0,1,2,3] row_mask:0xf bank_mask:0x1 bound_ctrl:1\n\t"
        "v_max3_u32 v46, v24, v25, v26\n\t"
        "v_max3_u32 v46, v46, v27, v28\n\t"
        "v_smfmac_f32_16x16x64_bf16 v[32:35], v[96:99], v[48:55], v44\n\t"
        "v_mov_b32_dpp v252, v28 quad_perm:[0,1,2,3] row_mask:0xf bank_mask:0x1 bound_ctrl:1\n\t"
        "v_mov_b32_dpp v253, v29 quad_perm:[0,1,2,3] row_mask:0xf bank_mask:0x1 bound_ctrl:1\n\t"
        "ds_read_b128 v[48:51], v45 offset:33792\n\t"
        "ds_read_b128 v[52:55], v45 offset:34816\n\t"
        "v_smfmac_f32_16x16x64_bf16 v[36:39], v[96:99], v[56:63], v44\n\t"
        "v_mov_b32_dpp v254, v30 quad_perm:[0,1,2,3] row_mask:0xf bank_mask:0x1 bound_ctrl:1\n\t"
        "v_mov_b32_dpp v255, v31 quad_perm:[0,1,2,3] row_mask:0xf bank_mask:0x1 bound_ctrl:1\n\t"
        "ds_read_b128 v[56:59], v45 offset:8192\n\t"
        "ds_read_b128 v[60:63], v45 offset:35840\n\t"
        "v_smfmac_f32_16x16x64_bf16 v[40:43], v[96:99], v[64:71], v44\n\t"
        "v_max3_u32 v46, v46, v29, v30\n\t"
        "v_max_u32 v46, v46, v31\n\t"
        "ds_read_b128 v[64:67], v45 offset:9216\n\t"
        "ds_read_b128 v[68:71], v45 offset:10240\n\t"
        "v_smfmac_f32_16x16x64_bf16 v[32:35], v[252:255], v[72:79], v44\n\t"
        "v_mov_b32_dpp v96, v24 row_shl:2 row_mask:0xf bank_mask:0x1 bound_ctrl:1\n\t"
        "v_mov_b32_dpp v97, v25 row_shl:2 row_mask:0xf bank_mask:0x1 bound_ctrl:1\n\t"
        "ds_read_b128 v[72:75], v45 offset:36864\n\t"
        "ds_read_b128 v[76:79], v45 offset:37888\n\t"
        "v_smfmac_f32_16x16x64_bf16 v[36:39], v[252:255], v[80:87], v44\n\t"
        "v_mov_b32_dpp v98, v26 row_shl:2 row_mask:0xf bank_mask:0x1 bound_ctrl:1\n\t"
        "v_mov_b32_dpp v99, v27 row_shl:2 row_mask:0xf bank_mask:0x1 bound_ctrl:1\n\t"
        "ds_read_b128 v[80:83], v45 offset:11264\n\t"
        "ds_read_b128 v[84:87], v45 offset:38912\n\t"
        "v_smfmac_f32_16x16x64_bf16 v[40:43], v[252:255], v[88:95], v44\n\t"
        "v_cmp_eq_u32 vcc, -1, v46\n\t"
        "ds_read_b128 v[88:91], v45 offset:12288\n\t"
        "ds_read_b128 v[92:95], v45 offset:13312\n\t"
        "v_smfmac_f32_16x16x64_bf16 v[32:35], v[96:99], v[220:227], v44\n\t"
        "v_mov_b32_dpp v252, v24 row_shl:4 row_mask:0xf bank_mask:0x1 bound_ctrl:1\n\t"
        "v_mov_b32_dpp v253, v25 row_shl:4 row_mask:0xf bank_mask:0x1 bound_ctrl:1\n\t"
        "ds_read_b128 v[220:223], v45 offset:39936\n\t"
        "ds_read_b128 v[224:227], v45 offset:40960\n\t"
        "v_smfmac_f32_16x16x64_bf16 v[36:39], v[96:99], v[228:235], v44\n\t"
        "v_mov_b32_dpp v254, v26 row_shl:4 row_mask:0xf bank_mask:0x1 bound_ctrl:1\n\t"
        "ds_read_b128 v[228:231], v45 offset:14336\n\t"
        "ds_read_b128 v[232:235], v45 offset:41984\n\t"
        "s_cbranch_vccnz Ly_%=\n\t"
        "Lz_%=:\n\t"
        "v_smfmac_f32_16x16x64_bf16 v[40:43], v[96:99], v[236:243], v44\n\t"
        "v_mov_b32_dpp v255, v27 row_shl:4 row_mask:0xf bank_mask:0x1 bound_ctrl:1\n\t"
        "ds_read_b128 v[236:239], v45 offset:15360\n\t"
        "ds_read_b128 v[240:243], v45 offset:16384\n\t"
        "v_smfmac_f32_16x16x64_bf16 v[32:35], v[252:255], v[244:251], v44\n\t"
        "v_mov_b32_dpp v96, v24 row_shl:6 row_mask:0xf bank_mask:0x1 bound_ctrl:1\n\t"
        "v_mov_b32_dpp v97, v25 row_shl:6 row_mask:0xf bank_mask:0x1 bound_ctrl:1\n\t"
        "ds_read_b128 v[244:247], v45 offset:43008\n\t"
        "ds_read_b128 v[248:251], v45 offset:44032\n\t"
        "s_waitcnt lgkmcnt(15)\n\t"
        "v_smfmac_f32_16x16x64_bf16 v[36:39], v[252:255], v[48:55], v44\n\t"
        "v_mov_b32_dpp v98, v26 row_shl:6 row_mask:0xf bank_mask:0x1 bound_ctrl:1\n\t"
        "ds_read_b128 v[48:51], v45 offset:17408\n\t"
        "ds_read_b128 v[52:55], v45 offset:45056\n\t"
        "v_smfmac_f32_16x16x64_bf16 v[40:43], v[252:255], v[56:63], v44\n\t"
        "v_mov_b32_dpp v99, v27 row_shl:6 row_mask:0xf bank_mask:0x1 bound_ctrl:1\n\t"
        "ds_read_b128 v[56:59], v45 offset:18432\n\t"
        "ds_read_b128 v[60:63], v45 offset:19456\n\t"
        "s_waitcnt lgkmcnt(15)\n\t"
        "v_smfmac_f32_16x16x64_bf16 v[32:35], v[96:99], v[64:71], v44\n\t"
        "v_mov_b32_dpp v252, v24 row_shl:8 row_mask:0xf bank_mask:0x1 bound_ctrl:1\n\t"
        "v_mov_b32_dpp v253, v25 row_shl:8 row_mask:0xf bank_mask:0x1 bound_ctrl:1\n\t"
        "ds_read_b128 v[64:67], v45 offset:46080\n\t"
        "ds_read_b128 v[68:71], v45 offset:47104\n\t"
        "v_smfmac_f32_16x16x64_bf16 v[36:39], v[96:99], v[72:79], v44\n\t"
        "v_mov_b32_dpp v254, v26 row_shl:8 row_mask:0xf bank_mask:0x1 bound_ctrl:1\n\t"
        "ds_read_b128 v[72:75], v45 offset:20480\n\t"
        "ds_read_b128 v[76:79], v45 offset:48128\n\t"
        "s_waitcnt lgkmcnt(15)\n\t"
        "v_smfmac_f32_16x16x64_bf16 v[40:43], v[96:99], v[80:87], v44\n\t"
        "v_mov_b32_dpp v255, v27 row_shl:8 row_mask:0xf bank_mask:0x1 bound_ctrl:1\n\t"
        "ds_read_b128 v[80:83], v45 offset:21504\n\t"
        "ds_read_b128 v[84:87], v45 offset:22528\n\t"
        "v_smfmac_f32_16x16x64_bf16 v[32:35], v[252:255], v[88:95], v44\n\t"
        "v_mov_b32_dpp v96, v24 row_shl:10 row_mask:0xf bank_mask:0x1 bound_ctrl:1\n\t"
        "v_mov_b32_dpp v97, v25 row_shl:10 row_mask:0xf bank_mask:0x1 bound_ctrl:1\n\t"
        "ds_read_b128 v[88:91], v45 offset:49152\n\t"
        "ds_read_b128 v[92:95], v45 offset:50176\n\t"
        "s_waitcnt lgkmcnt(15)\n\t"
        "v_smfmac_f32_16x16x64_bf16 v[36:39], v[252:255], v[220:227], v44\n\t"
        "v_mov_b32_dpp v98, v26 row_shl:10 row_mask:0xf bank_mask:0x1 bound_ctrl:1\n\t"
        "ds_read_b128 v[220:223], v45 offset:23552\n\t"
        "ds_read_b128 v[224:227], v45 offset:51200\n\t"
        "v_smfmac_f32_16x16x64_bf16 v[40:43], v[252:255], v[228:235], v44\n\t"
        "v_mov_b32_dpp v99, v27 row_shl:10 row_mask:0xf bank_mask:0x1 bound_ctrl:1\n\t"
        "s_waitcnt lgkmcnt(15)\n\t"
        "s_nop 0\n\t"
        "v_smfmac_f32_16x16x64_bf16 v[32:35], v[96:99], v[236:243], v44\n\t"
        "v_mov_b32_dpp v252, v24 row_shl:12 row_mask:0xf bank_mask:0x1 bound_ctrl:1\n\t"
        "v_mov_b32_dpp v253, v25 row_shl:12 row_mask:0xf bank_mask:0x1 bound_ctrl:1\n\t"
        "s_waitcnt lgkmcnt(14)\n\t"
        "v_smfmac_f32_16x16x64_bf16 v[36:39], v[96:99], v[244:251], v44\n\t"
        "v_mov_b32_dpp v254, v26 row_shl:12 row_mask:0xf bank_mask:0x1 bound_ctrl:1\n\t"
        "s_waitcnt lgkmcnt(12)\n\t"
        "v_smfmac_f32_16x16x64_bf16 v[40:43], v[96:99], v[48:55], v44\n\t"
        "v_mov_b32_dpp v255, v27 row_shl:12 row_mask:0xf bank_mask:0x1 bound_ctrl:1\n\t"
        "s_waitcnt lgkmcnt(10)\n\t"
        "s_nop 0\n\t"
        "v_smfmac_f32_16x16x64_bf16 v[32:35], v[252:255], v[56:63], v44\n\t"
        "v_mov_b32_dpp v96, v24 row_shl:14 row_mask:0xf bank_mask:0x1 bound_ctrl:1\n\t"
        "v_mov_b32_dpp v97, v25 row_shl:14 row_mask:0xf bank_mask:0x1 bound_ctrl:1\n\t"
        "s_waitcnt lgkmcnt(8)\n\t"
        "v_smfmac_f32_16x16x64_bf16 v[36:39], v[252:255], v[64:71], v44\n\t"
        "v_mov_b32_dpp v98, v26 row_shl:14 row_mask:0xf bank_mask:0x1 bound_ctrl:1\n\t"
        "s_waitcnt lgkmcnt(6)\n\t"
        "v_smfmac_f32_16x16x64_bf16 v[40:43], v[252:255], v[72:79], v44\n\t"
        "v_mov_b32_dpp v99, v27 row_shl:14 row_mask:0xf bank_mask:0x1 bound_ctrl:1\n\t"
        "s_waitcnt lgkmcnt(0)\n\t"
        "s_nop 0\n\t"
        "v_smfmac_f32_16x16x64_bf16 v[40:43], v[96:99], v[220:227], v44\n\t"
        "v_readlane_b32 %[sa0], v28, 2\n\t"
        "v_smfmac_f32_16x16x64_bf16 v[32:35], v[96:99], v[80:87], v44\n\t"
        "v_readlane_b32 %[sa1], v29, 2\n\t"
        "v_smfmac_f32_16x16x64_bf16 v[36:39], v[96:99], v[88:95], v44\n\t"
        "s_nop 3\n\t"
        "v_add_f32 v40, v40, v41\n\t"
        "s_nop 0\n\t"
        "v_add_f32 v46, v32, v33\n\t"
        "v_mov_b32_dpp v41, v40 row_shl:8 row_mask:0xf bank_mask:0xf bound_ctrl:1\n\t"
        "v_add_f32 v47, v36, v37\n\t"
        "v_permlane16_swap_b32 v46, v40\n\t"
        "s_nop 0\n\t"
        "v_permlane16_swap_b32 v47, v41\n\t"
        "v_fma_f32 v46, -%[sa0], v40, v46\n\t"
        "v_fma_f32 v47, -%[sa0], v41, v47\n\t"
        "v_mul_f32 v46, %[sa1], v46\n\t"
        "v_mul_f32 v47, %[sa1], v47\n\t"
        "v_mul_f32 v44, 0xbfb8aa3b, v46\n\t"
        "v_exp_f32 v44, v44\n\t"
        "v_mul_f32 v46, v46, v47\n\t"
        "v_add_f32 v44, 1.0, v44\n\t"
        "v_rcp_f32 v44, v44\n\t"
        "s_mov_b32 %[sm], %[sa0]\n\t"
        "s_mov_b32 %[srs], %[sa1]\n\t"
        "v_mul_f32 v46, v46, v44\n\t"
        "v_mov_b32 v60, 0\n\t"
        "v_mov_b32 v61, 0\n\t"
        "v_mov_b32_dpp v47, v46 row_shl:1 row_mask:0xf bank_mask:0xf bound_ctrl:1\n\t"
        "v_cvt_pk_bf16_f32 v46, v46, v47\n\t"
        "v_mov_b32 v62, 0\n\t"
        "v_readlane_b32 %[sa0], v46, 0\n\t"
        "v_mov_b32 v63, 0\n\t"
        "v_readlane_b32 %[sa1], v46, 2\n\t"
        "v_mov_b32 v64, 0\n\t"
        "v_readlane_b32 %[sa2], v46, 4\n\t"
        "v_mov_b32 v65, 0\n\t"
        "v_readlane_b32 %[sa3], v46, 6\n\t"
        "v_mov_b32 v66, 0\n\t"
        "v_readlane_b32 %[sa4], v46, 8\n\t"
        "v_mov_b32 v67, 0\n\t"
        "v_readlane_b32 %[sa5], v46, 10\n\t"
        "v_mov_b32 v68, 0\n\t"
        "v_readlane_b32 %[sa6], v46, 12\n\t"
        "v_readlane_b32 %[sa7], v46, 14\n\t"
        "v_readlane_b32 %[sa8], v46, 16\n\t"
        "v_readlane_b32 %[sa9], v46, 18\n\t"
        "v_readlane_b32 %[sa10], v46, 20\n\t"
        "v_readlane_b32 %[sa11], v46, 22\n\t"
        "v_dot2c_f32_bf16 v60, %[sa0], v112\n\t"
        "v_dot2c_f32_bf16 v61, %[sa0], v116\n\t"
        "v_dot2c_f32_bf16 v62, %[sa0], v113\n\t"
        "v_dot2c_f32_bf16 v63, %[sa0], v117\n\t"
        "v_dot2c_f32_bf16 v60, %[sa1], v120\n\t"
        "v_dot2c_f32_bf16 v61, %[sa1], v124\n\t"
        "v_dot2c_f32_bf16 v62, %[sa1], v121\n\t"
        "v_dot2c_f32_bf16 v63, %[sa1], v125\n\t"
        "v_dot2c_f32_bf16 v60, %[sa2], v128\n\t"
        "v_dot2c_f32_bf16 v61, %[sa2], v132\n\t"
        "v_dot2c_f32_bf16 v62, %[sa2], v129\n\t"
        "v_dot2c_f32_bf16 v63, %[sa2], v133\n\t"
        "v_dot2c_f32_bf16 v60, %[sa3], v136\n\t"
        "v_dot2c_f32_bf16 v61, %[sa3], v140\n\t"
        "v_dot2c_f32_bf16 v62, %[sa3], v137\n\t"
        "v_dot2c_f32_bf16 v63, %[sa3], v141\n\t"
        "v_dot2c_f32_bf16 v60, %[sa4], v144\n\t"
        "v_dot2c_f32_bf16 v61, %[sa4], v148\n\t"
        "v_dot2c_f32_bf16 v62, %[sa4], v145\n\t"
        "v_dot2c_f32_bf16 v63, %[sa4], v149\n\t"
        "v_dot2c_f32_bf16 v60, %[sa5], v152\n\t"
        "v_dot2c_f32_bf16 v61, %[sa5], v156\n\t"
        "v_dot2c_f32_bf16 v62, %[sa5], v153\n\t"
        "v_dot2c_f32_bf16 v63, %[sa5], v157\n\t"
        "v_dot2c_f32_bf16 v60, %[sa6], v160\n\t"
        "v_dot2c_f32_bf16 v61, %[sa6], v164\n\t"
        "v_dot2c_f32_bf16 v62, %[sa6], v161\n\t"
        "v_dot2c_f32_bf16 v63, %[sa6], v165\n\t"
        "v_dot2c_f32_bf16 v60, %[sa7], v168\n\t"
        "v_dot2c_f32_bf16 v61, %[sa7], v172\n\t"
        "v_dot2c_f32_bf16 v62, %[sa7], v169\n\t"
        "v_dot2c_f32_bf16 v63, %[sa7], v173\n\t"
        "v_dot2c_f32_bf16 v60, %[sa8], v176\n\t"
        "v_dot2c_f32_bf16 v61, %[sa8], v180\n\t"
        "v_dot2c_f32_bf16 v62, %[sa8], v177\n\t"
        "v_dot2c_f32_bf16 v63, %[sa8], v181\n\t"
        "v_dot2c_f32_bf16 v60, %[sa9], v184\n\t"
        "v_dot2c_f32_bf16 v61, %[sa9], v188\n\t"
        "v_dot2c_f32_bf16 v62, %[sa9], v185\n\t"
        "v_dot2c_f32_bf16 v63, %[sa9], v189\n\t"
        "v_dot2c_f32_bf16 v60, %[sa10], v192\n\t"
        "v_dot2c_f32_bf16 v61, %[sa10], v196\n\t"
        "v_dot2c_f32_bf16 v62, %[sa10], v193\n\t"
        "v_dot2c_f32_bf16 v63, %[sa10], v197\n\t"
        "v_dot2c_f32_bf16 v60, %[sa11], v200\n\t"
        "v_dot2c_f32_bf16 v61, %[sa11], v204\n\t"
        "v_dot2c_f32_bf16 v62, %[sa11], v201\n\t"
        "v_dot2c_f32_bf16 v63, %[sa11], v205\n\t"
        "v_dot2c_f32_bf16 v64, %[sa0], v114\n\t"
        "v_dot2c_f32_bf16 v65, %[sa0], v118\n\t"
        "v_dot2c_f32_bf16 v66, %[sa0], v115\n\t"
        "v_dot2c_f32_bf16 v67, %[sa0], v119\n\t"
        "v_dot2c_f32_bf16 v68, %[sa0], v208\n\t"
        "v_min_u32 v60, -2, v60\n\t"
        "v_min_u32 v61, -2, v61\n\t"
        "v_min_u32 v62, -2, v62\n\t"
        "v_min_u32 v63, -2, v63\n\t"
        "buffer_store_dwordx4 v[60:63], %[ya], %[ssrd], %[syb] offen sc1\n\t"
        "v_dot2c_f32_bf16 v64, %[sa1], v122\n\t"
        "v_dot2c_f32_bf16 v65, %[sa1], v126\n\t"
        "v_dot2c_f32_bf16 v66, %[sa1], v123\n\t"
        "v_dot2c_f32_bf16 v67, %[sa1], v127\n\t"
        "v_dot2c_f32_bf16 v68, %[sa1], v209\n\t"
        "v_dot2c_f32_bf16 v64, %[sa2], v130\n\t"
        "v_dot2c_f32_bf16 v65, %[sa2], v134\n\t"
        "v_dot2c_f32_bf16 v66, %[sa2], v131\n\t"
        "v_dot2c_f32_bf16 v67, %[sa2], v135\n\t"
        "v_dot2c_f32_bf16 v68, %[sa2], v210\n\t"
        "v_dot2c_f32_bf16 v64, %[sa3], v138\n\t"
        "v_dot2c_f32_bf16 v65, %[sa3], v142\n\t"
        "v_dot2c_f32_bf16 v66, %[sa3], v139\n\t"
        "v_dot2c_f32_bf16 v67, %[sa3], v143\n\t"
        "v_dot2c_f32_bf16 v68, %[sa3], v211\n\t"
        "v_dot2c_f32_bf16 v64, %[sa4], v146\n\t"
        "v_dot2c_f32_bf16 v65, %[sa4], v150\n\t"
        "v_dot2c_f32_bf16 v66, %[sa4], v147\n\t"
        "v_dot2c_f32_bf16 v67, %[sa4], v151\n\t"
        "v_dot2c_f32_bf16 v68, %[sa4], v212\n\t"
        "v_dot2c_f32_bf16 v64, %[sa5], v154\n\t"
        "v_dot2c_f32_bf16 v65, %[sa5], v158\n\t"
        "v_dot2c_f32_bf16 v66, %[sa5], v155\n\t"
        "v_dot2c_f32_bf16 v67, %[sa5], v159\n\t"
        "v_dot2c_f32_bf16 v68, %[sa5], v213\n\t"
        "v_dot2c_f32_bf16 v64, %[sa6], v162\n\t"
        "v_dot2c_f32_bf16 v65, %[sa6], v166\n\t"
        "v_dot2c_f32_bf16 v66, %[sa6], v163\n\t"
        "v_dot2c_f32_bf16 v67, %[sa6], v167\n\t"
        "v_dot2c_f32_bf16 v68, %[sa6], v214\n\t"
        "v_dot2c_f32_bf16 v64, %[sa7], v170\n\t"
        "v_dot2c_f32_bf16 v65, %[sa7], v174\n\t"
        "v_dot2c_f32_bf16 v66, %[sa7], v171\n\t"
        "v_dot2c_f32_bf16 v67, %[sa7], v175\n\t"
        "v_dot2c_f32_bf16 v68, %[sa7], v215\n\t"
        "v_dot2c_f32_bf16 v64, %[sa8], v178\n\t"
        "v_dot2c_f32_bf16 v65, %[sa8], v182\n\t"
        "v_dot2c_f32_bf16 v66, %[sa8], v179\n\t"
        "v_dot2c_f32_bf16 v67, %[sa8], v183\n\t"
        "v_dot2c_f32_bf16 v68, %[sa8], v216\n\t"
        "v_dot2c_f32_bf16 v64, %[sa9], v186\n\t"
        "v_dot2c_f32_bf16 v65, %[sa9], v190\n\t"
        "v_dot2c_f32_bf16 v66, %[sa9], v187\n\t"
        "v_dot2c_f32_bf16 v67, %[sa9], v191\n\t"
        "v_dot2c_f32_bf16 v68, %[sa9], v217\n\t"
        "v_dot2c_f32_bf16 v64, %[sa10], v194\n\t"
        "v_dot2c_f32_bf16 v65, %[sa10], v198\n\t"
        "v_dot2c_f32_bf16 v66, %[sa10], v195\n\t"
        "v_dot2c_f32_bf16 v67, %[sa10], v199\n\t"
        "v_dot2c_f32_bf16 v68, %[sa10], v218\n\t"
        "v_dot2c_f32_bf16 v64, %[sa11], v202\n\t"
        "v_dot2c_f32_bf16 v65, %[sa11], v206\n\t"
        "v_dot2c_f32_bf16 v66, %[sa11], v203\n\t"
        "v_dot2c_f32_bf16 v67, %[sa11], v207\n\t"
        "v_dot2c_f32_bf16 v68, %[sa11], v219\n\t"
        "v_min_u32 v64, -2, v64\n\t"
        "v_min_u32 v65, -2, v65\n\t"
        "v_min_u32 v66, -2, v66\n\t"
        "v_min_u32 v67, -2, v67\n\t"
        "v_min_u32 v68, -2, v68\n\t"
        "buffer_store_dwordx4 v[64:67], %[ya], %[ssrd], %[syc] offen sc1\n\t"
        "buffer_store_dword v68, %[ye], %[ssrd], %[syb] offen sc1\n\t"
        "v_add_u32 v45, 157472, %[c0]\n\t"
        "ds_write_b128 v45, v[24:27]\n\t"
        "s_mov_b32 exec_lo, 0x30003\n\t"
        "s_mov_b32 exec_hi, 0x30003\n\t"
        "ds_write_b128 v45, v[28:31] offset:1024\n\t"
        "v_mov_b32 v44, 153984\n\t"
        "v_mov_b32 v46, 1\n\t"
        "s_mov_b64 exec, 1\n\t"
        "ds_add_u32 v44, v46 offset:108\n\t"
        "s_mov_b64 exec, -1\n\t"
        "s_branch Le_%=\n\t"
        "Ly_%=:\n\t"
        "s_cmp_lt_i32 %[scn], 1\n\t"
        "s_cbranch_scc1 Lz_%=\n\t"
        "s_sub_u32 %[scn], %[scn], 16\n\t"
        "s_mov_b32 %[sre], 1\n\t"
        "s_nop 7\n\t"
        "s_nop 7\n\t"
        "s_waitcnt lgkmcnt(0)\n\t"
        "s_branch Ls_%=\n\t"
        "Lr_%=:\n\t"
        "ds_read_b64_tr_b16 v[64:65], v111 offset:32\n\t"
        "ds_read_b64_tr_b16 v[66:67], v111 offset:416\n\t"
        "ds_read_b64_tr_b16 v[68:69], v111 offset:1568\n\t"
        "ds_read_b64_tr_b16 v[70:71], v111 offset:1952\n\t"
        "ds_read_b64_tr_b16 v[72:73], v110 offset:24576\n\t"
        "ds_read_b64_tr_b16 v[74:75], v110 offset:24960\n\t"
        "ds_read_b64_tr_b16 v[76:77], v110 offset:26112\n\t"
        "ds_read_b64_tr_b16 v[78:79], v110 offset:26496\n\t"
        "ds_read_b64_tr_b16 v[80:81], v110 offset:52224\n\t"
        "ds_read_b64_tr_b16 v[82:83], v110 offset:52608\n\t"
        "ds_read_b64_tr_b16 v[84:85], v110 offset:53760\n\t"
        "ds_read_b64_tr_b16 v[86:87], v110 offset:54144\n\t"
        "ds_read_b64_tr_b16 v[88:89], v111 offset:24608\n\t"
        "ds_read_b64_tr_b16 v[90:91], v111 offset:24992\n\t"
        "ds_read_b64_tr_b16 v[92:93], v111 offset:26144\n\t"
        "ds_read_b64_tr_b16 v[94:95], v111 offset:26528\n\t"
        "ds_read_b64_tr_b16 v[220:221], v110 offset:3072\n\t"
        "ds_read_b64_tr_b16 v[222:223], v110 offset:3456\n\t"
        "ds_read_b64_tr_b16 v[224:225], v110 offset:4608\n\t"
        "ds_read_b64_tr_b16 v[226:227], v110 offset:4992\n\t"
        "ds_read_b64_tr_b16 v[228:229], v110 offset:30720\n\t"
        "ds_read_b64_tr_b16 v[230:231], v110 offset:31104\n\t"
        "ds_read_b64_tr_b16 v[232:233], v110 offset:32256\n\t"
        "ds_read_b64_tr_b16 v[234:235], v110 offset:32640\n\t"
        "ds_read_b64_tr_b16 v[236:237], v111 offset:3104\n\t"
        "ds_read_b64_tr_b16 v[238:239], v111 offset:3488\n\t"
        "ds_read_b64_tr_b16 v[240:241], v111 offset:4640\n\t"
        "ds_read_b64_tr_b16 v[242:243], v111 offset:5024\n\t"
        "ds_read_b128 v[244:247], v45 offset:6144\n\t"
        "ds_read_b128 v[248:251], v45 offset:7168\n\t"
        "s_waitcnt vmcnt(0)\n\t"
        "ds_read_b64_tr_b16 v[48:49], v110 offset:0\n\t"
        "ds_read_b64_tr_b16 v[50:51], v110 offset:384\n\t"
        "ds_read_b64_tr_b16 v[52:53], v110 offset:1536\n\t"
        "ds_read_b64_tr_b16 v[54:55], v110 offset:1920\n\t"
        "ds_read_b64_tr_b16 v[56:57], v110 offset:27648\n\t"
        "ds_read_b64_tr_b16 v[58:59], v110 offset:28032\n\t"
        "ds_read_b64_tr_b16 v[60:61], v110 offset:29184\n\t"
        "ds_read_b64_tr_b16 v[62:63], v110 offset:29568\n\t"
        "s_waitcnt lgkmcnt(0)\n\t"
        "s_branch Lb_%=\n\t"
        "Le_%=:\n\t"
        : [sa0] "=&s"(ap[0]), [sa1] "=&s"(ap[1]), [sa2] "=&s"(ap[2]), [sa3] "=&s"(ap[3]), [sa4] "=&s"(ap[4]), [sa5] "=&s"(ap[5]), [sa6] "=&s"(ap[6]), [sa7] "=&s"(ap[7]), [sa8] "=&s"(ap[8]), [sa9] "=&s"(ap[9]), [sa10] "=&s"(ap[10]), [sa11] "=&s"(ap[11]), [sm] "=&s"(m), [srs] "=&s"(rs), [sre] "+s"(re), [scn] "+s"(cn)
        : [ssrd] "s"(srd), [scr] "s"(cr), [c0] "v"(c0), [c1] "v"(c1), [ya] "v"(ya), [ye] "v"(ye),
          [syb] "s"(yb), [syc] "s"(yb + 262144), [spar] "s"(0xAAAAAAAAAAAAAAAAull)
        : "memory", "scc", "vcc", "v24", "v25", "v26", "v27", "v28", "v29", "v30", "v31", "v32", "v33", "v34", "v35", "v36", "v37", "v38", "v39", "v40", "v41", "v42", "v43", "v44", "v45", "v46", "v47");
}

DEV void poll_stats(u32x4 srd, u32 va, u32 so, float (&o)[3], u32& cn) {
    u32 t;
    asm volatile(
        "s_nop 4\n\t"
        "buffer_load_dwordx4 v[24:27], %[va], %[ssrd], %[so] offen offset:0 sc1\n\t"
        "buffer_load_dwordx4 v[28:31], %[va], %[ssrd], %[so] offen offset:1024 sc1\n\t"
        "buffer_load_dwordx4 v[32:35], %[va], %[ssrd], %[so] offen offset:256 sc1\n\t"
        "buffer_load_dwordx4 v[36:39], %[va], %[ssrd], %[so] offen offset:1280 sc1\n\t"
        "buffer_load_dwordx4 v[40:43], %[va], %[ssrd], %[so] offen offset:512 sc1\n\t"
        "buffer_load_dwordx4 v[44:47], %[va], %[ssrd], %[so] offen offset:1536 sc1\n\t"
        "buffer_load_dwordx4 v[48:51], %[va], %[ssrd], %[so] offen offset:768 sc1\n\t"
        "buffer_load_dwordx4 v[68:71], %[va], %[ssrd], %[so] offen offset:1792 sc1\n\t"
        "Lp_%=:\n\t"
        "s_waitcnt vmcnt(0)\n\t"
        "v_max3_u32 v27, v24, v25, v26\n\t"
        "v_max3_u32 v31, v28, v29, v30\n\t"
        "v_max3_u32 v35, v32, v33, v34\n\t"
        "v_max3_u32 v39, v36, v37, v38\n\t"
        "v_max3_u32 v43, v40, v41, v42\n\t"
        "v_max3_u32 v47, v44, v45, v46\n\t"
        "v_max3_u32 v51, v48, v49, v50\n\t"
        "v_max3_u32 v71, v68, v69, v70\n\t"
        "v_add_f32 v24, v24, v28\n\t"
        "v_add_f32 v32, v32, v36\n\t"
        "v_add_f32 v40, v40, v44\n\t"
        "v_add_f32 v48, v48, v68\n\t"
        "v_add_f32 v25, v25, v29\n\t"
        "v_add_f32 v33, v33, v37\n\t"
        "v_add_f32 v41, v41, v45\n\t"
        "v_add_f32 v49, v49, v69\n\t"
        "v_add_f32 v26, v26, v30\n\t"
        "v_add_f32 v34, v34, v38\n\t"
        "v_add_f32 v42, v42, v46\n\t"
        "v_add_f32 v50, v50, v70\n\t"
        "v_max3_u32 %[t], v27, v31, v35\n\t"
        "v_max3_u32 v39, v39, v43, v47\n\t"
        "v_max3_u32 %[t], %[t], v51, v71\n\t"
        "v_max_u32 %[t], %[t], v39\n\t"
        "v_cmp_eq_u32 vcc, -1, %[t]\n\t"
        "v_add_f32 v24, v24, v32\n\t"
        "v_add_f32 v40, v40, v48\n\t"
        "v_add_f32 v25, v25, v33\n\t"
        "v_add_f32 v41, v41, v49\n\t"
        "v_add_f32 v26, v26, v34\n\t"
        "v_add_f32 v42, v42, v50\n\t"
        "s_cbranch_vccz Lf0_%=\n\t"
        "buffer_load_dwordx4 v[24:27], %[va], %[ssrd], %[so] offen offset:0 sc1\n\t"
        "buffer_load_dwordx4 v[28:31], %[va], %[ssrd], %[so] offen offset:1024 sc1\n\t"
        "buffer_load_dwordx4 v[32:35], %[va], %[ssrd], %[so] offen offset:256 sc1\n\t"
        "buffer_load_dwordx4 v[36:39], %[va], %[ssrd], %[so] offen offset:1280 sc1\n\t"
        "buffer_load_dwordx4 v[40:43], %[va], %[ssrd], %[so] offen offset:512 sc1\n\t"
        "buffer_load_dwordx4 v[44:47], %[va], %[ssrd], %[so] offen offset:1536 sc1\n\t"
        "buffer_load_dwordx4 v[48:51], %[va], %[ssrd], %[so] offen offset:768 sc1\n\t"
        "buffer_load_dwordx4 v[68:71], %[va], %[ssrd], %[so] offen offset:1792 sc1\n\t"
        "s_sub_u32 %[scn], %[scn], 1\n\t"
        "s_cmp_lg_u32 %[scn], 0\n\t"
        "s_cbranch_scc1 Lp_%=\n\t"
        "s_waitcnt vmcnt(0)\n\t"
        "Lf0_%=:\n\t"
        "v_add_f32 %[o0], v24, v40\n\t"
        "v_add_f32 %[o1], v25, v41\n\t"
        "v_add_f32 %[o2], v26, v42\n\t"
        "Le_%=:\n\t"
        : [o0] "=&v"(o[0]), [o1] "=&v"(o[1]), [o2] "=&v"(o[2]), [t] "=&v"(t), [scn] "+s"(cn)
        : [ssrd] "s"(srd), [va] "v"(va), [so] "s"(so)
        : "memory", "scc", "vcc", "v24", "v25", "v26", "v27", "v28", "v29", "v30", "v31", "v32", "v33", "v34", "v35", "v36", "v37", "v38", "v39", "v40", "v41", "v42", "v43", "v44", "v45", "v46", "v47");
}
DEV void poll_stats1(u32x4 srd, u32 so, float (&o)[3], u32& cn) {
    u32 t;
    asm volatile(
        "s_nop 4\n\t"
        "buffer_load_dwordx4 v[24:27], v107, %[ssrd], %[so] offen sc1\n\t"
        "buffer_load_dwordx4 v[28:31], v107, %[ssrd], %[so] offen offset:1024 sc1\n\t"
        "Lp_%=:\n\t"
        "s_waitcnt vmcnt(0)\n\t"
        "v_max3_u32 %[t], v24, v25, v26\n\t"
        "v_max3_u32 %[t], %[t], v27, v28\n\t"
        "v_max3_u32 %[t], %[t], v29, v30\n\t"
        "v_max_u32 %[t], %[t], v31\n\t"
        "v_cmp_eq_u32 vcc, -1, %[t]\n\t"
        "s_cbranch_vccz Lf0_%=\n\t"
        "buffer_load_dwordx4 v[24:27], v107, %[ssrd], %[so] offen sc1\n\t"
        "buffer_load_dwordx4 v[28:31], v107, %[ssrd], %[so] offen offset:1024 sc1\n\t"
        "s_sub_u32 %[scn], %[scn], 1\n\t"
        "s_cmp_lg_u32 %[scn], 0\n\t"
        "s_cbranch_scc1 Lp_%=\n\t"
        "s_waitcnt vmcnt(0)\n\t"
        "Lf0_%=:\n\t"
        "v_add_f32 %[o0], v24, v28\n\t"
        "v_add_f32 %[o1], v25, v29\n\t"
        "v_add_f32 %[o2], v26, v30\n\t"
        "Le_%=:\n\t"
        : [o0] "=&v"(o[0]), [o1] "=&v"(o[1]), [o2] "=&v"(o[2]), [t] "=&v"(t), [scn] "+s"(cn)
        : [ssrd] "s"(srd), [so] "s"(so)
        : "memory", "scc", "vcc", "v24", "v25", "v26", "v27", "v28", "v29", "v30", "v31");
}
DEV void poll_keys2(u32x4 srd, u32 so, u32 tag, u32& o, u32& cn) {
    u32 t;
    asm volatile(
        "s_nop 4\n\t"
        "buffer_load_dwordx4 v[48:51], v107, %[ssrd], %[so] offen sc1\n\t"
        "Lp_%=:\n\t"
        "s_waitcnt vmcnt(0)\n\t"
        "v_xor_b32 %[t], %[stag], v49\n\t"
        "v_xor_b32 %[o0], %[stag], v51\n\t"
        "v_or_b32 %[t], %[t], %[o0]\n\t"
        "v_cmp_ne_u32 vcc, 0, %[t]\n\t"
        "s_cbranch_vccz Lf0_%=\n\t"
        "buffer_load_dwordx4 v[48:51], v107, %[ssrd], %[so] offen sc1\n\t"
        "s_sub_u32 %[scn], %[scn], 1\n\t"
        "s_cmp_lg_u32 %[scn], 0\n\t"
        "s_cbranch_scc1 Lp_%=\n\t"
        "s_waitcnt vmcnt(0)\n\t"
        "Lf0_%=:\n\t"
        "v_max_u32 %[o0], v48, v50\n\t"
        "Le_%=:\n\t"
        : [o0] "=&v"(o), [t] "=&v"(t), [scn] "+s"(cn)
        : [ssrd] "s"(srd), [so] "s"(so), [stag] "s"(tag)
        : "memory", "scc", "vcc");
}
DEV void poll_keys(u32x4 srd, u32 so, u32 tag, u64 lm, u32& o, u32& cn) {
    u32 t;
    asm volatile(
        "s_nop 4\n\t"
        "buffer_load_dwordx4 v[96:99], v107, %[ssrd], %[so] offen sc1\n\t"
        "v_add_u32 %[t], 98048, v107\n\t"
        "ds_read_b128 v[244:247], %[t] offset:6144\n\t"
        "ds_read_b128 v[248:251], %[t] offset:7168\n\t"
        "s_waitcnt lgkmcnt(0)\n\t"
        "Lp_%=:\n\t"
        "s_waitcnt vmcnt(0)\n\t"
        "v_xor_b32 %[t], %[stag], v97\n\t"
        "v_xor_b32 %[o0], %[stag], v99\n\t"
        "v_or_b32 %[t], %[t], %[o0]\n\t"
        "v_max_u32 %[o0], v96, v98\n\t"
        "v_cndmask_b32 %[t], %[t], 0, %[slm]\n\t"
        "v_cmp_ne_u32 vcc, 0, %[t]\n\t"
        "s_cbranch_vccz Lf0_%=\n\t"
        "buffer_load_dwordx4 v[96:99], v107, %[ssrd], %[so] offen sc1\n\t"
        "s_sub_u32 %[scn], %[scn], 1\n\t"
        "s_cmp_lg_u32 %[scn], 0\n\t"
        "s_cbranch_scc1 Lp_%=\n\t"
        "s_waitcnt vmcnt(0)\n\t"
        "Lf0_%=:\n\t"
        "v_cndmask_b32 %[o0], %[o0], 0, %[slm]\n\t"
        "Le_%=:\n\t"
        : [o0] "=&v"(o), [t] "=&v"(t), [scn] "+s"(cn)
        : [ssrd] "s"(srd), [so] "s"(so), [stag] "s"(tag), [slm] "s"(lm)
        : "memory", "scc", "vcc");
}
DEV void poll_flags(u32x4 srd, u32 fo, u32 tag, u32& cn) {
    u32 t, d0;
    asm volatile(
        "s_nop 4\n\t"
        "s_sleep 5\n\t"
        "buffer_load_dword v48, %[fo], %[ssrd], 0 offen sc1\n\t"
        "Lp_%=:\n\t"
        "s_waitcnt vmcnt(0)\n\t"
        "v_cmp_ne_u32 vcc, %[stag], v48\n\t"
        "s_cbranch_vccz Lf0_%=\n\t"
        "buffer_load_dword v48, %[fo], %[ssrd], 0 offen sc1\n\t"
        "s_sub_u32 %[scn], %[scn], 1\n\t"
        "s_cmp_lg_u32 %[scn], 0\n\t"
        "s_cbranch_scc1 Lp_%=\n\t"
        "s_waitcnt vmcnt(0)\n\t"
        "Lf0_%=:\n\t"
        "s_nop 0\n\t"
        "Le_%=:\n\t"
        : [d0] "=&s"(d0), [t] "=&v"(t), [scn] "+s"(cn)
        : [ssrd] "s"(srd), [fo] "v"(fo), [stag] "s"(tag)
        : "memory", "scc", "vcc");
}
DEV void poll_flagh(u32x4 srd, u32 fo, u32 tag, u32& cn) {
    u32 t, d0;
    asm volatile(
        "s_nop 4\n\t"
        "buffer_load_dword v48, %[fo], %[ssrd], 0 offen sc1\n\t"
        "Lp_%=:\n\t"
        "s_waitcnt vmcnt(0)\n\t"
        "v_cmp_ne_u32 vcc, %[stag], v48\n\t"
        "s_cbranch_vccz Lf0_%=\n\t"
        "buffer_load_dword v48, %[fo], %[ssrd], 0 offen sc1\n\t"
        "s_sub_u32 %[scn], %[scn], 1\n\t"
        "s_cmp_lg_u32 %[scn], 0\n\t"
        "s_cbranch_scc1 Lp_%=\n\t"
        "s_waitcnt vmcnt(0)\n\t"
        "Lf0_%=:\n\t"
        "s_nop 0\n\t"
        "Le_%=:\n\t"
        : [d0] "=&s"(d0), [t] "=&v"(t), [scn] "+s"(cn)
        : [ssrd] "s"(srd), [fo] "v"(fo), [stag] "s"(tag)
        : "memory", "scc", "vcc");
}
DEV void reduce(u32x4 srd, u32x4 out, u32 va, u32 so, int xa, float m, float rs, float sg, u32 xo, u32 oo, int fa, u32& ab, u32& cn) {
    u32 t, n;
    asm volatile(
        "ds_read_b32 v78, %[xa]\n\t"
        "v_mov_b32 v79, %[sm]\n\t"
        "v_mov_b32 v76, %[sg]\n\t"
        "v_mov_b32 v77, %[sg]\n\t"
        "v_mul_f32 v79, %[srs], -v79\n\t"
        "s_cmp_eq_u32 %[sab], 2\n\t"
        "s_cselect_b32 %[sn], 2, 1\n\t"
        "Lw_%=:\n\t"
        "ds_read_b32 %[t], %[fa]\n\t"
        "s_sleep 1\n\t"
        "s_sub_u32 %[sn], %[sn], 1\n\t"
        "s_waitcnt lgkmcnt(0)\n\t"
        "v_cmp_eq_u32 vcc, %[sab], %[t]\n\t"
        "s_cbranch_vccnz Le_%=\n\t"
        "s_cbranch_scc0 Lw_%=\n\t"
        "Lp_%=:\n\t"
        "buffer_load_dwordx4 v[48:51], %[va], %[ssrd], %[so0] offen sc1\n\t"
        "buffer_load_dwordx4 v[52:55], %[va], %[ssrd], %[so1] offen sc1\n\t"
        "buffer_load_dwordx4 v[56:59], %[va], %[ssrd], %[so2] offen sc1\n\t"
        "buffer_load_dwordx4 v[60:63], %[va], %[ssrd], %[so3] offen sc1\n\t"
        "v_lshlrev_b32 v72, 16, v78\n\t"
        "v_and_b32 v73, 0xffff0000, v78\n\t"
        "v_fma_f32 v74, v72, %[srs], v79\n\t"
        "v_fma_f32 v75, v73, %[srs], v79\n\t"
        "v_cvt_pk_bf16_f32 v75, v74, v75\n\t"
        "v_lshlrev_b32 v74, 16, v75\n\t"
        "v_and_b32 v75, 0xffff0000, v75\n\t"
        "s_cmp_eq_u32 %[sab], 2\n\t"
        "s_cselect_b32 %[sn], 3, 0\n\t"
        "Lx_%=:\n\t"
        "ds_read_b32 %[t], %[fa]\n\t"
        "s_sleep 1\n\t"
        "s_sub_u32 %[sn], %[sn], 1\n\t"
        "s_waitcnt lgkmcnt(0)\n\t"
        "v_cmp_eq_u32 vcc, %[sab], %[t]\n\t"
        "s_cbranch_vccnz Le_%=\n\t"
        "s_cbranch_scc0 Lx_%=\n\t"
        "s_waitcnt vmcnt(0)\n\t"
        "ds_read_b32 %[t], %[fa]\n\t"
        ".long 0xd3b24040, 0x18026930 ; v_pk_add_f32 v(64:65), v(48:49), v(52:53)\n\t"
        ".long 0xd3b24042, 0x18026d32 ; v_pk_add_f32 v(66:67), v(50:51), v(54:55)\n\t"
        ".long 0xd3b24044, 0x18027938 ; v_pk_add_f32 v(68:69), v(56:57), v(60:61)\n\t"
        ".long 0xd3b24046, 0x18027d3a ; v_pk_add_f32 v(70:71), v(58:59), v(62:63)\n\t"
        ".long 0xd3b24040, 0x18028940 ; v_pk_add_f32 v(64:65), v(64:65), v(68:69)\n\t"
        ".long 0xd3b24042, 0x18028d42 ; v_pk_add_f32 v(66:67), v(66:67), v(70:71)\n\t"
        "s_nop 1\n\t"
        "v_permlane32_swap_b32 v64, v66\n\t"
        "v_permlane32_swap_b32 v65, v67\n\t"
        ".long 0xd3b24040, 0x18028540 ; v_pk_add_f32 v(64:65), v(64:65), v(66:67)\n\t"
        "v_cmp_u_f32 vcc, v64, v65\n\t"
        "s_nop 0\n\t"
        "v_add_f32_dpp v64, v64, v64 row_ror:8 row_mask:0xf bank_mask:0xf\n\t"
        "v_add_f32_dpp v65, v65, v65 row_ror:8 row_mask:0xf bank_mask:0xf\n\t"
        "s_cbranch_vccnz Ln_%=\n\t"
        "Lf_%=:\n\t"
        "v_add_f32_dpp v64, v64, v64 row_ror:4 row_mask:0xf bank_mask:0xf\n\t"
        "v_add_f32_dpp v65, v65, v65 row_ror:4 row_mask:0xf bank_mask:0xf\n\t"
        "s_nop 0\n\t"
        "v_add_f32_dpp v64, v64, v64 row_ror:2 row_mask:0xf bank_mask:0xf\n\t"
        "v_add_f32_dpp v65, v65, v65 row_ror:2 row_mask:0xf bank_mask:0xf\n\t"
        "s_waitcnt lgkmcnt(0)\n\t"
        "v_add_f32_dpp v64, v64, v64 row_ror:1 row_mask:0xf bank_mask:0xf\n\t"
        "v_add_f32_dpp v65, v65, v65 row_ror:1 row_mask:0xf bank_mask:0xf\n\t"
        "v_readfirstlane_b32 %[sn], %[t]\n\t"
        "v_add_f32_dpp v64, v64, v64 row_bcast:15 row_mask:0xa bank_mask:0xf\n\t"
        "v_add_f32_dpp v65, v65, v65 row_bcast:15 row_mask:0xa bank_mask:0xf\n\t"
        "v_cvt_pk_bf16_f32 v66, v64, v65\n\t"
        "v_lshlrev_b32 v64, 16, v66\n\t"
        "v_and_b32 v65, 0xffff0000, v66\n\t"
        ".long 0xd3b14040, 0x18029940 ; v_pk_mul_f32 v(64:65), v(64:65), v(76:77)\n\t"
        "v_cvt_pk_bf16_f32 v66, v64, v65\n\t"
        "v_lshlrev_b32 v64, 16, v66\n\t"
        "v_and_b32 v65, 0xffff0000, v66\n\t"
        ".long 0xd3b24040, 0x18029540 ; v_pk_add_f32 v(64:65), v(64:65), v(74:75)\n\t"
        "s_or_b32 %[sn], %[sn], %[sab]\n\t"
        "v_cvt_pk_bf16_f32 v66, v64, v65\n\t"
        "v_lshlrev_b32 v64, 16, v66\n\t"
        "v_and_b32 v65, 0xffff0000, v66\n\t"
        ".long 0xd3b24040, 0x18029140 ; v_pk_add_f32 v(64:65), v(64:65), v(72:73)\n\t"
        "s_bitcmp1_b32 %[sn], 0\n\t"
        "v_cvt_pk_bf16_f32 v66, v64, v65\n\t"
        "s_cbranch_scc0 Lg_%=\n\t"
        "Lh_%=:\n\t"
        "buffer_store_dword v66, %[oo], %[sout], 0 offen\n\t"
        "v_min_u32 v67, -2, v66\n\t"
        "buffer_store_dword v67, %[xo], %[ssrd], 0 offen sc1\n\t"
        "s_mov_b32 %[sab], 0\n\t"
        "s_branch Le_%=\n\t"
        "Ln_%=:\n\t"
        "v_max3_u32 v48, v48, v49, v50\n\t"
        "v_max3_u32 v48, v48, v51, v52\n\t"
        "v_max3_u32 v48, v48, v53, v54\n\t"
        "v_max3_u32 v48, v48, v55, v56\n\t"
        "v_max3_u32 v48, v48, v57, v58\n\t"
        "v_max3_u32 v48, v48, v59, v60\n\t"
        "v_max3_u32 v48, v48, v61, v62\n\t"
        "v_max_u32 v48, v48, v63\n\t"
        "v_cmp_eq_u32 vcc, -1, v48\n\t"
        "s_cbranch_vccz Lf_%=\n\t"
        "s_cmp_lt_i32 %[scn], 1\n\t"
        "s_cbranch_scc1 Lf_%=\n\t"
        "s_sub_u32 %[scn], %[scn], 16\n\t"
        "s_branch Lp_%=\n\t"
        "Lg_%=:\n\t"
        "ds_read_b32 %[t], %[fa]\n\t"
        "s_cmp_lt_i32 %[scn], 1\n\t"
        "s_cbranch_scc1 Lh_%=\n\t"
        "s_sub_u32 %[scn], %[scn], 1\n\t"
        "s_waitcnt lgkmcnt(0)\n\t"
        "v_readfirstlane_b32 %[sn], %[t]\n\t"
        "s_cmp_eq_u32 %[sn], 1\n\t"
        "s_cbranch_scc1 Lh_%=\n\t"
        "s_cmp_eq_u32 %[sn], 0\n\t"
        "s_cbranch_scc1 Lg_%=\n\t"
        "Le_%=:\n\t"
        "s_waitcnt lgkmcnt(0)\n\t"
        : [t] "=&v"(t), [sn] "=&s"(n), [sab] "+s"(ab), [scn] "+s"(cn)
        : [ssrd] "s"(srd), [sout] "s"(out), [va] "v"(va), [xa] "v"(xa), [sm] "s"(m), [srs] "s"(rs), [sg] "v"(sg), [xo] "v"(xo), [oo] "v"(oo), [fa] "v"(fa), [so0] "s"(so + 0), [so1] "s"(so + 4096), [so2] "s"(so + 8192), [so3] "s"(so + 12288)
        : "memory", "scc", "vcc");
}
DEV void poll_row(u32x4 srd, u32 v1, u32 v2, u32 (&o)[6], u32& cn) {
    u32 t;
    asm volatile(
        "s_nop 4\n\t"
        "buffer_load_dwordx2 v[48:49], %[v1], %[ssrd], 0 offen sc1\n\t"
        "buffer_load_dwordx2 v[50:51], %[v1], %[ssrd], 0 offen offset:2048 sc1\n\t"
        "buffer_load_dwordx2 v[52:53], %[v2], %[ssrd], 0 offen sc1\n\t"
        "Lp_%=:\n\t"
        "s_waitcnt vmcnt(0)\n\t"
        "v_max3_u32 %[t], v48, v49, v50\n\t"
        "v_max3_u32 %[t], %[t], v51, v52\n\t"
        "v_max_u32 %[t], %[t], v53\n\t"
        "v_cmp_eq_u32 vcc, -1, %[t]\n\t"
        "s_cbranch_vccz Lf0_%=\n\t"
        "buffer_load_dwordx2 v[48:49], %[v1], %[ssrd], 0 offen sc1\n\t"
        "buffer_load_dwordx2 v[50:51], %[v1], %[ssrd], 0 offen offset:2048 sc1\n\t"
        "buffer_load_dwordx2 v[52:53], %[v2], %[ssrd], 0 offen sc1\n\t"
        "s_sub_u32 %[scn], %[scn], 1\n\t"
        "s_cmp_lg_u32 %[scn], 0\n\t"
        "s_cbranch_scc1 Lp_%=\n\t"
        "s_waitcnt vmcnt(0)\n\t"
        "Lf0_%=:\n\t"
        "v_mov_b32 %[o0], v48\n\t"
        "v_mov_b32 %[o1], v49\n\t"
        "v_mov_b32 %[o2], v50\n\t"
        "v_mov_b32 %[o3], v51\n\t"
        "v_mov_b32 %[o4], v52\n\t"
        "v_mov_b32 %[o5], v53\n\t"
        "Le_%=:\n\t"
        : [o0] "=&v"(o[0]), [o1] "=&v"(o[1]), [o2] "=&v"(o[2]), [o3] "=&v"(o[3]), [o4] "=&v"(o[4]), [o5] "=&v"(o[5]), [t] "=&v"(t), [scn] "+s"(cn)
        : [ssrd] "s"(srd), [v1] "v"(v1), [v2] "v"(v2)
        : "memory", "scc", "vcc");
}

DEV int ffn_slice(int b, int& nu) {
    const int j = b >> 3, t = j - 20, t3 = (t * 43) >> 7, r = t - 3 * t3;
    nu = (j >= 20 && r < 2) ? 24 : 16;
    return (b & 7) * 576 + (j < 20 ? j * 16 : 320 + t3 * 64 + r * 24);
}
DEV void gu_dma(const u16* wg, const u16* wu, int lane, int b, int i0, int i1) {
    int nu;
    const int f0 = ffn_slice(b, nu);
    u32 gv[3];
#pragma unroll
    for (int m = 0; m < 3; ++m) {
        const int q = 64 * m + lane, sg = (q * 171) >> 9, part = q - 3 * sg;
        const int kin = 32 * ((sg >> 1) & 1) + 8 * (sg >> 4) + (sg & 5) + 2 * ((sg >> 3) & 1);
        gv[m] = (u32)kin * 9216u + f0 * 2u + ((part == 2 && nu == 16) ? 16u : part * 16u);
    }
#pragma nounroll
    for (int i = i0; i < i1; ++i) {
        int gi = i + 2; if (i0 == 0) { gi += (0xC840 >> (((b >> 3) & 3) * 4)) & 15; if (gi >= 17) gi -= 15; } else if (gi >= 18) gi -= 18;
        const u32x4 sd = mk_srd(gi < 9 ? wg : wu, 5308416);
        asm volatile("" : "+s"(gi));
        int g9 = gi - 9 * ((gi * 57) >> 9);
        asm volatile("" : "+s"(g9));
        const u32 so = g9 * (64 * 9216), ld = L_G + gi * 3072;
#pragma unroll
        for (int m = 0; m < 3; ++m) dma16g(sd, gv[m], so, ld + m * 1024);
    }
}

DEV void xt_upd(u32 (&XT)[10], float rm, float rrs) {
#pragma unroll
    for (int i = 0; i < 10; ++i) {
        const float a = lo16(XT[i]), c = hi16(XT[i]);
        const u32 h = pk2((a - rm) * rrs, (c - rm) * rrs);
        XT[i] = pk2(a + lo16(h), c + hi16(h));
    }
}

DEV void gu_pf(const u16* wg, const u16* wu, int lane, int b, int i) {
    const int j = b >> 3;
    const u32x4 sg = mk_srd(j < 16 ? wg : wu, 5308416);
    const int q = i * 54 + lane, r = (q * 57) >> 9;
    const u32 o = lane < 54 ? (u32)((j & 15) * 36 + r) * 9216u + (b & 7) * 1152u + (q - 9 * r) * 128u : (u32)OOB;
    asm volatile("s_nop 4\n\tbuffer_load_dword v98, %0, %1, 0 offen" :: "v"(o), "s"(sg) : "memory");
}

DEV int k_pos(int p, int cb) { return p >= 9 * cb + 8 ? p : (p >= 9 * cb ? p - 9 * cb : p + 8); }
DEV u32 w_voff(int wp, int r16, int p, int cb) {
    int ch = 16 * wp + r16; if (ch > 107) ch = 107;
    const int sec = (ch * 57) >> 11, c = ch - 36 * sec;
    return (u32)(8 * p) * 3456u + (sec == 2 ? 0u : (u32)(sec + 1) * 1152u) + cb * 144u + c * 4u;
}

__global__ void NOPK __attribute__((amdgpu_num_vgpr(24))) __launch_bounds__(512, 2)
mega(const u16* __restrict__ x0, const u16* __restrict__ wqkv, const u16* __restrict__ wo, const u16* __restrict__ wr,
     const u16* __restrict__ wg, const u16* __restrict__ wu, const u16* __restrict__ wd, u16* __restrict__ out, char* __restrict__ ws) {
    const int w_ = __builtin_amdgcn_readfirstlane(threadIdx.x >> 6);
    const int b_ = blockIdx.x;
    {
        u64 pc; u32 off;
        asm volatile("s_getpc_b64 %0\n"
                     "Lcode0:\n\t"
                     "v_lshlrev_b32 %1, 7, %2\n\t"
                     "v_and_b32 v99, 3, %2\n\t"
                     "v_cmp_gt_u32 vcc, Lcode1-Lcode0, %1\n\t"
                     "s_mov_b64 exec, vcc\n\t"
                     "v_cmp_eq_u32 vcc, %3, v99\n\t"
                     "s_and_b64 exec, exec, vcc\n\t"
                     "s_cbranch_execz Lcode2\n\t"
                     "global_load_dword v99, %1, %0\n"
                     "Lcode2:\n\t"
                     "s_mov_b64 exec, -1"
                     : "=&s"(pc), "=&v"(off) : "v"(threadIdx.x), "s"((b_ >> 3) & 3) : "vcc", "scc", "memory");
    }
    const __amdgpu_buffer_rsrc_t rs = __builtin_amdgcn_make_buffer_rsrc((void*)ws, 0, WS_BYTES, 0x00020000);
    u32 tag_, tmo = 0;
    {
        IDS
        int u = rg * 4 + (w & 3);
        const bool uok = u < 126;
        if (u > 125) u = 125;
        const int su = (u * 147) >> 10, wp = u - 7 * su;
        u32 wv = w_voff(wp, r16, g, cb), so = (u32)su * 110592u;
        sts32(L_BC + lane * 4, 0u);
        asm volatile("" : "+v"(wv), "+s"(so));
        asm volatile("" :: "s"(x0), "s"(wqkv), "s"(wo), "s"(ws));
        wld8p(mk_srd(wqkv, 1990656), wv, so, w);
        bar();
        { const char* ep_ = ws; asm volatile("" : "+s"(ep_)); tag_ = *(const __attribute__((address_space(4))) u32*)(ep_ + OFF_EPOCH) + 1u; }
        const int kp = k_pos(4 * su + g, cb);
        wunit_st(mk_srd(ws, WS_BYTES), (uok && !(wp == 6 && r16 >= 12)) ? OFF_WI + ((cb * 7 + wp) * 18 + (kp >> 2)) * 2048 + ((kp & 3) * (wp == 6 ? 12 : 16) + r16) * 16 : OOB, w);
        x_dma(mk_srd(x0 - 9216, 2359296 + 18432), (u32)((rg * 64 + (lane >> 3)) * 1152) + (u32)(((lane ^ (lane >> 4)) & 7) << 4), rg == 0 ? OOB : 0,
              ((XCI >> (4 * w)) & 15) + cb, (XCN >> (4 * w)) & 15);
        if (w < 4) {
            const int fa = lane < WREP ? OFF_FLAG + ((lane * 8 + cb) * 32 + rg) * FST : OOB;
            if (w > 0) { VMWAIT(10); } else { VMWAIT(0); }
            BST32(tag_, lds_inc_rtn(L_BC + BC_FC) == 3u ? fa : OOB, 0);
            __builtin_amdgcn_s_setprio(0);
        }
#define PFI(w) ((w) >= 4 ? (w) - 4 : (w) + 2)
#define PFON(w) ((w) > 0 && (w) < 7 && PFI(w) < PFA)
        if (PFON(w)) gu_pf(wg, wu, lane, b, PFI(w));
        if (w == 6) {
#pragma unroll
            for (int i = 0; i < 3; ++i) {
                const int q = lane + 64 * i;
                if (q < 144) sts128((q >> 4) * XCH + (q & 15) * 16, (u32x4){ONES2, ONES2, ONES2, ONES2});
            }
        }
        lc_set(lane_consts(w_, b_));
    }
    MidS a;
    {
        IDS
        const int rt4 = w & 3, kh = w >> 2, hp = 2 * cb + kh;
        const u32 obase = OFF_OT + (rg * 4 + rt4) * 18432;
        const u32 wl = OFF_WOI + cb * 82944;
        const u32 lo = w < 4 ? 16384 + w * 6144 : (w < 7 ? 64512 + (w - 4) * 6144 : 57344), eo = kh * 41472 + rt4 * 4096;
        a.n7 = rt4 == 3 ? 1024 : 0;
        a.ob = obase + kh * 9216; a.st = obase + hp * 1024; a.tt = obase + 16384 + hp * 128;
        a.c0 = (rg == 0 && rt4 == 0) ? 0xff61b1e6u : 0u;
        a.l1d = kh ? (u32)-(L_WOE + 9 * 4608) : 5 * 4608; a.tvd = kh ? 128 : 8;
        a.wi = wl + lo; a.ld = lo >= 64512 ? lo - 64512 : L_WOE + lo; a.wx = wl + eo; a.ldx = L_WOE + eo;
        a.msk = (u32)(kh - 1); a.scr = rt4 == 2 ? L_SCR3 : L_SCR + (rt4 == 3 ? 2 : rt4) * 4608; a.pk = L_PK(rt4);
        a.stat = OFF_STATS + (rg * 4 + rt4) * 2048 + cb * 256;
        a.x1 = OFF_X1G + rg * 73728 + 9 * cb * 1024 + rt4 * 256;
        asm volatile("" : "+s"(a.ob), "+s"(a.st), "+s"(a.tt), "+s"(a.c0), "+s"(a.l1d), "+s"(a.tvd), "+s"(a.wi), "+s"(a.ld), "+s"(a.wx), "+s"(a.ldx), "+s"(a.msk),
                          "+s"(a.scr), "+s"(a.stat), "+s"(a.x1), "+s"(a.pk), "+s"(a.n7));
    }
    asm volatile("" : "+s"(tag_));
#define VA_MASK (CSL(w) < 6 ? ~0ull : 0x0000FFFFFFFFFFFFull)
#define V0_MASK (CSL(w) < 4 ? ~0ull : (CSL(w) == 4 ? 0xFFFFFFFFull : 0ull))
    a.lb = L_WOE + (w_ >> 2) * 9 * 4608;
    a.skip = 0;
    a.wc = L_BC + BC_WC + (w_ >> 2) * 4;
    u64 lm0 = 0;
#pragma nounroll
    for (int layer = 0; layer < 2; ++layer) {
    u32 tag = tag_; asm volatile("" : "+s"(tag));
    if (layer == 0) {
    if (w_ < 7) {
        IDS
        const u32x4 sws = mk_srd(ws, WS_BYTES);
        const u32 fo = w > 0 ? OOB : OFF_GFLAG + (lane < 18 ? lane * 8 + cb : (((lane & 8) && rg > 0) ? rg - 1 : rg) * 8 + (lane & 7)) * 16;
        int xt[3];
        {
            const int row = 16 + 16 * (w & 3) + r16;
#pragma unroll
            for (int p = 0; p < 3; ++p) {
                const int pc = 9 * cb + 4 * p + (p < 2 ? g : 0);
                xt[p] = (pc >> 3) * XCH + row * 128 + (((pc ^ (row >> 1)) & 7) << 4) + (p < 2 ? 0 : (g & 1) * 8);
            }
            asm volatile("" : "+v"(xt[0]), "+v"(xt[1]), "+v"(xt[2]));
        }
        if (w >= 1) {
            if (PFON(w)) { VMWAIT(1); } else { VMWAIT(0); }
            lds_inc(L_BC + BC_XC, lane);
        }
        if (w >= 4) {
            LWAIT0(L_BC + BC_XC, 6, 22);
            float xs[2], xq[2];
            xstats((w - 4) * 2048 + r16 * 128 + (((g ^ (r16 >> 1)) & 7) << 4), xs, xq);
#pragma unroll
            for (int t = 0; t < 2; ++t) {
                const int row = 16 * (w - 4 + 3 * t) + r16;
                float m = xs[t] * (1.f / 576.f);
                float rsd = ln_rs(fmaxf(xq[t] * (1.f / 576.f) - m * m, 0.f));
                m = ln_m(m);
                if (row < 2) { m = 0.f; rsd = 0.f; }
                if (g == (r16 >> 2) && row < 80) sts64(L_MR + row * 8, (u32x2){__float_as_uint(m), __float_as_uint(rsd)});
            }
            lds_inc(L_BC + BC_MC, lane);
        }
        if (w == 0) {
            u32 cn = SPIN_MAX;
            poll_flags(sws, OFF_FLAG + ((rg & (WREP - 1)) * 256 + cb * 32 + (lane & 31)) * FST, tag, cn);
            sts32v(L_BC + BC_GC, 1u);
            if (cn == 0) TMO(3);
        } else {
            LWAIT0(L_BC + BC_GC, 1, 23);
        }
        LWAIT0(L_BC + BC_XC, 6, 22);
        {
            const u32 ib = OFF_WI + (cb * 7 + CSL(w)) * 36864;
            u32 cn = SPIN_MAX;
            gemm0(sws, r16 * 128 + (((g ^ (r16 >> 1)) & 7) << 4), (CSL(w) == 6 ? g * 12 + (r16 < 12 ? r16 : 11) : lane) * 16, ib, xt, 2048 + 2048 * (w & 3), 3u << cb, g >= cb ? XCH : 0, g + 4 >= cb ? XCH : 0, fo, w > 0 ? 0u : tag, L_CS + w * 128, VA_MASK, V0_MASK, cn);
            if ((int)cn < 1) TMO(25);
        }
    } else {
        IDS
        __builtin_amdgcn_s_setprio(3);
        {
            u32 U[3][8];
            const int ks = rg < 18 ? rg : 0;
            const u32 rb = rg < 18 ? (u32)(ks < 16 ? 36 * ks + 4 * g : 36 * (8 * (ks - 16) + 2 * g) + 16) * 1152u + cb * 144u : (u32)OOB;
#pragma unroll
            for (int p = 0; p < 3; ++p) old8(U[p], mk_srd(wo, 663552), rb + p * 64u + (p < 2 ? r16 * 4u : ((r16 & 7) >> 1) * 4u), (ks < 16 ? 20 : 36) * 1152);
            u32 sw_ = SENT; asm volatile("" : "+v"(sw_));
            const u32x4 sv = {sw_, sw_, sw_, sw_};
            const int ob = OFF_OT + rg * 4 * 18432;
            LWAIT(L_BC + BC_FC, 4, 43);
#pragma unroll
            for (int i = 0; i < 8; ++i) BST128(sv, lane * 16 + (i & 1) * 1024, ob + (i >> 1) * 18432 + cb * 2048);
            BST128(sv, g * 18432 + r16 * 16, ob + 16384 + cb * 256);
            BST128(sv, g * 2048 + r16 * 16, OFF_STATS + rg * 4 * 2048 + cb * 256);
            const int wb = rg < 18 ? OFF_WOI + cb * 82944 + ks * 4608 : OOB;
#pragma unroll
            for (int p = 0; p < 3; ++p) {
                vmwait8n(U[p], 10);
                u32x4 f0, f1;
                wfrag(U[p], f0, f1);
                if (p < 2) { BST128(f0, wb + p * 2048 + lane * 16, 0); BST128(f1, wb + p * 2048 + 1024 + lane * 16, 0); }
                else {
                    const u32x4 f = (r16 & 1) ? f1 : f0;
                    BST64(((u32x2){r16 < 8 ? f.x : f.y, r16 < 8 ? f.z : f.w}), wb + 4096 + ((g >> 1) * 16 + r16) * 16 + (g & 1) * 8, 0);
                }
            }
        }
        u32 wq[6];
        {
            const u32x4 sr = mk_srd(wr, 1152);
#pragma unroll
            for (int i = 0; i < 5; ++i) bld32(wq[i], sr, lane * 4u, 0u, 256 * i);
            bld32(wq[5], sr, lane < 32 ? cb * 144u + lane * 4u : ((lane < 40 && !(lane & 2)) ? cb * 144u + 128u + ((lane >> 2) & 1) * 8u + (lane & 1) * 4u : (u32)OOB), 0u, 0);
        }
        { int nu; const int f0 = ffn_slice(b, nu); dload(mk_srd(wd, (f0 + nu) * 1152), lane * 2u, f0 * 1152u); }
        asm volatile("s_waitcnt vmcnt(%6)" : "+v"(wq[0]), "+v"(wq[1]), "+v"(wq[2]), "+v"(wq[3]), "+v"(wq[4]), "+v"(wq[5]) : "n"(48) : "memory");
        if (lane == 0) BST32(tag, OFF_GFLAG + (rg * 8 + cb) * 16, 0);
        {
            float sacc = 0.f;
#pragma unroll
            for (int q = 0; q < 5; ++q) sacc = dot2(wq[q], ONES2, sacc);
            sacc = gsum(rsum(sacc));
            if (lane < 48) sts32(L_WR + lane * 4, wq[5]);
            if (lane < 36) sts32(L_WR + 192 + lane * 4, ONES2);
            if (lane == 0) sts32(L_BC + BC_WRS, __float_as_uint(sacc));
        }
        asm volatile("s_nop 4\n\tbuffer_load_dword v98, %0, %1, 0 offen sc1" :: "v"(lane < 2 ? OFF_KEY + b * 256 + lane * 128 : (lane == 2 ? OFF_XFLAG + b * 128 : OOB)), "s"(mk_srd(ws, WS_BYTES)) : "memory");
        LWAIT(L_BC + BC_GC, 1, 31);
        __builtin_amdgcn_s_sleep(30);
        __builtin_amdgcn_s_setprio(3);
#pragma unroll
        for (int i = PFA; i < 6; ++i) gu_pf(wg, wu, lane, b, i);
        vmwaitn(6 - PFA);
        dpermx(); dperm(); gu_set();
        VMWAIT(0);
        __builtin_amdgcn_s_setprio(3);
        gu_dma(wg, wu, lane, b, 0, 15);
        {
            u32 sw_ = SENT; asm volatile("" : "+v"(sw_));
            const u32x4 sv = {sw_, sw_, sw_, sw_};
            const int l16 = lane * 16;
#pragma unroll
            for (int l = 0; l < 2; ++l) {
                BST128(sv, (r16 < 9 ? g * 1280 + cb * 144 + r16 * 16 : ((r16 == 9 && cb == 0) ? g * 1280 + 1152 : OOB)), OFF_CROW + (l * 128 + rg * 4) * 1280);
            }
            if (b < 144) {
#pragma unroll
                for (int i = 0; i < 4; ++i) BST128(sv, l16 + i * 1024, OFF_YP + b * 4096);
            }
            if (b >= 251) BST128(sv, l16, OFF_X2R + (b - 251) * 1024);
#pragma unroll
            for (int i = 0; i < 9; ++i) BST128(sv, l16 + (i & 3) * 1024, OFF_X1G + rg * 73728 + cb * 9216 + (i >> 2) * 4096);
            BST128(sv, g * 2048 + r16 * 16, OFF_STATS + STATS_B + rg * 4 * 2048 + cb * 256);
        }
        __builtin_amdgcn_s_setprio(0);
    }
    }
#define XFLAG_OFF (OFF_XFLAG + ((((lane & 8) && rg > 0) ? rg - 1 : rg) * 8 + (lane & 7)) * XFS)
#define WO_PRE() do { const u32x4 sws = mk_srd(ws, WS_BYTES); dma16x4_sc1(sws, lane * 16u, a.wx, a.ldx); if (2 > 1) { dma16x4_sc1(sws, lane * 16u, a.wi, a.ld); \
    dma16x2_sc1(sws, lane * 16u, a.wi + 4096, a.ld + 4096); } a.skip = 3; } while (0)
        bool xf;
        {
            IDS
            u32 cn = LSPIN_MAX / 4;
            u32 fo = (layer == 0 && (w & 6) == 4) ? XFLAG_OFF : OOB;
            a.srd = mk_srd(ws, WS_BYTES); a.layer = layer;
            att(a);
            if (layer == 0 && w == 7) BST32(tag, lane == 0 ? OFF_XFLAG + (rg * 8 + cb) * XFS : OOB, 0);
            mid(a, fo, cn);
            xf = __builtin_amdgcn_ballot_w64(fo != tag) != 0ull;
            a.stat += STATS_B;
            if (cn == 0) TMO(4);
        }
        if (layer == 0 && w_ == 7) { IDS gu_dma(wg, wu, lane, b, 15, 18); }
        if (layer == 0) {
            IDS
#pragma nounroll
            for (u32 m = (0x21004000u >> (4 * w)) & 7; m; m &= m - 1) { gu_tr(L_G + 6144 + __builtin_ctz(m) * 6144); lds_inc(L_BC + BC_PP, lane); }
        }
        if (w_ == (layer == 0 ? 6 : 3)) {
            IDS
            const int tile = rg * 4 + g;
            __builtin_amdgcn_s_setprio(3);
            float wrsum = __uint_as_float(lds32(L_BC + BC_WRS));
            const int cr = OFF_CROW + (layer * 128 + tile) * 1280;
            int ka = r16 < 4 ? OFF_KEY + (layer * 32 + cb * 4 + r16) * 1024 + tile * 8 : OOB;
            int pa_ = L_PK(g) + (r16 < 9 ? r16 : 0) * 256 + 240, ca_ = r16 < 9 ? cr + cb * 144 + r16 * 16 : OOB, sa_ = cb == 0 ? cr + 1152 : OOB;
            u32 idx = 2047 - (rg * 64 + lane), tv = tag, sv = g * 2048 + r16 * 16;
            asm volatile("" : "+v"(wrsum), "+v"(ka), "+v"(pa_), "+v"(ca_), "+v"(sa_), "+v"(idx), "+v"(tv), "+v"(sv));
            float t[3];
            u32 cn = SPIN_MAX;
            LWAIT0(L_BC + BC_PK, 4 * (layer + 1), 27);
            if (layer == 0) { gemm1p(); __builtin_amdgcn_s_sleep(3); }
            if (layer == 1) __builtin_amdgcn_s_sleep(8);
            poll_stats(mk_srd(ws, WS_BYTES), sv, OFF_STATS + layer * STATS_B + rg * 8192, t, cn);
            const float rm0 = t[0] * (1.f / 576.f);
            const float var = fmaxf(t[1] * (1.f / 576.f) - rm0 * rm0, 0.f);
            const float rm = ln_m(rm0), rrs = ln_rs(var);
            const float score = rrs * (t[2] - rm * wrsum);
            const u32 sb = pk2(0.f, score);
            const u32 key = ((sb ^ ((u32)((int)sb >> 31) | 0x80000000u)) & 0xffff0000u) | idx;
            const u32 best = rmax(key);
            BST64(((u32x2){best, tv}), ka, 0);
            CFENCE();
            {
                const u32x4 v = lds128(pa_ - (int)(best & 15u) * 16);
                sts64(L_RS + lane * 8, (u32x2){__float_as_uint(rm), __float_as_uint(rrs)});
                sts32(L_BC + BC_TB + g * 4, best);
                CFENCE();
                sts32v(L_BC + BC_KC, layer + 1);
                BST128(((u32x4){san(v.x), san(v.y), san(v.z), san(v.w)}), ca_, 0);
                BST128(((u32x4){san(__float_as_uint(rm)), san(__float_as_uint(rrs)), san(__float_as_uint(rm)), san(__float_as_uint(rrs))}), key == best ? sa_ : OOB, 0);
            }
            if (cn == 0) TMO(5);
            if (layer == 0) {
                sts64(L_MR + (16 + lane) * 8, ln_next(rm0, var, rm, rrs));
                lds_inc(L_BC + BC_MC, lane);
            }
            __builtin_amdgcn_s_setprio(0);
        }
        if (layer == 0 && w_ == 3) {
            IDS
            float t[3];
            u32 cn = SPIN_MAX;
            LWAIT0(L_BC + BC_PK, 4, 27);
            gemm1p();
            poll_stats1(mk_srd(ws, WS_BYTES), OFF_STATS + (rg > 0 ? rg * 4 - 1 : 3) * 2048, t, cn);
            if (cn == 0) TMO(15);
            const float hm = gsum(t[0]) * (1.f / 576.f);
            const float hv = fmaxf(gsum(t[1]) * (1.f / 576.f) - hm * hm, 0.f);
            if (r16 > 1) sts64(L_MR + r16 * 8, ln_next(hm, hv, ln_m(hm), ln_rs(hv)));
            lds_inc(L_BC + BC_MC, lane);
        }
        if (layer == 0 && (w_ == 4 || w_ == 5)) {
            IDS
            u32 cn = SPIN_MAX;
            const int h = w - 4;
            if (xf) poll_flagh(mk_srd(ws, WS_BYTES), XFLAG_OFF, tag, cn);
            if (cn == 0) TMO(6);
            u32 own = OFF_X1G + rg * 73728, xp = own - (rg > 0 ? 73728 : 0) + h * 36864 - 8192, xq = xp + (h ? 0 : cb * 9216 + 8192);
            int th = 9 * cb - 8 * h, hh = 9 * cb + 8 - 36 * h;
            asm volatile("" : "+s"(own), "+s"(xp), "+s"(xq), "+s"(th), "+s"(hh));
            LWAIT0(L_BC + BC_PK, 4, 27);
            __builtin_amdgcn_s_setprio(3);
            gemm1p();
            cn = SPIN_MAX;
            xgather(mk_srd(ws, WS_BYTES), own, xp, xq, th, hh, 8192 + h * 8192, L_XH + h * 9216, cn);
            if ((int)cn < 1) TMO(39);
            __builtin_amdgcn_s_setprio(0);
        }
        if (w_ < 4) {
            if (layer == 1) {
                IDS
                const int grow = rg * 64 + w * 16 + r16;
                LWAIT(L_BC + BC_KC, 2, 24);
                const u32 best = lds32(L_BC + BC_TB + w * 4);
                const bool isb = (best & 0x7ffu) == (u32)(2047 - grow);
                const u32x2 mrs = lds64(L_RS + (16 * w + r16) * 8);
                u32 XT[10];
                xt_get(XT); xt_upd(XT, __uint_as_float(mrs.x), __uint_as_float(mrs.y));
                const __amdgpu_buffer_rsrc_t ro = __builtin_amdgcn_make_buffer_rsrc((void*)out, 0, 2359296, 0x00020000);
                const int ob = grow * 1152 + cb * 144;
                const u32x4 v0 = {XT[0], XT[1], XT[2], XT[3]}, v1 = {XT[4], XT[5], XT[6], XT[7]};
                const u32x2 v2 = {XT[8], XT[9]};
                if (!isb) {
                    __builtin_amdgcn_raw_buffer_store_b128(v0, ro, ob + g * 16, 0, 2);
                    __builtin_amdgcn_raw_buffer_store_b128(v1, ro, ob + 64 + g * 16, 0, 2);
                    if (g < 2) __builtin_amdgcn_raw_buffer_store_b64(v2, ro, ob + 128 + g * 8, 0, 2);
                }
                LWAIT(L_BC + BC_FIN, 1, 13);
                if (isb && best != lds32v(L_BC + BC_BEST1)) {
                    __builtin_amdgcn_raw_buffer_store_b128(v0, ro, ob + g * 16, 0, 0);
                    __builtin_amdgcn_raw_buffer_store_b128(v1, ro, ob + 64 + g * 16, 0, 0);
                    if (g < 2) __builtin_amdgcn_raw_buffer_store_b64(v2, ro, ob + 128 + g * 8, 0, 0);
                }
            }
        } else if (w_ == 4 && layer == 1) {
            IDS
            __builtin_amdgcn_s_setprio(3);
            u32 k, cn = SPIN_MAX;
            LWAIT(L_BC + BC_SP, 1, 17);
            poll_keys2(mk_srd(ws, WS_BYTES), OFF_KEY + (32 + (b & 31)) * 1024, tag, k, cn);
            if (cn == 0) TMO(9);
            k = rmax(k);
            const u32 best = umax2(umax2(__builtin_amdgcn_readlane(k, 0), __builtin_amdgcn_readlane(k, 16)), umax2(__builtin_amdgcn_readlane(k, 32), __builtin_amdgcn_readlane(k, 48)));
            sts32v(L_BC + BC_BEST1, best);
            sts32v(L_BC + BC_FIN, best == lds32v(L_BC + BC_SP) ? 1u : 2u);
        }
        if (w_ < 7) {
            if (layer == 0) {
                u32 cn = LSPIN_MAX, best, lc;
                {
                    IDS
                    int fb = L_BC;
                    lc = 2061 - 64 * rg;
                    asm volatile("" : "+v"(fb), "+s"(lc));
                    if (w < 4 && w != 3) { LWAIT0(L_BC + BC_PK, 4, 27); gemm1p(); }
                    LWAIT0(L_BC + BC_G0, 2, 16);
                    gemm1a(g * 1024 + r16 * 16, fb, L_CS + w * 128, VA_MASK, V0_MASK, w == 3 ? 5 : 4, best, cn);
                }
                if (w_ < 4 || best == 0u || lc - (best & 0x7ffu) < 78u) {
                IDS
                if (w < 4) __builtin_amdgcn_s_setprio(0);
                if (best == 0u) {
                    LWAIT(L_BC + BC_BEST, 1, 28);
                    best = __builtin_amdgcn_readfirstlane(lds32v(L_BC + BC_BEST));
                }
                if (w == 0) {
                    const u64 lm = LATE_MASK(2047 - (int)(best & 0x7ffu));
                    if (b == 255) BST32(tag, lane == 0 ? OFF_EPOCH : OOB, 0);
                    u32 sw_ = SENT; asm volatile("" : "+v"(sw_));
                    const u32x4 sv = {sw_, sw_, sw_, sw_};
#pragma nounroll
                    for (int t = 0; t < 3; ++t) {
                        const bool on = t == 2 ? b >= 112 : (b < 144 && ((lm >> (2 * (rg ^ (16 * t)))) & 1) == (u64)t);
                        const int o = t == 2 ? OFF_YP2 + (b - 112) * 4096 : OFF_YP + YP_B + (b ^ (4 * t)) * 4096;
                        if (on) {
#pragma unroll
                            for (int i = 0; i < 4; ++i) BST128(sv, lane * 16 + i * 1024, o);
                        }
                    }
                }
                if (w < 3) {
                    LWAIT(L_BC + BC_C1, 7, 48);
                    WO_PRE();
                }
                if (lc - (best & 0x7ffu) < 78u)
                    gemm1b(L_BC, L_XR + cb * 144 + g * 16, L_XR + cb * 144 + 128 + (g & 1) * 8, w + 1, L_CS + w * 128, VA_MASK, V0_MASK, cn);
                rs_get(L_RS + (16 * (w & 3) + r16) * 8);
                }
                if ((int)cn < 1) TMO(16);
            }
        } else {
            IDS
            if (layer == 1) __builtin_amdgcn_s_setprio(3);
            int ridx;
#pragma nounroll
            for (int rd = layer; ; ++rd) {
                IDS
                u32 c0 = r16 * 64 + g * 16, c1 = r16 < 2 ? c0 + 1024 : 1152;
                u32 ya = (lane >> 2) * 16384u + (lane & 3) * 16u, ye = (32 + g) * 16384u + r16 * 4u;
                asm volatile("" : "+v"(c0), "+v"(c1), "+v"(ya), "+v"(ye));
                const u32x4 sws = mk_srd(ws, WS_BYTES);
                u32 yp = rd == 2 ? OFF_YP2 : OFF_YP + rd * YP_B;
                asm volatile("" : "+s"(yp));
                u32 best;
                if (rd == 2) best = __builtin_amdgcn_readfirstlane(lds32v(L_BC + BC_BEST1));
                else {
                    u32 k, cn = SPIN_MAX;
                    const bool late = (lm0 >> (2 * rg)) & 1;
                    u32 ko = OFF_KEY + (layer * 32 + (b & 31)) * 1024;
                    asm volatile("" : "+s"(ko));
                    VMWAIT(0);
                    __builtin_amdgcn_s_setprio(3);
                    gu_pre();
                    if (!late) {
                        LWAIT(L_BC + BC_KC, layer + 1, 29);
                        if (layer == 0) __builtin_amdgcn_s_sleep(5);
                        if (layer == 1) __builtin_amdgcn_s_sleep(9);
                    }
                    if (layer == 0) LWAIT0(L_BC + BC_PP, 3, 33);
                    poll_keys(sws, ko, tag, lm0, k, cn);
                    if (cn == 0) TMO(7);
                    k = rmax(k);
                    best = umax2(umax2(__builtin_amdgcn_readlane(k, 0), __builtin_amdgcn_readlane(k, 16)), umax2(__builtin_amdgcn_readlane(k, 32), __builtin_amdgcn_readlane(k, 48)));
                }
                ridx = 2047 - (int)(best & 0x7ffu);
                sts32v(L_BC + BC_BEST + rd * 8, best);
                float fm, frs;
                {
                    u32 cn = LSPIN_MAX;
                    ffn(sws, OFF_CROW + (layer * 128 + (ridx >> 4)) * 1280, c0, c1, ya, ye, yp + b * 64, rd >> 1, fm, frs, cn);
                    if ((int)cn < 1) TMO(14);
                }
                u32 ab = 3 - (rd & 1);
                if (b < 144) {
                    const int q = (b & 7) * 18 + (b >> 3), c0 = q < 128 ? 8 * (q & 63) + 4 * (q >> 6) : 4 * q;
                    const u32 sbits = best >> 16;
                    const u32 sraw = (sbits & 0x8000u) ? (sbits & 0x7fffu) : (~sbits & 0xffffu);
                    const float sc = __uint_as_float(sraw << 16);
                    const float sg = rbf(__builtin_amdgcn_rcpf(1.f + __builtin_amdgcn_exp2f(-sc * 1.4426950408889634f)));
                    const u32 so = (lane & 31) == 16 ? 2 * (g & 2) : OOB;
                    u32 cn = LSPIN_MAX;
                    reduce(mk_srd(ws, WS_BYTES), mk_srd(out, 2359296), lane * 64u, yp + (q >> 2) * 16384 + (q & 3) * 16, L_XR + 2 * c0 + 2 * (g & 2), fm, frs, sg,
                           so + (layer == 0 ? OFF_X2R + 32 * q : OOB), so + (layer == 1 ? ridx * 1152 + 2 * c0 : OOB), L_BC + BC_FIN, ab, cn);
                    if ((int)cn < 1) TMO(8);
                } else if (rd == 1) {
                    LWAIT0(L_BC + BC_FIN, 1, 18);
                    ab = __builtin_amdgcn_readfirstlane(lds32v(L_BC + BC_FIN));
                }
                if (ab != 2u) break;
            }
            lm0 = LATE_MASK(ridx);
            const int prow = ridx - (rg * 64 - 16);
            const bool affected = (lm0 >> (2 * rg)) & 1;
            if (layer == 0 && affected) {
                LWAIT0(L_BC + BC_G0 + 16, 2, 41);
                if (prow >= 16) sts32v(L_RS + (prow - 16) * 8 + 4, 0u);
                u32 o[6], cn = SPIN_MAX;
                int h0 = L_XH + k_pos(lane, cb) * 256, h1 = L_XH + k_pos(64 + (r16 >> 1), cb) * 256 + (r16 & 1) * 8;
                u32 gw = 1u | (u32)prow << 8;
                asm volatile("" : "+v"(h0), "+v"(h1), "+v"(gw));
                poll_row(mk_srd(ws, WS_BYTES), OFF_X2R + lane * 32, OFF_X2R + 4096 + r16 * 32, o, cn);
                if (cn == 0) TMO(2);
                u32x4 c0 = {o[0], o[1], o[2], o[3]}; u32x2 c1 = {o[4], o[5]};
                sts128(h0, c0);
                sts64(h1, c1);
                CFENCE();
                sts32v(L_BC + BC_GO, gw);
                CFENCE();
                sts128(L_XR + lane * 16, c0);
                sts64(L_XR + 1024 + r16 * 8, c1);
                if (lane >= 16) c1 = (u32x2){0u, 0u};
                float psx = 0.f, psq = 0.f;
#pragma unroll
                for (int i = 0; i < 4; ++i) {
                    psx = dot2(c0[i], ONES2, psx); psq = dot2(c0[i], c0[i], psq);
                    if (i < 2) { psx = dot2(c1[i], ONES2, psx); psq = dot2(c1[i], c1[i], psq); }
                }
                psx = gsum(rsum(psx)); psq = gsum(rsum(psq));
                CFENCE();
                const float pm0 = psx * (1.f / 576.f);
                const float prs = ln_rs(fmaxf(psq * (1.f / 576.f) - pm0 * pm0, 0.f)), pm = ln_m(pm0);
                sts32v(L_MR, __float_as_uint(pm)); sts32v(L_MR + 4, __float_as_uint(prs)); sts32v(L_BC + BC_PST, 255u);
            }
            __builtin_amdgcn_s_setprio(0);
        }
    }
    if (tmo != 0u) BST32(tmo, OFF_TMO, 0);
    asm volatile("Lcode1:");
}

extern "C" void kernel_launch(void* const* d_in, const int* in_sizes, int n_in, void* d_out, int out_size, void* d_ws, size_t ws_size,
                              hipStream_t stream) {
    (void)in_sizes; (void)n_in; (void)out_size; (void)ws_size;
    hipFuncSetAttribute((const void*)mega, hipFuncAttributeMaxDynamicSharedMemorySize, LDS_BYTES);
    mega<<<256, 512, LDS_BYTES, stream>>>((const u16*)d_in[0], (const u16*)d_in[1], (const u16*)d_in[2], (const u16*)d_in[4],
                                          (const u16*)d_in[5], (const u16*)d_in[6], (const u16*)d_in[7], (u16*)d_out, (char*)d_ws);
}
```

```cpp
#include <hip/hip_runtime.h>
#include <stdint.h>

#define WREP 8
#define FST 32
#define NTD " nt"
#define XFS 32
#define XCI 0x04208760u
#define XCN 0x02221110u
#define PFA 3

#if defined(__HIP_DEVICE_COMPILE__)
#define NOPK __attribute__((target("no-packed-fp32-ops")))
#else
#define NOPK
#endif
#define DEV __device__ __forceinline__ NOPK
typedef unsigned short u16;
typedef unsigned int u32;
typedef unsigned long long u64;
#define __uint_as_float(x) __builtin_bit_cast(float, (u32)(x))
#define __float_as_uint(x) __builtin_bit_cast(u32, (float)(x))
#define fmaxf(a, b) __builtin_fmaxf((a), (b))
typedef __attribute__((ext_vector_type(2))) float f32x2;
typedef __attribute__((ext_vector_type(2))) __bf16 bf16x2;
typedef __attribute__((ext_vector_type(2))) u32 u32x2;
typedef __attribute__((ext_vector_type(4))) u32 u32x4;

extern __shared__ __attribute__((aligned(16))) char smem[];

#define SPIN_MAX 20000
#define LSPIN_MAX 400000

#define WS_BYTES 0x1000000
#define OFF_EPOCH 0
#define OFF_TMO 256
#define OFF_FLAG 0xF0000
#define OFF_GFLAG 4096
#define OFF_XFLAG 0x18000
#define OFF_KEY 16384
#define OFF_X2R 81920
#define OFF_STATS 0x20000
#define STATS_B 0x40000
#define OFF_CROW 0xA0000
#define OFF_YP 0x100000
#define YP_B 589824
#define OFF_OT 0x240000
#define OFF_X1G 0x6C0000
#define OFF_WI 0x900000
#define OFF_WOI 0xB00000
#define OFF_YP2 0xD00000
#define OOB 0x7FFFFF00

#define LDS_BYTES 163840
#define XCH 10240
#define L_XH 73728
#define L_KV 0
#define KVS 304
#define L_Q 24320
#define L_WOE 33536
#define L_SCR 18432
#define L_G 98048
#define L_U 125696
#define L_MISC 153344
#define L_MR (L_MISC)
#define L_BC (L_MISC + 640)
#define BC_BEST 0
#define BC_SP 8
#define BC_WRS 4
#define BC_GC 16
#define BC_C1 20
#define BC_GO 28
#define BC_PST 32
#define BC_FIN 36
#define BC_BEST1 40
#define BC_XC 44
#define BC_ZERO 48
#define BC_G0 96
#define BC_TB 168
#define BC_KC 64
#define BC_MC 160
#define BC_FC 76
#define BC_PP 56
#define BC_PK 84
#define BC_PC 152
#define BC_WC 144
#define L_RS 92160
#define L_PK(t) ((t) < 2 ? 92672 + (t) * 2304 : ((t) == 2 ? L_SCR3 : L_HROW))
#define L_WR (L_MISC + 9888)
#define L_CS (L_MISC + 928)
#define L_HROW (L_MISC + 1824)
#define L_XR (L_MISC + 4128)
#define L_SCR3 (L_MISC + 5280)

#define SENT 0xFFFFFFFFu

DEV void bar() {
    asm volatile("s_waitcnt lgkmcnt(0)" ::: "memory");
    __builtin_amdgcn_s_barrier();
    asm volatile("" ::: "memory");
}
#define VMWAIT(n) asm volatile("s_waitcnt vmcnt(" #n ")" ::: "memory")
DEV void vmwaitn(const int n) { asm volatile("s_waitcnt vmcnt(%0)" :: "n"(n) : "memory"); }
DEV void vmwait8n(u32 (&d)[8], const int n) {
    asm volatile("s_waitcnt vmcnt(%8)" : "+v"(d[0]), "+v"(d[1]), "+v"(d[2]), "+v"(d[3]), "+v"(d[4]), "+v"(d[5]), "+v"(d[6]), "+v"(d[7]) : "n"(n) : "memory");
}
DEV u32x4 mk_srd(const void* p, u32 bytes) {
    u64 a = (u64)p;
    asm volatile("" : "+s"(a));
    return (u32x4){(u32)a, (u32)(a >> 32) & 0xffffu, bytes, 0x00020000u};
}
DEV void dma16g(u32x4 srd, u32 voff, u32 soff, u32 lds_dst) {
    asm volatile("s_mov_b32 m0, %0\n\ts_nop 3\n\tbuffer_load_dwordx4 %1, %2, %3 offen lds" :: "s"(lds_dst), "v"(voff), "s"(srd), "s"(soff) : "memory");
}
DEV void dma16x4_sc1(u32x4 srd, u32 voff, u32 soff, u32 lds_dst) {
    asm volatile("s_mov_b32 m0, %0\n\ts_nop 3\n\tbuffer_load_dwordx4 %1, %2, %3 offen sc1 lds\n\tbuffer_load_dwordx4 %1, %2, %3 offen offset:1024 sc1 lds\n\t"
                 "buffer_load_dwordx4 %1, %2, %3 offen offset:2048 sc1 lds\n\tbuffer_load_dwordx4 %1, %2, %3 offen offset:3072 sc1 lds"
                 :: "s"(lds_dst), "v"(voff), "s"(srd), "s"(soff) : "memory");
}
DEV void dma16x2_sc1(u32x4 srd, u32 voff, u32 soff, u32 lds_dst) {
    asm volatile("s_mov_b32 m0, %0\n\ts_nop 3\n\tbuffer_load_dwordx4 %1, %2, %3 offen sc1 lds\n\tbuffer_load_dwordx4 %1, %2, %3 offen offset:1024 sc1 lds"
                 :: "s"(lds_dst), "v"(voff), "s"(srd), "s"(soff) : "memory");
}
DEV void bld32(u32& d, u32x4 srd, u32 voff, u32 soff, const int imm) {
    asm volatile("s_nop 4\n\tbuffer_load_dword %0, %1, %2, %3 offen offset:%4" : "=v"(d) : "v"(voff), "s"(srd), "s"(soff), "n"(imm));
}
DEV void old8(u32 (&d)[8], u32x4 srd, u32 voff, u32 s2) {
    asm volatile("s_nop 4\n\tbuffer_load_dword %0, %8, %9, 0 offen nt\n\tbuffer_load_dword %1, %8, %9, 0 offen offset:1152 nt\n\t"
                 "buffer_load_dword %2, %8, %9, 0 offen offset:2304 nt\n\tbuffer_load_dword %3, %8, %9, 0 offen offset:3456 nt\n\t"
                 "buffer_load_dword %4, %8, %9, %10 offen nt\n\tbuffer_load_dword %5, %8, %9, %10 offen offset:1152 nt\n\t"
                 "buffer_load_dword %6, %8, %9, %10 offen offset:2304 nt\n\tbuffer_load_dword %7, %8, %9, %10 offen offset:3456 nt"
                 : "=&v"(d[0]), "=&v"(d[1]), "=&v"(d[2]), "=&v"(d[3]), "=&v"(d[4]), "=&v"(d[5]), "=&v"(d[6]), "=&v"(d[7])
                 : "v"(voff), "s"(srd), "s"(s2));
}
DEV void wld8p(u32x4 srd, u32 voff, u32 soff, int w) {
    u32 t;
    asm volatile("s_cmp_gt_u32 %4, 3\n\ts_cbranch_scc1 Lw8_%=\n\ts_setprio 3\n\ts_nop 1\n\t"
                 "buffer_load_dword v48, %1, %2, %3 offen nt\n\tbuffer_load_dword v49, %1, %2, %3 offen offset:3456 nt\n\ts_add_u32 %0, %3, 6912\n\t"
                 "buffer_load_dword v50, %1, %2, %0 offen nt\n\tbuffer_load_dword v51, %1, %2, %0 offen offset:3456 nt\n\ts_add_u32 %0, %3, 13824\n\t"
                 "buffer_load_dword v52, %1, %2, %0 offen nt\n\tbuffer_load_dword v53, %1, %2, %0 offen offset:3456 nt\n\ts_add_u32 %0, %3, 20736\n\t"
                 "buffer_load_dword v54, %1, %2, %0 offen nt\n\tbuffer_load_dword v55, %1, %2, %0 offen offset:3456 nt\n"
                 "Lw8_%=:"
                 : "=&s"(t) : "v"(voff), "s"(srd), "s"(soff), "s"(w) : "scc", "memory");
}
DEV void x_dma(u32x4 srd, u32 xv, u32 orm, u32 c, u32 n) {
    u32 z1, z2, z3, y0, y1, so, t;
    asm volatile("s_cmp_eq_u32 %8, 0\n\ts_cbranch_scc1 Lx1_%=\n\t"
                 "v_xor_b32 %2, 64, %9\n\tv_or_b32 %3, %11, %9\n\tv_add_u32 %0, 0x2000, %2\n\tv_add_u32 %1, 0x4000, %9\n\tv_or_b32 %4, %11, %0\n\tv_add_u32 %2, 0x6000, %2\n"
                 "Lx0_%=:\n\t"
                 "s_cmp_gt_u32 %7, 8\n\ts_cselect_b32 %6, 9, 0\n\ts_sub_u32 %7, %7, %6\n\ts_mul_i32 m0, %7, 10240\n\ts_lshl_b32 %5, %7, 7\n\t"
                 "s_mov_b32 exec_lo, 0xffff0000\n\tbuffer_load_dwordx4 %3, %10, %5 offen lds\n\ts_mov_b32 exec_lo, -1\n\t"
                 "buffer_load_dwordx4 %4, %10, %5 offen offset:1024 lds\n\tbuffer_load_dwordx4 %1, %10, %5 offen offset:2048 lds\n\tbuffer_load_dwordx4 %2, %10, %5 offen offset:3072 lds\n\t"
                 "s_add_u32 m0, m0, 4096\n\ts_add_u32 %5, %5, 36864\n\t"
                 "buffer_load_dwordx4 %9, %10, %5 offen lds\n\tbuffer_load_dwordx4 %0, %10, %5 offen offset:1024 lds\n\t"
                 "buffer_load_dwordx4 %1, %10, %5 offen offset:2048 lds\n\tbuffer_load_dwordx4 %2, %10, %5 offen offset:3072 lds\n\t"
                 "s_add_u32 m0, m0, 4096\n\ts_add_u32 %5, %5, 36864\n\t"
                 "buffer_load_dwordx4 %9, %10, %5 offen lds\n\tbuffer_load_dwordx4 %0, %10, %5 offen offset:1024 lds\n\t"
                 "s_add_u32 %7, %7, 1\n\ts_sub_u32 %8, %8, 1\n\ts_cmp_lg_u32 %8, 0\n\ts_cbranch_scc1 Lx0_%=\n"
                 "Lx1_%=:"
                 : "=&v"(z1), "=&v"(z2), "=&v"(z3), "=&v"(y0), "=&v"(y1), "=&s"(so), "=&s"(t), "+s"(c), "+s"(n) : "v"(xv), "s"(srd), "s"(orm) : "scc", "memory");
}
DEV void wunit_st(u32x4 srd, u32 ib, int w) {
    asm volatile("s_cmp_gt_u32 %2, 3\n\ts_cbranch_scc1 Lu_%=\n\ts_waitcnt vmcnt(0)\n\t"
                 "v_perm_b32 v56, v49, v48, %3\n\tv_perm_b32 v57, v51, v50, %3\n\tv_perm_b32 v58, v53, v52, %3\n\tv_perm_b32 v59, v55, v54, %3\n\t"
                 "buffer_store_dwordx4 v[56:59], %0, %1, 0 offen sc1\n\t"
                 "v_perm_b32 v60, v49, v48, %4\n\tv_perm_b32 v61, v51, v50, %4\n\tv_perm_b32 v62, v53, v52, %4\n\tv_perm_b32 v63, v55, v54, %4\n\t"
                 "buffer_store_dwordx4 v[60:63], %0, %1, 0 offen offset:1024 sc1\n"
                 "Lu_%=:" :: "v"(ib), "s"(srd), "s"(w), "s"(0x05040100u), "s"(0x07060302u) : "scc", "memory");
}
DEV u32 plo(u32 hi, u32 lo) { return __builtin_amdgcn_perm(hi, lo, 0x05040100u); }
DEV u32 phi(u32 hi, u32 lo) { return __builtin_amdgcn_perm(hi, lo, 0x07060302u); }
DEV void wfrag(const u32 (&d)[8], u32x4& f0, u32x4& f1) {
    f0 = (u32x4){plo(d[1], d[0]), plo(d[3], d[2]), plo(d[5], d[4]), plo(d[7], d[6])};
    f1 = (u32x4){phi(d[1], d[0]), phi(d[3], d[2]), phi(d[5], d[4]), phi(d[7], d[6])};
}
DEV u32 pk2(float a, float b) { f32x2 v = {a, b}; return __builtin_bit_cast(u32, __builtin_convertvector(v, bf16x2)); }
DEV float rbf(float a) { return (float)(__bf16)a; }
DEV float ln_rs(float var) { return __builtin_amdgcn_rcpf(rbf(__builtin_amdgcn_sqrtf(rbf(rbf(var) + 1e-5f)))); }
DEV float ln_m(float m) { return rbf(m); }
DEV u32x2 ln_next(float m, float var, float mb, float rs) {
    const float c = 1.f + rs;
    const float mb2 = ln_m(c * m - mb * rs);
    return (u32x2){__float_as_uint((mb * rs + mb2) * __builtin_amdgcn_rcpf(c)), __float_as_uint(c * ln_rs(c * c * var))};
}
DEV float lo16(u32 v) { return __uint_as_float(v << 16); }
DEV float hi16(u32 v) { return __uint_as_float(v & 0xffff0000u); }
DEV u32 san(u32 v) { return v < 0xFFFFFFFEu ? v : 0xFFFFFFFEu; }
DEV u32 umax2(u32 a, u32 b) { return a > b ? a : b; }
DEV float dot2(u32 a, u32 b, float c) {
    return __builtin_amdgcn_fdot2_f32_bf16(__builtin_bit_cast(bf16x2, a), __builtin_bit_cast(bf16x2, b), c, false);
}
DEV float gsum(float v) {
    u32 u = __float_as_uint(v);
    auto a = __builtin_amdgcn_permlane16_swap(u, u, false, false);
    v = __uint_as_float(a[0]) + __uint_as_float(a[1]);
    u = __float_as_uint(v);
    auto c = __builtin_amdgcn_permlane32_swap(u, u, false, false);
    return __uint_as_float(c[0]) + __uint_as_float(c[1]);
}
#define DPP(v, ctrl) ((u32)__builtin_amdgcn_update_dpp(0, (int)(v), (ctrl), 0xf, 0xf, false))
DEV u32 rmax(u32 v) {
    v = umax2(v, DPP(v, 0x128)); v = umax2(v, DPP(v, 0x124)); v = umax2(v, DPP(v, 0x122)); v = umax2(v, DPP(v, 0x121));
    return v;
}
DEV float rsum(float v) {
    v += __uint_as_float(DPP(__float_as_uint(v), 0x128)); v += __uint_as_float(DPP(__float_as_uint(v), 0x124));
    v += __uint_as_float(DPP(__float_as_uint(v), 0x122)); v += __uint_as_float(DPP(__float_as_uint(v), 0x121));
    return v;
}
DEV u32 lds32(int off) { return *(const u32*)(smem + off); }
typedef __attribute__((address_space(3))) volatile u32* lds_vu32;
DEV u32 lds32v(int off) { return *(lds_vu32)(smem + off); }
DEV u32x2 lds64(int off) { return *(const u32x2*)(smem + off); }
DEV u32x4 lds128(int off) { return *(const u32x4*)(smem + off); }
DEV void sts32(int off, u32 v) { *(u32*)(smem + off) = v; }
DEV void sts32v(int off, u32 v) { *(lds_vu32)(smem + off) = v; }
DEV void sts64(int off, u32x2 v) { *(u32x2*)(smem + off) = v; }
DEV void sts128(int off, u32x4 v) { *(u32x4*)(smem + off) = v; }
DEV void lds_inc(int off, int lane) {
    (void)lane;
    asm volatile("s_mov_b64 exec, 1\n\tds_add_u32 %0, %1\n\ts_mov_b64 exec, -1" :: "v"(off), "v"(1u) : "memory");
}
DEV u32 lds_inc_rtn(int off) {
    u32 old;
    asm volatile("s_mov_b64 exec, 1\n\tds_add_rtn_u32 %0, %1, %2\n\ts_mov_b64 exec, -1\n\ts_waitcnt lgkmcnt(0)" : "=&v"(old) : "v"(off), "v"(1u) : "memory");
    return __builtin_amdgcn_readfirstlane(old);
}
#define BST128(v, off, soff) __builtin_amdgcn_raw_buffer_store_b128((v), rs, (off) + (soff), 0, 16)
#define BST64(v, off, soff) __builtin_amdgcn_raw_buffer_store_b64((v), rs, (off), (soff), 16)
#define BST32(v, off, soff) __builtin_amdgcn_raw_buffer_store_b32((v), rs, (off), (soff), 16)
#define CFENCE() asm volatile("" ::: "memory")
#define SB() __builtin_amdgcn_sched_barrier(0)
#define TMO(code) (tmo = (code))
#define LWAIT(off, target, code) do { int it_ = 0; _Pragma("nounroll") for (; it_ < LSPIN_MAX; ++it_) { if (lds32v(off) >= (u32)(target)) break; __builtin_amdgcn_s_sleep(1); } \
    if (it_ == LSPIN_MAX) TMO(code); CFENCE(); } while (0)

#define LWAIT0(off, target, code) do { int it_ = 0; _Pragma("nounroll") for (; it_ < LSPIN_MAX; ++it_) { if (lds32v(off) >= (u32)(target)) break; } \
    if (it_ == LSPIN_MAX) TMO(code); CFENCE(); } while (0)

#define ONES2 0x3F803F80u
#define LATE_MASK(r) ((((r) & 63) >= 50 ? 15ull : 3ull) << (((r) >> 6) * 2))
#define RG_OF(b) (((b) & 7) * 4 + (((b) >> 3) & 3))
#define CB_OF(b) ((b) >> 5)
#define IDS \
    int lane; asm volatile("v_mbcnt_lo_u32_b32 %0, -1, 0\n\tv_mbcnt_hi_u32_b32 %0, -1, %0" : "=v"(lane)); \
    int w = w_; asm volatile("" : "+s"(w)); \
    int b = b_; asm volatile("" : "+s"(b)); \
    const int rg = RG_OF(b), cb = CB_OF(b); \
    const int r16 = lane & 15, g = lane >> 4; \
    (void)rg; (void)cb; (void)r16; (void)g; (void)w;

#define CSL(w) ((0x4653210 >> (4 * (w))) & 15)
struct LaneC { int ka, kt, qa, qt, va, ta, l0; u32 av, tv0, ot; int sd, sstr; };
DEV LaneC lane_consts(int w_, int b_) {
    IDS
    LaneC c;
    const int rt4 = w & 3, pp = w >> 2;
    const int kvb = L_KV + 16 * rt4 * KVS + 72 * pp;
    const int qb = L_Q + (16 * rt4 + r16) * 144 + 72 * pp;
    const int trr = kvb + (4 * g + (r16 >> 2)) * KVS + 144;
    c.kt = kvb + r16 * KVS + 32; c.ka = kvb + r16 * KVS + g * 8;
    c.qa = qb + g * 8; c.qt = g == 2 ? qb + 32 : L_BC + BC_ZERO;
    c.va = trr + (r16 & 3) * 8; c.ta = trr + 32;
    c.l0 = 4096 + (g >> 1) * 4608 + (lane & 31) * 16;
    c.av = lane * 16;
    c.tv0 = 16384 + g * 256 + r16 * 8;
    c.ot = 0;
    const int P = 4 * CSL(w) + g;
    c.sstr = P >= 18 ? 144 : KVS;
    c.sd = (P >= 18 ? (L_Q - 16 * 144 + (P - 18) * 16) : (L_KV + P * 16)) + r16 * c.sstr;
    c.sstr *= 16;
    return c;
}

struct MidS { u32x4 srd; u32 ob, st, tt, c0, l1d, tvd, wi, ld, wx, ldx, msk, scr, stat, x1, layer, pk, n7, skip, wc, lb; };

DEV void lc_set(const LaneC& c) {
    asm volatile("v_mov_b32 v100, %0\n\t" "v_mov_b32 v101, %1\n\t" "v_mov_b32 v102, %2\n\t" "v_mov_b32 v103, %3\n\t" "v_mov_b32 v104, %4\n\t" "v_mov_b32 v105, %5\n\t" "v_mov_b32 v106, %6\n\t" "v_mov_b32 v107, %7\n\t" "v_mov_b32 v108, %8\n\t" "v_mov_b32 v110, %10\n\t" "v_mov_b32 v111, %11\n\t"
        :: "v"(c.ka), "v"(c.kt), "v"(c.qa), "v"(c.qt), "v"(c.va), "v"(c.ta), "v"(c.l0), "v"(c.av), "v"(c.tv0), "v"(c.ot), "v"(c.sd), "v"(c.sstr));
}
DEV void xt_get(u32 (&x)[10]) {
    asm volatile("v_mov_b32 %0, v88\n\t" "v_mov_b32 %1, v89\n\t" "v_mov_b32 %2, v90\n\t" "v_mov_b32 %3, v91\n\t" "v_mov_b32 %4, v92\n\t" "v_mov_b32 %5, v93\n\t" "v_mov_b32 %6, v94\n\t" "v_mov_b32 %7, v95\n\t" "v_mov_b32 %8, v96\n\t" "v_mov_b32 %9, v97\n\t"
        : "=v"(x[0]), "=v"(x[1]), "=v"(x[2]), "=v"(x[3]), "=v"(x[4]), "=v"(x[5]), "=v"(x[6]), "=v"(x[7]), "=v"(x[8]), "=v"(x[9]));
}
DEV void rs_get(int a) { asm volatile("ds_read_b64 v[98:99], %0" :: "v"(a) : "memory"); }

DEV void gemm0(u32x4 srd, int xa, u32 iv, u32 si, int (&xt)[3], u32 ro, u32 cm, u32 c0, u32 c1, u32 fo, u32 fexp, u32 cs, u64 va, u64 v0, u32& cn) {
    int xb; u32 fl, sf;
    asm volatile(
        "s_nop 4\n\t"
        "s_setprio 2\n\t"
        "buffer_load_dwordx4 v[112:115], %[iv], %[ssrd], %[si] offen offset:0 sc1\n\t"
        "buffer_load_dwordx4 v[116:119], %[iv], %[ssrd], %[si] offen offset:1024 sc1\n\t"
        "buffer_load_dwordx4 v[120:123], %[iv], %[ssrd], %[si] offen offset:2048 sc1\n\t"
        "buffer_load_dwordx4 v[124:127], %[iv], %[ssrd], %[si] offen offset:3072 sc1\n\t"
        "s_add_u32 %[si], %[si], 4096\n\t"
        "buffer_load_dwordx4 v[128:131], %[iv], %[ssrd], %[si] offen offset:0 sc1\n\t"
        "buffer_load_dwordx4 v[132:135], %[iv], %[ssrd], %[si] offen offset:1024 sc1\n\t"
        "buffer_load_dwordx4 v[136:139], %[iv], %[ssrd], %[si] offen offset:2048 sc1\n\t"
        "buffer_load_dwordx4 v[140:143], %[iv], %[ssrd], %[si] offen offset:3072 sc1\n\t"
        "s_add_u32 %[si], %[si], 4096\n\t"
        "buffer_load_dwordx4 v[144:147], %[iv], %[ssrd], %[si] offen offset:0 sc1\n\t"
        "buffer_load_dwordx4 v[148:151], %[iv], %[ssrd], %[si] offen offset:1024 sc1\n\t"
        "ds_read_b128 v[88:91], %[x0]\n\t"
        "ds_read_b128 v[92:95], %[x1]\n\t"
        "ds_read_b64 v[96:97], %[x2]\n\t"
        "v_xor_b32 %[xb], 64, %[xa]\n\t"
        "v_subrev_u32 %[x0], %[sro], %[x0]\n\t"
        "v_subrev_u32 %[x1], %[sro], %[x1]\n\t"
        "ds_read_b128 v[24:27], %[x0] offset:0\n\t"
        "ds_read_b128 v[28:31], %[x0] offset:2048\n\t"
        "ds_read_b128 v[32:35], %[x0] offset:4096\n\t"
        "ds_read_b128 v[36:39], %[x0] offset:6144\n\t"
        "ds_read_b128 v[40:43], %[x0] offset:8192\n\t"
        "ds_read_b128 v[44:47], %[x1] offset:0\n\t"
        "s_waitcnt vmcnt(8) lgkmcnt(5)\n\t"
        "v_mfma_f32_16x16x32_bf16 v[48:51], v[112:115], v[24:27], 0\n\t"
        "v_mfma_f32_16x16x32_bf16 v[68:71], v[116:119], v[24:27], 0\n\t"
        "ds_read_b128 v[24:27], %[x1] offset:2048\n\t"
        "s_waitcnt lgkmcnt(5)\n\t"
        "v_mfma_f32_16x16x32_bf16 v[52:55], v[112:115], v[28:31], 0\n\t"
        "v_mfma_f32_16x16x32_bf16 v[72:75], v[116:119], v[28:31], 0\n\t"
        "buffer_load_dwordx4 v[152:155], %[iv], %[ssrd], %[si] offen offset:2048 sc1\n\t"
        "buffer_load_dwordx4 v[156:159], %[iv], %[ssrd], %[si] offen offset:3072 sc1\n\t"
        "ds_read_b128 v[28:31], %[x1] offset:4096\n\t"
        "s_waitcnt lgkmcnt(5)\n\t"
        "v_mfma_f32_16x16x32_bf16 v[56:59], v[112:115], v[32:35], 0\n\t"
        "v_mfma_f32_16x16x32_bf16 v[76:79], v[116:119], v[32:35], 0\n\t"
        "ds_read_b128 v[32:35], %[x1] offset:6144\n\t"
        "s_waitcnt lgkmcnt(5)\n\t"
        "v_mfma_f32_16x16x32_bf16 v[60:63], v[112:115], v[36:39], 0\n\t"
        "v_mfma_f32_16x16x32_bf16 v[80:83], v[116:119], v[36:39], 0\n\t"
        "ds_read_b128 v[36:39], %[x1] offset:8192\n\t"
        "s_waitcnt lgkmcnt(5)\n\t"
        "v_mfma_f32_16x16x32_bf16 v[64:67], v[112:115], v[40:43], 0\n\t"
        "v_mfma_f32_16x16x32_bf16 v[84:87], v[116:119], v[40:43], 0\n\t"
        "s_bitcmp1_b32 %[scm], 0\n\t"
        "s_cbranch_scc1 Lc0_%=\n\t"
        "Ld0_%=:\n\t"
        "ds_read_b128 v[40:43], %[xa] offset:0\n\t"
        "s_waitcnt vmcnt(8) lgkmcnt(5)\n\t"
        "v_mfma_f32_16x16x32_bf16 v[48:51], v[120:123], v[44:47], v[48:51]\n\t"
        "v_mfma_f32_16x16x32_bf16 v[68:71], v[124:127], v[44:47], v[68:71]\n\t"
        "ds_read_b128 v[44:47], %[xa] offset:2048\n\t"
        "s_waitcnt lgkmcnt(5)\n\t"
        "v_mfma_f32_16x16x32_bf16 v[52:55], v[120:123], v[24:27], v[52:55]\n\t"
        "v_mfma_f32_16x16x32_bf16 v[72:75], v[124:127], v[24:27], v[72:75]\n\t"
        "s_add_u32 %[si], %[si], 4096\n\t"
        "buffer_load_dwordx4 v[160:163], %[iv], %[ssrd], %[si] offen offset:0 sc1\n\t"
        "buffer_load_dwordx4 v[164:167], %[iv], %[ssrd], %[si] offen offset:1024 sc1\n\t"
        "ds_read_b128 v[24:27], %[xa] offset:4096\n\t"
        "s_waitcnt lgkmcnt(5)\n\t"
        "v_mfma_f32_16x16x32_bf16 v[56:59], v[120:123], v[28:31], v[56:59]\n\t"
        "v_mfma_f32_16x16x32_bf16 v[76:79], v[124:127], v[28:31], v[76:79]\n\t"
        "ds_read_b128 v[28:31], %[xa] offset:6144\n\t"
        "s_waitcnt lgkmcnt(5)\n\t"
        "v_mfma_f32_16x16x32_bf16 v[60:63], v[120:123], v[32:35], v[60:63]\n\t"
        "v_mfma_f32_16x16x32_bf16 v[80:83], v[124:127], v[32:35], v[80:83]\n\t"
        "ds_read_b128 v[32:35], %[xa] offset:8192\n\t"
        "s_waitcnt lgkmcnt(5)\n\t"
        "v_mfma_f32_16x16x32_bf16 v[64:67], v[120:123], v[36:39], v[64:67]\n\t"
        "v_mfma_f32_16x16x32_bf16 v[84:87], v[124:127], v[36:39], v[84:87]\n\t"
        "ds_read_b128 v[36:39], %[xb] offset:0\n\t"
        "s_waitcnt vmcnt(8) lgkmcnt(5)\n\t"
        "v_mfma_f32_16x16x32_bf16 v[48:51], v[128:131], v[40:43], v[48:51]\n\t"
        "v_mfma_f32_16x16x32_bf16 v[68:71], v[132:135], v[40:43], v[68:71]\n\t"
        "ds_read_b128 v[40:43], %[xb] offset:2048\n\t"
        "s_waitcnt lgkmcnt(5)\n\t"
        "v_mfma_f32_16x16x32_bf16 v[52:55], v[128:131], v[44:47], v[52:55]\n\t"
        "v_mfma_f32_16x16x32_bf16 v[72:75], v[132:135], v[44:47], v[72:75]\n\t"
        "buffer_load_dwordx4 v[168:171], %[iv], %[ssrd], %[si] offen offset:2048 sc1\n\t"
        "buffer_load_dwordx4 v[172:175], %[iv], %[ssrd], %[si] offen offset:3072 sc1\n\t"
        "ds_read_b128 v[44:47], %[xb] offset:4096\n\t"
        "s_waitcnt lgkmcnt(5)\n\t"
        "v_mfma_f32_16x16x32_bf16 v[56:59], v[128:131], v[24:27], v[56:59]\n\t"
        "v_mfma_f32_16x16x32_bf16 v[76:79], v[132:135], v[24:27], v[76:79]\n\t"
        "ds_read_b128 v[24:27], %[xb] offset:6144\n\t"
        "s_waitcnt lgkmcnt(5)\n\t"
        "v_mfma_f32_16x16x32_bf16 v[60:63], v[128:131], v[28:31], v[60:63]\n\t"
        "v_mfma_f32_16x16x32_bf16 v[80:83], v[132:135], v[28:31], v[80:83]\n\t"
        "ds_read_b128 v[28:31], %[xb] offset:8192\n\t"
        "s_waitcnt lgkmcnt(5)\n\t"
        "v_mfma_f32_16x16x32_bf16 v[64:67], v[128:131], v[32:35], v[64:67]\n\t"
        "v_mfma_f32_16x16x32_bf16 v[84:87], v[132:135], v[32:35], v[84:87]\n\t"
        "s_bitcmp1_b32 %[scm], 1\n\t"
        "s_cbranch_scc1 Lc1_%=\n\t"
        "Ld1_%=:\n\t"
        "ds_read_b128 v[32:35], %[xa] offset:10240\n\t"
        "s_waitcnt vmcnt(8) lgkmcnt(5)\n\t"
        "v_mfma_f32_16x16x32_bf16 v[48:51], v[136:139], v[36:39], v[48:51]\n\t"
        "v_mfma_f32_16x16x32_bf16 v[68:71], v[140:143], v[36:39], v[68:71]\n\t"
        "ds_read_b128 v[36:39], %[xa] offset:12288\n\t"
        "s_waitcnt lgkmcnt(5)\n\t"
        "v_mfma_f32_16x16x32_bf16 v[52:55], v[136:139], v[40:43], v[52:55]\n\t"
        "v_mfma_f32_16x16x32_bf16 v[72:75], v[140:143], v[40:43], v[72:75]\n\t"
        "s_add_u32 %[si], %[si], 4096\n\t"
        "buffer_load_dwordx4 v[176:179], %[iv], %[ssrd], %[si] offen offset:0 sc1\n\t"
        "buffer_load_dwordx4 v[180:183], %[iv], %[ssrd], %[si] offen offset:1024 sc1\n\t"
        "ds_read_b128 v[40:43], %[xa] offset:14336\n\t"
        "s_waitcnt lgkmcnt(5)\n\t"
        "v_mfma_f32_16x16x32_bf16 v[56:59], v[136:139], v[44:47], v[56:59]\n\t"
        "v_mfma_f32_16x16x32_bf16 v[76:79], v[140:143], v[44:47], v[76:79]\n\t"
        "ds_read_b128 v[44:47], %[xa] offset:16384\n\t"
        "s_waitcnt lgkmcnt(5)\n\t"
        "v_mfma_f32_16x16x32_bf16 v[60:63], v[136:139], v[24:27], v[60:63]\n\t"
        "v_mfma_f32_16x16x32_bf16 v[80:83], v[140:143], v[24:27], v[80:83]\n\t"
        "ds_read_b128 v[24:27], %[xa] offset:18432\n\t"
        "s_waitcnt lgkmcnt(5)\n\t"
        "v_mfma_f32_16x16x32_bf16 v[64:67], v[136:139], v[28:31], v[64:67]\n\t"
        "v_mfma_f32_16x16x32_bf16 v[84:87], v[140:143], v[28:31], v[84:87]\n\t"
        "ds_read_b128 v[28:31], %[xb] offset:10240\n\t"
        "s_waitcnt vmcnt(8) lgkmcnt(5)\n\t"
        "v_mfma_f32_16x16x32_bf16 v[48:51], v[144:147], v[32:35], v[48:51]\n\t"
        "v_mfma_f32_16x16x32_bf16 v[68:71], v[148:151], v[32:35], v[68:71]\n\t"
        "ds_read_b128 v[32:35], %[xb] offset:12288\n\t"
        "s_waitcnt lgkmcnt(5)\n\t"
        "v_mfma_f32_16x16x32_bf16 v[52:55], v[144:147], v[36:39], v[52:55]\n\t"
        "v_mfma_f32_16x16x32_bf16 v[72:75], v[148:151], v[36:39], v[72:75]\n\t"
        "buffer_load_dwordx4 v[184:187], %[iv], %[ssrd], %[si] offen offset:2048 sc1\n\t"
        "buffer_load_dwordx4 v[188:191], %[iv], %[ssrd], %[si] offen offset:3072 sc1\n\t"
        "ds_read_b128 v[36:39], %[xb] offset:14336\n\t"
        "s_waitcnt lgkmcnt(5)\n\t"
        "v_mfma_f32_16x16x32_bf16 v[56:59], v[144:147], v[40:43], v[56:59]\n\t"
        "v_mfma_f32_16x16x32_bf16 v[76:79], v[148:151], v[40:43], v[76:79]\n\t"
        "ds_read_b128 v[40:43], %[xb] offset:16384\n\t"
        "s_waitcnt lgkmcnt(5)\n\t"
        "v_mfma_f32_16x16x32_bf16 v[60:63], v[144:147], v[44:47], v[60:63]\n\t"
        "v_mfma_f32_16x16x32_bf16 v[80:83], v[148:151], v[44:47], v[80:83]\n\t"
        "ds_read_b128 v[44:47], %[xb] offset:18432\n\t"
        "s_waitcnt lgkmcnt(5)\n\t"
        "v_mfma_f32_16x16x32_bf16 v[64:67], v[144:147], v[24:27], v[64:67]\n\t"
        "v_mfma_f32_16x16x32_bf16 v[84:87], v[148:151], v[24:27], v[84:87]\n\t"
        "s_bitcmp1_b32 %[scm], 2\n\t"
        "s_cbranch_scc1 Lc2_%=\n\t"
        "Ld2_%=:\n\t"
        "ds_read_b128 v[24:27], %[xa] offset:20480\n\t"
        "s_waitcnt vmcnt(8) lgkmcnt(5)\n\t"
        "v_mfma_f32_16x16x32_bf16 v[48:51], v[152:155], v[28:31], v[48:51]\n\t"
        "v_mfma_f32_16x16x32_bf16 v[68:71], v[156:159], v[28:31], v[68:71]\n\t"
        "ds_read_b128 v[28:31], %[xa] offset:22528\n\t"
        "s_waitcnt lgkmcnt(5)\n\t"
        "v_mfma_f32_16x16x32_bf16 v[52:55], v[152:155], v[32:35], v[52:55]\n\t"
        "v_mfma_f32_16x16x32_bf16 v[72:75], v[156:159], v[32:35], v[72:75]\n\t"
        "s_add_u32 %[si], %[si], 4096\n\t"
        "buffer_load_dwordx4 v[192:195], %[iv], %[ssrd], %[si] offen offset:0 sc1\n\t"
        "buffer_load_dwordx4 v[196:199], %[iv], %[ssrd], %[si] offen offset:1024 sc1\n\t"
        "ds_read_b128 v[32:35], %[xa] offset:24576\n\t"
        "s_waitcnt lgkmcnt(5)\n\t"
        "v_mfma_f32_16x16x32_bf16 v[56:59], v[152:155], v[36:39], v[56:59]\n\t"
        "v_mfma_f32_16x16x32_bf16 v[76:79], v[156:159], v[36:39], v[76:79]\n\t"
        "ds_read_b128 v[36:39], %[xa] offset:26624\n\t"
        "s_waitcnt lgkmcnt(5)\n\t"
        "v_mfma_f32_16x16x32_bf16 v[60:63], v[152:155], v[40:43], v[60:63]\n\t"
        "v_mfma_f32_16x16x32_bf16 v[80:83], v[156:159], v[40:43], v[80:83]\n\t"
        "ds_read_b128 v[40:43], %[xa] offset:28672\n\t"
        "s_waitcnt lgkmcnt(5)\n\t"
        "v_mfma_f32_16x16x32_bf16 v[64:67], v[152:155], v[44:47], v[64:67]\n\t"
        "v_mfma_f32_16x16x32_bf16 v[84:87], v[156:159], v[44:47], v[84:87]\n\t"
        "ds_read_b128 v[44:47], %[xb] offset:20480\n\t"
        "s_setprio 1\n\t"
        "s_waitcnt vmcnt(8) lgkmcnt(5)\n\t"
        "v_mfma_f32_16x16x32_bf16 v[48:51], v[160:163], v[24:27], v[48:51]\n\t"
        "v_mfma_f32_16x16x32_bf16 v[68:71], v[164:167], v[24:27], v[68:71]\n\t"
        "ds_read_b128 v[24:27], %[xb] offset:22528\n\t"
        "s_waitcnt lgkmcnt(5)\n\t"
        "v_mfma_f32_16x16x32_bf16 v[52:55], v[160:163], v[28:31], v[52:55]\n\t"
        "v_mfma_f32_16x16x32_bf16 v[72:75], v[164:167], v[28:31], v[72:75]\n\t"
        "buffer_load_dwordx4 v[200:203], %[iv], %[ssrd], %[si] offen offset:2048 sc1\n\t"
        "buffer_load_dwordx4 v[204:207], %[iv], %[ssrd], %[si] offen offset:3072 sc1\n\t"
        "ds_read_b128 v[28:31], %[xb] offset:24576\n\t"
        "s_waitcnt lgkmcnt(5)\n\t"
        "v_mfma_f32_16x16x32_bf16 v[56:59], v[160:163], v[32:35], v[56:59]\n\t"
        "v_mfma_f32_16x16x32_bf16 v[76:79], v[164:167], v[32:35], v[76:79]\n\t"
        "ds_read_b128 v[32:35], %[xb] offset:26624\n\t"
        "s_waitcnt lgkmcnt(5)\n\t"
        "v_mfma_f32_16x16x32_bf16 v[60:63], v[160:163], v[36:39], v[60:63]\n\t"
        "v_mfma_f32_16x16x32_bf16 v[80:83], v[164:167], v[36:39], v[80:83]\n\t"
        "ds_read_b128 v[36:39], %[xb] offset:28672\n\t"
        "s_waitcnt lgkmcnt(5)\n\t"
        "v_mfma_f32_16x16x32_bf16 v[64:67], v[160:163], v[40:43], v[64:67]\n\t"
        "v_mfma_f32_16x16x32_bf16 v[84:87], v[164:167], v[40:43], v[84:87]\n\t"
        "s_bitcmp1_b32 %[scm], 3\n\t"
        "s_cbranch_scc1 Lc3_%=\n\t"
        "Ld3_%=:\n\t"
        "ds_read_b128 v[40:43], %[xa] offset:30720\n\t"
        "s_waitcnt vmcnt(8) lgkmcnt(5)\n\t"
        "v_mfma_f32_16x16x32_bf16 v[48:51], v[168:171], v[44:47], v[48:51]\n\t"
        "v_mfma_f32_16x16x32_bf16 v[68:71], v[172:175], v[44:47], v[68:71]\n\t"
        "ds_read_b128 v[44:47], %[xa] offset:32768\n\t"
        "s_waitcnt lgkmcnt(5)\n\t"
        "v_mfma_f32_16x16x32_bf16 v[52:55], v[168:171], v[24:27], v[52:55]\n\t"
        "v_mfma_f32_16x16x32_bf16 v[72:75], v[172:175], v[24:27], v[72:75]\n\t"
        "s_add_u32 %[si], %[si], 4096\n\t"
        "buffer_load_dwordx4 v[208:211], %[iv], %[ssrd], %[si] offen offset:0 sc1\n\t"
        "buffer_load_dwordx4 v[212:215], %[iv], %[ssrd], %[si] offen offset:1024 sc1\n\t"
        "ds_read_b128 v[24:27], %[xa] offset:34816\n\t"
        "s_waitcnt lgkmcnt(5)\n\t"
        "v_mfma_f32_16x16x32_bf16 v[56:59], v[168:171], v[28:31], v[56:59]\n\t"
        "v_mfma_f32_16x16x32_bf16 v[76:79], v[172:175], v[28:31], v[76:79]\n\t"
        "ds_read_b128 v[28:31], %[xa] offset:36864\n\t"
        "s_waitcnt lgkmcnt(5)\n\t"
        "v_mfma_f32_16x16x32_bf16 v[60:63], v[168:171], v[32:35], v[60:63]\n\t"
        "v_mfma_f32_16x16x32_bf16 v[80:83], v[172:175], v[32:35], v[80:83]\n\t"
        "ds_read_b128 v[32:35], %[xa] offset:38912\n\t"
        "s_waitcnt lgkmcnt(5)\n\t"
        "v_mfma_f32_16x16x32_bf16 v[64:67], v[168:171], v[36:39], v[64:67]\n\t"
        "v_mfma_f32_16x16x32_bf16 v[84:87], v[172:175], v[36:39], v[84:87]\n\t"
        "ds_read_b128 v[36:39], %[xb] offset:30720\n\t"
        "s_waitcnt vmcnt(8) lgkmcnt(5)\n\t"
        "v_mfma_f32_16x16x32_bf16 v[48:51], v[176:179], v[40:43], v[48:51]\n\t"
        "v_mfma_f32_16x16x32_bf16 v[68:71], v[180:183], v[40:43], v[68:71]\n\t"
        "ds_read_b128 v[40:43], %[xb] offset:32768\n\t"
        "s_waitcnt lgkmcnt(5)\n\t"
        "v_mfma_f32_16x16x32_bf16 v[52:55], v[176:179], v[44:47], v[52:55]\n\t"
        "v_mfma_f32_16x16x32_bf16 v[72:75], v[180:183], v[44:47], v[72:75]\n\t"
        "buffer_load_dwordx4 v[216:219], %[iv], %[ssrd], %[si] offen offset:2048 sc1\n\t"
        "buffer_load_dwordx4 v[220:223], %[iv], %[ssrd], %[si] offen offset:3072 sc1\n\t"
        "ds_read_b128 v[44:47], %[xb] offset:34816\n\t"
        "s_waitcnt lgkmcnt(5)\n\t"
        "v_mfma_f32_16x16x32_bf16 v[56:59], v[176:179], v[24:27], v[56:59]\n\t"
        "v_mfma_f32_16x16x32_bf16 v[76:79], v[180:183], v[24:27], v[76:79]\n\t"
        "ds_read_b128 v[24:27], %[xb] offset:36864\n\t"
        "s_waitcnt lgkmcnt(5)\n\t"
        "v_mfma_f32_16x16x32_bf16 v[60:63], v[176:179], v[28:31], v[60:63]\n\t"
        "v_mfma_f32_16x16x32_bf16 v[80:83], v[180:183], v[28:31], v[80:83]\n\t"
        "ds_read_b128 v[28:31], %[xb] offset:38912\n\t"
        "s_waitcnt lgkmcnt(5)\n\t"
        "v_mfma_f32_16x16x32_bf16 v[64:67], v[176:179], v[32:35], v[64:67]\n\t"
        "v_mfma_f32_16x16x32_bf16 v[84:87], v[180:183], v[32:35], v[84:87]\n\t"
        "s_bitcmp1_b32 %[scm], 4\n\t"
        "s_cbranch_scc1 Lc4_%=\n\t"
        "Ld4_%=:\n\t"
        "ds_read_b128 v[32:35], %[xa] offset:40960\n\t"
        "v_mov_b32 %[x2], 153984\n\t"
        "v_mov_b32 %[x0], 1\n\t"
        "s_mov_b64 exec, 1\n\t"
        "ds_add_u32 %[x2], %[x0] offset:%[omc]+4\n\t"
        "s_mov_b64 exec, -1\n\t"
        "s_waitcnt vmcnt(8) lgkmcnt(6)\n\t"
        "v_mfma_f32_16x16x32_bf16 v[48:51], v[184:187], v[36:39], v[48:51]\n\t"
        "v_mfma_f32_16x16x32_bf16 v[68:71], v[188:191], v[36:39], v[68:71]\n\t"
        "ds_read_b128 v[36:39], %[xa] offset:43008\n\t"
        "s_waitcnt lgkmcnt(6)\n\t"
        "v_mfma_f32_16x16x32_bf16 v[52:55], v[184:187], v[40:43], v[52:55]\n\t"
        "v_mfma_f32_16x16x32_bf16 v[72:75], v[188:191], v[40:43], v[72:75]\n\t"
        "s_add_u32 %[si], %[si], 4096\n\t"
        "buffer_load_dwordx4 v[224:227], %[iv], %[ssrd], %[si] offen offset:0 sc1\n\t"
        "buffer_load_dwordx4 v[228:231], %[iv], %[ssrd], %[si] offen offset:1024 sc1\n\t"
        "ds_read_b128 v[40:43], %[xa] offset:45056\n\t"
        "s_waitcnt lgkmcnt(6)\n\t"
        "v_mfma_f32_16x16x32_bf16 v[56:59], v[184:187], v[44:47], v[56:59]\n\t"
        "v_mfma_f32_16x16x32_bf16 v[76:79], v[188:191], v[44:47], v[76:79]\n\t"
        "ds_read_b128 v[44:47], %[xa] offset:47104\n\t"
        "s_waitcnt lgkmcnt(6)\n\t"
        "v_mfma_f32_16x16x32_bf16 v[60:63], v[184:187], v[24:27], v[60:63]\n\t"
        "v_mfma_f32_16x16x32_bf16 v[80:83], v[188:191], v[24:27], v[80:83]\n\t"
        "ds_read_b128 v[24:27], %[xa] offset:49152\n\t"
        "s_waitcnt lgkmcnt(6)\n\t"
        "v_mfma_f32_16x16x32_bf16 v[64:67], v[184:187], v[28:31], v[64:67]\n\t"
        "v_mfma_f32_16x16x32_bf16 v[84:87], v[188:191], v[28:31], v[84:87]\n\t"
        "ds_read_b128 v[28:31], %[xb] offset:40960\n\t"
        "s_waitcnt vmcnt(8) lgkmcnt(6)\n\t"
        "v_mfma_f32_16x16x32_bf16 v[48:51], v[192:195], v[32:35], v[48:51]\n\t"
        "v_mfma_f32_16x16x32_bf16 v[68:71], v[196:199], v[32:35], v[68:71]\n\t"
        "ds_read_b128 v[32:35], %[xb] offset:43008\n\t"
        "s_waitcnt lgkmcnt(5)\n\t"
        "v_mfma_f32_16x16x32_bf16 v[52:55], v[192:195], v[36:39], v[52:55]\n\t"
        "v_mfma_f32_16x16x32_bf16 v[72:75], v[196:199], v[36:39], v[72:75]\n\t"
        "buffer_load_dwordx4 v[232:235], %[iv], %[ssrd], %[si] offen offset:2048 sc1\n\t"
        "buffer_load_dwordx4 v[236:239], %[iv], %[ssrd], %[si] offen offset:3072 sc1\n\t"
        "ds_read_b128 v[36:39], %[xb] offset:45056\n\t"
        "s_waitcnt lgkmcnt(5)\n\t"
        "v_mfma_f32_16x16x32_bf16 v[56:59], v[192:195], v[40:43], v[56:59]\n\t"
        "v_mfma_f32_16x16x32_bf16 v[76:79], v[196:199], v[40:43], v[76:79]\n\t"
        "ds_read_b128 v[40:43], %[xb] offset:47104\n\t"
        "s_waitcnt lgkmcnt(5)\n\t"
        "v_mfma_f32_16x16x32_bf16 v[60:63], v[192:195], v[44:47], v[60:63]\n\t"
        "v_mfma_f32_16x16x32_bf16 v[80:83], v[196:199], v[44:47], v[80:83]\n\t"
        "ds_read_b128 v[44:47], %[xb] offset:49152\n\t"
        "s_waitcnt lgkmcnt(5)\n\t"
        "v_mfma_f32_16x16x32_bf16 v[64:67], v[192:195], v[24:27], v[64:67]\n\t"
        "v_mfma_f32_16x16x32_bf16 v[84:87], v[196:199], v[24:27], v[84:87]\n\t"
        "s_bitcmp1_b32 %[scm], 5\n\t"
        "s_cbranch_scc1 Lc5_%=\n\t"
        "Ld5_%=:\n\t"
        "v_add_u32 %[xa], 51200, %[xa]\n\t"
        "ds_read_b128 v[24:27], %[xa] offset:0\n\t"
        "s_waitcnt vmcnt(8) lgkmcnt(5)\n\t"
        "v_mfma_f32_16x16x32_bf16 v[48:51], v[200:203], v[28:31], v[48:51]\n\t"
        "v_mfma_f32_16x16x32_bf16 v[68:71], v[204:207], v[28:31], v[68:71]\n\t"
        "ds_read_b128 v[28:31], %[xa] offset:2048\n\t"
        "s_waitcnt lgkmcnt(5)\n\t"
        "v_mfma_f32_16x16x32_bf16 v[52:55], v[200:203], v[32:35], v[52:55]\n\t"
        "v_mfma_f32_16x16x32_bf16 v[72:75], v[204:207], v[32:35], v[72:75]\n\t"
        "s_add_u32 %[si], %[si], 4096\n\t"
        "buffer_load_dwordx4 v[240:243], %[iv], %[ssrd], %[si] offen offset:0 sc1\n\t"
        "buffer_load_dwordx4 v[244:247], %[iv], %[ssrd], %[si] offen offset:1024 sc1\n\t"
        "ds_read_b128 v[32:35], %[xa] offset:4096\n\t"
        "s_waitcnt lgkmcnt(5)\n\t"
        "v_mfma_f32_16x16x32_bf16 v[56:59], v[200:203], v[36:39], v[56:59]\n\t"
        "v_mfma_f32_16x16x32_bf16 v[76:79], v[204:207], v[36:39], v[76:79]\n\t"
        "ds_read_b128 v[36:39], %[xa] offset:6144\n\t"
        "s_waitcnt lgkmcnt(5)\n\t"
        "v_mfma_f32_16x16x32_bf16 v[60:63], v[200:203], v[40:43], v[60:63]\n\t"
        "v_mfma_f32_16x16x32_bf16 v[80:83], v[204:207], v[40:43], v[80:83]\n\t"
        "ds_read_b128 v[40:43], %[xa] offset:8192\n\t"
        "s_waitcnt lgkmcnt(5)\n\t"
        "v_mfma_f32_16x16x32_bf16 v[64:67], v[200:203], v[44:47], v[64:67]\n\t"
        "v_mfma_f32_16x16x32_bf16 v[84:87], v[204:207], v[44:47], v[84:87]\n\t"
        "v_add_u32 %[xb], 51200, %[xb]\n\t"
        "ds_read_b128 v[44:47], %[xb] offset:0\n\t"
        "s_setprio 0\n\t"
        "s_waitcnt vmcnt(8) lgkmcnt(5)\n\t"
        "v_mfma_f32_16x16x32_bf16 v[48:51], v[208:211], v[24:27], v[48:51]\n\t"
        "v_mfma_f32_16x16x32_bf16 v[68:71], v[212:215], v[24:27], v[68:71]\n\t"
        "ds_read_b128 v[24:27], %[xb] offset:2048\n\t"
        "s_waitcnt lgkmcnt(5)\n\t"
        "v_mfma_f32_16x16x32_bf16 v[52:55], v[208:211], v[28:31], v[52:55]\n\t"
        "v_mfma_f32_16x16x32_bf16 v[72:75], v[212:215], v[28:31], v[72:75]\n\t"
        "buffer_load_dwordx4 v[248:251], %[iv], %[ssrd], %[si] offen offset:2048 sc1\n\t"
        "buffer_load_dwordx4 v[252:255], %[iv], %[ssrd], %[si] offen offset:3072 sc1\n\t"
        "ds_read_b128 v[28:31], %[xb] offset:4096\n\t"
        "s_waitcnt lgkmcnt(5)\n\t"
        "v_mfma_f32_16x16x32_bf16 v[56:59], v[208:211], v[32:35], v[56:59]\n\t"
        "v_mfma_f32_16x16x32_bf16 v[76:79], v[212:215], v[32:35], v[76:79]\n\t"
        "ds_read_b128 v[32:35], %[xb] offset:6144\n\t"
        "s_waitcnt lgkmcnt(5)\n\t"
        "v_mfma_f32_16x16x32_bf16 v[60:63], v[208:211], v[36:39], v[60:63]\n\t"
        "v_mfma_f32_16x16x32_bf16 v[80:83], v[212:215], v[36:39], v[80:83]\n\t"
        "ds_read_b128 v[36:39], %[xb] offset:8192\n\t"
        "s_waitcnt lgkmcnt(5)\n\t"
        "v_mfma_f32_16x16x32_bf16 v[64:67], v[208:211], v[40:43], v[64:67]\n\t"
        "v_mfma_f32_16x16x32_bf16 v[84:87], v[212:215], v[40:43], v[84:87]\n\t"
        "s_bitcmp1_b32 %[scm], 6\n\t"
        "s_cbranch_scc1 Lc6_%=\n\t"
        "Ld6_%=:\n\t"
        "ds_read_b128 v[40:43], %[xa] offset:10240\n\t"
        "s_waitcnt vmcnt(8) lgkmcnt(5)\n\t"
        "v_mfma_f32_16x16x32_bf16 v[48:51], v[216:219], v[44:47], v[48:51]\n\t"
        "v_mfma_f32_16x16x32_bf16 v[68:71], v[220:223], v[44:47], v[68:71]\n\t"
        "ds_read_b128 v[44:47], %[xa] offset:12288\n\t"
        "s_waitcnt lgkmcnt(5)\n\t"
        "v_mfma_f32_16x16x32_bf16 v[52:55], v[216:219], v[24:27], v[52:55]\n\t"
        "v_mfma_f32_16x16x32_bf16 v[72:75], v[220:223], v[24:27], v[72:75]\n\t"
        "buffer_load_dword %[fl], %[fo], %[ssrd], 0 offen sc1\n\t"
        "ds_read_b128 v[24:27], %[xa] offset:14336\n\t"
        "s_waitcnt lgkmcnt(5)\n\t"
        "v_mfma_f32_16x16x32_bf16 v[56:59], v[216:219], v[28:31], v[56:59]\n\t"
        "v_mfma_f32_16x16x32_bf16 v[76:79], v[220:223], v[28:31], v[76:79]\n\t"
        "ds_read_b128 v[28:31], %[xa] offset:16384\n\t"
        "s_waitcnt lgkmcnt(5)\n\t"
        "v_mfma_f32_16x16x32_bf16 v[60:63], v[216:219], v[32:35], v[60:63]\n\t"
        "v_mfma_f32_16x16x32_bf16 v[80:83], v[220:223], v[32:35], v[80:83]\n\t"
        "ds_read_b128 v[32:35], %[xa] offset:18432\n\t"
        "s_waitcnt lgkmcnt(5)\n\t"
        "v_mfma_f32_16x16x32_bf16 v[64:67], v[216:219], v[36:39], v[64:67]\n\t"
        "v_mfma_f32_16x16x32_bf16 v[84:87], v[220:223], v[36:39], v[84:87]\n\t"
        "ds_read_b128 v[36:39], %[xb] offset:10240\n\t"
        "s_waitcnt vmcnt(7) lgkmcnt(5)\n\t"
        "v_mfma_f32_16x16x32_bf16 v[48:51], v[224:227], v[40:43], v[48:51]\n\t"
        "v_mfma_f32_16x16x32_bf16 v[68:71], v[228:231], v[40:43], v[68:71]\n\t"
        "ds_read_b128 v[40:43], %[xb] offset:12288\n\t"
        "s_waitcnt lgkmcnt(5)\n\t"
        "v_mfma_f32_16x16x32_bf16 v[52:55], v[224:227], v[44:47], v[52:55]\n\t"
        "v_mfma_f32_16x16x32_bf16 v[72:75], v[228:231], v[44:47], v[72:75]\n\t"
        "ds_read_b128 v[44:47], %[xb] offset:14336\n\t"
        "s_waitcnt lgkmcnt(5)\n\t"
        "v_mfma_f32_16x16x32_bf16 v[56:59], v[224:227], v[24:27], v[56:59]\n\t"
        "v_mfma_f32_16x16x32_bf16 v[76:79], v[228:231], v[24:27], v[76:79]\n\t"
        "ds_read_b128 v[24:27], %[xb] offset:16384\n\t"
        "s_waitcnt lgkmcnt(5)\n\t"
        "v_mfma_f32_16x16x32_bf16 v[60:63], v[224:227], v[28:31], v[60:63]\n\t"
        "v_mfma_f32_16x16x32_bf16 v[80:83], v[228:231], v[28:31], v[80:83]\n\t"
        "ds_read_b128 v[28:31], %[xb] offset:18432\n\t"
        "s_waitcnt lgkmcnt(5)\n\t"
        "v_mfma_f32_16x16x32_bf16 v[64:67], v[224:227], v[32:35], v[64:67]\n\t"
        "v_mfma_f32_16x16x32_bf16 v[84:87], v[228:231], v[32:35], v[84:87]\n\t"
        "s_bitcmp1_b32 %[scm], 7\n\t"
        "s_cbranch_scc1 Lc7_%=\n\t"
        "Ld7_%=:\n\t"
        "ds_read_b128 v[32:35], %[xa] offset:20480\n\t"
        "s_waitcnt vmcnt(5) lgkmcnt(5)\n\t"
        "v_mfma_f32_16x16x32_bf16 v[48:51], v[232:235], v[36:39], v[48:51]\n\t"
        "v_mfma_f32_16x16x32_bf16 v[68:71], v[236:239], v[36:39], v[68:71]\n\t"
        "ds_read_b128 v[36:39], %[xa] offset:22528\n\t"
        "s_waitcnt lgkmcnt(5)\n\t"
        "v_mfma_f32_16x16x32_bf16 v[52:55], v[232:235], v[40:43], v[52:55]\n\t"
        "v_mfma_f32_16x16x32_bf16 v[72:75], v[236:239], v[40:43], v[72:75]\n\t"
        "ds_read_b128 v[40:43], %[xa] offset:24576\n\t"
        "s_waitcnt lgkmcnt(5)\n\t"
        "v_mfma_f32_16x16x32_bf16 v[56:59], v[232:235], v[44:47], v[56:59]\n\t"
        "v_mfma_f32_16x16x32_bf16 v[76:79], v[236:239], v[44:47], v[76:79]\n\t"
        "ds_read_b128 v[44:47], %[xa] offset:26624\n\t"
        "s_waitcnt lgkmcnt(5)\n\t"
        "v_mfma_f32_16x16x32_bf16 v[60:63], v[232:235], v[24:27], v[60:63]\n\t"
        "v_mfma_f32_16x16x32_bf16 v[80:83], v[236:239], v[24:27], v[80:83]\n\t"
        "ds_read_b128 v[24:27], %[xa] offset:28672\n\t"
        "s_waitcnt lgkmcnt(5)\n\t"
        "v_mfma_f32_16x16x32_bf16 v[64:67], v[232:235], v[28:31], v[64:67]\n\t"
        "v_mfma_f32_16x16x32_bf16 v[84:87], v[236:239], v[28:31], v[84:87]\n\t"
        "ds_read_b128 v[28:31], %[xb] offset:20480\n\t"
        "s_waitcnt vmcnt(3) lgkmcnt(5)\n\t"
        "v_mfma_f32_16x16x32_bf16 v[48:51], v[240:243], v[32:35], v[48:51]\n\t"
        "v_mfma_f32_16x16x32_bf16 v[68:71], v[244:247], v[32:35], v[68:71]\n\t"
        "ds_read_b128 v[32:35], %[xb] offset:22528\n\t"
        "s_waitcnt lgkmcnt(5)\n\t"
        "v_mfma_f32_16x16x32_bf16 v[52:55], v[240:243], v[36:39], v[52:55]\n\t"
        "v_mfma_f32_16x16x32_bf16 v[72:75], v[244:247], v[36:39], v[72:75]\n\t"
        "ds_read_b128 v[36:39], %[xb] offset:24576\n\t"
        "v_and_b32 %[x0], 0xf0, v107\n\t"
        "v_lshrrev_b32 %[x0], 1, %[x0]\n\t"
        "v_add_u32 %[x0], 153344, %[x0]\n\t"
        "v_lshrrev_b32 %[x1], 8, v107\n\t"
        "v_lshl_add_u32 %[x1], %[x1], 5, %[scs]\n\t"
        "s_waitcnt lgkmcnt(5)\n\t"
        "v_mfma_f32_16x16x32_bf16 v[56:59], v[240:243], v[40:43], v[56:59]\n\t"
        "v_mfma_f32_16x16x32_bf16 v[76:79], v[244:247], v[40:43], v[76:79]\n\t"
        "ds_read_b128 v[40:43], %[xb] offset:26624\n\t"
        "s_waitcnt lgkmcnt(5)\n\t"
        "v_mfma_f32_16x16x32_bf16 v[60:63], v[240:243], v[44:47], v[60:63]\n\t"
        "v_mfma_f32_16x16x32_bf16 v[80:83], v[244:247], v[44:47], v[80:83]\n\t"
        "ds_read_b128 v[44:47], %[xb] offset:28672\n\t"
        "s_waitcnt lgkmcnt(5)\n\t"
        "v_mfma_f32_16x16x32_bf16 v[64:67], v[240:243], v[24:27], v[64:67]\n\t"
        "v_mfma_f32_16x16x32_bf16 v[84:87], v[244:247], v[24:27], v[84:87]\n\t"
        "s_waitcnt vmcnt(1) lgkmcnt(4)\n\t"
        "v_mfma_f32_16x16x32_bf16 v[48:51], v[248:251], v[28:31], v[48:51]\n\t"
        "v_mfma_f32_16x16x32_bf16 v[68:71], v[252:255], v[28:31], v[68:71]\n\t"
        "s_waitcnt lgkmcnt(3)\n\t"
        "v_mfma_f32_16x16x32_bf16 v[52:55], v[248:251], v[32:35], v[52:55]\n\t"
        "v_mfma_f32_16x16x32_bf16 v[72:75], v[252:255], v[32:35], v[72:75]\n\t"
        "ds_read_b64 v[34:35], %[x2] offset:%[omc]\n\t"
        "ds_read_b64 v[24:25], %[x0] offset:0\n\t"
        "ds_read_b64 v[26:27], %[x0] offset:128\n\t"
        "ds_read_b64 v[28:29], %[x0] offset:256\n\t"
        "ds_read_b64 v[30:31], %[x0] offset:384\n\t"
        "ds_read_b64 v[32:33], %[x0] offset:512\n\t"
        "s_waitcnt lgkmcnt(8)\n\t"
        "v_mfma_f32_16x16x32_bf16 v[56:59], v[248:251], v[36:39], v[56:59]\n\t"
        "v_mfma_f32_16x16x32_bf16 v[76:79], v[252:255], v[36:39], v[76:79]\n\t"
        "s_waitcnt lgkmcnt(7)\n\t"
        "v_mfma_f32_16x16x32_bf16 v[60:63], v[248:251], v[40:43], v[60:63]\n\t"
        "v_mfma_f32_16x16x32_bf16 v[80:83], v[252:255], v[40:43], v[80:83]\n\t"
        "s_waitcnt lgkmcnt(6)\n\t"
        "v_mfma_f32_16x16x32_bf16 v[64:67], v[248:251], v[44:47], v[64:67]\n\t"
        "v_mfma_f32_16x16x32_bf16 v[84:87], v[252:255], v[44:47], v[84:87]\n\t"
        "v_mov_b32_dpp v36, v48 row_newbcast:0 row_mask:0xf bank_mask:0xf bound_ctrl:1\n\t"
        "v_mov_b32_dpp v37, v49 row_newbcast:0 row_mask:0xf bank_mask:0xf bound_ctrl:1\n\t"
        "v_mov_b32_dpp v38, v50 row_newbcast:0 row_mask:0xf bank_mask:0xf bound_ctrl:1\n\t"
        "v_mov_b32_dpp v39, v51 row_newbcast:0 row_mask:0xf bank_mask:0xf bound_ctrl:1\n\t"
        "v_mov_b32_dpp v40, v68 row_newbcast:0 row_mask:0xf bank_mask:0xf bound_ctrl:1\n\t"
        "v_mov_b32_dpp v41, v69 row_newbcast:0 row_mask:0xf bank_mask:0xf bound_ctrl:1\n\t"
        "v_mov_b32_dpp v42, v70 row_newbcast:0 row_mask:0xf bank_mask:0xf bound_ctrl:1\n\t"
        "v_mov_b32_dpp v43, v71 row_newbcast:0 row_mask:0xf bank_mask:0xf bound_ctrl:1\n\t"
        "Lm_%=:\n\t"
        "s_waitcnt vmcnt(0) lgkmcnt(0)\n\t"
        "v_readfirstlane_b32 %[sf], v34\n\t"
        "v_cmp_ne_u32 vcc, %[sexp], %[fl]\n\t"
        "s_cmp_lt_u32 %[sf], 3\n\t"
        "s_cbranch_scc1 Ln_%=\n\t"
        "s_cbranch_vccnz Ln_%=\n\t"
        "Lo_%=:\n\t"
        "s_mov_b64 exec, %[sm16]\n\t"
        "ds_write_b128 %[x1], v[36:39]\n\t"
        "ds_write_b128 %[x1], v[40:43] offset:16\n\t"
        "s_mov_b64 exec, -1\n\t"
        "v_fma_f32 v48, -v24, v36, v48\n\t"
        "v_fma_f32 v49, -v24, v37, v49\n\t"
        "v_fma_f32 v50, -v24, v38, v50\n\t"
        "v_fma_f32 v51, -v24, v39, v51\n\t"
        "v_fma_f32 v68, -v24, v40, v68\n\t"
        "v_fma_f32 v69, -v24, v41, v69\n\t"
        "v_fma_f32 v70, -v24, v42, v70\n\t"
        "v_fma_f32 v71, -v24, v43, v71\n\t"
        "v_mul_f32 v48, v25, v48\n\t"
        "v_mul_f32 v49, v25, v49\n\t"
        "v_mul_f32 v50, v25, v50\n\t"
        "v_mul_f32 v51, v25, v51\n\t"
        "v_mul_f32 v68, v25, v68\n\t"
        "v_mul_f32 v69, v25, v69\n\t"
        "v_mul_f32 v70, v25, v70\n\t"
        "v_mul_f32 v71, v25, v71\n\t"
        "v_cvt_pk_bf16_f32 v48, v48, v68\n\t"
        "v_cvt_pk_bf16_f32 v49, v49, v69\n\t"
        "v_cvt_pk_bf16_f32 v50, v50, v70\n\t"
        "v_cvt_pk_bf16_f32 v51, v51, v71\n\t"
        "v_add_u32 v68, v110, v111\n\t"
        "v_add_u32 v69, v68, v111\n\t"
        "v_add_u32 v70, v69, v111\n\t"
        "v_add_u32 v71, v70, v111\n\t"
        "v_fma_f32 v52, -v26, v36, v52\n\t"
        "v_fma_f32 v53, -v26, v37, v53\n\t"
        "v_fma_f32 v54, -v26, v38, v54\n\t"
        "v_fma_f32 v55, -v26, v39, v55\n\t"
        "v_fma_f32 v72, -v26, v40, v72\n\t"
        "v_fma_f32 v73, -v26, v41, v73\n\t"
        "v_fma_f32 v74, -v26, v42, v74\n\t"
        "v_fma_f32 v75, -v26, v43, v75\n\t"
        "v_mul_f32 v52, v27, v52\n\t"
        "v_mul_f32 v53, v27, v53\n\t"
        "v_mul_f32 v54, v27, v54\n\t"
        "v_mul_f32 v55, v27, v55\n\t"
        "v_mul_f32 v72, v27, v72\n\t"
        "v_mul_f32 v73, v27, v73\n\t"
        "v_mul_f32 v74, v27, v74\n\t"
        "v_mul_f32 v75, v27, v75\n\t"
        "v_cvt_pk_bf16_f32 v52, v52, v72\n\t"
        "v_cvt_pk_bf16_f32 v53, v53, v73\n\t"
        "v_cvt_pk_bf16_f32 v54, v54, v74\n\t"
        "v_cvt_pk_bf16_f32 v55, v55, v75\n\t"
        "v_fma_f32 v56, -v28, v36, v56\n\t"
        "v_fma_f32 v57, -v28, v37, v57\n\t"
        "v_fma_f32 v58, -v28, v38, v58\n\t"
        "v_fma_f32 v59, -v28, v39, v59\n\t"
        "v_fma_f32 v76, -v28, v40, v76\n\t"
        "v_fma_f32 v77, -v28, v41, v77\n\t"
        "v_fma_f32 v78, -v28, v42, v78\n\t"
        "v_fma_f32 v79, -v28, v43, v79\n\t"
        "v_mul_f32 v56, v29, v56\n\t"
        "v_mul_f32 v57, v29, v57\n\t"
        "v_mul_f32 v58, v29, v58\n\t"
        "v_mul_f32 v59, v29, v59\n\t"
        "v_mul_f32 v76, v29, v76\n\t"
        "v_mul_f32 v77, v29, v77\n\t"
        "v_mul_f32 v78, v29, v78\n\t"
        "v_mul_f32 v79, v29, v79\n\t"
        "v_cvt_pk_bf16_f32 v56, v56, v76\n\t"
        "v_cvt_pk_bf16_f32 v57, v57, v77\n\t"
        "v_cvt_pk_bf16_f32 v58, v58, v78\n\t"
        "v_cvt_pk_bf16_f32 v59, v59, v79\n\t"
        "v_fma_f32 v60, -v30, v36, v60\n\t"
        "v_fma_f32 v61, -v30, v37, v61\n\t"
        "v_fma_f32 v62, -v30, v38, v62\n\t"
        "v_fma_f32 v63, -v30, v39, v63\n\t"
        "v_fma_f32 v80, -v30, v40, v80\n\t"
        "v_fma_f32 v81, -v30, v41, v81\n\t"
        "v_fma_f32 v82, -v30, v42, v82\n\t"
        "v_fma_f32 v83, -v30, v43, v83\n\t"
        "v_mul_f32 v60, v31, v60\n\t"
        "v_mul_f32 v61, v31, v61\n\t"
        "v_mul_f32 v62, v31, v62\n\t"
        "v_mul_f32 v63, v31, v63\n\t"
        "v_mul_f32 v80, v31, v80\n\t"
        "v_mul_f32 v81, v31, v81\n\t"
        "v_mul_f32 v82, v31, v82\n\t"
        "v_mul_f32 v83, v31, v83\n\t"
        "v_cvt_pk_bf16_f32 v60, v60, v80\n\t"
        "v_cvt_pk_bf16_f32 v61, v61, v81\n\t"
        "v_cvt_pk_bf16_f32 v62, v62, v82\n\t"
        "v_cvt_pk_bf16_f32 v63, v63, v83\n\t"
        "v_fma_f32 v64, -v32, v36, v64\n\t"
        "v_fma_f32 v65, -v32, v37, v65\n\t"
        "v_fma_f32 v66, -v32, v38, v66\n\t"
        "v_fma_f32 v67, -v32, v39, v67\n\t"
        "v_fma_f32 v84, -v32, v40, v84\n\t"
        "v_fma_f32 v85, -v32, v41, v85\n\t"
        "v_fma_f32 v86, -v32, v42, v86\n\t"
        "v_fma_f32 v87, -v32, v43, v87\n\t"
        "v_mul_f32 v64, v33, v64\n\t"
        "v_mul_f32 v65, v33, v65\n\t"
        "v_mul_f32 v66, v33, v66\n\t"
        "v_mul_f32 v67, v33, v67\n\t"
        "v_mul_f32 v84, v33, v84\n\t"
        "v_mul_f32 v85, v33, v85\n\t"
        "v_mul_f32 v86, v33, v86\n\t"
        "v_mul_f32 v87, v33, v87\n\t"
        "v_cvt_pk_bf16_f32 v64, v64, v84\n\t"
        "v_cvt_pk_bf16_f32 v65, v65, v85\n\t"
        "v_cvt_pk_bf16_f32 v66, v66, v86\n\t"
        "v_cvt_pk_bf16_f32 v67, v67, v87\n\t"
        "Lu_%=:\n\t"
        "v_readfirstlane_b32 %[sf], v35\n\t"
        "s_cmp_ge_u32 %[sf], 7\n\t"
        "s_cbranch_scc1 Lv_%=\n\t"
        "s_cmp_lt_i32 %[scn], 1\n\t"
        "s_cbranch_scc1 Lv_%=\n\t"
        "s_sub_u32 %[scn], %[scn], 1\n\t"
        "s_sleep 4\n\t"
        "ds_read_b32 v35, %[x2] offset:%[omc]+4\n\t"
        "s_waitcnt lgkmcnt(0)\n\t"
        "s_branch Lu_%=\n\t"
        "Lv_%=:\n\t"
        "s_mov_b64 exec, %[sva]\n\t"
        "ds_write_b128 v68, v[52:55]\n\t"
        "ds_write_b128 v69, v[56:59]\n\t"
        "ds_write_b128 v70, v[60:63]\n\t"
        "ds_write_b128 v71, v[64:67]\n\t"
        "s_mov_b64 exec, %[sv0]\n\t"
        "ds_write_b128 v110, v[48:51]\n\t"
        "s_mov_b64 exec, -1\n\t"
        "s_branch Le_%=\n\t"
        "Ln_%=:\n\t"
        "s_cmp_lt_i32 %[scn], 1\n\t"
        "s_cbranch_scc1 Lo_%=\n\t"
        "s_sub_u32 %[scn], %[scn], 1\n\t"
        "buffer_load_dword %[fl], %[fo], %[ssrd], 0 offen sc1\n\t"
        "ds_read_b64 v[34:35], %[x2] offset:%[omc]\n\t"
        "ds_read_b64 v[24:25], %[x0] offset:0\n\t"
        "ds_read_b64 v[26:27], %[x0] offset:128\n\t"
        "ds_read_b64 v[28:29], %[x0] offset:256\n\t"
        "ds_read_b64 v[30:31], %[x0] offset:384\n\t"
        "ds_read_b64 v[32:33], %[x0] offset:512\n\t"
        "s_branch Lm_%=\n\t"
        "Lc0_%=:\n\t"
        "v_add_u32 %[xa], %[c0], %[xa]\n\t"
        "v_add_u32 %[xb], %[c1], %[xb]\n\t"
        "v_xor_b32 %[c0], 10240, %[c0]\n\t"
        "v_xor_b32 %[c1], 10240, %[c1]\n\t"
        "s_branch Ld0_%=\n\t"
        "Lc1_%=:\n\t"
        "v_add_u32 %[xa], %[c0], %[xa]\n\t"
        "v_add_u32 %[xb], %[c1], %[xb]\n\t"
        "v_xor_b32 %[c0], 10240, %[c0]\n\t"
        "v_xor_b32 %[c1], 10240, %[c1]\n\t"
        "s_branch Ld1_%=\n\t"
        "Lc2_%=:\n\t"
        "v_add_u32 %[xa], %[c0], %[xa]\n\t"
        "v_add_u32 %[xb], %[c1], %[xb]\n\t"
        "v_xor_b32 %[c0], 10240, %[c0]\n\t"
        "v_xor_b32 %[c1], 10240, %[c1]\n\t"
        "s_branch Ld2_%=\n\t"
        "Lc3_%=:\n\t"
        "v_add_u32 %[xa], %[c0], %[xa]\n\t"
        "v_add_u32 %[xb], %[c1], %[xb]\n\t"
        "v_xor_b32 %[c0], 10240, %[c0]\n\t"
        "v_xor_b32 %[c1], 10240, %[c1]\n\t"
        "s_branch Ld3_%=\n\t"
        "Lc4_%=:\n\t"
        "v_add_u32 %[xa], %[c0], %[xa]\n\t"
        "v_add_u32 %[xb], %[c1], %[xb]\n\t"
        "v_xor_b32 %[c0], 10240, %[c0]\n\t"
        "v_xor_b32 %[c1], 10240, %[c1]\n\t"
        "s_branch Ld4_%=\n\t"
        "Lc5_%=:\n\t"
        "v_add_u32 %[xa], %[c0], %[xa]\n\t"
        "v_add_u32 %[xb], %[c1], %[xb]\n\t"
        "v_xor_b32 %[c0], 10240, %[c0]\n\t"
        "v_xor_b32 %[c1], 10240, %[c1]\n\t"
        "s_branch Ld5_%=\n\t"
        "Lc6_%=:\n\t"
        "v_add_u32 %[xa], %[c0], %[xa]\n\t"
        "v_add_u32 %[xb], %[c1], %[xb]\n\t"
        "v_xor_b32 %[c0], 10240, %[c0]\n\t"
        "v_xor_b32 %[c1], 10240, %[c1]\n\t"
        "s_branch Ld6_%=\n\t"
        "Lc7_%=:\n\t"
        "v_add_u32 %[xa], %[c0], %[xa]\n\t"
        "v_add_u32 %[xb], %[c1], %[xb]\n\t"
        "v_xor_b32 %[c0], 10240, %[c0]\n\t"
        "v_xor_b32 %[c1], 10240, %[c1]\n\t"
        "s_branch Ld7_%=\n\t"
        "Le_%=:\n\t"
        : [xa] "+v"(xa), [xb] "=&v"(xb), [c0] "+v"(c0), [c1] "+v"(c1), [si] "+s"(si), [fl] "=&v"(fl), [sf] "=&s"(sf), [scn] "+s"(cn), [x0] "+v"(xt[0]), [x1] "+v"(xt[1]), [x2] "+v"(xt[2])
        : [ssrd] "s"(srd), [iv] "v"(iv), [sro] "s"(ro), [scm] "s"(cm), [fo] "v"(fo), [sexp] "s"(fexp), [scs] "s"(cs), [sva] "s"(va), [sv0] "s"(v0), [sm16] "s"(0x0001000100010001ull), [omc] "n"(BC_MC)
        : "memory", "scc", "vcc", "v255", "v24", "v25", "v26", "v27", "v28", "v29", "v30", "v31", "v32", "v33", "v34", "v35", "v36", "v37", "v38", "v39", "v40", "v41", "v42", "v43", "v44", "v45", "v46", "v47");
}

DEV void gemm1a(int xo0, int fb, u32 cs, u64 va, u64 v0, u32 thr, u32& sf, u32& cn) {
    int xa, xh; u32 one, fl;
    asm volatile(
        "v_add_u32 %[xa], 36864, %[xo0]\n\t"
        "v_mov_b32 %[one], 1\n\t"
        "s_setprio 2\n\t"
        "ds_read_b128 v[24:27], %[xo0] offset:8192\n\t"
        "ds_read_b128 v[28:31], %[xo0] offset:8448\n\t"
        "ds_read_b128 v[32:35], %[xo0] offset:8704\n\t"
        "ds_read_b128 v[36:39], %[xo0] offset:8960\n\t"
        "ds_read_b128 v[40:43], %[xo0] offset:12288\n\t"
        "ds_read_b128 v[44:47], %[xo0] offset:12544\n\t"
        "ds_read_b128 v[8:11], %[xo0] offset:12800\n\t"
        "ds_read_b128 v[12:15], %[xo0] offset:13056\n\t"
        "ds_read_b128 v[16:19], %[xo0] offset:16384\n\t"
        "ds_read_b128 v[20:23], %[xo0] offset:16640\n\t"
        "ds_read_b128 v[48:51], %[xo0] offset:16896\n\t"
        "ds_read_b128 v[68:71], %[xo0] offset:17152\n\t"
        "s_waitcnt lgkmcnt(11)\n\t"
        "v_mfma_f32_16x16x32_bf16 v[52:55], v[128:131], v[24:27], v[52:55]\n\t"
        "v_mfma_f32_16x16x32_bf16 v[72:75], v[132:135], v[24:27], v[72:75]\n\t"
        "s_waitcnt lgkmcnt(10)\n\t"
        "v_mfma_f32_16x16x32_bf16 v[56:59], v[128:131], v[28:31], v[56:59]\n\t"
        "ds_read_b128 v[24:27], %[xo0] offset:20480\n\t"
        "v_mfma_f32_16x16x32_bf16 v[76:79], v[132:135], v[28:31], v[76:79]\n\t"
        "s_waitcnt lgkmcnt(10)\n\t"
        "v_mfma_f32_16x16x32_bf16 v[60:63], v[128:131], v[32:35], v[60:63]\n\t"
        "ds_read_b128 v[28:31], %[xo0] offset:20736\n\t"
        "v_mfma_f32_16x16x32_bf16 v[80:83], v[132:135], v[32:35], v[80:83]\n\t"
        "s_waitcnt lgkmcnt(10)\n\t"
        "v_mfma_f32_16x16x32_bf16 v[64:67], v[128:131], v[36:39], v[64:67]\n\t"
        "ds_read_b128 v[32:35], %[xo0] offset:20992\n\t"
        "v_mfma_f32_16x16x32_bf16 v[84:87], v[132:135], v[36:39], v[84:87]\n\t"
        "s_waitcnt lgkmcnt(10)\n\t"
        "v_mfma_f32_16x16x32_bf16 v[52:55], v[136:139], v[40:43], v[52:55]\n\t"
        "ds_read_b128 v[36:39], %[xo0] offset:21248\n\t"
        "v_mfma_f32_16x16x32_bf16 v[72:75], v[140:143], v[40:43], v[72:75]\n\t"
        "s_waitcnt lgkmcnt(10)\n\t"
        "v_mfma_f32_16x16x32_bf16 v[56:59], v[136:139], v[44:47], v[56:59]\n\t"
        "ds_read_b32 %[fl], %[fb] offset:%[og0]+4\n\t"
        "v_mfma_f32_16x16x32_bf16 v[76:79], v[140:143], v[44:47], v[76:79]\n\t"
        "s_waitcnt lgkmcnt(10)\n\t"
        "v_mfma_f32_16x16x32_bf16 v[60:63], v[136:139], v[8:11], v[60:63]\n\t"
        "v_mfma_f32_16x16x32_bf16 v[80:83], v[140:143], v[8:11], v[80:83]\n\t"
        "s_waitcnt lgkmcnt(9)\n\t"
        "v_mfma_f32_16x16x32_bf16 v[64:67], v[136:139], v[12:15], v[64:67]\n\t"
        "v_mfma_f32_16x16x32_bf16 v[84:87], v[140:143], v[12:15], v[84:87]\n\t"
        "s_waitcnt lgkmcnt(8)\n\t"
        "v_mfma_f32_16x16x32_bf16 v[52:55], v[144:147], v[16:19], v[52:55]\n\t"
        "v_mfma_f32_16x16x32_bf16 v[72:75], v[148:151], v[16:19], v[72:75]\n\t"
        "s_waitcnt lgkmcnt(7)\n\t"
        "v_mfma_f32_16x16x32_bf16 v[56:59], v[144:147], v[20:23], v[56:59]\n\t"
        "v_mfma_f32_16x16x32_bf16 v[76:79], v[148:151], v[20:23], v[76:79]\n\t"
        "s_waitcnt lgkmcnt(6)\n\t"
        "v_mfma_f32_16x16x32_bf16 v[60:63], v[144:147], v[48:51], v[60:63]\n\t"
        "s_waitcnt lgkmcnt(0)\n\t"
        "v_cmp_gt_u32 vcc, 2, %[fl]\n\t"
        "v_mfma_f32_16x16x32_bf16 v[80:83], v[148:151], v[48:51], v[80:83]\n\t"
        "v_mfma_f32_16x16x32_bf16 v[64:67], v[144:147], v[68:71], v[64:67]\n\t"
        "s_cbranch_vccnz Ls16_%=\n\t"
        "Lg16_%=:\n\t"
        "ds_read_b128 v[40:43], %[xo0] offset:24576\n\t"
        "v_mfma_f32_16x16x32_bf16 v[84:87], v[148:151], v[68:71], v[84:87]\n\t"
        "ds_read_b128 v[44:47], %[xo0] offset:24832\n\t"
        "v_mfma_f32_16x16x32_bf16 v[52:55], v[152:155], v[24:27], v[52:55]\n\t"
        "ds_read_b128 v[8:11], %[xo0] offset:25088\n\t"
        "v_mfma_f32_16x16x32_bf16 v[72:75], v[156:159], v[24:27], v[72:75]\n\t"
        "ds_read_b128 v[12:15], %[xo0] offset:25344\n\t"
        "v_mfma_f32_16x16x32_bf16 v[56:59], v[152:155], v[28:31], v[56:59]\n\t"
        "ds_read_b128 v[16:19], %[xo0] offset:28672\n\t"
        "v_mfma_f32_16x16x32_bf16 v[76:79], v[156:159], v[28:31], v[76:79]\n\t"
        "ds_read_b128 v[20:23], %[xo0] offset:28928\n\t"
        "v_mfma_f32_16x16x32_bf16 v[60:63], v[152:155], v[32:35], v[60:63]\n\t"
        "ds_read_b128 v[48:51], %[xo0] offset:29184\n\t"
        "v_mfma_f32_16x16x32_bf16 v[80:83], v[156:159], v[32:35], v[80:83]\n\t"
        "ds_read_b128 v[68:71], %[xo0] offset:29440\n\t"
        "v_mfma_f32_16x16x32_bf16 v[64:67], v[152:155], v[36:39], v[64:67]\n\t"
        "ds_read_b128 v[24:27], %[xo0] offset:32768\n\t"
        "v_mfma_f32_16x16x32_bf16 v[84:87], v[156:159], v[36:39], v[84:87]\n\t"
        "ds_read_b128 v[28:31], %[xo0] offset:33024\n\t"
        "s_waitcnt lgkmcnt(9)\n\t"
        "v_mfma_f32_16x16x32_bf16 v[52:55], v[160:163], v[40:43], v[52:55]\n\t"
        "ds_read_b128 v[32:35], %[xo0] offset:33280\n\t"
        "v_mfma_f32_16x16x32_bf16 v[72:75], v[164:167], v[40:43], v[72:75]\n\t"
        "ds_read_b128 v[36:39], %[xo0] offset:33536\n\t"
        "s_mov_b64 exec, 1\n\t"
        "ds_add_u32 %[fb], %[one] offset:%[omc]+4\n\t"
        "s_mov_b64 exec, -1\n\t"
        "s_waitcnt lgkmcnt(11)\n\t"
        "v_and_b32 %[xo0], 0xf0, v107\n\t"
        "v_lshrrev_b32 %[xo0], 1, %[xo0]\n\t"
        "v_mfma_f32_16x16x32_bf16 v[56:59], v[160:163], v[44:47], v[56:59]\n\t"
        "ds_read_b128 v[40:43], %[xa] offset:0\n\t"
        "v_add_u32 %[xo0], 153344, %[xo0]\n\t"
        "v_lshrrev_b32 %[xh], 8, v107\n\t"
        "v_mfma_f32_16x16x32_bf16 v[76:79], v[164:167], v[44:47], v[76:79]\n\t"
        "s_waitcnt lgkmcnt(11)\n\t"
        "v_lshl_add_u32 %[xh], %[xh], 5, %[scs]\n\t"
        "v_mfma_f32_16x16x32_bf16 v[60:63], v[160:163], v[8:11], v[60:63]\n\t"
        "ds_read_b128 v[44:47], %[xa] offset:256\n\t"
        "v_mfma_f32_16x16x32_bf16 v[80:83], v[164:167], v[8:11], v[80:83]\n\t"
        "s_waitcnt lgkmcnt(11)\n\t"
        "v_mfma_f32_16x16x32_bf16 v[64:67], v[160:163], v[12:15], v[64:67]\n\t"
        "ds_read_b128 v[8:11], %[xa] offset:512\n\t"
        "v_mfma_f32_16x16x32_bf16 v[84:87], v[164:167], v[12:15], v[84:87]\n\t"
        "s_waitcnt lgkmcnt(11)\n\t"
        "v_mfma_f32_16x16x32_bf16 v[52:55], v[168:171], v[16:19], v[52:55]\n\t"
        "ds_read_b128 v[12:15], %[xa] offset:768\n\t"
        "v_mfma_f32_16x16x32_bf16 v[72:75], v[172:175], v[16:19], v[72:75]\n\t"
        "s_waitcnt lgkmcnt(11)\n\t"
        "v_mfma_f32_16x16x32_bf16 v[56:59], v[168:171], v[20:23], v[56:59]\n\t"
        "ds_read_b32 %[fl], %[fb] offset:%[og0]+8\n\t"
        "v_mfma_f32_16x16x32_bf16 v[76:79], v[172:175], v[20:23], v[76:79]\n\t"
        "s_waitcnt lgkmcnt(11)\n\t"
        "v_mfma_f32_16x16x32_bf16 v[60:63], v[168:171], v[48:51], v[60:63]\n\t"
        "v_mfma_f32_16x16x32_bf16 v[80:83], v[172:175], v[48:51], v[80:83]\n\t"
        "s_waitcnt lgkmcnt(10)\n\t"
        "v_mfma_f32_16x16x32_bf16 v[64:67], v[168:171], v[68:71], v[64:67]\n\t"
        "v_mfma_f32_16x16x32_bf16 v[84:87], v[172:175], v[68:71], v[84:87]\n\t"
        "s_waitcnt lgkmcnt(9)\n\t"
        "v_mfma_f32_16x16x32_bf16 v[52:55], v[176:179], v[24:27], v[52:55]\n\t"
        "v_mfma_f32_16x16x32_bf16 v[72:75], v[180:183], v[24:27], v[72:75]\n\t"
        "s_waitcnt lgkmcnt(8)\n\t"
        "v_mfma_f32_16x16x32_bf16 v[56:59], v[176:179], v[28:31], v[56:59]\n\t"
        "v_mfma_f32_16x16x32_bf16 v[76:79], v[180:183], v[28:31], v[76:79]\n\t"
        "s_waitcnt lgkmcnt(7)\n\t"
        "v_mfma_f32_16x16x32_bf16 v[60:63], v[176:179], v[32:35], v[60:63]\n\t"
        "s_waitcnt lgkmcnt(0)\n\t"
        "v_cmp_gt_u32 vcc, 2, %[fl]\n\t"
        "v_mfma_f32_16x16x32_bf16 v[80:83], v[180:183], v[32:35], v[80:83]\n\t"
        "v_mfma_f32_16x16x32_bf16 v[64:67], v[176:179], v[36:39], v[64:67]\n\t"
        "s_cbranch_vccnz Ls32_%=\n\t"
        "Lg32_%=:\n\t"
        "ds_read_b128 v[16:19], %[xa] offset:4096\n\t"
        "v_mfma_f32_16x16x32_bf16 v[84:87], v[180:183], v[36:39], v[84:87]\n\t"
        "ds_read_b128 v[20:23], %[xa] offset:4352\n\t"
        "v_mfma_f32_16x16x32_bf16 v[52:55], v[184:187], v[40:43], v[52:55]\n\t"
        "ds_read_b128 v[48:51], %[xa] offset:4608\n\t"
        "v_mfma_f32_16x16x32_bf16 v[72:75], v[188:191], v[40:43], v[72:75]\n\t"
        "ds_read_b128 v[68:71], %[xa] offset:4864\n\t"
        "v_mfma_f32_16x16x32_bf16 v[56:59], v[184:187], v[44:47], v[56:59]\n\t"
        "ds_read_b128 v[24:27], %[xa] offset:8192\n\t"
        "v_mfma_f32_16x16x32_bf16 v[76:79], v[188:191], v[44:47], v[76:79]\n\t"
        "ds_read_b128 v[28:31], %[xa] offset:8448\n\t"
        "v_mfma_f32_16x16x32_bf16 v[60:63], v[184:187], v[8:11], v[60:63]\n\t"
        "ds_read_b128 v[32:35], %[xa] offset:8704\n\t"
        "v_mfma_f32_16x16x32_bf16 v[80:83], v[188:191], v[8:11], v[80:83]\n\t"
        "ds_read_b128 v[36:39], %[xa] offset:8960\n\t"
        "v_mfma_f32_16x16x32_bf16 v[64:67], v[184:187], v[12:15], v[64:67]\n\t"
        "ds_read_b32 %[fl], %[fb] offset:%[og0]+12\n\t"
        "ds_read_b128 v[40:43], %[xa] offset:12288\n\t"
        "v_mfma_f32_16x16x32_bf16 v[84:87], v[188:191], v[12:15], v[84:87]\n\t"
        "ds_read_b128 v[44:47], %[xa] offset:16384\n\t"
        "s_waitcnt lgkmcnt(10)\n\t"
        "v_mfma_f32_16x16x32_bf16 v[52:55], v[192:195], v[16:19], v[52:55]\n\t"
        "v_mfma_f32_16x16x32_bf16 v[72:75], v[196:199], v[16:19], v[72:75]\n\t"
        "s_waitcnt lgkmcnt(9)\n\t"
        "v_mfma_f32_16x16x32_bf16 v[56:59], v[192:195], v[20:23], v[56:59]\n\t"
        "v_mfma_f32_16x16x32_bf16 v[76:79], v[196:199], v[20:23], v[76:79]\n\t"
        "s_waitcnt lgkmcnt(8)\n\t"
        "v_mfma_f32_16x16x32_bf16 v[60:63], v[192:195], v[48:51], v[60:63]\n\t"
        "ds_read_b128 v[8:11], %[xh] offset:0\n\t"
        "v_mfma_f32_16x16x32_bf16 v[80:83], v[196:199], v[48:51], v[80:83]\n\t"
        "ds_read_b128 v[12:15], %[xh] offset:16\n\t"
        "s_waitcnt lgkmcnt(9)\n\t"
        "v_mfma_f32_16x16x32_bf16 v[64:67], v[192:195], v[68:71], v[64:67]\n\t"
        "ds_read_b64 v[98:99], %[fb] offset:%[omc]\n\t"
        "v_mfma_f32_16x16x32_bf16 v[84:87], v[196:199], v[68:71], v[84:87]\n\t"
        "ds_read_b64 v[16:17], %[xo0] offset:128\n\t"
        "s_waitcnt lgkmcnt(10)\n\t"
        "v_mfma_f32_16x16x32_bf16 v[52:55], v[200:203], v[24:27], v[52:55]\n\t"
        "s_waitcnt lgkmcnt(6)\n\t"
        "v_cmp_gt_u32 vcc, %[sthr], %[fl]\n\t"
        "ds_read_b64 v[18:19], %[xo0] offset:256\n\t"
        "v_mfma_f32_16x16x32_bf16 v[72:75], v[204:207], v[24:27], v[72:75]\n\t"
        "ds_read_b64 v[20:21], %[xo0] offset:384\n\t"
        "v_mfma_f32_16x16x32_bf16 v[56:59], v[200:203], v[28:31], v[56:59]\n\t"
        "s_cbranch_vccnz Ls42_%=\n\t"
        "Lg42_%=:\n\t"
        "ds_read_b128 v[48:51], %[xa] offset:20480\n\t"
        "v_mfma_f32_16x16x32_bf16 v[76:79], v[204:207], v[28:31], v[76:79]\n\t"
        "ds_read_b64 v[22:23], %[xo0] offset:512\n\t"
        "v_mfma_f32_16x16x32_bf16 v[60:63], v[200:203], v[32:35], v[60:63]\n\t"
        "ds_read_b128 v[68:71], %[xa] offset:24576\n\t"
        "v_mfma_f32_16x16x32_bf16 v[80:83], v[204:207], v[32:35], v[80:83]\n\t"
        "ds_read_b128 v[24:27], %[xa] offset:28672\n\t"
        "v_mfma_f32_16x16x32_bf16 v[64:67], v[200:203], v[36:39], v[64:67]\n\t"
        "ds_read_b128 v[28:31], %[xa] offset:32768\n\t"
        "v_mfma_f32_16x16x32_bf16 v[84:87], v[204:207], v[36:39], v[84:87]\n\t"
        "ds_read_b128 v[32:35], %[xa] offset:12544\n\t"
        "s_waitcnt lgkmcnt(13)\n\t"
        "v_mfma_f32_16x16x32_bf16 v[52:55], v[208:211], v[40:43], v[52:55]\n\t"
        "ds_read_b128 v[36:39], %[xa] offset:16640\n\t"
        "v_mfma_f32_16x16x32_bf16 v[72:75], v[212:215], v[40:43], v[72:75]\n\t"
        "s_waitcnt lgkmcnt(13)\n\t"
        "v_mfma_f32_16x16x32_bf16 v[52:55], v[216:219], v[44:47], v[52:55]\n\t"
        "ds_read_b128 v[40:43], %[xa] offset:20736\n\t"
        "v_mfma_f32_16x16x32_bf16 v[72:75], v[220:223], v[44:47], v[72:75]\n\t"
        "s_waitcnt lgkmcnt(7)\n\t"
        "v_mfma_f32_16x16x32_bf16 v[52:55], v[224:227], v[48:51], v[52:55]\n\t"
        "ds_read_b128 v[44:47], %[xa] offset:24832\n\t"
        "v_add_u32 %[xh], 73728, v107\n\t"
        "v_mfma_f32_16x16x32_bf16 v[72:75], v[228:231], v[48:51], v[72:75]\n\t"
        "s_waitcnt lgkmcnt(6)\n\t"
        "v_mfma_f32_16x16x32_bf16 v[52:55], v[232:235], v[68:71], v[52:55]\n\t"
        "ds_read_b128 v[48:51], %[xa] offset:28928\n\t"
        "v_mfma_f32_16x16x32_bf16 v[72:75], v[236:239], v[68:71], v[72:75]\n\t"
        "s_waitcnt lgkmcnt(6)\n\t"
        "v_mfma_f32_16x16x32_bf16 v[52:55], v[240:243], v[24:27], v[52:55]\n\t"
        "ds_read_b128 v[68:71], %[xa] offset:33024\n\t"
        "v_mfma_f32_16x16x32_bf16 v[72:75], v[244:247], v[24:27], v[72:75]\n\t"
        "s_waitcnt lgkmcnt(6)\n\t"
        "v_mfma_f32_16x16x32_bf16 v[52:55], v[248:251], v[28:31], v[52:55]\n\t"
        "v_cmp_gt_u32 vcc, 5, v98\n\t"
        "ds_read_b128 v[24:27], %[xa] offset:12800\n\t"
        "v_mfma_f32_16x16x32_bf16 v[72:75], v[252:255], v[28:31], v[72:75]\n\t"
        "s_waitcnt lgkmcnt(6)\n\t"
        "v_mfma_f32_16x16x32_bf16 v[56:59], v[208:211], v[32:35], v[56:59]\n\t"
        "s_cbranch_vccnz Ln_%=\n\t"
        "Lo_%=:\n\t"
        "ds_read_b128 v[28:31], %[xa] offset:16896\n\t"
        "v_mfma_f32_16x16x32_bf16 v[76:79], v[212:215], v[32:35], v[76:79]\n\t"
        "s_waitcnt lgkmcnt(6)\n\t"
        "v_mfma_f32_16x16x32_bf16 v[56:59], v[216:219], v[36:39], v[56:59]\n\t"
        "ds_read_b128 v[32:35], %[xa] offset:20992\n\t"
        "v_fma_f32 v52, -v16, v8, v52\n\t"
        "v_fma_f32 v53, -v16, v9, v53\n\t"
        "v_mfma_f32_16x16x32_bf16 v[76:79], v[220:223], v[36:39], v[76:79]\n\t"
        "s_waitcnt lgkmcnt(6)\n\t"
        "v_fma_f32 v54, -v16, v10, v54\n\t"
        "v_fma_f32 v55, -v16, v11, v55\n\t"
        "v_mfma_f32_16x16x32_bf16 v[56:59], v[224:227], v[40:43], v[56:59]\n\t"
        "ds_read_b128 v[36:39], %[xa] offset:25088\n\t"
        "v_fma_f32 v72, -v16, v12, v72\n\t"
        "v_fma_f32 v73, -v16, v13, v73\n\t"
        "v_mfma_f32_16x16x32_bf16 v[76:79], v[228:231], v[40:43], v[76:79]\n\t"
        "s_waitcnt lgkmcnt(6)\n\t"
        "v_fma_f32 v74, -v16, v14, v74\n\t"
        "v_fma_f32 v75, -v16, v15, v75\n\t"
        "v_mfma_f32_16x16x32_bf16 v[56:59], v[232:235], v[44:47], v[56:59]\n\t"
        "ds_read_b128 v[40:43], %[xa] offset:29184\n\t"
        "v_mul_f32 v52, v17, v52\n\t"
        "v_mul_f32 v53, v17, v53\n\t"
        "v_mfma_f32_16x16x32_bf16 v[76:79], v[236:239], v[44:47], v[76:79]\n\t"
        "s_waitcnt lgkmcnt(6)\n\t"
        "v_mul_f32 v54, v17, v54\n\t"
        "v_mul_f32 v55, v17, v55\n\t"
        "v_mfma_f32_16x16x32_bf16 v[56:59], v[240:243], v[48:51], v[56:59]\n\t"
        "ds_read_b128 v[44:47], %[xa] offset:33280\n\t"
        "v_mul_f32 v72, v17, v72\n\t"
        "v_mul_f32 v73, v17, v73\n\t"
        "v_mfma_f32_16x16x32_bf16 v[76:79], v[244:247], v[48:51], v[76:79]\n\t"
        "s_waitcnt lgkmcnt(6)\n\t"
        "v_mul_f32 v74, v17, v74\n\t"
        "v_mul_f32 v75, v17, v75\n\t"
        "v_mfma_f32_16x16x32_bf16 v[56:59], v[248:251], v[68:71], v[56:59]\n\t"
        "ds_read_b128 v[48:51], %[xa] offset:13056\n\t"
        "v_cvt_pk_bf16_f32 v52, v52, v72\n\t"
        "v_cvt_pk_bf16_f32 v53, v53, v73\n\t"
        "v_mfma_f32_16x16x32_bf16 v[76:79], v[252:255], v[68:71], v[76:79]\n\t"
        "s_waitcnt lgkmcnt(6)\n\t"
        "v_cvt_pk_bf16_f32 v54, v54, v74\n\t"
        "v_cvt_pk_bf16_f32 v55, v55, v75\n\t"
        "v_mfma_f32_16x16x32_bf16 v[60:63], v[208:211], v[24:27], v[60:63]\n\t"
        "v_add_u32 v16, v110, v111\n\t"
        "v_mfma_f32_16x16x32_bf16 v[80:83], v[212:215], v[24:27], v[80:83]\n\t"
        "ds_read_b128 v[72:75], %[xa] offset:17152\n\t"
        "s_waitcnt lgkmcnt(6)\n\t"
        "s_cmp_eq_u64 %[sv0], 0\n\t"
        "s_cbranch_scc1 Lq_%=\n\t"
        "v_mfma_f32_16x16x32_bf16 v[60:63], v[216:219], v[28:31], v[60:63]\n\t"
        "ds_read_b32 %[fl], %[fb] offset:%[og0]+16\n\t"
        "ds_read_b128 v[24:27], %[xa] offset:21248\n\t"
        "v_fma_f32 v56, -v18, v8, v56\n\t"
        "v_fma_f32 v57, -v18, v9, v57\n\t"
        "v_mfma_f32_16x16x32_bf16 v[80:83], v[220:223], v[28:31], v[80:83]\n\t"
        "s_waitcnt lgkmcnt(7)\n\t"
        "v_fma_f32 v58, -v18, v10, v58\n\t"
        "v_fma_f32 v59, -v18, v11, v59\n\t"
        "v_mfma_f32_16x16x32_bf16 v[60:63], v[224:227], v[32:35], v[60:63]\n\t"
        "ds_read_b128 v[28:31], %[xa] offset:25344\n\t"
        "v_fma_f32 v76, -v18, v12, v76\n\t"
        "v_fma_f32 v77, -v18, v13, v77\n\t"
        "v_mfma_f32_16x16x32_bf16 v[80:83], v[228:231], v[32:35], v[80:83]\n\t"
        "s_waitcnt lgkmcnt(7)\n\t"
        "v_fma_f32 v78, -v18, v14, v78\n\t"
        "v_fma_f32 v79, -v18, v15, v79\n\t"
        "v_mfma_f32_16x16x32_bf16 v[60:63], v[232:235], v[36:39], v[60:63]\n\t"
        "ds_read_b128 v[32:35], %[xa] offset:29440\n\t"
        "v_mul_f32 v56, v19, v56\n\t"
        "v_mul_f32 v57, v19, v57\n\t"
        "v_mfma_f32_16x16x32_bf16 v[80:83], v[236:239], v[36:39], v[80:83]\n\t"
        "s_waitcnt lgkmcnt(7)\n\t"
        "v_mul_f32 v58, v19, v58\n\t"
        "v_mul_f32 v59, v19, v59\n\t"
        "v_mfma_f32_16x16x32_bf16 v[60:63], v[240:243], v[40:43], v[60:63]\n\t"
        "ds_read_b128 v[36:39], %[xa] offset:33536\n\t"
        "s_mov_b64 exec, 1\n\t"
        "ds_add_u32 %[fb], %[one] offset:%[oc1]\n\t"
        "s_mov_b64 exec, -1\n\t"
        "v_mul_f32 v76, v19, v76\n\t"
        "v_mul_f32 v77, v19, v77\n\t"
        "v_mfma_f32_16x16x32_bf16 v[80:83], v[244:247], v[40:43], v[80:83]\n\t"
        "s_waitcnt lgkmcnt(8)\n\t"
        "v_mul_f32 v78, v19, v78\n\t"
        "v_mul_f32 v79, v19, v79\n\t"
        "v_mfma_f32_16x16x32_bf16 v[60:63], v[248:251], v[44:47], v[60:63]\n\t"
        "v_cvt_pk_bf16_f32 v56, v56, v76\n\t"
        "v_cvt_pk_bf16_f32 v57, v57, v77\n\t"
        "v_mfma_f32_16x16x32_bf16 v[80:83], v[252:255], v[44:47], v[80:83]\n\t"
        "s_waitcnt lgkmcnt(7)\n\t"
        "v_cvt_pk_bf16_f32 v58, v58, v78\n\t"
        "v_cvt_pk_bf16_f32 v59, v59, v79\n\t"
        "v_mfma_f32_16x16x32_bf16 v[64:67], v[208:211], v[48:51], v[64:67]\n\t"
        "s_waitcnt lgkmcnt(5)\n\t"
        "v_cmp_gt_u32 vcc, 2, %[fl]\n\t"
        "v_add_u32 v18, v16, v111\n\t"
        "v_mfma_f32_16x16x32_bf16 v[84:87], v[212:215], v[48:51], v[84:87]\n\t"
        "v_mfma_f32_16x16x32_bf16 v[64:67], v[216:219], v[72:75], v[64:67]\n\t"
        "s_cbranch_vccnz Ls64_%=\n\t"
        "Lg64_%=:\n\t"
        "ds_read_b128 v[40:43], %[xh] offset:0\n\t"
        "v_fma_f32 v60, -v20, v8, v60\n\t"
        "v_fma_f32 v61, -v20, v9, v61\n\t"
        "v_mfma_f32_16x16x32_bf16 v[84:87], v[220:223], v[72:75], v[84:87]\n\t"
        "ds_read_b128 v[44:47], %[xh] offset:1024\n\t"
        "s_waitcnt lgkmcnt(6)\n\t"
        "v_fma_f32 v62, -v20, v10, v62\n\t"
        "v_fma_f32 v63, -v20, v11, v63\n\t"
        "v_mfma_f32_16x16x32_bf16 v[64:67], v[224:227], v[24:27], v[64:67]\n\t"
        "ds_read_b128 v[76:79], %[xh] offset:2048\n\t"
        "v_fma_f32 v80, -v20, v12, v80\n\t"
        "v_fma_f32 v81, -v20, v13, v81\n\t"
        "v_mfma_f32_16x16x32_bf16 v[84:87], v[228:231], v[24:27], v[84:87]\n\t"
        "ds_read_b128 v[72:75], %[xh] offset:3072\n\t"
        "s_waitcnt lgkmcnt(7)\n\t"
        "v_fma_f32 v82, -v20, v14, v82\n\t"
        "v_fma_f32 v83, -v20, v15, v83\n\t"
        "v_mfma_f32_16x16x32_bf16 v[64:67], v[232:235], v[28:31], v[64:67]\n\t"
        "ds_read_b128 v[24:27], %[xh] offset:4096\n\t"
        "v_mul_f32 v60, v21, v60\n\t"
        "v_mul_f32 v61, v21, v61\n\t"
        "v_mfma_f32_16x16x32_bf16 v[84:87], v[236:239], v[28:31], v[84:87]\n\t"
        "s_waitcnt lgkmcnt(7)\n\t"
        "v_mul_f32 v62, v21, v62\n\t"
        "v_mul_f32 v63, v21, v63\n\t"
        "v_mfma_f32_16x16x32_bf16 v[64:67], v[240:243], v[32:35], v[64:67]\n\t"
        "ds_read_b128 v[28:31], %[xh] offset:5120\n\t"
        "v_mul_f32 v80, v21, v80\n\t"
        "v_mul_f32 v81, v21, v81\n\t"
        "v_mfma_f32_16x16x32_bf16 v[84:87], v[244:247], v[32:35], v[84:87]\n\t"
        "s_waitcnt lgkmcnt(7)\n\t"
        "v_mul_f32 v82, v21, v82\n\t"
        "v_mul_f32 v83, v21, v83\n\t"
        "v_mfma_f32_16x16x32_bf16 v[64:67], v[248:251], v[36:39], v[64:67]\n\t"
        "ds_read_b128 v[32:35], %[xh] offset:6144\n\t"
        "v_cvt_pk_bf16_f32 v60, v60, v80\n\t"
        "v_cvt_pk_bf16_f32 v61, v61, v81\n\t"
        "v_mfma_f32_16x16x32_bf16 v[84:87], v[252:255], v[36:39], v[84:87]\n\t"
        "s_waitcnt lgkmcnt(6)\n\t"
        "v_cvt_pk_bf16_f32 v62, v62, v82\n\t"
        "v_cvt_pk_bf16_f32 v63, v63, v83\n\t"
        "v_mfma_f32_16x16x32_bf16 v[48:51], v[112:115], v[40:43], 0\n\t"
        "ds_read_b128 v[36:39], %[xh] offset:7168\n\t"
        "v_add_u32 v20, v18, v111\n\t"
        "v_mfma_f32_16x16x32_bf16 v[68:71], v[116:119], v[40:43], 0\n\t"
        "ds_read_b128 v[80:83], %[xh] offset:8192\n\t"
        "s_waitcnt lgkmcnt(7)\n\t"
        "v_mfma_f32_16x16x32_bf16 v[48:51], v[120:123], v[44:47], v[48:51]\n\t"
        "ds_read_b128 v[40:43], %[xh] offset:9216\n\t"
        "v_fma_f32 v64, -v22, v8, v64\n\t"
        "v_fma_f32 v65, -v22, v9, v65\n\t"
        "v_mfma_f32_16x16x32_bf16 v[68:71], v[124:127], v[44:47], v[68:71]\n\t"
        "s_waitcnt lgkmcnt(7)\n\t"
        "v_fma_f32 v66, -v22, v10, v66\n\t"
        "v_fma_f32 v67, -v22, v11, v67\n\t"
        "v_mfma_f32_16x16x32_bf16 v[48:51], v[128:131], v[76:79], v[48:51]\n\t"
        "ds_read_b128 v[44:47], %[xh] offset:10240\n\t"
        "v_fma_f32 v84, -v22, v12, v84\n\t"
        "v_fma_f32 v85, -v22, v13, v85\n\t"
        "v_mfma_f32_16x16x32_bf16 v[68:71], v[132:135], v[76:79], v[68:71]\n\t"
        "s_waitcnt lgkmcnt(7)\n\t"
        "v_fma_f32 v86, -v22, v14, v86\n\t"
        "v_fma_f32 v87, -v22, v15, v87\n\t"
        "v_mfma_f32_16x16x32_bf16 v[48:51], v[136:139], v[72:75], v[48:51]\n\t"
        "ds_read_b128 v[76:79], %[xh] offset:11264\n\t"
        "v_mul_f32 v64, v23, v64\n\t"
        "v_mul_f32 v65, v23, v65\n\t"
        "v_mfma_f32_16x16x32_bf16 v[68:71], v[140:143], v[72:75], v[68:71]\n\t"
        "s_waitcnt lgkmcnt(7)\n\t"
        "v_mul_f32 v66, v23, v66\n\t"
        "v_mul_f32 v67, v23, v67\n\t"
        "v_mfma_f32_16x16x32_bf16 v[48:51], v[144:147], v[24:27], v[48:51]\n\t"
        "ds_read_b128 v[72:75], %[xh] offset:12288\n\t"
        "v_mul_f32 v84, v23, v84\n\t"
        "v_mul_f32 v85, v23, v85\n\t"
        "v_mfma_f32_16x16x32_bf16 v[68:71], v[148:151], v[24:27], v[68:71]\n\t"
        "s_waitcnt lgkmcnt(7)\n\t"
        "v_mul_f32 v86, v23, v86\n\t"
        "v_mul_f32 v87, v23, v87\n\t"
        "v_mfma_f32_16x16x32_bf16 v[48:51], v[152:155], v[28:31], v[48:51]\n\t"
        "ds_read_b128 v[24:27], %[xh] offset:13312\n\t"
        "v_cvt_pk_bf16_f32 v64, v64, v84\n\t"
        "v_cvt_pk_bf16_f32 v65, v65, v85\n\t"
        "v_mfma_f32_16x16x32_bf16 v[68:71], v[156:159], v[28:31], v[68:71]\n\t"
        "s_waitcnt lgkmcnt(7)\n\t"
        "v_cvt_pk_bf16_f32 v66, v66, v86\n\t"
        "v_cvt_pk_bf16_f32 v67, v67, v87\n\t"
        "v_mfma_f32_16x16x32_bf16 v[48:51], v[160:163], v[32:35], v[48:51]\n\t"
        "ds_read_b128 v[28:31], %[xh] offset:14336\n\t"
        "v_add_u32 v22, v20, v111\n\t"
        "v_mfma_f32_16x16x32_bf16 v[68:71], v[164:167], v[32:35], v[68:71]\n\t"
        "s_waitcnt lgkmcnt(7)\n\t"
        "v_mfma_f32_16x16x32_bf16 v[48:51], v[168:171], v[36:39], v[48:51]\n\t"
        "ds_read_b128 v[32:35], %[xh] offset:15360\n\t"
        "v_mfma_f32_16x16x32_bf16 v[68:71], v[172:175], v[36:39], v[68:71]\n\t"
        "s_waitcnt lgkmcnt(7)\n\t"
        "v_mfma_f32_16x16x32_bf16 v[48:51], v[176:179], v[80:83], v[48:51]\n\t"
        "ds_read_b128 v[36:39], %[xh] offset:16384\n\t"
        "v_mfma_f32_16x16x32_bf16 v[68:71], v[180:183], v[80:83], v[68:71]\n\t"
        "s_waitcnt lgkmcnt(7)\n\t"
        "v_mfma_f32_16x16x32_bf16 v[48:51], v[184:187], v[40:43], v[48:51]\n\t"
        "ds_read_b128 v[80:83], %[xh] offset:17408\n\t"
        "v_mfma_f32_16x16x32_bf16 v[68:71], v[188:191], v[40:43], v[68:71]\n\t"
        "ds_read_b64 v[84:85], %[xo0]\n\t"
        "s_waitcnt lgkmcnt(8)\n\t"
        "v_mfma_f32_16x16x32_bf16 v[48:51], v[192:195], v[44:47], v[48:51]\n\t"
        "ds_read_b32 v99, %[fb] offset:%[omc]+4\n\t"
        "v_mfma_f32_16x16x32_bf16 v[68:71], v[196:199], v[44:47], v[68:71]\n\t"
        "ds_read_b32 v98, %[fb]\n\t"
        "s_waitcnt lgkmcnt(9)\n\t"
        "v_mfma_f32_16x16x32_bf16 v[48:51], v[200:203], v[76:79], v[48:51]\n\t"
        "v_mfma_f32_16x16x32_bf16 v[68:71], v[204:207], v[76:79], v[68:71]\n\t"
        "s_waitcnt lgkmcnt(8)\n\t"
        "v_mfma_f32_16x16x32_bf16 v[48:51], v[208:211], v[72:75], v[48:51]\n\t"
        "v_mfma_f32_16x16x32_bf16 v[68:71], v[212:215], v[72:75], v[68:71]\n\t"
        "s_waitcnt lgkmcnt(7)\n\t"
        "v_mfma_f32_16x16x32_bf16 v[48:51], v[216:219], v[24:27], v[48:51]\n\t"
        "v_mfma_f32_16x16x32_bf16 v[68:71], v[220:223], v[24:27], v[68:71]\n\t"
        "s_waitcnt lgkmcnt(6)\n\t"
        "v_mfma_f32_16x16x32_bf16 v[48:51], v[224:227], v[28:31], v[48:51]\n\t"
        "v_mfma_f32_16x16x32_bf16 v[68:71], v[228:231], v[28:31], v[68:71]\n\t"
        "s_waitcnt lgkmcnt(5)\n\t"
        "v_mfma_f32_16x16x32_bf16 v[48:51], v[232:235], v[32:35], v[48:51]\n\t"
        "v_mfma_f32_16x16x32_bf16 v[68:71], v[236:239], v[32:35], v[68:71]\n\t"
        "s_waitcnt lgkmcnt(4)\n\t"
        "v_mfma_f32_16x16x32_bf16 v[48:51], v[240:243], v[36:39], v[48:51]\n\t"
        "v_mfma_f32_16x16x32_bf16 v[68:71], v[244:247], v[36:39], v[68:71]\n\t"
        "s_waitcnt lgkmcnt(3)\n\t"
        "v_mfma_f32_16x16x32_bf16 v[48:51], v[248:251], v[80:83], v[48:51]\n\t"
        "v_mfma_f32_16x16x32_bf16 v[68:71], v[252:255], v[80:83], v[68:71]\n\t"
        "s_waitcnt lgkmcnt(0)\n\t"
        "s_nop 5\n\t"
        "v_fma_f32 v48, -v84, v8, v48\n\t"
        "v_fma_f32 v49, -v84, v9, v49\n\t"
        "v_fma_f32 v50, -v84, v10, v50\n\t"
        "v_fma_f32 v51, -v84, v11, v51\n\t"
        "v_fma_f32 v68, -v84, v12, v68\n\t"
        "v_fma_f32 v69, -v84, v13, v69\n\t"
        "v_fma_f32 v70, -v84, v14, v70\n\t"
        "v_fma_f32 v71, -v84, v15, v71\n\t"
        "v_mul_f32 v48, v85, v48\n\t"
        "v_mul_f32 v49, v85, v49\n\t"
        "v_mul_f32 v50, v85, v50\n\t"
        "v_mul_f32 v51, v85, v51\n\t"
        "v_mul_f32 v68, v85, v68\n\t"
        "v_mul_f32 v69, v85, v69\n\t"
        "v_mul_f32 v70, v85, v70\n\t"
        "v_mul_f32 v71, v85, v71\n\t"
        "v_cvt_pk_bf16_f32 v48, v48, v68\n\t"
        "v_cvt_pk_bf16_f32 v49, v49, v69\n\t"
        "v_cvt_pk_bf16_f32 v50, v50, v70\n\t"
        "v_cvt_pk_bf16_f32 v51, v51, v71\n\t"
        "s_branch Lu_%=\n\t"
        "Lq_%=:\n\t"
        "v_mfma_f32_16x16x32_bf16 v[60:63], v[216:219], v[28:31], v[60:63]\n\t"
        "ds_read_b128 v[24:27], %[xa] offset:21248\n\t"
        "v_fma_f32 v56, -v18, v8, v56\n\t"
        "v_fma_f32 v57, -v18, v9, v57\n\t"
        "v_mfma_f32_16x16x32_bf16 v[80:83], v[220:223], v[28:31], v[80:83]\n\t"
        "s_waitcnt lgkmcnt(6)\n\t"
        "v_fma_f32 v58, -v18, v10, v58\n\t"
        "v_fma_f32 v59, -v18, v11, v59\n\t"
        "v_mfma_f32_16x16x32_bf16 v[60:63], v[224:227], v[32:35], v[60:63]\n\t"
        "ds_read_b128 v[28:31], %[xa] offset:25344\n\t"
        "v_fma_f32 v76, -v18, v12, v76\n\t"
        "v_fma_f32 v77, -v18, v13, v77\n\t"
        "v_mfma_f32_16x16x32_bf16 v[80:83], v[228:231], v[32:35], v[80:83]\n\t"
        "s_waitcnt lgkmcnt(6)\n\t"
        "v_fma_f32 v78, -v18, v14, v78\n\t"
        "v_fma_f32 v79, -v18, v15, v79\n\t"
        "v_mfma_f32_16x16x32_bf16 v[60:63], v[232:235], v[36:39], v[60:63]\n\t"
        "ds_read_b128 v[32:35], %[xa] offset:29440\n\t"
        "v_mul_f32 v56, v19, v56\n\t"
        "v_mul_f32 v57, v19, v57\n\t"
        "v_mfma_f32_16x16x32_bf16 v[80:83], v[236:239], v[36:39], v[80:83]\n\t"
        "s_waitcnt lgkmcnt(6)\n\t"
        "v_mul_f32 v58, v19, v58\n\t"
        "v_mul_f32 v59, v19, v59\n\t"
        "v_mfma_f32_16x16x32_bf16 v[60:63], v[240:243], v[40:43], v[60:63]\n\t"
        "ds_read_b128 v[36:39], %[xa] offset:33536\n\t"
        "s_mov_b64 exec, 1\n\t"
        "ds_add_u32 %[fb], %[one] offset:%[oc1]\n\t"
        "s_mov_b64 exec, -1\n\t"
        "v_mul_f32 v76, v19, v76\n\t"
        "v_mul_f32 v77, v19, v77\n\t"
        "v_mfma_f32_16x16x32_bf16 v[80:83], v[244:247], v[40:43], v[80:83]\n\t"
        "s_waitcnt lgkmcnt(7)\n\t"
        "v_mul_f32 v78, v19, v78\n\t"
        "v_mul_f32 v79, v19, v79\n\t"
        "v_mfma_f32_16x16x32_bf16 v[60:63], v[248:251], v[44:47], v[60:63]\n\t"
        "v_cvt_pk_bf16_f32 v56, v56, v76\n\t"
        "v_cvt_pk_bf16_f32 v57, v57, v77\n\t"
        "v_mfma_f32_16x16x32_bf16 v[80:83], v[252:255], v[44:47], v[80:83]\n\t"
        "s_waitcnt lgkmcnt(6)\n\t"
        "v_cvt_pk_bf16_f32 v58, v58, v78\n\t"
        "v_cvt_pk_bf16_f32 v59, v59, v79\n\t"
        "v_mfma_f32_16x16x32_bf16 v[64:67], v[208:211], v[48:51], v[64:67]\n\t"
        "v_add_u32 v18, v16, v111\n\t"
        "v_mfma_f32_16x16x32_bf16 v[84:87], v[212:215], v[48:51], v[84:87]\n\t"
        "s_waitcnt lgkmcnt(5)\n\t"
        "v_mfma_f32_16x16x32_bf16 v[64:67], v[216:219], v[72:75], v[64:67]\n\t"
        "v_fma_f32 v60, -v20, v8, v60\n\t"
        "v_fma_f32 v61, -v20, v9, v61\n\t"
        "v_mfma_f32_16x16x32_bf16 v[84:87], v[220:223], v[72:75], v[84:87]\n\t"
        "s_waitcnt lgkmcnt(4)\n\t"
        "v_fma_f32 v62, -v20, v10, v62\n\t"
        "v_fma_f32 v63, -v20, v11, v63\n\t"
        "v_mfma_f32_16x16x32_bf16 v[64:67], v[224:227], v[24:27], v[64:67]\n\t"
        "v_fma_f32 v80, -v20, v12, v80\n\t"
        "v_fma_f32 v81, -v20, v13, v81\n\t"
        "v_mfma_f32_16x16x32_bf16 v[84:87], v[228:231], v[24:27], v[84:87]\n\t"
        "s_waitcnt lgkmcnt(3)\n\t"
        "v_fma_f32 v82, -v20, v14, v82\n\t"
        "v_fma_f32 v83, -v20, v15, v83\n\t"
        "v_mfma_f32_16x16x32_bf16 v[64:67], v[232:235], v[28:31], v[64:67]\n\t"
        "v_mul_f32 v60, v21, v60\n\t"
        "v_mul_f32 v61, v21, v61\n\t"
        "v_mfma_f32_16x16x32_bf16 v[84:87], v[236:239], v[28:31], v[84:87]\n\t"
        "s_waitcnt lgkmcnt(2)\n\t"
        "v_mul_f32 v62, v21, v62\n\t"
        "v_mul_f32 v63, v21, v63\n\t"
        "v_mfma_f32_16x16x32_bf16 v[64:67], v[240:243], v[32:35], v[64:67]\n\t"
        "v_mul_f32 v80, v21, v80\n\t"
        "v_mul_f32 v81, v21, v81\n\t"
        "v_mfma_f32_16x16x32_bf16 v[84:87], v[244:247], v[32:35], v[84:87]\n\t"
        "s_waitcnt lgkmcnt(1)\n\t"
        "v_mul_f32 v82, v21, v82\n\t"
        "v_mul_f32 v83, v21, v83\n\t"
        "v_mfma_f32_16x16x32_bf16 v[64:67], v[248:251], v[36:39], v[64:67]\n\t"
        "v_cvt_pk_bf16_f32 v60, v60, v80\n\t"
        "v_cvt_pk_bf16_f32 v61, v61, v81\n\t"
        "v_mfma_f32_16x16x32_bf16 v[84:87], v[252:255], v[36:39], v[84:87]\n\t"
        "v_cvt_pk_bf16_f32 v62, v62, v82\n\t"
        "v_cvt_pk_bf16_f32 v63, v63, v83\n\t"
        "ds_read_b32 v99, %[fb] offset:%[omc]+4\n\t"
        "ds_read_b32 v98, %[fb]\n\t"
        "v_add_u32 v20, v18, v111\n\t"
        "v_fma_f32 v64, -v22, v8, v64\n\t"
        "v_fma_f32 v65, -v22, v9, v65\n\t"
        "v_fma_f32 v66, -v22, v10, v66\n\t"
        "v_fma_f32 v67, -v22, v11, v67\n\t"
        "v_fma_f32 v84, -v22, v12, v84\n\t"
        "v_fma_f32 v85, -v22, v13, v85\n\t"
        "v_fma_f32 v86, -v22, v14, v86\n\t"
        "v_fma_f32 v87, -v22, v15, v87\n\t"
        "v_mul_f32 v64, v23, v64\n\t"
        "v_mul_f32 v65, v23, v65\n\t"
        "v_mul_f32 v66, v23, v66\n\t"
        "v_mul_f32 v67, v23, v67\n\t"
        "v_mul_f32 v84, v23, v84\n\t"
        "v_mul_f32 v85, v23, v85\n\t"
        "v_mul_f32 v86, v23, v86\n\t"
        "v_mul_f32 v87, v23, v87\n\t"
        "v_cvt_pk_bf16_f32 v64, v64, v84\n\t"
        "v_cvt_pk_bf16_f32 v65, v65, v85\n\t"
        "v_cvt_pk_bf16_f32 v66, v66, v86\n\t"
        "v_cvt_pk_bf16_f32 v67, v67, v87\n\t"
        "v_add_u32 v22, v20, v111\n\t"
        "s_waitcnt lgkmcnt(0)\n\t"
        "Lu_%=:\n\t"
        "v_readfirstlane_b32 %[sf], v99\n\t"
        "s_cmp_ge_u32 %[sf], 14\n\t"
        "s_cbranch_scc1 Lv_%=\n\t"
        "s_cmp_lt_i32 %[scn], 1\n\t"
        "s_cbranch_scc1 Lv_%=\n\t"
        "s_sub_u32 %[scn], %[scn], 1\n\t"
        "s_sleep 4\n\t"
        "ds_read_b32 v99, %[fb] offset:%[omc]+4\n\t"
        "s_waitcnt lgkmcnt(0)\n\t"
        "s_branch Lu_%=\n\t"
        "Lv_%=:\n\t"
        "s_mov_b64 exec, %[sva]\n\t"
        "ds_write_b128 v16, v[52:55]\n\t"
        "ds_write_b128 v18, v[56:59]\n\t"
        "ds_write_b128 v20, v[60:63]\n\t"
        "ds_write_b128 v22, v[64:67]\n\t"
        "s_mov_b64 exec, %[sv0]\n\t"
        "ds_write_b128 v110, v[48:51]\n\t"
        "s_mov_b64 exec, -1\n\t"
        "v_readfirstlane_b32 %[sf], v98\n\t"
        "s_branch Le_%=\n\t"
        "Ls16_%=:\n\t"
        "s_cmp_lt_i32 %[scn], 1\n\t"
        "s_cbranch_scc1 Lg16_%=\n\t"
        "s_sub_u32 %[scn], %[scn], 1\n\t"
        "ds_read_b32 %[fl], %[fb] offset:%[og0]+4\n\t"
        "s_waitcnt lgkmcnt(0)\n\t"
        "v_cmp_gt_u32 vcc, 2, %[fl]\n\t"
        "s_cbranch_vccnz Ls16_%=\n\t"
        "s_branch Lg16_%=\n\t"
        "Ls32_%=:\n\t"
        "s_cmp_lt_i32 %[scn], 1\n\t"
        "s_cbranch_scc1 Lg32_%=\n\t"
        "s_sub_u32 %[scn], %[scn], 1\n\t"
        "ds_read_b32 %[fl], %[fb] offset:%[og0]+8\n\t"
        "s_waitcnt lgkmcnt(0)\n\t"
        "v_cmp_gt_u32 vcc, 2, %[fl]\n\t"
        "s_cbranch_vccnz Ls32_%=\n\t"
        "s_branch Lg32_%=\n\t"
        "Ls42_%=:\n\t"
        "s_cmp_lt_i32 %[scn], 1\n\t"
        "s_cbranch_scc1 Lg42_%=\n\t"
        "s_sub_u32 %[scn], %[scn], 1\n\t"
        "ds_read_b32 %[fl], %[fb] offset:%[og0]+12\n\t"
        "s_waitcnt lgkmcnt(0)\n\t"
        "v_cmp_gt_u32 vcc, %[sthr], %[fl]\n\t"
        "s_cbranch_vccnz Ls42_%=\n\t"
        "s_branch Lg42_%=\n\t"
        "Ls64_%=:\n\t"
        "s_cmp_lt_i32 %[scn], 1\n\t"
        "s_cbranch_scc1 Lg64_%=\n\t"
        "s_sub_u32 %[scn], %[scn], 1\n\t"
        "ds_read_b32 %[fl], %[fb] offset:%[og0]+16\n\t"
        "s_waitcnt lgkmcnt(0)\n\t"
        "v_cmp_gt_u32 vcc, 2, %[fl]\n\t"
        "s_cbranch_vccnz Ls64_%=\n\t"
        "s_branch Lg64_%=\n\t"
        "Ln_%=:\n\t"
        "s_cmp_lt_i32 %[scn], 1\n\t"
        "s_cbranch_scc1 Lo_%=\n\t"
        "s_sub_u32 %[scn], %[scn], 1\n\t"
        "s_sleep 1\n\t"
        "ds_read_b64 v[98:99], %[fb] offset:%[omc]\n\t"
        "ds_read_b64 v[16:17], %[xo0] offset:128\n\t"
        "ds_read_b64 v[18:19], %[xo0] offset:256\n\t"
        "ds_read_b64 v[20:21], %[xo0] offset:384\n\t"
        "ds_read_b64 v[22:23], %[xo0] offset:512\n\t"
        "s_waitcnt lgkmcnt(0)\n\t"
        "v_cmp_gt_u32 vcc, 5, v98\n\t"
        "s_cbranch_vccnz Ln_%=\n\t"
        "s_branch Lo_%=\n\t"
        "Le_%=:\n\t"
        : [xa] "=&v"(xa), [xh] "=&v"(xh), [xo0] "+v"(xo0), [one] "=&v"(one), [fl] "=&v"(fl), [sf] "=&s"(sf), [scn] "+s"(cn)
        : [fb] "v"(fb), [scs] "s"(cs), [sva] "s"(va), [sv0] "s"(v0), [sthr] "s"(thr), [omc] "n"(BC_MC), [oc1] "n"(BC_C1), [og0] "n"(BC_G0)
        : "memory", "scc", "vcc", "v8", "v9", "v10", "v11", "v12", "v13", "v14", "v15", "v16", "v17", "v18", "v19", "v20", "v21", "v22", "v23", "v24", "v25", "v26", "v27", "v28", "v29", "v30", "v31", "v32", "v33", "v34", "v35", "v36", "v37", "v38", "v39", "v40", "v41", "v42", "v43", "v44", "v45", "v46", "v47");
}

DEV void gemm1p() {
    int a0, a1;
    asm volatile(
        "v_add_u32 %[a0], 92672, v107\n\t"
        "v_add_u32 %[a1], 153344, v107\n\t"
        "ds_read_b128 v[24:27], %[a0] offset:0\n\t"
        "ds_read_b128 v[28:31], %[a0] offset:2304\n\t"
        "ds_read_b128 v[32:35], %[a1] offset:5280\n\t"
        "ds_read_b128 v[36:39], %[a1] offset:1824\n\t"
        "ds_read_b128 v[40:43], %[a0] offset:1024\n\t"
        "ds_read_b128 v[44:47], %[a0] offset:3328\n\t"
        "ds_read_b128 v[48:51], %[a1] offset:6304\n\t"
        "ds_read_b128 v[68:71], %[a1] offset:2848\n\t"
        "s_waitcnt lgkmcnt(7)\n\t"
        "v_mfma_f32_16x16x32_bf16 v[52:55], v[112:115], v[24:27], 0\n\t"
        "v_mfma_f32_16x16x32_bf16 v[72:75], v[116:119], v[24:27], 0\n\t"
        "s_waitcnt lgkmcnt(6)\n\t"
        "v_mfma_f32_16x16x32_bf16 v[56:59], v[112:115], v[28:31], 0\n\t"
        "v_mfma_f32_16x16x32_bf16 v[76:79], v[116:119], v[28:31], 0\n\t"
        "s_waitcnt lgkmcnt(5)\n\t"
        "v_mfma_f32_16x16x32_bf16 v[60:63], v[112:115], v[32:35], 0\n\t"
        "v_mfma_f32_16x16x32_bf16 v[80:83], v[116:119], v[32:35], 0\n\t"
        "s_waitcnt lgkmcnt(4)\n\t"
        "v_mfma_f32_16x16x32_bf16 v[64:67], v[112:115], v[36:39], 0\n\t"
        "v_mfma_f32_16x16x32_bf16 v[84:87], v[116:119], v[36:39], 0\n\t"
        "s_waitcnt lgkmcnt(3)\n\t"
        "v_mfma_f32_16x16x32_bf16 v[52:55], v[120:123], v[40:43], v[52:55]\n\t"
        "v_mfma_f32_16x16x32_bf16 v[72:75], v[124:127], v[40:43], v[72:75]\n\t"
        "s_waitcnt lgkmcnt(2)\n\t"
        "v_mfma_f32_16x16x32_bf16 v[56:59], v[120:123], v[44:47], v[56:59]\n\t"
        "v_mfma_f32_16x16x32_bf16 v[76:79], v[124:127], v[44:47], v[76:79]\n\t"
        "s_waitcnt lgkmcnt(1)\n\t"
        "v_mfma_f32_16x16x32_bf16 v[60:63], v[120:123], v[48:51], v[60:63]\n\t"
        "v_mfma_f32_16x16x32_bf16 v[80:83], v[124:127], v[48:51], v[80:83]\n\t"
        "s_waitcnt lgkmcnt(0)\n\t"
        "v_mfma_f32_16x16x32_bf16 v[64:67], v[120:123], v[68:71], v[64:67]\n\t"
        "v_mfma_f32_16x16x32_bf16 v[84:87], v[124:127], v[68:71], v[84:87]\n\t"
        "s_nop 7\n\t"
        : [a0] "=&v"(a0), [a1] "=&v"(a1) :: "memory", "v24", "v25", "v26", "v27", "v28", "v29", "v30", "v31", "v32", "v33", "v34", "v35", "v36", "v37", "v38", "v39", "v40", "v41", "v42", "v43", "v44", "v45", "v46", "v47");
}

DEV void gemm1b(int fb, int xr, int xq, u32 w1, u32 cs, u64 va, u64 v0, u32& cn) {
    u32 sf, st; u64 mw, mx;
    asm volatile(
        "v_add_u32 v36, 73728, v107\n\t"
        "v_mov_b32 v35, 153344\n\t"
        "v_lshrrev_b32 v37, 8, v107\n\t"
        "v_lshl_add_u32 v37, v37, 5, %[scs]\n\t"
        "s_waitcnt lgkmcnt(0)\n\t"
        "ds_read_b128 v[48:51], v37 offset:0\n\t"
        "ds_read_b128 v[68:71], v37 offset:16\n\t"
        "v_lshrrev_b32 v37, 4, v111\n\t"
        "Lw_%=:\n\t"
        "ds_read_b32 v34, %[fb] offset:%[ogo]\n\t"
        "ds_read_b128 v[52:55], v36 offset:0\n\t"
        "ds_read_b128 v[56:59], v36 offset:1024\n\t"
        "ds_read_b128 v[60:63], v36 offset:2048\n\t"
        "ds_read_b128 v[64:67], v36 offset:3072\n\t"
        "s_sub_u32 %[scn], %[scn], 1\n\t"
        "s_waitcnt lgkmcnt(4)\n\t"
        "v_readfirstlane_b32 %[sf], v34\n\t"
        "s_cmp_lt_i32 %[scn], 1\n\t"
        "s_cbranch_scc1 Lg_%=\n\t"
        "s_cmp_lg_u32 %[sf], 0\n\t"
        "s_cbranch_scc1 Lg_%=\n\t"
        "s_sleep 1\n\t"
        "s_branch Lw_%=\n\t"
        "Lg_%=:\n\t"
        "ds_read_b128 v[72:75], v36 offset:4096\n\t"
        "ds_read_b128 v[76:79], v36 offset:5120\n\t"
        "ds_read_b128 v[80:83], v36 offset:6144\n\t"
        "ds_read_b128 v[84:87], v36 offset:7168\n\t"
        "s_lshr_b32 %[sf], %[sf], 8\n\t"
        "s_and_b32 %[st], %[sf], 15\n\t"
        "s_lshl_b64 %[smx], %[sm16], %[st]\n\t"
        "s_cmp_ge_u32 %[sf], 16\n\t"
        "s_cselect_b64 %[smw], %[sva], %[sv0]\n\t"
        "s_and_b64 %[smw], %[smw], %[sm16]\n\t"
        "s_lshr_b32 %[st], %[sf], 4\n\t"
        "s_cmp_eq_u32 %[st], %[sw1]\n\t"
        "s_cselect_b64 %[smx], %[smx], 0\n\t"
        "s_cmp_eq_u64 %[smw], 0\n\t"
        "s_cbranch_scc1 Le_%=\n\t"
        "s_waitcnt lgkmcnt(7)\n\t"
        "v_mfma_f32_16x16x32_bf16 v[24:27], v[112:115], v[52:55], 0\n\t"
        "v_mfma_f32_16x16x32_bf16 v[28:31], v[116:119], v[52:55], 0\n\t"
        "ds_read_b128 v[52:55], v36 offset:8192\n\t"
        "s_waitcnt lgkmcnt(7)\n\t"
        "v_mfma_f32_16x16x32_bf16 v[24:27], v[120:123], v[56:59], v[24:27]\n\t"
        "v_mfma_f32_16x16x32_bf16 v[28:31], v[124:127], v[56:59], v[28:31]\n\t"
        "ds_read_b128 v[56:59], v36 offset:9216\n\t"
        "v_mad_u32_u24 v37, v37, %[sf], v110\n\t"
        "s_waitcnt lgkmcnt(7)\n\t"
        "v_mfma_f32_16x16x32_bf16 v[24:27], v[128:131], v[60:63], v[24:27]\n\t"
        "v_mfma_f32_16x16x32_bf16 v[28:31], v[132:135], v[60:63], v[28:31]\n\t"
        "ds_read_b128 v[60:63], v36 offset:10240\n\t"
        "s_waitcnt lgkmcnt(7)\n\t"
        "v_mfma_f32_16x16x32_bf16 v[24:27], v[136:139], v[64:67], v[24:27]\n\t"
        "v_mfma_f32_16x16x32_bf16 v[28:31], v[140:143], v[64:67], v[28:31]\n\t"
        "ds_read_b128 v[64:67], v36 offset:11264\n\t"
        "s_waitcnt lgkmcnt(7)\n\t"
        "v_mfma_f32_16x16x32_bf16 v[24:27], v[144:147], v[72:75], v[24:27]\n\t"
        "v_mfma_f32_16x16x32_bf16 v[28:31], v[148:151], v[72:75], v[28:31]\n\t"
        "ds_read_b128 v[72:75], v36 offset:12288\n\t"
        "s_waitcnt lgkmcnt(7)\n\t"
        "v_mfma_f32_16x16x32_bf16 v[24:27], v[152:155], v[76:79], v[24:27]\n\t"
        "v_mfma_f32_16x16x32_bf16 v[28:31], v[156:159], v[76:79], v[28:31]\n\t"
        "ds_read_b128 v[76:79], v36 offset:13312\n\t"
        "s_waitcnt lgkmcnt(7)\n\t"
        "v_mfma_f32_16x16x32_bf16 v[24:27], v[160:163], v[80:83], v[24:27]\n\t"
        "v_mfma_f32_16x16x32_bf16 v[28:31], v[164:167], v[80:83], v[28:31]\n\t"
        "ds_read_b128 v[80:83], v36 offset:14336\n\t"
        "s_waitcnt lgkmcnt(7)\n\t"
        "v_mfma_f32_16x16x32_bf16 v[24:27], v[168:171], v[84:87], v[24:27]\n\t"
        "v_mfma_f32_16x16x32_bf16 v[28:31], v[172:175], v[84:87], v[28:31]\n\t"
        "ds_read_b128 v[84:87], v36 offset:15360\n\t"
        "s_waitcnt lgkmcnt(7)\n\t"
        "v_mfma_f32_16x16x32_bf16 v[24:27], v[176:179], v[52:55], v[24:27]\n\t"
        "v_mfma_f32_16x16x32_bf16 v[28:31], v[180:183], v[52:55], v[28:31]\n\t"
        "ds_read_b128 v[52:55], v36 offset:16384\n\t"
        "s_waitcnt lgkmcnt(7)\n\t"
        "v_mfma_f32_16x16x32_bf16 v[24:27], v[184:187], v[56:59], v[24:27]\n\t"
        "v_mfma_f32_16x16x32_bf16 v[28:31], v[188:191], v[56:59], v[28:31]\n\t"
        "ds_read_b128 v[56:59], v36 offset:17408\n\t"
        "s_waitcnt lgkmcnt(7)\n\t"
        "v_mfma_f32_16x16x32_bf16 v[24:27], v[192:195], v[60:63], v[24:27]\n\t"
        "v_mfma_f32_16x16x32_bf16 v[28:31], v[196:199], v[60:63], v[28:31]\n\t"
        "s_waitcnt lgkmcnt(6)\n\t"
        "v_mfma_f32_16x16x32_bf16 v[24:27], v[200:203], v[64:67], v[24:27]\n\t"
        "v_mfma_f32_16x16x32_bf16 v[28:31], v[204:207], v[64:67], v[28:31]\n\t"
        "s_waitcnt lgkmcnt(5)\n\t"
        "v_mfma_f32_16x16x32_bf16 v[24:27], v[208:211], v[72:75], v[24:27]\n\t"
        "v_mfma_f32_16x16x32_bf16 v[28:31], v[212:215], v[72:75], v[28:31]\n\t"
        "ds_read_b32 v34, %[fb] offset:%[ops]\n\t"
        "ds_read_b64 v[32:33], v35\n\t"
        "s_waitcnt lgkmcnt(6)\n\t"
        "v_mfma_f32_16x16x32_bf16 v[24:27], v[216:219], v[76:79], v[24:27]\n\t"
        "v_mfma_f32_16x16x32_bf16 v[28:31], v[220:223], v[76:79], v[28:31]\n\t"
        "s_waitcnt lgkmcnt(5)\n\t"
        "v_mfma_f32_16x16x32_bf16 v[24:27], v[224:227], v[80:83], v[24:27]\n\t"
        "v_mfma_f32_16x16x32_bf16 v[28:31], v[228:231], v[80:83], v[28:31]\n\t"
        "s_waitcnt lgkmcnt(4)\n\t"
        "v_mfma_f32_16x16x32_bf16 v[24:27], v[232:235], v[84:87], v[24:27]\n\t"
        "v_mfma_f32_16x16x32_bf16 v[28:31], v[236:239], v[84:87], v[28:31]\n\t"
        "s_waitcnt lgkmcnt(3)\n\t"
        "v_mfma_f32_16x16x32_bf16 v[24:27], v[240:243], v[52:55], v[24:27]\n\t"
        "v_mfma_f32_16x16x32_bf16 v[28:31], v[244:247], v[52:55], v[28:31]\n\t"
        "s_waitcnt lgkmcnt(2)\n\t"
        "v_mfma_f32_16x16x32_bf16 v[24:27], v[248:251], v[56:59], v[24:27]\n\t"
        "v_mfma_f32_16x16x32_bf16 v[28:31], v[252:255], v[56:59], v[28:31]\n\t"
        "Lm_%=:\n\t"
        "s_waitcnt lgkmcnt(0)\n\t"
        "v_readfirstlane_b32 %[st], v34\n\t"
        "s_cmp_ge_u32 %[st], %[sf]\n\t"
        "s_cbranch_scc1 Lo_%=\n\t"
        "s_cmp_lt_i32 %[scn], 1\n\t"
        "s_cbranch_scc1 Lo_%=\n\t"
        "s_sub_u32 %[scn], %[scn], 1\n\t"
        "ds_read_b32 v34, %[fb] offset:%[ops]\n\t"
        "ds_read_b64 v[32:33], v35\n\t"
        "s_branch Lm_%=\n\t"
        "Lo_%=:\n\t"
        "s_mov_b64 exec, %[smx]\n\t"
        "ds_read_b128 v[88:91], %[xr]\n\t"
        "ds_read_b128 v[92:95], %[xr] offset:64\n\t"
        "ds_read_b64 v[96:97], %[xq]\n\t"
        "s_mov_b64 exec, -1\n\t"
        "v_fma_f32 v24, -v32, v48, v24\n\t"
        "v_fma_f32 v25, -v32, v49, v25\n\t"
        "v_fma_f32 v26, -v32, v50, v26\n\t"
        "v_fma_f32 v27, -v32, v51, v27\n\t"
        "v_fma_f32 v28, -v32, v68, v28\n\t"
        "v_fma_f32 v29, -v32, v69, v29\n\t"
        "v_fma_f32 v30, -v32, v70, v30\n\t"
        "v_fma_f32 v31, -v32, v71, v31\n\t"
        "v_mul_f32 v24, v33, v24\n\t"
        "v_mul_f32 v25, v33, v25\n\t"
        "v_mul_f32 v26, v33, v26\n\t"
        "v_mul_f32 v27, v33, v27\n\t"
        "v_mul_f32 v28, v33, v28\n\t"
        "v_mul_f32 v29, v33, v29\n\t"
        "v_mul_f32 v30, v33, v30\n\t"
        "v_mul_f32 v31, v33, v31\n\t"
        "v_cvt_pk_bf16_f32 v24, v24, v28\n\t"
        "v_cvt_pk_bf16_f32 v25, v25, v29\n\t"
        "v_cvt_pk_bf16_f32 v26, v26, v30\n\t"
        "v_cvt_pk_bf16_f32 v27, v27, v31\n\t"
        "s_mov_b64 exec, %[smw]\n\t"
        "ds_write_b128 v37, v[24:27]\n\t"
        "s_mov_b64 exec, -1\n\t"
        "Le_%=:\n\t"
        "s_waitcnt lgkmcnt(0)\n\t"
        : [sf] "=&s"(sf), [st] "=&s"(st), [smw] "=&s"(mw), [smx] "=&s"(mx), [scn] "+s"(cn)
        : [fb] "v"(fb), [xr] "v"(xr), [xq] "v"(xq), [sw1] "s"(w1), [scs] "s"(cs), [sva] "s"(va), [sv0] "s"(v0), [sm16] "s"(0x0001000100010001ull), [ogo] "n"(BC_GO), [ops] "n"(BC_PST)
        : "memory", "scc", "v24", "v25", "v26", "v27", "v28", "v29", "v30", "v31", "v32", "v33", "v34", "v35", "v36", "v37");
}

DEV void att(const MidS& a) {
    u32 tm;
    asm volatile(
        "s_mov_b32 m0, %[sldx]\n\t"
        "s_nop 3\n\t"
        "s_waitcnt lgkmcnt(0)\n\t"
        "s_barrier\n\t"
        "s_setprio 0\n\t"
        "ds_read_b64 v[36:37], v102 offset:0\n\t"
        "ds_read_b32 v38, v103 offset:0\n\t"
        "ds_read_b64 v[20:21], v100 offset:0\n\t"
        "ds_read_b32 v22, v101 offset:0\n\t"
        "ds_read_b64 v[24:25], v100 offset:4864\n\t"
        "ds_read_b32 v26, v101 offset:4864\n\t"
        "ds_read_b64 v[40:41], v102 offset:40\n\t"
        "ds_read_b32 v42, v103 offset:4\n\t"
        "ds_read_b64 v[28:29], v100 offset:40\n\t"
        "ds_read_b32 v30, v101 offset:4\n\t"
        "ds_read_b64 v[32:33], v100 offset:4904\n\t"
        "ds_read_b32 v34, v101 offset:4868\n\t"
        "ds_read_b64_tr_b16 v[52:53], v105\n\t"
        "ds_read_b64_tr_b16 v[54:55], v105 offset:4864\n\t"
        "s_bitcmp1_b32 %[sskip], 0\n\t"
        "s_cbranch_scc1 Le0_%=\n\t"
        "buffer_load_dwordx4 v107, %[ssrd], %[swx] offen sc1 lds\n\t"
        "buffer_load_dwordx4 v107, %[ssrd], %[swx] offen offset:1024 sc1 lds\n\t"
        "Le0_%=:\n\t"
        "v_mov_b32 v23, 0\n\t"
        "v_mov_b32 v27, 0\n\t"
        "v_mov_b32 v31, 0\n\t"
        "v_mov_b32 v35, 0\n\t"
        "v_mov_b32 v39, 0\n\t"
        "v_mov_b32 v43, 0\n\t"
        "v_mov_b32 v60, %[sc0]\n\t"
        "v_mov_b32 v61, %[sc0]\n\t"
        "v_mov_b32 v62, %[sc0]\n\t"
        "v_mov_b32 v63, %[sc0]\n\t"
        "v_mov_b32 v68, %[sc0]\n\t"
        "v_mov_b32 v69, %[sc0]\n\t"
        "v_mov_b32 v70, %[sc0]\n\t"
        "v_mov_b32 v71, %[sc0]\n\t"
        "s_waitcnt lgkmcnt(10)\n\t"
        "v_mfma_f32_16x16x32_bf16 v[60:63], v[20:23], v[36:39], v[60:63]\n\t"
        "ds_read_b64_tr_b16 v[44:45], v104\n\t"
        "ds_read_b64_tr_b16 v[46:47], v104 offset:4864\n\t"
        "s_waitcnt lgkmcnt(10)\n\t"
        "v_mfma_f32_16x16x32_bf16 v[64:67], v[24:27], v[36:39], 0\n\t"
        "ds_read_b64_tr_b16 v[48:49], v104 offset:40\n\t"
        "ds_read_b64_tr_b16 v[50:51], v104 offset:4904\n\t"
        "s_waitcnt lgkmcnt(8)\n\t"
        "v_mfma_f32_16x16x32_bf16 v[68:71], v[28:31], v[40:43], v[68:71]\n\t"
        "s_waitcnt lgkmcnt(6)\n\t"
        "v_mfma_f32_16x16x32_bf16 v[72:75], v[32:35], v[40:43], 0\n\t"
        "s_cbranch_scc1 Le1_%=\n\t"
        "buffer_load_dwordx4 v107, %[ssrd], %[swx] offen offset:2048 sc1 lds\n\t"
        "buffer_load_dwordx4 v107, %[ssrd], %[swx] offen offset:3072 sc1 lds\n\t"
        "Le1_%=:\n\t"
        "v_mov_b32 v56, 0x0c0c0100\n\t"
        "v_mov_b32 v57, 0x01000c0c\n\t"
        "v_mov_b32 v58, 0x0c0c0c0c\n\t"
        "v_cndmask_b32 v56, v56, v57, %[spar]\n\t"
        "v_add_u32 v57, 0x0202, v56\n\t"
        "v_add_u32 v59, 0x02020000, v56\n\t"
        "v_cndmask_b32 v57, v57, v59, %[spar]\n\t"
        "v_cndmask_b32 v76, v56, v58, %[ssa]\n\t"
        "v_cndmask_b32 v78, v58, v56, %[ssa]\n\t"
        "v_cndmask_b32 v77, v57, v58, %[ssb]\n\t"
        "v_cndmask_b32 v79, v58, v57, %[ssb]\n\t"
        "v_mov_b32 v17, 0x3f803f80\n\t"
        "v_cndmask_b32 v28, v60, v61, %[spar]\n\t"
        "v_cndmask_b32 v36, v68, v69, %[spar]\n\t"
        "v_cndmask_b32 v29, v62, v63, %[spar]\n\t"
        "v_cndmask_b32 v37, v70, v71, %[spar]\n\t"
        "v_cndmask_b32 v30, v64, v65, %[spar]\n\t"
        "v_cndmask_b32 v38, v72, v73, %[spar]\n\t"
        "v_cndmask_b32 v31, v66, v67, %[spar]\n\t"
        "v_cndmask_b32 v39, v74, v75, %[spar]\n\t"
        "v_cndmask_b32 v28, v28, v30, %[ssa]\n\t"
        "v_cndmask_b32 v36, v36, v38, %[ssa]\n\t"
        "v_cndmask_b32 v29, v29, v31, %[ssb]\n\t"
        "v_cndmask_b32 v37, v37, v39, %[ssb]\n\t"
        "v_max_f32 v30, v28, v29\n\t"
        "v_max_f32 v38, v36, v37\n\t"
        "v_max_f32 v31, v28, v29\n\t"
        "v_max_f32 v39, v36, v37\n\t"
        "s_waitcnt lgkmcnt(4)\n\t"
        "v_permlane16_swap_b32 v30, v31\n\t"
        "v_permlane16_swap_b32 v38, v39\n\t"
        "v_max_f32 v32, v30, v31\n\t"
        "v_max_f32 v40, v38, v39\n\t"
        "v_max_f32 v33, v30, v31\n\t"
        "v_max_f32 v41, v38, v39\n\t"
        "v_cndmask_b32 v56, v17, v52, %[sl2]\n\t"
        "v_permlane32_swap_b32 v32, v33\n\t"
        "v_permlane32_swap_b32 v40, v41\n\t"
        "v_max_f32 v30, v32, v33\n\t"
        "v_max_f32 v38, v40, v41\n\t"
        "v_sub_f32 v28, v28, v30\n\t"
        "v_sub_f32 v36, v36, v38\n\t"
        "v_sub_f32 v29, v29, v30\n\t"
        "v_sub_f32 v37, v37, v38\n\t"
        "v_mul_f32 v28, 0x3eae1a93, v28\n\t"
        "v_mul_f32 v36, 0x3eae1a93, v36\n\t"
        "v_mul_f32 v29, 0x3eae1a93, v29\n\t"
        "v_mul_f32 v37, 0x3eae1a93, v37\n\t"
        "v_exp_f32 v28, v28\n\t"
        "v_exp_f32 v36, v36\n\t"
        "v_exp_f32 v29, v29\n\t"
        "v_exp_f32 v37, v37\n\t"
        "v_cndmask_b32 v57, v17, v53, %[sl2]\n\t"
        "v_cndmask_b32 v58, v17, v54, %[sl2]\n\t"
        "v_cndmask_b32 v59, v17, v55, %[sl2]\n\t"
        "v_cndmask_b32 v52, v52, v17, %[sl2]\n\t"
        "v_cndmask_b32 v53, v53, v17, %[sl2]\n\t"
        "v_cndmask_b32 v54, v54, v17, %[sl2]\n\t"
        "v_cndmask_b32 v55, v55, v17, %[sl2]\n\t"
        "v_cvt_pk_bf16_f32 v28, v28, v29\n\t"
        "v_cvt_pk_bf16_f32 v36, v36, v37\n\t"
        "v_perm_b32 v20, v28, v28, v76\n\t"
        "v_perm_b32 v21, v28, v28, v77\n\t"
        "v_perm_b32 v22, v28, v28, v78\n\t"
        "v_perm_b32 v23, v28, v28, v79\n\t"
        "v_perm_b32 v24, v36, v36, v76\n\t"
        "v_perm_b32 v25, v36, v36, v77\n\t"
        "v_perm_b32 v26, v36, v36, v78\n\t"
        "v_perm_b32 v27, v36, v36, v79\n\t"
        "s_waitcnt lgkmcnt(0)\n\t"
        "v_mfma_f32_16x16x32_bf16 v[64:67], v[52:55], v[20:23], 0\n\t"
        "v_mfma_f32_16x16x32_bf16 v[60:63], v[44:47], v[20:23], 0\n\t"
        "v_mfma_f32_16x16x32_bf16 v[72:75], v[56:59], v[24:27], 0\n\t"
        "v_mfma_f32_16x16x32_bf16 v[68:71], v[48:51], v[24:27], 0\n\t"
        "v_lshrrev_b32 v17, 1, v107\n\t"
        "s_nop 3\n\t"
        "v_rcp_f32 v36, v66\n\t"
        "s_nop 0\n\t"
        "v_mul_f32 v60, v60, v36\n\t"
        "v_mul_f32 v61, v61, v36\n\t"
        "v_mul_f32 v62, v62, v36\n\t"
        "v_mul_f32 v63, v63, v36\n\t"
        "v_rcp_f32 v37, v72\n\t"
        "v_cvt_pk_bf16_f32 v80, v60, v61\n\t"
        "v_cvt_pk_bf16_f32 v81, v62, v63\n\t"
        "v_mul_f32 v68, v68, v37\n\t"
        "v_mul_f32 v69, v69, v37\n\t"
        "v_mul_f32 v70, v70, v37\n\t"
        "v_mul_f32 v71, v71, v37\n\t"
        "v_bfi_b32 v80, 1, %[slay], v80\n\t"
        "v_cvt_pk_bf16_f32 v82, v68, v69\n\t"
        "v_cvt_pk_bf16_f32 v83, v70, v71\n\t"
        "v_mul_f32 v64, v64, v36\n\t"
        "v_mul_f32 v65, v65, v36\n\t"
        "v_bfi_b32 v82, 1, %[slay], v82\n\t"
        "buffer_store_dwordx4 v[80:83], v107, %[ssrd], %[sst] offen sc1\n\t"
        "v_mul_f32 v74, v74, v37\n\t"
        "v_mul_f32 v75, v75, v37\n\t"
        "v_cvt_pk_bf16_f32 v84, v64, v65\n\t"
        "v_cvt_pk_bf16_f32 v85, v74, v75\n\t"
        "v_bfi_b32 v84, 1, %[slay], v84\n\t"
        "s_mov_b64 exec, 0xffff\n\t"
        "buffer_store_dwordx2 v[84:85], v17, %[ssrd], %[stt] offen sc1\n\t"
        "s_mov_b64 exec, -1\n\t"
        "s_waitcnt vmcnt(2)\n\t"
        "s_barrier\n\t"
        "s_cmp_eq_u32 %[smsk], 0\n\t"
        "s_cbranch_scc0 Lpr_%=\n\t"
        "s_setprio 1\n\t"
        "Lpr_%=:\n\t"
        : [stm] "=&s"(tm)
        : [ssrd] "s"(a.srd), [sst] "s"(a.st), [stt] "s"(a.tt), [sc0] "s"(a.c0), [swx] "s"(a.wx), [sldx] "s"(a.ldx), [sskip] "s"(a.skip), [smsk] "s"(a.msk), [swi] "s"(a.wi), [sld] "s"(a.ld), [sn7] "s"(a.n7), [slay] "s"(a.layer),
          [spar] "s"(0xAAAAAAAAAAAAAAAAull), [ssa] "s"(0xf000ff00fff0ffffull), [ssb] "s"(0xc000fc00ffc0fffcull), [sl2] "s"(0xCCCCCCCCCCCCCCCCull)
        : "memory", "scc", "vcc", "v14", "v15", "v16", "v17", "v18", "v19", "v20", "v21", "v22", "v23", "v24", "v25", "v26", "v27", "v28", "v29", "v30", "v31", "v32", "v33", "v34", "v35", "v36", "v37", "v38", "v39", "v40", "v41", "v42", "v43", "v44", "v45", "v46", "v47");
}
DEV void mid(const MidS& a, u32& fo, u32& cn) {
    u32 ob4, ob4x;
    asm volatile(
        "s_nop 4\n\t"
        "v_mov_b32 v40, v80\n\t"
        "v_mov_b32 v41, v81\n\t"
        "v_mov_b32 v42, v82\n\t"
        "v_mov_b32 v43, v83\n\t"
        "s_bitcmp1_b32 %[sskip], 1\n\t"
        "s_cbranch_scc1 L6_%=\n\t"
        "s_mov_b32 m0, %[sld]\n\t"
        "s_add_u32 %[sob4x], %[swi], 4096\n\t"
        "buffer_load_dwordx4 v107, %[ssrd], %[swi] offen offset:0 sc1 lds\n\t"
        "buffer_load_dwordx4 v107, %[ssrd], %[swi] offen offset:1024 sc1 lds\n\t"
        "buffer_load_dwordx4 v107, %[ssrd], %[swi] offen offset:2048 sc1 lds\n\t"
        "buffer_load_dwordx4 v107, %[ssrd], %[swi] offen offset:3072 sc1 lds\n\t"
        "s_add_u32 m0, m0, 4096\n\t"
        "s_cmp_eq_u32 %[sn7], 0\n\t"
        "buffer_load_dwordx4 v107, %[ssrd], %[sob4x] offen offset:0 sc1 lds\n\t"
        "buffer_load_dwordx4 v107, %[ssrd], %[sob4x] offen offset:1024 sc1 lds\n\t"
        "s_cbranch_scc1 L6_%=\n\t"
        "buffer_load_dwordx4 v107, %[ssrd], %[sob4x] offen offset:2048 sc1 lds\n\t"
        "L6_%=:\n\t"
        "s_and_b32 %[sob4x], %[smsk], 0xffff0000\n\t"
        "s_cmp_eq_u32 %[slay], 0\n\t"
        "s_cbranch_scc0 Lod0_%=\n\t"
        "s_sleep 3\n\t"
        "Lod0_%=:\n\t"
        "s_xor_b32 %[sob4], %[stvd], 2048\n\t"
        "s_add_u32 %[sob4], %[sob4], %[sob]\n\t"
        "buffer_load_dwordx4 v[44:47], v107, %[ssrd], %[sob4] offen sc1\n\t"
        "v_lshlrev_b32 v20, 16, v88\n\t"
        "v_and_b32 v24, %[sob4x], v88\n\t"
        "v_and_b32 v20, %[smsk], v20\n\t"
        "v_lshlrev_b32 v21, 16, v89\n\t"
        "v_and_b32 v25, %[sob4x], v89\n\t"
        "v_and_b32 v21, %[smsk], v21\n\t"
        "v_lshlrev_b32 v22, 16, v90\n\t"
        "v_and_b32 v26, %[sob4x], v90\n\t"
        "v_and_b32 v22, %[smsk], v22\n\t"
        "v_lshlrev_b32 v23, 16, v91\n\t"
        "v_and_b32 v27, %[sob4x], v91\n\t"
        "v_and_b32 v23, %[smsk], v23\n\t"
        "v_lshlrev_b32 v28, 16, v92\n\t"
        "v_and_b32 v32, %[sob4x], v92\n\t"
        "v_and_b32 v28, %[smsk], v28\n\t"
        "v_lshlrev_b32 v29, 16, v93\n\t"
        "v_and_b32 v33, %[sob4x], v93\n\t"
        "v_and_b32 v29, %[smsk], v29\n\t"
        "v_lshlrev_b32 v30, 16, v94\n\t"
        "v_and_b32 v34, %[sob4x], v94\n\t"
        "v_and_b32 v30, %[smsk], v30\n\t"
        "v_lshlrev_b32 v31, 16, v95\n\t"
        "v_and_b32 v35, %[sob4x], v95\n\t"
        "v_and_b32 v31, %[smsk], v31\n\t"
        "v_lshlrev_b32 v36, 16, v96\n\t"
        "v_and_b32 v37, %[sob4x], v96\n\t"
        "v_and_b32 v36, %[smsk], v36\n\t"
        "v_lshlrev_b32 v38, 16, v97\n\t"
        "v_and_b32 v39, %[sob4x], v97\n\t"
        "v_and_b32 v38, %[smsk], v38\n\t"
        "s_and_b32 %[sob4x], %[smsk], %[slay]\n\t"
        "s_cbranch_scc0 Lu_%=\n\t"
        "v_sub_f32 v88, v20, v98\n\t"
        "v_sub_f32 v86, v24, v98\n\t"
        "v_sub_f32 v89, v21, v98\n\t"
        "v_sub_f32 v87, v25, v98\n\t"
        "v_mul_f32 v88, v99, v88\n\t"
        "v_mul_f32 v86, v99, v86\n\t"
        "v_mul_f32 v89, v99, v89\n\t"
        "v_mul_f32 v87, v99, v87\n\t"
        "v_cvt_pk_bf16_f32 v86, v88, v86\n\t"
        "v_cvt_pk_bf16_f32 v87, v89, v87\n\t"
        "v_lshlrev_b32 v88, 16, v86\n\t"
        "v_and_b32 v86, 0xffff0000, v86\n\t"
        "v_lshlrev_b32 v89, 16, v87\n\t"
        "v_and_b32 v87, 0xffff0000, v87\n\t"
        "v_add_f32 v20, v20, v88\n\t"
        "v_add_f32 v24, v24, v86\n\t"
        "v_add_f32 v21, v21, v89\n\t"
        "v_add_f32 v25, v25, v87\n\t"
        "v_cvt_pk_bf16_f32 v88, v20, v24\n\t"
        "v_cvt_pk_bf16_f32 v89, v21, v25\n\t"
        "v_lshlrev_b32 v20, 16, v88\n\t"
        "v_and_b32 v24, 0xffff0000, v88\n\t"
        "v_lshlrev_b32 v21, 16, v89\n\t"
        "v_and_b32 v25, 0xffff0000, v89\n\t"
        "v_sub_f32 v90, v22, v98\n\t"
        "v_sub_f32 v86, v26, v98\n\t"
        "v_sub_f32 v91, v23, v98\n\t"
        "v_sub_f32 v87, v27, v98\n\t"
        "v_mul_f32 v90, v99, v90\n\t"
        "v_mul_f32 v86, v99, v86\n\t"
        "v_mul_f32 v91, v99, v91\n\t"
        "v_mul_f32 v87, v99, v87\n\t"
        "v_cvt_pk_bf16_f32 v86, v90, v86\n\t"
        "v_cvt_pk_bf16_f32 v87, v91, v87\n\t"
        "v_lshlrev_b32 v90, 16, v86\n\t"
        "v_and_b32 v86, 0xffff0000, v86\n\t"
        "v_lshlrev_b32 v91, 16, v87\n\t"
        "v_and_b32 v87, 0xffff0000, v87\n\t"
        "v_add_f32 v22, v22, v90\n\t"
        "v_add_f32 v26, v26, v86\n\t"
        "v_add_f32 v23, v23, v91\n\t"
        "v_add_f32 v27, v27, v87\n\t"
        "v_cvt_pk_bf16_f32 v90, v22, v26\n\t"
        "v_cvt_pk_bf16_f32 v91, v23, v27\n\t"
        "v_lshlrev_b32 v22, 16, v90\n\t"
        "v_and_b32 v26, 0xffff0000, v90\n\t"
        "v_lshlrev_b32 v23, 16, v91\n\t"
        "v_and_b32 v27, 0xffff0000, v91\n\t"
        "v_sub_f32 v92, v28, v98\n\t"
        "v_sub_f32 v86, v32, v98\n\t"
        "v_sub_f32 v93, v29, v98\n\t"
        "v_sub_f32 v87, v33, v98\n\t"
        "v_mul_f32 v92, v99, v92\n\t"
        "v_mul_f32 v86, v99, v86\n\t"
        "v_mul_f32 v93, v99, v93\n\t"
        "v_mul_f32 v87, v99, v87\n\t"
        "v_cvt_pk_bf16_f32 v86, v92, v86\n\t"
        "v_cvt_pk_bf16_f32 v87, v93, v87\n\t"
        "v_lshlrev_b32 v92, 16, v86\n\t"
        "v_and_b32 v86, 0xffff0000, v86\n\t"
        "v_lshlrev_b32 v93, 16, v87\n\t"
        "v_and_b32 v87, 0xffff0000, v87\n\t"
        "v_add_f32 v28, v28, v92\n\t"
        "v_add_f32 v32, v32, v86\n\t"
        "v_add_f32 v29, v29, v93\n\t"
        "v_add_f32 v33, v33, v87\n\t"
        "v_cvt_pk_bf16_f32 v92, v28, v32\n\t"
        "v_cvt_pk_bf16_f32 v93, v29, v33\n\t"
        "v_lshlrev_b32 v28, 16, v92\n\t"
        "v_and_b32 v32, 0xffff0000, v92\n\t"
        "v_lshlrev_b32 v29, 16, v93\n\t"
        "v_and_b32 v33, 0xffff0000, v93\n\t"
        "v_sub_f32 v94, v30, v98\n\t"
        "v_sub_f32 v86, v34, v98\n\t"
        "v_sub_f32 v95, v31, v98\n\t"
        "v_sub_f32 v87, v35, v98\n\t"
        "v_mul_f32 v94, v99, v94\n\t"
        "v_mul_f32 v86, v99, v86\n\t"
        "v_mul_f32 v95, v99, v95\n\t"
        "v_mul_f32 v87, v99, v87\n\t"
        "v_cvt_pk_bf16_f32 v86, v94, v86\n\t"
        "v_cvt_pk_bf16_f32 v87, v95, v87\n\t"
        "v_lshlrev_b32 v94, 16, v86\n\t"
        "v_and_b32 v86, 0xffff0000, v86\n\t"
        "v_lshlrev_b32 v95, 16, v87\n\t"
        "v_and_b32 v87, 0xffff0000, v87\n\t"
        "v_add_f32 v30, v30, v94\n\t"
        "v_add_f32 v34, v34, v86\n\t"
        "v_add_f32 v31, v31, v95\n\t"
        "v_add_f32 v35, v35, v87\n\t"
        "v_cvt_pk_bf16_f32 v94, v30, v34\n\t"
        "v_cvt_pk_bf16_f32 v95, v31, v35\n\t"
        "v_lshlrev_b32 v30, 16, v94\n\t"
        "v_and_b32 v34, 0xffff0000, v94\n\t"
        "v_lshlrev_b32 v31, 16, v95\n\t"
        "v_and_b32 v35, 0xffff0000, v95\n\t"
        "v_sub_f32 v96, v36, v98\n\t"
        "v_sub_f32 v86, v37, v98\n\t"
        "v_sub_f32 v97, v38, v98\n\t"
        "v_sub_f32 v87, v39, v98\n\t"
        "v_mul_f32 v96, v99, v96\n\t"
        "v_mul_f32 v86, v99, v86\n\t"
        "v_mul_f32 v97, v99, v97\n\t"
        "v_mul_f32 v87, v99, v87\n\t"
        "v_cvt_pk_bf16_f32 v86, v96, v86\n\t"
        "v_cvt_pk_bf16_f32 v87, v97, v87\n\t"
        "v_lshlrev_b32 v96, 16, v86\n\t"
        "v_and_b32 v86, 0xffff0000, v86\n\t"
        "v_lshlrev_b32 v97, 16, v87\n\t"
        "v_and_b32 v87, 0xffff0000, v87\n\t"
        "v_add_f32 v36, v36, v96\n\t"
        "v_add_f32 v37, v37, v86\n\t"
        "v_add_f32 v38, v38, v97\n\t"
        "v_add_f32 v39, v39, v87\n\t"
        "v_cvt_pk_bf16_f32 v96, v36, v37\n\t"
        "v_cvt_pk_bf16_f32 v97, v38, v39\n\t"
        "v_lshlrev_b32 v36, 16, v96\n\t"
        "v_and_b32 v37, 0xffff0000, v96\n\t"
        "v_lshlrev_b32 v38, 16, v97\n\t"
        "v_and_b32 v39, 0xffff0000, v97\n\t"
        "Lu_%=:\n\t"
        "s_mov_b32 exec_lo, 0\n\t"
        "v_mov_b32 v36, 0\n\t"
        "v_mov_b32 v37, 0\n\t"
        "v_mov_b32 v38, 0\n\t"
        "v_mov_b32 v39, 0\n\t"
        "s_mov_b32 exec_lo, -1\n\t"
        "v_bfe_i32 v13, v107, 7, 1\n\t"
        "v_and_b32 v13, 0xaaaa, v13\n\t"
        "v_or_b32 v13, 0x4444, v13\n\t"
        "v_mov_b32 v15, %[swc]\n\t"
        "v_mov_b32 v16, 1\n\t"
        "v_add_u32 v18, %[slb], v107\n\t"
        "v_add_u32 v14, %[slb], v106\n\t"
        "ds_read_b128 v[76:79], v18 offset:0\n\t"
        "ds_read_b128 v[80:83], v18 offset:1024\n\t"
        "ds_read_b128 v[84:87], v18 offset:2048\n\t"
        "ds_read_b128 v[88:91], v18 offset:3072\n\t"
        "ds_read_b128 v[92:95], v18 offset:4608\n\t"
        "ds_read_b128 v[96:99], v18 offset:5632\n\t"
        "s_waitcnt lgkmcnt(5)\n\t"
        "v_mfma_f32_16x16x32_bf16 v[20:23], v[76:79], v[40:43], v[20:23]\n\t"
        "s_xor_b32 %[sob4], %[stvd], 4096\n\t"
        "s_add_u32 %[sob4], %[sob4], %[sob]\n\t"
        "buffer_load_dwordx4 v[48:51], v107, %[ssrd], %[sob4] offen sc1\n\t"
        "ds_read_b128 v[76:79], v18 offset:6656\n\t"
        "s_waitcnt lgkmcnt(5)\n\t"
        "v_mfma_f32_16x16x32_bf16 v[24:27], v[80:83], v[40:43], v[24:27]\n\t"
        "s_xor_b32 %[sob4], %[stvd], 6144\n\t"
        "s_add_u32 %[sob4], %[sob4], %[sob]\n\t"
        "buffer_load_dwordx4 v[52:55], v107, %[ssrd], %[sob4] offen sc1\n\t"
        "ds_read_b128 v[80:83], v18 offset:7680\n\t"
        "s_waitcnt lgkmcnt(5)\n\t"
        "v_mfma_f32_16x16x32_bf16 v[28:31], v[84:87], v[40:43], v[28:31]\n\t"
        "ds_read_b128 v[84:87], v14 offset:0\n\t"
        "s_waitcnt lgkmcnt(5)\n\t"
        "v_mfma_f32_16x16x32_bf16 v[32:35], v[88:91], v[40:43], v[32:35]\n\t"
        "s_waitcnt vmcnt(3)\n\t"
        "s_mov_b64 exec, 1\n\t"
        "ds_add_u32 v15, v16\n\t"
        "s_mov_b64 exec, -1\n\t"
        "s_waitcnt vmcnt(2)\n\t"
        "v_bitop3_b32 v17, v44, v46, %[slay] bitop3:0x7e\n\t"
        "v_and_b32 v17, 1, v17\n\t"
        "v_cmp_eq_u32 vcc, 1, v17\n\t"
        "s_cbranch_vccnz Lx1_%=\n\t"
        "Ld1_%=:\n\t"
        "ds_read_b32 v16, v15\n\t"
        "ds_read_b128 v[88:91], v18 offset:9216\n\t"
        "s_waitcnt lgkmcnt(7)\n\t"
        "v_mfma_f32_16x16x32_bf16 v[20:23], v[92:95], v[44:47], v[20:23]\n\t"
        "s_xor_b32 %[sob4], %[stvd], 8192\n\t"
        "s_add_u32 %[sob4], %[sob4], %[sob]\n\t"
        "buffer_load_dwordx4 v[56:59], v107, %[ssrd], %[sob4] offen sc1\n\t"
        "ds_read_b128 v[92:95], v18 offset:10240\n\t"
        "s_waitcnt lgkmcnt(7)\n\t"
        "v_mfma_f32_16x16x32_bf16 v[24:27], v[96:99], v[44:47], v[24:27]\n\t"
        "ds_read_b128 v[96:99], v18 offset:11264\n\t"
        "s_waitcnt lgkmcnt(7)\n\t"
        "v_mfma_f32_16x16x32_bf16 v[28:31], v[76:79], v[44:47], v[28:31]\n\t"
        "s_waitcnt vmcnt(2)\n\t"
        "v_bitop3_b32 v17, v48, v50, %[slay] bitop3:0x7e\n\t"
        "v_and_b32 v17, 1, v17\n\t"
        "v_cmp_eq_u32 vcc, 1, v17\n\t"
        "ds_read_b128 v[76:79], v18 offset:12288\n\t"
        "s_waitcnt lgkmcnt(7)\n\t"
        "v_mfma_f32_16x16x32_bf16 v[32:35], v[80:83], v[44:47], v[32:35]\n\t"
        "s_waitcnt lgkmcnt(4)\n\t"
        "Lwc_%=:\n\t"
        "v_readfirstlane_b32 %[sob4x], v16\n\t"
        "s_cmp_lt_u32 %[sob4x], %[swt]\n\t"
        "s_cbranch_scc1 Lws_%=\n\t"
        "Lwg_%=:\n\t"
        "ds_read_b128 v[80:83], v18 offset:13824\n\t"
        "v_smfmac_f32_16x16x64_bf16 v[36:39], v[84:87], v[40:47], v13\n\t"
        "s_cbranch_vccnz Lx2_%=\n\t"
        "Ld2_%=:\n\t"
        "ds_read_b128 v[84:87], v18 offset:14848\n\t"
        "ds_read_b128 v[40:43], v18 offset:15872\n\t"
        "s_waitcnt lgkmcnt(6)\n\t"
        "v_mfma_f32_16x16x32_bf16 v[20:23], v[88:91], v[48:51], v[20:23]\n\t"
        "s_xor_b32 %[sob4], %[stvd], 10240\n\t"
        "s_add_u32 %[sob4], %[sob4], %[sob]\n\t"
        "buffer_load_dwordx4 v[60:63], v107, %[ssrd], %[sob4] offen sc1\n\t"
        "ds_read_b128 v[44:47], v18 offset:16896\n\t"
        "ds_read_b128 v[88:91], v14 offset:9216\n\t"
        "s_waitcnt lgkmcnt(7)\n\t"
        "v_mfma_f32_16x16x32_bf16 v[24:27], v[92:95], v[48:51], v[24:27]\n\t"
        "s_waitcnt vmcnt(2)\n\t"
        "v_bitop3_b32 v17, v52, v54, %[slay] bitop3:0x7e\n\t"
        "v_and_b32 v17, 1, v17\n\t"
        "v_cmp_eq_u32 vcc, 1, v17\n\t"
        "ds_read_b128 v[92:95], v18 offset:18432\n\t"
        "s_waitcnt lgkmcnt(7)\n\t"
        "v_mfma_f32_16x16x32_bf16 v[28:31], v[96:99], v[48:51], v[28:31]\n\t"
        "ds_read_b128 v[96:99], v18 offset:19456\n\t"
        "s_waitcnt lgkmcnt(7)\n\t"
        "v_mfma_f32_16x16x32_bf16 v[32:35], v[76:79], v[48:51], v[32:35]\n\t"
        "s_cbranch_vccnz Lx3_%=\n\t"
        "Ld3_%=:\n\t"
        "ds_read_b128 v[76:79], v18 offset:20480\n\t"
        "s_waitcnt lgkmcnt(7)\n\t"
        "v_mfma_f32_16x16x32_bf16 v[20:23], v[80:83], v[52:55], v[20:23]\n\t"
        "s_xor_b32 %[sob4], %[stvd], 12288\n\t"
        "s_add_u32 %[sob4], %[sob4], %[sob]\n\t"
        "buffer_load_dwordx4 v[64:67], v107, %[ssrd], %[sob4] offen sc1\n\t"
        "ds_read_b128 v[80:83], v18 offset:21504\n\t"
        "s_waitcnt lgkmcnt(7)\n\t"
        "v_mfma_f32_16x16x32_bf16 v[24:27], v[84:87], v[52:55], v[24:27]\n\t"
        "v_mov_b32 v84, 0\n\t"
        "v_mov_b32 v85, 0\n\t"
        "v_mov_b32 v86, 0\n\t"
        "v_mov_b32 v87, 0\n\t"
        "s_mov_b32 exec_lo, 0\n\t"
        "ds_read_b128 v[84:87], v14 offset:13824\n\t"
        "s_mov_b32 exec_lo, -1\n\t"
        "s_waitcnt lgkmcnt(7)\n\t"
        "v_mfma_f32_16x16x32_bf16 v[28:31], v[40:43], v[52:55], v[28:31]\n\t"
        "s_waitcnt vmcnt(2)\n\t"
        "v_bitop3_b32 v17, v56, v58, %[slay] bitop3:0x7e\n\t"
        "v_and_b32 v17, 1, v17\n\t"
        "v_cmp_eq_u32 vcc, 1, v17\n\t"
        "v_add_u32 v18, %[sl1d], v18\n\t"
        "v_add_u32 v14, %[sl1d], v14\n\t"
        "ds_read_b128 v[40:43], v18 offset:0\n\t"
        "s_waitcnt lgkmcnt(7)\n\t"
        "v_mfma_f32_16x16x32_bf16 v[32:35], v[44:47], v[52:55], v[32:35]\n\t"
        "ds_read_b128 v[44:47], v18 offset:1024\n\t"
        "s_waitcnt lgkmcnt(7)\n\t"
        "v_smfmac_f32_16x16x64_bf16 v[36:39], v[88:91], v[48:55], v13\n\t"
        "s_cbranch_vccnz Lx4_%=\n\t"
        "Ld4_%=:\n\t"
        "ds_read_b128 v[88:91], v18 offset:2048\n\t"
        "ds_read_b128 v[48:51], v18 offset:3072\n\t"
        "s_waitcnt lgkmcnt(8)\n\t"
        "v_mfma_f32_16x16x32_bf16 v[20:23], v[92:95], v[56:59], v[20:23]\n\t"
        "s_xor_b32 %[sob4], %[stvd], 14336\n\t"
        "s_add_u32 %[sob4], %[sob4], %[sob]\n\t"
        "buffer_load_dwordx4 v[68:71], v107, %[ssrd], %[sob4] offen sc1\n\t"
        "ds_read_b128 v[92:95], v18 offset:4608\n\t"
        "s_waitcnt lgkmcnt(8)\n\t"
        "v_mfma_f32_16x16x32_bf16 v[24:27], v[96:99], v[56:59], v[24:27]\n\t"
        "ds_read_b128 v[96:99], v18 offset:5632\n\t"
        "s_waitcnt lgkmcnt(8)\n\t"
        "v_mfma_f32_16x16x32_bf16 v[28:31], v[76:79], v[56:59], v[28:31]\n\t"
        "s_waitcnt vmcnt(2)\n\t"
        "v_bitop3_b32 v17, v60, v62, %[slay] bitop3:0x7e\n\t"
        "v_and_b32 v17, 1, v17\n\t"
        "v_cmp_eq_u32 vcc, 1, v17\n\t"
        "ds_read_b128 v[76:79], v18 offset:6656\n\t"
        "s_waitcnt lgkmcnt(8)\n\t"
        "v_mfma_f32_16x16x32_bf16 v[32:35], v[80:83], v[56:59], v[32:35]\n\t"
        "ds_read_b128 v[80:83], v18 offset:7680\n\t"
        "s_waitcnt lgkmcnt(8)\n\t"
        "v_smfmac_f32_16x16x64_bf16 v[36:39], v[84:87], v[52:59], v13\n\t"
        "s_cbranch_vccnz Lx5_%=\n\t"
        "Ld5_%=:\n\t"
        "ds_read_b128 v[84:87], v14 offset:0\n\t"
        "s_waitcnt lgkmcnt(8)\n\t"
        "v_mfma_f32_16x16x32_bf16 v[20:23], v[40:43], v[60:63], v[20:23]\n\t"
        "buffer_load_dwordx2 v[72:73], v108, %[ssrd], %[sob] offen sc1\n\t"
        "buffer_load_dwordx2 v[74:75], v108, %[ssrd], %[sob] offen offset:128 sc1\n\t"
        "ds_read_b128 v[52:55], v18 offset:9216\n\t"
        "s_waitcnt lgkmcnt(8)\n\t"
        "v_mfma_f32_16x16x32_bf16 v[24:27], v[44:47], v[60:63], v[24:27]\n\t"
        "s_waitcnt vmcnt(3)\n\t"
        "v_bitop3_b32 v17, v64, v66, %[slay] bitop3:0x7e\n\t"
        "v_and_b32 v17, 1, v17\n\t"
        "v_cmp_eq_u32 vcc, 1, v17\n\t"
        "ds_read_b128 v[56:59], v18 offset:10240\n\t"
        "s_waitcnt lgkmcnt(8)\n\t"
        "v_mfma_f32_16x16x32_bf16 v[28:31], v[88:91], v[60:63], v[28:31]\n\t"
        "ds_read_b128 v[40:43], v18 offset:11264\n\t"
        "s_waitcnt lgkmcnt(8)\n\t"
        "v_mfma_f32_16x16x32_bf16 v[32:35], v[48:51], v[60:63], v[32:35]\n\t"
        "s_cbranch_vccnz Lx6_%=\n\t"
        "Ld6_%=:\n\t"
        "ds_read_b128 v[44:47], v18 offset:12288\n\t"
        "s_waitcnt lgkmcnt(8)\n\t"
        "v_mfma_f32_16x16x32_bf16 v[20:23], v[92:95], v[64:67], v[20:23]\n\t"
        "ds_read_b128 v[88:91], v14 offset:9216\n\t"
        "s_waitcnt lgkmcnt(8)\n\t"
        "v_mfma_f32_16x16x32_bf16 v[24:27], v[96:99], v[64:67], v[24:27]\n\t"
        "ds_read_b128 v[48:51], v18 offset:13824\n\t"
        "s_waitcnt lgkmcnt(8)\n\t"
        "v_mfma_f32_16x16x32_bf16 v[28:31], v[76:79], v[64:67], v[28:31]\n\t"
        "s_waitcnt vmcnt(2)\n\t"
        "v_bitop3_b32 v17, v68, v70, %[slay] bitop3:0x7e\n\t"
        "v_and_b32 v17, 1, v17\n\t"
        "v_cmp_eq_u32 vcc, 1, v17\n\t"
        "ds_read_b128 v[92:95], v18 offset:14848\n\t"
        "s_waitcnt lgkmcnt(8)\n\t"
        "v_mfma_f32_16x16x32_bf16 v[32:35], v[80:83], v[64:67], v[32:35]\n\t"
        "ds_read_b128 v[96:99], v18 offset:15872\n\t"
        "s_waitcnt lgkmcnt(8)\n\t"
        "v_smfmac_f32_16x16x64_bf16 v[36:39], v[84:87], v[60:67], v13\n\t"
        "s_cbranch_vccnz Lx7_%=\n\t"
        "Ld7_%=:\n\t"
        "ds_read_b128 v[76:79], v18 offset:16896\n\t"
        "s_waitcnt lgkmcnt(8)\n\t"
        "v_mfma_f32_16x16x32_bf16 v[20:23], v[52:55], v[68:71], v[20:23]\n\t"
        "buffer_load_dword %[fo], %[fo], %[ssrd], 0 offen sc1\n\t"
        "s_waitcnt lgkmcnt(7)\n\t"
        "v_mfma_f32_16x16x32_bf16 v[24:27], v[56:59], v[68:71], v[24:27]\n\t"
        "s_waitcnt vmcnt(1)\n\t"
        "v_bitop3_b32 v17, v72, v74, %[slay] bitop3:0x7e\n\t"
        "v_and_b32 v17, 1, v17\n\t"
        "v_cmp_eq_u32 vcc, 1, v17\n\t"
        "s_waitcnt lgkmcnt(6)\n\t"
        "v_mfma_f32_16x16x32_bf16 v[28:31], v[40:43], v[68:71], v[28:31]\n\t"
        "s_waitcnt lgkmcnt(5)\n\t"
        "v_mfma_f32_16x16x32_bf16 v[32:35], v[44:47], v[68:71], v[32:35]\n\t"
        "s_cbranch_vccnz Lx8_%=\n\t"
        "Ld8_%=:\n\t"
        "s_waitcnt lgkmcnt(4)\n\t"
        "v_smfmac_f32_16x16x64_bf16 v[36:39], v[88:91], v[68:75], v13\n\t"
        "s_waitcnt lgkmcnt(3)\n\t"
        "v_mfma_f32_16x16x32_bf16 v[20:23], v[48:51], v[72:75], v[20:23]\n\t"
        "v_add_u32 v18, %[sscr], v107\n\t"
        "v_mov_b32 v15, 153984\n\t"
        "s_cmp_eq_u32 %[smsk], 0\n\t"
        "s_waitcnt lgkmcnt(2)\n\t"
        "v_mfma_f32_16x16x32_bf16 v[24:27], v[92:95], v[72:75], v[24:27]\n\t"
        "v_lshrrev_b32 v14, 1, v107\n\t"
        "v_add_u32 v14, %[sscr], v14\n\t"
        "s_waitcnt lgkmcnt(1)\n\t"
        "v_mfma_f32_16x16x32_bf16 v[28:31], v[96:99], v[72:75], v[28:31]\n\t"
        "v_permlane32_swap_b32 v36, v38\n\t"
        "v_permlane32_swap_b32 v37, v39\n\t"
        "s_waitcnt lgkmcnt(0)\n\t"
        "v_mfma_f32_16x16x32_bf16 v[32:35], v[76:79], v[72:75], v[32:35]\n\t"
        "v_add_f32 v36, v36, v38\n\t"
        "v_add_f32 v37, v37, v39\n\t"
        "s_cbranch_scc0 Lk0_%=\n\t"
        "v_mov_b32 v16, 1\n\t"
        "ds_write_b64 v14, v[36:37] offset:4096\n\t"
        "ds_write_b128 v18, v[20:23] offset:0\n\t"
        "ds_write_b128 v18, v[24:27] offset:1024\n\t"
        "ds_write_b128 v18, v[28:31] offset:2048\n\t"
        "ds_write_b128 v18, v[32:35] offset:3072\n\t"
        "s_mov_b64 exec, 1\n\t"
        "ds_add_u32 v15, v16 offset:%[opc]\n\t"
        "s_mov_b64 exec, -1\n\t"
        "s_waitcnt vmcnt(0)\n\t"
        "s_branch Lend_%=\n\t"
        "Lx1_%=:\n\t"
        "s_cmp_eq_u32 %[scn], 0\n\t"
        "s_cbranch_scc1 Ld1_%=\n\t"
        "s_sub_u32 %[scn], %[scn], 1\n\t"
        "s_xor_b32 %[sob4], %[stvd], 2048\n\t"
        "s_add_u32 %[sob4], %[sob4], %[sob]\n\t"
        "buffer_load_dwordx4 v[44:47], v107, %[ssrd], %[sob4] offen sc1\n\t"
        "s_xor_b32 %[sob4], %[stvd], 4096\n\t"
        "s_add_u32 %[sob4], %[sob4], %[sob]\n\t"
        "buffer_load_dwordx4 v[48:51], v107, %[ssrd], %[sob4] offen sc1\n\t"
        "s_xor_b32 %[sob4], %[stvd], 6144\n\t"
        "s_add_u32 %[sob4], %[sob4], %[sob]\n\t"
        "buffer_load_dwordx4 v[52:55], v107, %[ssrd], %[sob4] offen sc1\n\t"
        "s_waitcnt vmcnt(2)\n\t"
        "v_bitop3_b32 v17, v44, v46, %[slay] bitop3:0x7e\n\t"
        "v_and_b32 v17, 1, v17\n\t"
        "v_cmp_eq_u32 vcc, 1, v17\n\t"
        "s_cbranch_vccnz Lx1_%=\n\t"
        "s_branch Ld1_%=\n\t"
        "Lx2_%=:\n\t"
        "s_cmp_eq_u32 %[scn], 0\n\t"
        "s_cbranch_scc1 Ld2_%=\n\t"
        "s_sub_u32 %[scn], %[scn], 1\n\t"
        "s_xor_b32 %[sob4], %[stvd], 4096\n\t"
        "s_add_u32 %[sob4], %[sob4], %[sob]\n\t"
        "buffer_load_dwordx4 v[48:51], v107, %[ssrd], %[sob4] offen sc1\n\t"
        "s_xor_b32 %[sob4], %[stvd], 6144\n\t"
        "s_add_u32 %[sob4], %[sob4], %[sob]\n\t"
        "buffer_load_dwordx4 v[52:55], v107, %[ssrd], %[sob4] offen sc1\n\t"
        "s_xor_b32 %[sob4], %[stvd], 8192\n\t"
        "s_add_u32 %[sob4], %[sob4], %[sob]\n\t"
        "buffer_load_dwordx4 v[56:59], v107, %[ssrd], %[sob4] offen sc1\n\t"
        "s_waitcnt vmcnt(2)\n\t"
        "v_bitop3_b32 v17, v48, v50, %[slay] bitop3:0x7e\n\t"
        "v_and_b32 v17, 1, v17\n\t"
        "v_cmp_eq_u32 vcc, 1, v17\n\t"
        "s_cbranch_vccnz Lx2_%=\n\t"
        "s_branch Ld2_%=\n\t"
        "Lx3_%=:\n\t"
        "s_cmp_eq_u32 %[scn], 0\n\t"
        "s_cbranch_scc1 Ld3_%=\n\t"
        "s_sub_u32 %[scn], %[scn], 1\n\t"
        "s_xor_b32 %[sob4], %[stvd], 6144\n\t"
        "s_add_u32 %[sob4], %[sob4], %[sob]\n\t"
        "buffer_load_dwordx4 v[52:55], v107, %[ssrd], %[sob4] offen sc1\n\t"
        "s_xor_b32 %[sob4], %[stvd], 8192\n\t"
        "s_add_u32 %[sob4], %[sob4], %[sob]\n\t"
        "buffer_load_dwordx4 v[56:59], v107, %[ssrd], %[sob4] offen sc1\n\t"
        "s_xor_b32 %[sob4], %[stvd], 10240\n\t"
        "s_add_u32 %[sob4], %[sob4], %[sob]\n\t"
        "buffer_load_dwordx4 v[60:63], v107, %[ssrd], %[sob4] offen sc1\n\t"
        "s_waitcnt vmcnt(2)\n\t"
        "v_bitop3_b32 v17, v52, v54, %[slay] bitop3:0x7e\n\t"
        "v_and_b32 v17, 1, v17\n\t"
        "v_cmp_eq_u32 vcc, 1, v17\n\t"
        "s_cbranch_vccnz Lx3_%=\n\t"
        "s_branch Ld3_%=\n\t"
        "Lx4_%=:\n\t"
        "s_cmp_eq_u32 %[scn], 0\n\t"
        "s_cbranch_scc1 Ld4_%=\n\t"
        "s_sub_u32 %[scn], %[scn], 1\n\t"
        "s_xor_b32 %[sob4], %[stvd], 8192\n\t"
        "s_add_u32 %[sob4], %[sob4], %[sob]\n\t"
        "buffer_load_dwordx4 v[56:59], v107, %[ssrd], %[sob4] offen sc1\n\t"
        "s_xor_b32 %[sob4], %[stvd], 10240\n\t"
        "s_add_u32 %[sob4], %[sob4], %[sob]\n\t"
        "buffer_load_dwordx4 v[60:63], v107, %[ssrd], %[sob4] offen sc1\n\t"
        "s_xor_b32 %[sob4], %[stvd], 12288\n\t"
        "s_add_u32 %[sob4], %[sob4], %[sob]\n\t"
        "buffer_load_dwordx4 v[64:67], v107, %[ssrd], %[sob4] offen sc1\n\t"
        "s_waitcnt vmcnt(2)\n\t"
        "v_bitop3_b32 v17, v56, v58, %[slay] bitop3:0x7e\n\t"
        "v_and_b32 v17, 1, v17\n\t"
        "v_cmp_eq_u32 vcc, 1, v17\n\t"
        "s_cbranch_vccnz Lx4_%=\n\t"
        "s_branch Ld4_%=\n\t"
        "Lx5_%=:\n\t"
        "s_cmp_eq_u32 %[scn], 0\n\t"
        "s_cbranch_scc1 Ld5_%=\n\t"
        "s_sub_u32 %[scn], %[scn], 1\n\t"
        "s_xor_b32 %[sob4], %[stvd], 10240\n\t"
        "s_add_u32 %[sob4], %[sob4], %[sob]\n\t"
        "buffer_load_dwordx4 v[60:63], v107, %[ssrd], %[sob4] offen sc1\n\t"
        "s_xor_b32 %[sob4], %[stvd], 12288\n\t"
        "s_add_u32 %[sob4], %[sob4], %[sob]\n\t"
        "buffer_load_dwordx4 v[64:67], v107, %[ssrd], %[sob4] offen sc1\n\t"
        "s_xor_b32 %[sob4], %[stvd], 14336\n\t"
        "s_add_u32 %[sob4], %[sob4], %[sob]\n\t"
        "buffer_load_dwordx4 v[68:71], v107, %[ssrd], %[sob4] offen sc1\n\t"
        "s_waitcnt vmcnt(2)\n\t"
        "v_bitop3_b32 v17, v60, v62, %[slay] bitop3:0x7e\n\t"
        "v_and_b32 v17, 1, v17\n\t"
        "v_cmp_eq_u32 vcc, 1, v17\n\t"
        "s_cbranch_vccnz Lx5_%=\n\t"
        "s_branch Ld5_%=\n\t"
        "Lx6_%=:\n\t"
        "s_cmp_eq_u32 %[scn], 0\n\t"
        "s_cbranch_scc1 Ld6_%=\n\t"
        "s_sub_u32 %[scn], %[scn], 1\n\t"
        "s_xor_b32 %[sob4], %[stvd], 12288\n\t"
        "s_add_u32 %[sob4], %[sob4], %[sob]\n\t"
        "buffer_load_dwordx4 v[64:67], v107, %[ssrd], %[sob4] offen sc1\n\t"
        "s_xor_b32 %[sob4], %[stvd], 14336\n\t"
        "s_add_u32 %[sob4], %[sob4], %[sob]\n\t"
        "buffer_load_dwordx4 v[68:71], v107, %[ssrd], %[sob4] offen sc1\n\t"
        "buffer_load_dwordx2 v[72:73], v108, %[ssrd], %[sob] offen sc1\n\t"
        "buffer_load_dwordx2 v[74:75], v108, %[ssrd], %[sob] offen offset:128 sc1\n\t"
        "s_waitcnt vmcnt(3)\n\t"
        "v_bitop3_b32 v17, v64, v66, %[slay] bitop3:0x7e\n\t"
        "v_and_b32 v17, 1, v17\n\t"
        "v_cmp_eq_u32 vcc, 1, v17\n\t"
        "s_cbranch_vccnz Lx6_%=\n\t"
        "s_branch Ld6_%=\n\t"
        "Lx7_%=:\n\t"
        "s_cmp_eq_u32 %[scn], 0\n\t"
        "s_cbranch_scc1 Ld7_%=\n\t"
        "s_sub_u32 %[scn], %[scn], 1\n\t"
        "s_xor_b32 %[sob4], %[stvd], 14336\n\t"
        "s_add_u32 %[sob4], %[sob4], %[sob]\n\t"
        "buffer_load_dwordx4 v[68:71], v107, %[ssrd], %[sob4] offen sc1\n\t"
        "buffer_load_dwordx2 v[72:73], v108, %[ssrd], %[sob] offen sc1\n\t"
        "buffer_load_dwordx2 v[74:75], v108, %[ssrd], %[sob] offen offset:128 sc1\n\t"
        "s_waitcnt vmcnt(2)\n\t"
        "v_bitop3_b32 v17, v68, v70, %[slay] bitop3:0x7e\n\t"
        "v_and_b32 v17, 1, v17\n\t"
        "v_cmp_eq_u32 vcc, 1, v17\n\t"
        "s_cbranch_vccnz Lx7_%=\n\t"
        "s_branch Ld7_%=\n\t"
        "Lx8_%=:\n\t"
        "s_cmp_eq_u32 %[scn], 0\n\t"
        "s_cbranch_scc1 Ld8_%=\n\t"
        "s_sub_u32 %[scn], %[scn], 1\n\t"
        "buffer_load_dwordx2 v[72:73], v108, %[ssrd], %[sob] offen sc1\n\t"
        "buffer_load_dwordx2 v[74:75], v108, %[ssrd], %[sob] offen offset:128 sc1\n\t"
        "s_waitcnt vmcnt(0)\n\t"
        "v_bitop3_b32 v17, v72, v74, %[slay] bitop3:0x7e\n\t"
        "v_and_b32 v17, 1, v17\n\t"
        "v_cmp_eq_u32 vcc, 1, v17\n\t"
        "s_cbranch_vccnz Lx8_%=\n\t"
        "s_branch Ld8_%=\n\t"
        "Lws_%=:\n\t"
        "s_cmp_eq_u32 %[scn], 0\n\t"
        "s_cbranch_scc1 Lwg_%=\n\t"
        "s_sub_u32 %[scn], %[scn], 1\n\t"
        "s_sleep 1\n\t"
        "ds_read_b32 v16, v15\n\t"
        "s_waitcnt lgkmcnt(0)\n\t"
        "s_branch Lwc_%=\n\t"
        "Lk0_%=:\n\t"
        "s_waitcnt vmcnt(0)\n\t"
        "Lp_%=:\n\t"
        "ds_read_b32 v16, v15 offset:%[opc]\n\t"
        "ds_read_b128 v[52:55], v18 offset:0\n\t"
        "ds_read_b128 v[56:59], v18 offset:1024\n\t"
        "ds_read_b128 v[60:63], v18 offset:2048\n\t"
        "ds_read_b128 v[64:67], v18 offset:3072\n\t"
        "ds_read_b64 v[68:69], v14 offset:4096\n\t"
        "v_lshrrev_b32 v19, 8, v107\n\t"
        "v_lshl_add_u32 v19, v19, 4, %[swr]\n\t"
        "v_mov_b32 v17, 163424\n\t"
        "v_cndmask_b32 v19, v17, v19, %[sl1]\n\t"
        "ds_read_b128 v[40:43], v19 offset:0\n\t"
        "ds_read_b128 v[44:47], v19 offset:64\n\t"
        "ds_read_b128 v[48:51], v19 offset:128\n\t"
        "v_mov_b32 v97, 0\n\t"
        "v_mov_b32 v98, 0\n\t"
        "v_mov_b32 v99, 0\n\t"
        "v_and_b32 v84, 0xf0, v107\n\t"
        "v_mov_b32 v83, 0\n\t"
        "s_waitcnt lgkmcnt(8)\n\t"
        "v_readfirstlane_b32 %[sob4x], v16\n\t"
        "s_cmp_ge_u32 %[sob4x], %[swt]\n\t"
        "s_cbranch_scc1 Lq_%=\n\t"
        "s_cmp_eq_u32 %[scn], 0\n\t"
        "s_cbranch_scc1 Lq_%=\n\t"
        "s_sub_u32 %[scn], %[scn], 1\n\t"
        "s_waitcnt lgkmcnt(0)\n\t"
        "s_branch Lp_%=\n\t"
        "Lq_%=:\n\t"
        "s_waitcnt lgkmcnt(6)\n\t"
        "v_add_f32 v20, v20, v52\n\t"
        "v_add_f32 v21, v21, v53\n\t"
        "v_add_f32 v22, v22, v54\n\t"
        "v_add_f32 v23, v23, v55\n\t"
        "v_add_f32 v24, v24, v56\n\t"
        "v_add_f32 v25, v25, v57\n\t"
        "v_add_f32 v26, v26, v58\n\t"
        "v_add_f32 v27, v27, v59\n\t"
        "v_cvt_pk_bf16_f32 v88, v20, v24\n\t"
        "v_cvt_pk_bf16_f32 v89, v21, v25\n\t"
        "v_cvt_pk_bf16_f32 v90, v22, v26\n\t"
        "v_cvt_pk_bf16_f32 v91, v23, v27\n\t"
        "s_waitcnt lgkmcnt(4)\n\t"
        "v_add_f32 v28, v28, v60\n\t"
        "v_add_f32 v29, v29, v61\n\t"
        "v_add_f32 v30, v30, v62\n\t"
        "v_add_f32 v31, v31, v63\n\t"
        "v_add_f32 v32, v32, v64\n\t"
        "v_add_f32 v33, v33, v65\n\t"
        "v_add_f32 v34, v34, v66\n\t"
        "v_add_f32 v35, v35, v67\n\t"
        "v_cvt_pk_bf16_f32 v92, v28, v32\n\t"
        "v_cvt_pk_bf16_f32 v93, v29, v33\n\t"
        "v_cvt_pk_bf16_f32 v94, v30, v34\n\t"
        "v_cvt_pk_bf16_f32 v95, v31, v35\n\t"
        "s_waitcnt lgkmcnt(0)\n\t"
        "v_mfma_f32_16x16x32_bf16 v[72:75], v[40:43], v[88:91], 0\n\t"
        "v_mfma_f32_16x16x32_bf16 v[76:79], v[88:91], v[88:91], 0\n\t"
        "s_waitcnt lgkmcnt(3)\n\t"
        "v_add_f32 v36, v36, v68\n\t"
        "v_add_f32 v37, v37, v69\n\t"
        "v_cvt_pk_bf16_f32 v96, v36, v37\n\t"
        "s_nop 1\n\t"
        "v_permlane32_swap_b32 v96, v97\n\t"
        "v_mfma_f32_16x16x32_bf16 v[72:75], v[44:47], v[92:95], v[72:75]\n\t"
        "v_mfma_f32_16x16x32_bf16 v[76:79], v[92:95], v[92:95], v[76:79]\n\t"
        "v_mfma_f32_16x16x32_bf16 v[72:75], v[48:51], v[96:99], v[72:75]\n\t"
        "v_mfma_f32_16x16x32_bf16 v[76:79], v[96:99], v[96:99], v[76:79]\n\t"
        "v_and_b32 v17, 0x300, v107\n\t"
        "v_lshrrev_b32 v19, 5, v17\n\t"
        "v_lshl_add_u32 v17, v17, 2, v84\n\t"
        "v_add_u32 v19, v19, v84\n\t"
        "s_add_u32 %[sob4], %[sx1], 4096\n\t"
        "s_add_u32 %[sob4x], %[sx1], 8192\n\t"
        "s_cmp_lg_u32 %[slay], 0\n\t"
        "v_min_u32 v80, -2, v72\n\t"
        "v_min_u32 v82, -2, v73\n\t"
        "v_cndmask_b32 v76, v76, v77, %[spar]\n\t"
        "v_cndmask_b32 v78, v78, v79, %[spar]\n\t"
        "v_cndmask_b32 v76, v76, v78, %[sl2]\n\t"
        "v_min_u32 v81, -2, v76\n\t"
        "s_mov_b64 exec, %[sdiag]\n\t"
        "buffer_store_dwordx4 v[80:83], v84, %[ssrd], %[sstat] offen sc1\n\t"
        "s_mov_b64 exec, -1\n\t"
        "s_cbranch_scc1 Lpk_%=\n\t"
        "v_min_u32 v88, -2, v88\n\t"
        "v_min_u32 v89, -2, v89\n\t"
        "v_min_u32 v90, -2, v90\n\t"
        "v_min_u32 v91, -2, v91\n\t"
        "buffer_store_dwordx4 v[88:91], v17, %[ssrd], %[sx1] offen sc1\n\t"
        "v_min_u32 v92, -2, v92\n\t"
        "v_min_u32 v93, -2, v93\n\t"
        "v_min_u32 v94, -2, v94\n\t"
        "v_min_u32 v95, -2, v95\n\t"
        "v_min_u32 v96, -2, v96\n\t"
        "v_min_u32 v97, -2, v97\n\t"
        "buffer_store_dwordx4 v[92:95], v17, %[ssrd], %[sob4] offen sc1\n\t"
        "s_mov_b32 exec_hi, 0\n\t"
        "buffer_store_dwordx2 v[96:97], v19, %[ssrd], %[sob4x] offen sc1\n\t"
        "s_mov_b32 exec_hi, -1\n\t"
        "Lpk_%=:\n\t"
        "v_add_u32 v18, %[spk], v107\n\t"
        "v_add_u32 v19, %[spk], v19\n\t"
        "v_mov_b32 v16, 1\n\t"
        "ds_write_b128 v18, v[88:91]\n\t"
        "ds_write_b128 v18, v[92:95] offset:1024\n\t"
        "s_mov_b32 exec_hi, 0\n\t"
        "ds_write_b64 v19, v[96:97] offset:2048\n\t"
        "s_mov_b32 exec_lo, 1\n\t"
        "ds_add_u32 v15, v16 offset:%[opk]\n\t"
        "s_mov_b64 exec, -1\n\t"
        "Lend_%=:\n\t"
        "s_setprio 0\n\t"
        : [sob4] "=&s"(ob4), [sob4x] "=&s"(ob4x), [fo] "+v"(fo), [scn] "+s"(cn)
        : [ssrd] "s"(a.srd), [sob] "s"(a.ob), [sl1d] "s"(a.l1d), [stvd] "s"(a.tvd), [swi] "s"(a.wi), [sld] "s"(a.ld),
          [smsk] "s"(a.msk), [sscr] "s"(a.scr), [sstat] "s"(a.stat), [sx1] "s"(a.x1), [slay] "s"(a.layer), [spk] "s"(a.pk), [sn7] "s"(a.n7), [sskip] "s"(a.skip), [swc] "s"(a.wc), [swt] "s"(4 * a.layer + 4), [slb] "s"(a.lb), [opc] "n"(BC_PC), [opk] "n"(BC_PK), [swr] "s"(163232),
          [spar] "s"(0xAAAAAAAAAAAAAAAAull), [sl2] "s"(0xCCCCCCCCCCCCCCCCull), [sl1] "s"(0x2222222222222222ull), [sdiag] "s"(0xf0000f0000f0000full)
        : "memory", "scc", "vcc", "v13", "v14", "v15", "v16", "v17", "v18", "v19", "v20", "v21", "v22", "v23", "v24", "v25", "v26", "v27", "v28", "v29", "v30", "v31", "v32", "v33", "v34", "v35", "v36", "v37", "v38", "v39", "v40", "v41", "v42", "v43", "v44", "v45", "v46", "v47");
}

DEV void dload(u32x4 srd, u32 a2, u32 so) {
    asm volatile(
        "s_nop 4\n\t"
        "buffer_load_dwordx4 v[112:115], v107, %[ssrd], %[so] offen offset:0" NTD "\n\t"
        "buffer_load_dwordx4 v[116:119], v107, %[ssrd], %[so] offen offset:1152" NTD "\n\t"
        "buffer_load_dwordx4 v[120:123], v107, %[ssrd], %[so] offen offset:2304" NTD "\n\t"
        "buffer_load_ushort v220, %[a2], %[ssrd], %[so] offen offset:1024" NTD "\n\t"
        "buffer_load_ushort v221, %[a2], %[ssrd], %[so] offen offset:2176" NTD "\n\t"
        "buffer_load_ushort v222, %[a2], %[ssrd], %[so] offen offset:3328" NTD "\n\t"
        "s_add_u32 %[so], %[so], 3456\n\t"
        "buffer_load_dwordx4 v[124:127], v107, %[ssrd], %[so] offen offset:0" NTD "\n\t"
        "buffer_load_dwordx4 v[128:131], v107, %[ssrd], %[so] offen offset:1152" NTD "\n\t"
        "buffer_load_dwordx4 v[132:135], v107, %[ssrd], %[so] offen offset:2304" NTD "\n\t"
        "buffer_load_ushort v223, %[a2], %[ssrd], %[so] offen offset:1024" NTD "\n\t"
        "buffer_load_ushort v224, %[a2], %[ssrd], %[so] offen offset:2176" NTD "\n\t"
        "buffer_load_ushort v225, %[a2], %[ssrd], %[so] offen offset:3328" NTD "\n\t"
        "s_add_u32 %[so], %[so], 3456\n\t"
        "buffer_load_dwordx4 v[136:139], v107, %[ssrd], %[so] offen offset:0" NTD "\n\t"
        "buffer_load_dwordx4 v[140:143], v107, %[ssrd], %[so] offen offset:1152" NTD "\n\t"
        "buffer_load_dwordx4 v[144:147], v107, %[ssrd], %[so] offen offset:2304" NTD "\n\t"
        "buffer_load_ushort v226, %[a2], %[ssrd], %[so] offen offset:1024" NTD "\n\t"
        "buffer_load_ushort v227, %[a2], %[ssrd], %[so] offen offset:2176" NTD "\n\t"
        "buffer_load_ushort v228, %[a2], %[ssrd], %[so] offen offset:3328" NTD "\n\t"
        "s_add_u32 %[so], %[so], 3456\n\t"
        "buffer_load_dwordx4 v[148:151], v107, %[ssrd], %[so] offen offset:0" NTD "\n\t"
        "buffer_load_dwordx4 v[152:155], v107, %[ssrd], %[so] offen offset:1152" NTD "\n\t"
        "buffer_load_dwordx4 v[156:159], v107, %[ssrd], %[so] offen offset:2304" NTD "\n\t"
        "buffer_load_ushort v229, %[a2], %[ssrd], %[so] offen offset:1024" NTD "\n\t"
        "buffer_load_ushort v230, %[a2], %[ssrd], %[so] offen offset:2176" NTD "\n\t"
        "buffer_load_ushort v231, %[a2], %[ssrd], %[so] offen offset:3328" NTD "\n\t"
        "s_add_u32 %[so], %[so], 3456\n\t"
        "buffer_load_dwordx4 v[160:163], v107, %[ssrd], %[so] offen offset:0" NTD "\n\t"
        "buffer_load_dwordx4 v[164:167], v107, %[ssrd], %[so] offen offset:1152" NTD "\n\t"
        "buffer_load_dwordx4 v[168:171], v107, %[ssrd], %[so] offen offset:2304" NTD "\n\t"
        "buffer_load_ushort v232, %[a2], %[ssrd], %[so] offen offset:1024" NTD "\n\t"
        "buffer_load_ushort v233, %[a2], %[ssrd], %[so] offen offset:2176" NTD "\n\t"
        "buffer_load_ushort v234, %[a2], %[ssrd], %[so] offen offset:3328" NTD "\n\t"
        "s_add_u32 %[so], %[so], 3456\n\t"
        "buffer_load_dwordx4 v[172:175], v107, %[ssrd], %[so] offen offset:0" NTD "\n\t"
        "buffer_load_dwordx4 v[176:179], v107, %[ssrd], %[so] offen offset:1152" NTD "\n\t"
        "buffer_load_dwordx4 v[180:183], v107, %[ssrd], %[so] offen offset:2304" NTD "\n\t"
        "buffer_load_ushort v235, %[a2], %[ssrd], %[so] offen offset:1024" NTD "\n\t"
        "buffer_load_ushort v236, %[a2], %[ssrd], %[so] offen offset:2176" NTD "\n\t"
        "buffer_load_ushort v237, %[a2], %[ssrd], %[so] offen offset:3328" NTD "\n\t"
        "s_add_u32 %[so], %[so], 3456\n\t"
        "buffer_load_dwordx4 v[184:187], v107, %[ssrd], %[so] offen offset:0" NTD "\n\t"
        "buffer_load_dwordx4 v[188:191], v107, %[ssrd], %[so] offen offset:1152" NTD "\n\t"
        "buffer_load_dwordx4 v[192:195], v107, %[ssrd], %[so] offen offset:2304" NTD "\n\t"
        "buffer_load_ushort v238, %[a2], %[ssrd], %[so] offen offset:1024" NTD "\n\t"
        "buffer_load_ushort v239, %[a2], %[ssrd], %[so] offen offset:2176" NTD "\n\t"
        "buffer_load_ushort v240, %[a2], %[ssrd], %[so] offen offset:3328" NTD "\n\t"
        "s_add_u32 %[so], %[so], 3456\n\t"
        "buffer_load_dwordx4 v[196:199], v107, %[ssrd], %[so] offen offset:0" NTD "\n\t"
        "buffer_load_dwordx4 v[200:203], v107, %[ssrd], %[so] offen offset:1152" NTD "\n\t"
        "buffer_load_dwordx4 v[204:207], v107, %[ssrd], %[so] offen offset:2304" NTD "\n\t"
        "buffer_load_ushort v241, %[a2], %[ssrd], %[so] offen offset:1024" NTD "\n\t"
        "buffer_load_ushort v242, %[a2], %[ssrd], %[so] offen offset:2176" NTD "\n\t"
        "buffer_load_ushort v243, %[a2], %[ssrd], %[so] offen offset:3328" NTD "\n\t"
        "s_add_u32 %[so], %[so], 3456\n\t"
        : [so] "+s"(so) : [ssrd] "s"(srd), [a2] "v"(a2) : "memory", "scc");
}

DEV void dpermx() {
    asm volatile(
        "v_perm_b32 v208, v221, v220, %[slo]\n\t"
        "v_perm_b32 v209, v223, v222, %[slo]\n\t"
        "v_perm_b32 v210, v225, v224, %[slo]\n\t"
        "v_perm_b32 v211, v227, v226, %[slo]\n\t"
        "v_perm_b32 v212, v229, v228, %[slo]\n\t"
        "v_perm_b32 v213, v231, v230, %[slo]\n\t"
        "v_perm_b32 v214, v233, v232, %[slo]\n\t"
        "v_perm_b32 v215, v235, v234, %[slo]\n\t"
        "v_perm_b32 v216, v237, v236, %[slo]\n\t"
        "v_perm_b32 v217, v239, v238, %[slo]\n\t"
        "v_perm_b32 v218, v241, v240, %[slo]\n\t"
        "v_perm_b32 v219, v243, v242, %[slo]\n\t"
        :: [slo] "s"(0x05040100u) : "memory");
}
DEV void dperm() {
    u32 t;
    asm volatile(
        "v_perm_b32 %[t], v116, v112, %[slo]\n\t"
        "v_perm_b32 v116, v116, v112, %[shi]\n\t"
        "v_mov_b32 v112, %[t]\n\t"
        "v_perm_b32 %[t], v117, v113, %[slo]\n\t"
        "v_perm_b32 v117, v117, v113, %[shi]\n\t"
        "v_mov_b32 v113, %[t]\n\t"
        "v_perm_b32 %[t], v118, v114, %[slo]\n\t"
        "v_perm_b32 v118, v118, v114, %[shi]\n\t"
        "v_mov_b32 v114, %[t]\n\t"
        "v_perm_b32 %[t], v119, v115, %[slo]\n\t"
        "v_perm_b32 v119, v119, v115, %[shi]\n\t"
        "v_mov_b32 v115, %[t]\n\t"
        "v_perm_b32 %[t], v124, v120, %[slo]\n\t"
        "v_perm_b32 v124, v124, v120, %[shi]\n\t"
        "v_mov_b32 v120, %[t]\n\t"
        "v_perm_b32 %[t], v125, v121, %[slo]\n\t"
        "v_perm_b32 v125, v125, v121, %[shi]\n\t"
        "v_mov_b32 v121, %[t]\n\t"
        "v_perm_b32 %[t], v126, v122, %[slo]\n\t"
        "v_perm_b32 v126, v126, v122, %[shi]\n\t"
        "v_mov_b32 v122, %[t]\n\t"
        "v_perm_b32 %[t], v127, v123, %[slo]\n\t"
        "v_perm_b32 v127, v127, v123, %[shi]\n\t"
        "v_mov_b32 v123, %[t]\n\t"
        "v_perm_b32 %[t], v132, v128, %[slo]\n\t"
        "v_perm_b32 v132, v132, v128, %[shi]\n\t"
        "v_mov_b32 v128, %[t]\n\t"
        "v_perm_b32 %[t], v133, v129, %[slo]\n\t"
        "v_perm_b32 v133, v133, v129, %[shi]\n\t"
        "v_mov_b32 v129, %[t]\n\t"
        "v_perm_b32 %[t], v134, v130, %[slo]\n\t"
        "v_perm_b32 v134, v134, v130, %[shi]\n\t"
        "v_mov_b32 v130, %[t]\n\t"
        "v_perm_b32 %[t], v135, v131, %[slo]\n\t"
        "v_perm_b32 v135, v135, v131, %[shi]\n\t"
        "v_mov_b32 v131, %[t]\n\t"
        "v_perm_b32 %[t], v140, v136, %[slo]\n\t"
        "v_perm_b32 v140, v140, v136, %[shi]\n\t"
        "v_mov_b32 v136, %[t]\n\t"
        "v_perm_b32 %[t], v141, v137, %[slo]\n\t"
        "v_perm_b32 v141, v141, v137, %[shi]\n\t"
        "v_mov_b32 v137, %[t]\n\t"
        "v_perm_b32 %[t], v142, v138, %[slo]\n\t"
        "v_perm_b32 v142, v142, v138, %[shi]\n\t"
        "v_mov_b32 v138, %[t]\n\t"
        "v_perm_b32 %[t], v143, v139, %[slo]\n\t"
        "v_perm_b32 v143, v143, v139, %[shi]\n\t"
        "v_mov_b32 v139, %[t]\n\t"
        "v_perm_b32 %[t], v148, v144, %[slo]\n\t"
        "v_perm_b32 v148, v148, v144, %[shi]\n\t"
        "v_mov_b32 v144, %[t]\n\t"
        "v_perm_b32 %[t], v149, v145, %[slo]\n\t"
        "v_perm_b32 v149, v149, v145, %[shi]\n\t"
        "v_mov_b32 v145, %[t]\n\t"
        "v_perm_b32 %[t], v150, v146, %[slo]\n\t"
        "v_perm_b32 v150, v150, v146, %[shi]\n\t"
        "v_mov_b32 v146, %[t]\n\t"
        "v_perm_b32 %[t], v151, v147, %[slo]\n\t"
        "v_perm_b32 v151, v151, v147, %[shi]\n\t"
        "v_mov_b32 v147, %[t]\n\t"
        "v_perm_b32 %[t], v156, v152, %[slo]\n\t"
        "v_perm_b32 v156, v156, v152, %[shi]\n\t"
        "v_mov_b32 v152, %[t]\n\t"
        "v_perm_b32 %[t], v157, v153, %[slo]\n\t"
        "v_perm_b32 v157, v157, v153, %[shi]\n\t"
        "v_mov_b32 v153, %[t]\n\t"
        "v_perm_b32 %[t], v158, v154, %[slo]\n\t"
        "v_perm_b32 v158, v158, v154, %[shi]\n\t"
        "v_mov_b32 v154, %[t]\n\t"
        "v_perm_b32 %[t], v159, v155, %[slo]\n\t"
        "v_perm_b32 v159, v159, v155, %[shi]\n\t"
        "v_mov_b32 v155, %[t]\n\t"
        "v_perm_b32 %[t], v164, v160, %[slo]\n\t"
        "v_perm_b32 v164, v164, v160, %[shi]\n\t"
        "v_mov_b32 v160, %[t]\n\t"
        "v_perm_b32 %[t], v165, v161, %[slo]\n\t"
        "v_perm_b32 v165, v165, v161, %[shi]\n\t"
        "v_mov_b32 v161, %[t]\n\t"
        "v_perm_b32 %[t], v166, v162, %[slo]\n\t"
        "v_perm_b32 v166, v166, v162, %[shi]\n\t"
        "v_mov_b32 v162, %[t]\n\t"
        "v_perm_b32 %[t], v167, v163, %[slo]\n\t"
        "v_perm_b32 v167, v167, v163, %[shi]\n\t"
        "v_mov_b32 v163, %[t]\n\t"
        "v_perm_b32 %[t], v172, v168, %[slo]\n\t"
        "v_perm_b32 v172, v172, v168, %[shi]\n\t"
        "v_mov_b32 v168, %[t]\n\t"
        "v_perm_b32 %[t], v173, v169, %[slo]\n\t"
        "v_perm_b32 v173, v173, v169, %[shi]\n\t"
        "v_mov_b32 v169, %[t]\n\t"
        "v_perm_b32 %[t], v174, v170, %[slo]\n\t"
        "v_perm_b32 v174, v174, v170, %[shi]\n\t"
        "v_mov_b32 v170, %[t]\n\t"
        "v_perm_b32 %[t], v175, v171, %[slo]\n\t"
        "v_perm_b32 v175, v175, v171, %[shi]\n\t"
        "v_mov_b32 v171, %[t]\n\t"
        "v_perm_b32 %[t], v180, v176, %[slo]\n\t"
        "v_perm_b32 v180, v180, v176, %[shi]\n\t"
        "v_mov_b32 v176, %[t]\n\t"
        "v_perm_b32 %[t], v181, v177, %[slo]\n\t"
        "v_perm_b32 v181, v181, v177, %[shi]\n\t"
        "v_mov_b32 v177, %[t]\n\t"
        "v_perm_b32 %[t], v182, v178, %[slo]\n\t"
        "v_perm_b32 v182, v182, v178, %[shi]\n\t"
        "v_mov_b32 v178, %[t]\n\t"
        "v_perm_b32 %[t], v183, v179, %[slo]\n\t"
        "v_perm_b32 v183, v183, v179, %[shi]\n\t"
        "v_mov_b32 v179, %[t]\n\t"
        "v_perm_b32 %[t], v188, v184, %[slo]\n\t"
        "v_perm_b32 v188, v188, v184, %[shi]\n\t"
        "v_mov_b32 v184, %[t]\n\t"
        "v_perm_b32 %[t], v189, v185, %[slo]\n\t"
        "v_perm_b32 v189, v189, v185, %[shi]\n\t"
        "v_mov_b32 v185, %[t]\n\t"
        "v_perm_b32 %[t], v190, v186, %[slo]\n\t"
        "v_perm_b32 v190, v190, v186, %[shi]\n\t"
        "v_mov_b32 v186, %[t]\n\t"
        "v_perm_b32 %[t], v191, v187, %[slo]\n\t"
        "v_perm_b32 v191, v191, v187, %[shi]\n\t"
        "v_mov_b32 v187, %[t]\n\t"
        "v_perm_b32 %[t], v196, v192, %[slo]\n\t"
        "v_perm_b32 v196, v196, v192, %[shi]\n\t"
        "v_mov_b32 v192, %[t]\n\t"
        "v_perm_b32 %[t], v197, v193, %[slo]\n\t"
        "v_perm_b32 v197, v197, v193, %[shi]\n\t"
        "v_mov_b32 v193, %[t]\n\t"
        "v_perm_b32 %[t], v198, v194, %[slo]\n\t"
        "v_perm_b32 v198, v198, v194, %[shi]\n\t"
        "v_mov_b32 v194, %[t]\n\t"
        "v_perm_b32 %[t], v199, v195, %[slo]\n\t"
        "v_perm_b32 v199, v199, v195, %[shi]\n\t"
        "v_mov_b32 v195, %[t]\n\t"
        "v_perm_b32 %[t], v204, v200, %[slo]\n\t"
        "v_perm_b32 v204, v204, v200, %[shi]\n\t"
        "v_mov_b32 v200, %[t]\n\t"
        "v_perm_b32 %[t], v205, v201, %[slo]\n\t"
        "v_perm_b32 v205, v205, v201, %[shi]\n\t"
        "v_mov_b32 v201, %[t]\n\t"
        "v_perm_b32 %[t], v206, v202, %[slo]\n\t"
        "v_perm_b32 v206, v206, v202, %[shi]\n\t"
        "v_mov_b32 v202, %[t]\n\t"
        "v_perm_b32 %[t], v207, v203, %[slo]\n\t"
        "v_perm_b32 v207, v207, v203, %[shi]\n\t"
        "v_mov_b32 v203, %[t]\n\t"
        : [t] "=&v"(t) : [slo] "s"(0x05040100u), [shi] "s"(0x07060302u) : "memory");
}

DEV void xstats(int xa, float (&sx)[2], float (&sq)[2]) {
    int xb; u32 sc;
    asm volatile(
        "v_mov_b32 v24, 0x3f803f80\n\t"
        "v_mov_b32 v25, 0x3f803f80\n\t"
        "v_mov_b32 v26, 0x3f803f80\n\t"
        "v_mov_b32 v27, 0x3f803f80\n\t"
        "v_mov_b32 v48, 0\n\t"
        "v_mov_b32 v49, 0\n\t"
        "v_mov_b32 v50, 0\n\t"
        "v_mov_b32 v51, 0\n\t"
        "v_mov_b32 v52, 0\n\t"
        "v_mov_b32 v53, 0\n\t"
        "v_mov_b32 v54, 0\n\t"
        "v_mov_b32 v55, 0\n\t"
        "v_mov_b32 v56, 0\n\t"
        "v_mov_b32 v57, 0\n\t"
        "v_mov_b32 v58, 0\n\t"
        "v_mov_b32 v59, 0\n\t"
        "v_mov_b32 v60, 0\n\t"
        "v_mov_b32 v61, 0\n\t"
        "v_mov_b32 v62, 0\n\t"
        "v_mov_b32 v63, 0\n\t"
        "v_xor_b32 %[xb], 64, %[xa]\n\t"
        "s_mov_b32 %[sc], 3\n\t"
        "Ll_%=:\n\t"
        "ds_read_b128 v[208:211], %[xa] offset:0\n\t"
        "ds_read_b128 v[212:215], %[xa] offset:6144\n\t"
        "ds_read_b128 v[216:219], %[xb] offset:0\n\t"
        "ds_read_b128 v[220:223], %[xb] offset:6144\n\t"
        "ds_read_b128 v[224:227], %[xa] offset:10240\n\t"
        "ds_read_b128 v[228:231], %[xa] offset:16384\n\t"
        "ds_read_b128 v[232:235], %[xb] offset:10240\n\t"
        "ds_read_b128 v[236:239], %[xb] offset:16384\n\t"
        "ds_read_b128 v[240:243], %[xa] offset:20480\n\t"
        "ds_read_b128 v[244:247], %[xa] offset:26624\n\t"
        "ds_read_b128 v[248:251], %[xb] offset:20480\n\t"
        "ds_read_b128 v[252:255], %[xb] offset:26624\n\t"
        "v_add_u32 %[xa], 30720, %[xa]\n\t"
        "v_add_u32 %[xb], 30720, %[xb]\n\t"
        "s_waitcnt lgkmcnt(11)\n\t"
        "v_mfma_f32_16x16x32_bf16 v[48:51], v[24:27], v[208:211], v[48:51]\n\t"
        "v_mfma_f32_16x16x32_bf16 v[56:59], v[208:211], v[208:211], v[56:59]\n\t"
        "s_waitcnt lgkmcnt(10)\n\t"
        "v_mfma_f32_16x16x32_bf16 v[52:55], v[24:27], v[212:215], v[52:55]\n\t"
        "v_mfma_f32_16x16x32_bf16 v[60:63], v[212:215], v[212:215], v[60:63]\n\t"
        "s_waitcnt lgkmcnt(9)\n\t"
        "v_mfma_f32_16x16x32_bf16 v[48:51], v[24:27], v[216:219], v[48:51]\n\t"
        "v_mfma_f32_16x16x32_bf16 v[56:59], v[216:219], v[216:219], v[56:59]\n\t"
        "s_waitcnt lgkmcnt(8)\n\t"
        "v_mfma_f32_16x16x32_bf16 v[52:55], v[24:27], v[220:223], v[52:55]\n\t"
        "v_mfma_f32_16x16x32_bf16 v[60:63], v[220:223], v[220:223], v[60:63]\n\t"
        "s_waitcnt lgkmcnt(7)\n\t"
        "v_mfma_f32_16x16x32_bf16 v[48:51], v[24:27], v[224:227], v[48:51]\n\t"
        "v_mfma_f32_16x16x32_bf16 v[56:59], v[224:227], v[224:227], v[56:59]\n\t"
        "s_waitcnt lgkmcnt(6)\n\t"
        "v_mfma_f32_16x16x32_bf16 v[52:55], v[24:27], v[228:231], v[52:55]\n\t"
        "v_mfma_f32_16x16x32_bf16 v[60:63], v[228:231], v[228:231], v[60:63]\n\t"
        "s_waitcnt lgkmcnt(5)\n\t"
        "v_mfma_f32_16x16x32_bf16 v[48:51], v[24:27], v[232:235], v[48:51]\n\t"
        "v_mfma_f32_16x16x32_bf16 v[56:59], v[232:235], v[232:235], v[56:59]\n\t"
        "s_waitcnt lgkmcnt(4)\n\t"
        "v_mfma_f32_16x16x32_bf16 v[52:55], v[24:27], v[236:239], v[52:55]\n\t"
        "v_mfma_f32_16x16x32_bf16 v[60:63], v[236:239], v[236:239], v[60:63]\n\t"
        "s_waitcnt lgkmcnt(3)\n\t"
        "v_mfma_f32_16x16x32_bf16 v[48:51], v[24:27], v[240:243], v[48:51]\n\t"
        "v_mfma_f32_16x16x32_bf16 v[56:59], v[240:243], v[240:243], v[56:59]\n\t"
        "s_waitcnt lgkmcnt(2)\n\t"
        "v_mfma_f32_16x16x32_bf16 v[52:55], v[24:27], v[244:247], v[52:55]\n\t"
        "v_mfma_f32_16x16x32_bf16 v[60:63], v[244:247], v[244:247], v[60:63]\n\t"
        "s_waitcnt lgkmcnt(1)\n\t"
        "v_mfma_f32_16x16x32_bf16 v[48:51], v[24:27], v[248:251], v[48:51]\n\t"
        "v_mfma_f32_16x16x32_bf16 v[56:59], v[248:251], v[248:251], v[56:59]\n\t"
        "s_waitcnt lgkmcnt(0)\n\t"
        "v_mfma_f32_16x16x32_bf16 v[52:55], v[24:27], v[252:255], v[52:55]\n\t"
        "v_mfma_f32_16x16x32_bf16 v[60:63], v[252:255], v[252:255], v[60:63]\n\t"
        "s_sub_u32 %[sc], %[sc], 1\n\t"
        "s_cmp_lg_u32 %[sc], 0\n\t"
        "s_cbranch_scc1 Ll_%=\n\t"
        "s_nop 7\n\t"
        "v_mov_b32 %[x0], v48\n\t"
        "v_cndmask_b32 v56, v56, v57, %[spar]\n\t"
        "v_cndmask_b32 v58, v58, v59, %[spar]\n\t"
        "v_cndmask_b32 %[q0], v56, v58, %[sl2]\n\t"
        "v_mov_b32 %[x1], v52\n\t"
        "v_cndmask_b32 v60, v60, v61, %[spar]\n\t"
        "v_cndmask_b32 v62, v62, v63, %[spar]\n\t"
        "v_cndmask_b32 %[q1], v60, v62, %[sl2]\n\t"
        : [xa] "+v"(xa), [xb] "=&v"(xb), [sc] "=&s"(sc), [x0] "=&v"(sx[0]), [q0] "=&v"(sq[0]), [x1] "=&v"(sx[1]), [q1] "=&v"(sq[1])
        : [spar] "s"(0xAAAAAAAAAAAAAAAAull), [sl2] "s"(0xCCCCCCCCCCCCCCCCull)
        : "memory", "scc", "v24", "v25", "v26", "v27");
}

DEV void xgather(u32x4 srd, u32 xo, u32 xp, u32 xq, int th, int hh, u32 ld, u32 lh, u32& cn) {
    u32 cr, sa, sb, g0, g1, g2;
    asm volatile(
        "s_nop 4\n\t"
        "s_sleep 5\n\t"
        "s_add_u32 m0, %[sld], 0\n\t"
        "s_add_u32 %[sb], %[sxo], m0\n\t"
        "s_sub_u32 %[sa], %[sb], 8192\n\t"
        "s_cmp_gt_i32 %[sth], 0\n\t"
        "s_cselect_b32 %[scr], %[sa], %[sb]\n\t"
        "buffer_load_dwordx4 v107, %[ssrd], %[scr] offen offset:0 sc1 lds\n\t"
        "s_cmp_gt_i32 %[sth], 1\n\t"
        "s_cselect_b32 %[scr], %[sa], %[sb]\n\t"
        "buffer_load_dwordx4 v107, %[ssrd], %[scr] offen offset:1024 sc1 lds\n\t"
        "s_cmp_gt_i32 %[sth], 2\n\t"
        "s_cselect_b32 %[scr], %[sa], %[sb]\n\t"
        "buffer_load_dwordx4 v107, %[ssrd], %[scr] offen offset:2048 sc1 lds\n\t"
        "s_cmp_gt_i32 %[sth], 3\n\t"
        "s_cselect_b32 %[scr], %[sa], %[sb]\n\t"
        "buffer_load_dwordx4 v107, %[ssrd], %[scr] offen offset:3072 sc1 lds\n\t"
        "s_add_u32 m0, %[sld], 4096\n\t"
        "s_add_u32 %[sb], %[sxo], m0\n\t"
        "s_sub_u32 %[sa], %[sb], 8192\n\t"
        "s_cmp_gt_i32 %[sth], 4\n\t"
        "s_cselect_b32 %[scr], %[sa], %[sb]\n\t"
        "buffer_load_dwordx4 v107, %[ssrd], %[scr] offen offset:0 sc1 lds\n\t"
        "s_cmp_gt_i32 %[sth], 5\n\t"
        "s_cselect_b32 %[scr], %[sa], %[sb]\n\t"
        "buffer_load_dwordx4 v107, %[ssrd], %[scr] offen offset:1024 sc1 lds\n\t"
        "s_cmp_gt_i32 %[sth], 6\n\t"
        "s_cselect_b32 %[scr], %[sa], %[sb]\n\t"
        "buffer_load_dwordx4 v107, %[ssrd], %[scr] offen offset:2048 sc1 lds\n\t"
        "s_cmp_gt_i32 %[sth], 7\n\t"
        "s_cselect_b32 %[scr], %[sa], %[sb]\n\t"
        "buffer_load_dwordx4 v107, %[ssrd], %[scr] offen offset:3072 sc1 lds\n\t"
        "v_and_b32 %[g0], 0x300, v107\n\t"
        "v_and_b32 %[g1], 0xf0, v107\n\t"
        "v_lshrrev_b32 v96, 8, v107\n\t"
        "v_lshl_add_u32 %[g0], %[g0], 2, %[g1]\n\t"
        "v_add_u32 %[g0], 768, %[g0]\n\t"
        "v_sub_u32 v96, %[shh], v96\n\t"
        "v_add_u32 v97, 0x2000, %[g0]\n\t"
        "v_add_u32 %[g1], %[sld], v107\n\t"
        "v_mov_b32 %[g2], 153984\n\t"
        "s_sleep 4\n\t"
        "s_add_u32 m0, %[sld], 16384\n\t"
        "s_add_u32 %[sb], %[sxo], m0\n\t"
        "s_sub_u32 %[sa], %[sb], 8192\n\t"
        "s_cmp_gt_i32 %[sth], 16\n\t"
        "s_cselect_b32 %[scr], %[sa], %[sb]\n\t"
        "buffer_load_dwordx4 v107, %[ssrd], %[scr] offen offset:0 sc1 lds\n\t"
        "s_cmp_gt_i32 %[sth], 17\n\t"
        "s_cselect_b32 %[scr], %[sa], %[sb]\n\t"
        "buffer_load_dwordx4 v107, %[ssrd], %[scr] offen offset:1024 sc1 lds\n\t"
        "s_cmp_gt_i32 %[sth], 18\n\t"
        "s_cselect_b32 %[scr], %[sa], %[sb]\n\t"
        "buffer_load_dwordx4 v107, %[ssrd], %[scr] offen offset:2048 sc1 lds\n\t"
        "s_cmp_gt_i32 %[sth], 19\n\t"
        "s_cselect_b32 %[scr], %[sa], %[sb]\n\t"
        "buffer_load_dwordx4 v107, %[ssrd], %[scr] offen offset:3072 sc1 lds\n\t"
        "s_add_u32 m0, %[sld], 20480\n\t"
        "s_add_u32 %[sb], %[sxo], m0\n\t"
        "s_sub_u32 %[sa], %[sb], 8192\n\t"
        "s_cmp_gt_i32 %[sth], 20\n\t"
        "s_cselect_b32 %[scr], %[sa], %[sb]\n\t"
        "buffer_load_dwordx4 v107, %[ssrd], %[scr] offen offset:0 sc1 lds\n\t"
        "s_cmp_gt_i32 %[sth], 21\n\t"
        "s_cselect_b32 %[scr], %[sa], %[sb]\n\t"
        "buffer_load_dwordx4 v107, %[ssrd], %[scr] offen offset:1024 sc1 lds\n\t"
        "s_cmp_gt_i32 %[sth], 22\n\t"
        "s_cselect_b32 %[scr], %[sa], %[sb]\n\t"
        "buffer_load_dwordx4 v107, %[ssrd], %[scr] offen offset:2048 sc1 lds\n\t"
        "s_cmp_gt_i32 %[sth], 23\n\t"
        "s_cselect_b32 %[scr], %[sa], %[sb]\n\t"
        "buffer_load_dwordx4 v107, %[ssrd], %[scr] offen offset:3072 sc1 lds\n\t"
        "s_waitcnt vmcnt(8)\n\t"
        "ds_read_b128 v[24:27], %[g1] offset:0\n\t"
        "ds_read_b128 v[28:31], %[g1] offset:1024\n\t"
        "ds_read_b128 v[32:35], %[g1] offset:2048\n\t"
        "ds_read_b128 v[36:39], %[g1] offset:3072\n\t"
        "ds_read_b128 v[40:43], %[g1] offset:4096\n\t"
        "ds_read_b128 v[44:47], %[g1] offset:5120\n\t"
        "ds_read_b128 v[88:91], %[g1] offset:6144\n\t"
        "ds_read_b128 v[92:95], %[g1] offset:7168\n\t"
        "Lh0_%=:\n\t"
        "s_waitcnt lgkmcnt(0)\n\t"
        "v_max3_u32 v24, v24, v25, v26\n\t"
        "v_max3_u32 v24, v24, v27, v28\n\t"
        "v_max3_u32 v24, v24, v29, v30\n\t"
        "v_max3_u32 v24, v24, v31, v32\n\t"
        "v_max3_u32 v24, v24, v33, v34\n\t"
        "v_max3_u32 v24, v24, v35, v36\n\t"
        "v_max3_u32 v24, v24, v37, v38\n\t"
        "v_max3_u32 v24, v24, v39, v40\n\t"
        "v_max3_u32 v24, v24, v41, v42\n\t"
        "v_max3_u32 v24, v24, v43, v44\n\t"
        "v_max3_u32 v24, v24, v45, v46\n\t"
        "v_max3_u32 v24, v24, v47, v88\n\t"
        "v_max3_u32 v24, v24, v89, v90\n\t"
        "v_max3_u32 v24, v24, v91, v92\n\t"
        "v_max3_u32 v24, v24, v93, v94\n\t"
        "v_max_u32 v24, v24, v95\n\t"
        "v_cmp_eq_u32 vcc, -1, v24\n\t"
        "v_mov_b32 v25, 1\n\t"
        "s_cbranch_vccnz Lx0_%=\n\t"
        "Ld0_%=:\n\t"
        "s_mov_b64 exec, 1\n\t"
        "ds_add_u32 %[g2], v25 offset:%[og0]+0\n\t"
        "s_mov_b64 exec, -1\n\t"
        "s_add_u32 m0, %[sld], 32768\n\t"
        "s_add_u32 %[sb], %[sxo], m0\n\t"
        "s_sub_u32 %[sa], %[sb], 8192\n\t"
        "s_cmp_gt_i32 %[sth], 32\n\t"
        "s_cselect_b32 %[scr], %[sa], %[sb]\n\t"
        "buffer_load_dwordx4 v107, %[ssrd], %[scr] offen offset:0 sc1 lds\n\t"
        "s_cmp_gt_i32 %[sth], 33\n\t"
        "s_cselect_b32 %[scr], %[sa], %[sb]\n\t"
        "buffer_load_dwordx4 v107, %[ssrd], %[scr] offen offset:1024 sc1 lds\n\t"
        "s_cmp_gt_i32 %[sth], 34\n\t"
        "s_cselect_b32 %[scr], %[sa], %[sb]\n\t"
        "buffer_load_dwordx4 v107, %[ssrd], %[scr] offen offset:2048 sc1 lds\n\t"
        "s_cmp_gt_i32 %[sth], 35\n\t"
        "s_cselect_b32 %[scr], %[sa], %[sb]\n\t"
        "buffer_load_dwordx4 v107, %[ssrd], %[scr] offen offset:3072 sc1 lds\n\t"
        "s_add_u32 m0, %[sld], 36864\n\t"
        "s_add_u32 %[sb], %[sxo], m0\n\t"
        "s_sub_u32 %[sa], %[sb], 8192\n\t"
        "s_cmp_gt_i32 %[sth], 36\n\t"
        "s_cselect_b32 %[scr], %[sa], %[sb]\n\t"
        "buffer_load_dwordx4 v107, %[ssrd], %[scr] offen offset:0 sc1 lds\n\t"
        "s_cmp_gt_i32 %[sth], 37\n\t"
        "s_cselect_b32 %[scr], %[sa], %[sb]\n\t"
        "buffer_load_dwordx4 v107, %[ssrd], %[scr] offen offset:1024 sc1 lds\n\t"
        "s_cmp_gt_i32 %[sth], 38\n\t"
        "s_cselect_b32 %[scr], %[sa], %[sb]\n\t"
        "buffer_load_dwordx4 v107, %[ssrd], %[scr] offen offset:2048 sc1 lds\n\t"
        "s_cmp_gt_i32 %[sth], 39\n\t"
        "s_cselect_b32 %[scr], %[sa], %[sb]\n\t"
        "buffer_load_dwordx4 v107, %[ssrd], %[scr] offen offset:3072 sc1 lds\n\t"
        "s_waitcnt vmcnt(8)\n\t"
        "ds_read_b128 v[24:27], %[g1] offset:16384\n\t"
        "ds_read_b128 v[28:31], %[g1] offset:17408\n\t"
        "ds_read_b128 v[32:35], %[g1] offset:18432\n\t"
        "ds_read_b128 v[36:39], %[g1] offset:19456\n\t"
        "ds_read_b128 v[40:43], %[g1] offset:20480\n\t"
        "ds_read_b128 v[44:47], %[g1] offset:21504\n\t"
        "ds_read_b128 v[88:91], %[g1] offset:22528\n\t"
        "ds_read_b128 v[92:95], %[g1] offset:23552\n\t"
        "s_add_u32 m0, %[sld], 49152\n\t"
        "s_add_u32 %[sb], %[sxo], m0\n\t"
        "s_sub_u32 %[sa], %[sb], 8192\n\t"
        "s_cmp_gt_i32 %[sth], 48\n\t"
        "s_cselect_b32 %[scr], %[sa], %[sb]\n\t"
        "buffer_load_dwordx4 v107, %[ssrd], %[scr] offen offset:0 sc1 lds\n\t"
        "s_cmp_gt_i32 %[sth], 49\n\t"
        "s_cselect_b32 %[scr], %[sa], %[sb]\n\t"
        "buffer_load_dwordx4 v107, %[ssrd], %[scr] offen offset:1024 sc1 lds\n\t"
        "s_cmp_gt_i32 %[sth], 50\n\t"
        "s_cselect_b32 %[scr], %[sa], %[sb]\n\t"
        "buffer_load_dwordx4 v107, %[ssrd], %[scr] offen offset:2048 sc1 lds\n\t"
        "s_cmp_gt_i32 %[sth], 51\n\t"
        "s_cselect_b32 %[scr], %[sa], %[sb]\n\t"
        "buffer_load_dwordx4 v107, %[ssrd], %[scr] offen offset:3072 sc1 lds\n\t"
        "s_add_u32 m0, %[sld], 53248\n\t"
        "s_add_u32 %[sb], %[sxo], m0\n\t"
        "s_sub_u32 %[sa], %[sb], 8192\n\t"
        "s_cmp_gt_i32 %[sth], 52\n\t"
        "s_cselect_b32 %[scr], %[sa], %[sb]\n\t"
        "buffer_load_dwordx4 v107, %[ssrd], %[scr] offen offset:0 sc1 lds\n\t"
        "s_cmp_gt_i32 %[sth], 53\n\t"
        "s_cselect_b32 %[scr], %[sa], %[sb]\n\t"
        "buffer_load_dwordx4 v107, %[ssrd], %[scr] offen offset:1024 sc1 lds\n\t"
        "s_cmp_gt_i32 %[sth], 54\n\t"
        "s_cselect_b32 %[scr], %[sa], %[sb]\n\t"
        "buffer_load_dwordx4 v107, %[ssrd], %[scr] offen offset:2048 sc1 lds\n\t"
        "s_cmp_gt_i32 %[sth], 55\n\t"
        "s_cselect_b32 %[scr], %[sa], %[sb]\n\t"
        "buffer_load_dwordx4 v107, %[ssrd], %[scr] offen offset:3072 sc1 lds\n\t"
        "Lh1_%=:\n\t"
        "s_waitcnt lgkmcnt(0)\n\t"
        "v_max3_u32 v24, v24, v25, v26\n\t"
        "v_max3_u32 v24, v24, v27, v28\n\t"
        "v_max3_u32 v24, v24, v29, v30\n\t"
        "v_max3_u32 v24, v24, v31, v32\n\t"
        "v_max3_u32 v24, v24, v33, v34\n\t"
        "v_max3_u32 v24, v24, v35, v36\n\t"
        "v_max3_u32 v24, v24, v37, v38\n\t"
        "v_max3_u32 v24, v24, v39, v40\n\t"
        "v_max3_u32 v24, v24, v41, v42\n\t"
        "v_max3_u32 v24, v24, v43, v44\n\t"
        "v_max3_u32 v24, v24, v45, v46\n\t"
        "v_max3_u32 v24, v24, v47, v88\n\t"
        "v_max3_u32 v24, v24, v89, v90\n\t"
        "v_max3_u32 v24, v24, v91, v92\n\t"
        "v_max3_u32 v24, v24, v93, v94\n\t"
        "v_max_u32 v24, v24, v95\n\t"
        "v_cmp_eq_u32 vcc, -1, v24\n\t"
        "v_mov_b32 v25, 1\n\t"
        "s_cbranch_vccnz Lx1_%=\n\t"
        "Ld1_%=:\n\t"
        "s_mov_b64 exec, 1\n\t"
        "ds_add_u32 %[g2], v25 offset:%[og0]+4\n\t"
        "s_mov_b64 exec, -1\n\t"
        "s_waitcnt vmcnt(8)\n\t"
        "ds_read_b128 v[24:27], %[g1] offset:32768\n\t"
        "ds_read_b128 v[28:31], %[g1] offset:33792\n\t"
        "ds_read_b128 v[32:35], %[g1] offset:34816\n\t"
        "ds_read_b128 v[36:39], %[g1] offset:35840\n\t"
        "ds_read_b128 v[40:43], %[g1] offset:36864\n\t"
        "ds_read_b128 v[44:47], %[g1] offset:37888\n\t"
        "ds_read_b128 v[88:91], %[g1] offset:38912\n\t"
        "ds_read_b128 v[92:95], %[g1] offset:39936\n\t"
        "v_cmp_lt_i32 vcc, 0, v96\n\t"
        "s_add_u32 m0, %[slh], 0\n\t"
        "s_add_u32 %[scr], %[sxq], 0\n\t"
        "v_cndmask_b32 v98, v97, %[g0], vcc\n\t"
        "buffer_load_dwordx4 v98, %[ssrd], %[scr] offen sc1 lds\n\t"
        "v_cmp_lt_i32 vcc, 4, v96\n\t"
        "s_add_u32 m0, %[slh], 1024\n\t"
        "s_add_u32 %[scr], %[sxq], 4096\n\t"
        "v_cndmask_b32 v99, v97, %[g0], vcc\n\t"
        "buffer_load_dwordx4 v99, %[ssrd], %[scr] offen sc1 lds\n\t"
        "v_cmp_lt_i32 vcc, 8, v96\n\t"
        "s_add_u32 m0, %[slh], 2048\n\t"
        "s_add_u32 %[scr], %[sxp], 8192\n\t"
        "v_cndmask_b32 v98, v97, %[g0], vcc\n\t"
        "buffer_load_dwordx4 v98, %[ssrd], %[scr] offen sc1 lds\n\t"
        "v_cmp_lt_i32 vcc, 12, v96\n\t"
        "s_add_u32 m0, %[slh], 3072\n\t"
        "s_add_u32 %[scr], %[sxp], 12288\n\t"
        "v_cndmask_b32 v99, v97, %[g0], vcc\n\t"
        "buffer_load_dwordx4 v99, %[ssrd], %[scr] offen sc1 lds\n\t"
        "v_cmp_lt_i32 vcc, 16, v96\n\t"
        "s_add_u32 m0, %[slh], 4096\n\t"
        "s_add_u32 %[scr], %[sxp], 16384\n\t"
        "v_cndmask_b32 v98, v97, %[g0], vcc\n\t"
        "buffer_load_dwordx4 v98, %[ssrd], %[scr] offen sc1 lds\n\t"
        "Lh2_%=:\n\t"
        "s_waitcnt lgkmcnt(0)\n\t"
        "v_max3_u32 v24, v24, v25, v26\n\t"
        "v_max3_u32 v24, v24, v27, v28\n\t"
        "v_max3_u32 v24, v24, v29, v30\n\t"
        "v_max3_u32 v24, v24, v31, v32\n\t"
        "v_max3_u32 v24, v24, v33, v34\n\t"
        "v_max3_u32 v24, v24, v35, v36\n\t"
        "v_max3_u32 v24, v24, v37, v38\n\t"
        "v_max3_u32 v24, v24, v39, v40\n\t"
        "v_max3_u32 v24, v24, v41, v42\n\t"
        "v_max3_u32 v24, v24, v43, v44\n\t"
        "v_max3_u32 v24, v24, v45, v46\n\t"
        "v_max3_u32 v24, v24, v47, v88\n\t"
        "v_max3_u32 v24, v24, v89, v90\n\t"
        "v_max3_u32 v24, v24, v91, v92\n\t"
        "v_max3_u32 v24, v24, v93, v94\n\t"
        "v_max_u32 v24, v24, v95\n\t"
        "v_cmp_eq_u32 vcc, -1, v24\n\t"
        "v_mov_b32 v25, 1\n\t"
        "s_cbranch_vccnz Lx2_%=\n\t"
        "Ld2_%=:\n\t"
        "s_mov_b64 exec, 1\n\t"
        "ds_add_u32 %[g2], v25 offset:%[og0]+8\n\t"
        "s_mov_b64 exec, -1\n\t"
        "s_waitcnt vmcnt(5)\n\t"
        "ds_read_b128 v[24:27], %[g1] offset:49152\n\t"
        "ds_read_b128 v[28:31], %[g1] offset:50176\n\t"
        "ds_read_b128 v[32:35], %[g1] offset:51200\n\t"
        "ds_read_b128 v[36:39], %[g1] offset:52224\n\t"
        "ds_read_b128 v[40:43], %[g1] offset:53248\n\t"
        "ds_read_b128 v[44:47], %[g1] offset:54272\n\t"
        "ds_read_b128 v[88:91], %[g1] offset:55296\n\t"
        "ds_read_b128 v[92:95], %[g1] offset:56320\n\t"
        "v_cmp_lt_i32 vcc, 20, v96\n\t"
        "s_add_u32 m0, %[slh], 5120\n\t"
        "s_add_u32 %[scr], %[sxp], 20480\n\t"
        "v_cndmask_b32 v99, v97, %[g0], vcc\n\t"
        "buffer_load_dwordx4 v99, %[ssrd], %[scr] offen sc1 lds\n\t"
        "v_cmp_lt_i32 vcc, 24, v96\n\t"
        "s_add_u32 m0, %[slh], 6144\n\t"
        "s_add_u32 %[scr], %[sxp], 24576\n\t"
        "v_cndmask_b32 v98, v97, %[g0], vcc\n\t"
        "buffer_load_dwordx4 v98, %[ssrd], %[scr] offen sc1 lds\n\t"
        "v_cmp_lt_i32 vcc, 28, v96\n\t"
        "s_add_u32 m0, %[slh], 7168\n\t"
        "s_add_u32 %[scr], %[sxp], 28672\n\t"
        "v_cndmask_b32 v99, v97, %[g0], vcc\n\t"
        "buffer_load_dwordx4 v99, %[ssrd], %[scr] offen sc1 lds\n\t"
        "v_cmp_lt_i32 vcc, 32, v96\n\t"
        "s_add_u32 m0, %[slh], 8192\n\t"
        "s_add_u32 %[scr], %[sxp], 32768\n\t"
        "v_cndmask_b32 v98, v97, %[g0], vcc\n\t"
        "buffer_load_dwordx4 v98, %[ssrd], %[scr] offen sc1 lds\n\t"
        "Lh3_%=:\n\t"
        "s_waitcnt lgkmcnt(0)\n\t"
        "v_max3_u32 v24, v24, v25, v26\n\t"
        "v_max3_u32 v24, v24, v27, v28\n\t"
        "v_max3_u32 v24, v24, v29, v30\n\t"
        "v_max3_u32 v24, v24, v31, v32\n\t"
        "v_max3_u32 v24, v24, v33, v34\n\t"
        "v_max3_u32 v24, v24, v35, v36\n\t"
        "v_max3_u32 v24, v24, v37, v38\n\t"
        "v_max3_u32 v24, v24, v39, v40\n\t"
        "v_max3_u32 v24, v24, v41, v42\n\t"
        "v_max3_u32 v24, v24, v43, v44\n\t"
        "v_max3_u32 v24, v24, v45, v46\n\t"
        "v_max3_u32 v24, v24, v47, v88\n\t"
        "v_max3_u32 v24, v24, v89, v90\n\t"
        "v_max3_u32 v24, v24, v91, v92\n\t"
        "v_max3_u32 v24, v24, v93, v94\n\t"
        "v_max_u32 v24, v24, v95\n\t"
        "v_cmp_eq_u32 vcc, -1, v24\n\t"
        "v_mov_b32 v25, 2\n\t"
        "s_cbranch_vccnz Lx3_%=\n\t"
        "Ld3_%=:\n\t"
        "s_mov_b64 exec, 1\n\t"
        "ds_add_u32 %[g2], v25 offset:%[og0]+12\n\t"
        "s_mov_b64 exec, -1\n\t"
        "s_waitcnt vmcnt(4)\n\t"
        "v_add_u32 %[g1], %[slh], v107\n\t"
        "ds_read_b128 v[24:27], %[g1] offset:0\n\t"
        "ds_read_b128 v[28:31], %[g1] offset:1024\n\t"
        "ds_read_b128 v[32:35], %[g1] offset:2048\n\t"
        "ds_read_b128 v[36:39], %[g1] offset:3072\n\t"
        "ds_read_b128 v[40:43], %[g1] offset:4096\n\t"
        "Lh4_%=:\n\t"
        "s_waitcnt lgkmcnt(0)\n\t"
        "v_max3_u32 v24, v24, v25, v26\n\t"
        "v_max3_u32 v24, v24, v27, v28\n\t"
        "v_max3_u32 v24, v24, v29, v30\n\t"
        "v_max3_u32 v24, v24, v31, v32\n\t"
        "v_max3_u32 v24, v24, v33, v34\n\t"
        "v_max3_u32 v24, v24, v35, v36\n\t"
        "v_max3_u32 v24, v24, v37, v38\n\t"
        "v_max3_u32 v24, v24, v39, v40\n\t"
        "v_max3_u32 v24, v24, v41, v42\n\t"
        "v_max_u32 v24, v24, v43\n\t"
        "v_cmp_eq_u32 vcc, -1, v24\n\t"
        "v_mov_b32 v25, 1\n\t"
        "s_cbranch_vccnz Lx4_%=\n\t"
        "Ld4_%=:\n\t"
        "s_waitcnt vmcnt(0)\n\t"
        "ds_read_b128 v[24:27], %[g1] offset:5120\n\t"
        "ds_read_b128 v[28:31], %[g1] offset:6144\n\t"
        "ds_read_b128 v[32:35], %[g1] offset:7168\n\t"
        "ds_read_b128 v[36:39], %[g1] offset:8192\n\t"
        "Lh5_%=:\n\t"
        "s_waitcnt lgkmcnt(0)\n\t"
        "v_max3_u32 v24, v24, v25, v26\n\t"
        "v_max3_u32 v24, v24, v27, v28\n\t"
        "v_max3_u32 v24, v24, v29, v30\n\t"
        "v_max3_u32 v24, v24, v31, v32\n\t"
        "v_max3_u32 v24, v24, v33, v34\n\t"
        "v_max3_u32 v24, v24, v35, v36\n\t"
        "v_max3_u32 v24, v24, v37, v38\n\t"
        "v_max_u32 v24, v24, v39\n\t"
        "v_cmp_eq_u32 vcc, -1, v24\n\t"
        "v_mov_b32 v25, 1\n\t"
        "s_cbranch_vccnz Lx5_%=\n\t"
        "Ld5_%=:\n\t"
        "s_mov_b64 exec, 1\n\t"
        "ds_add_u32 %[g2], v25 offset:%[og0]+16\n\t"
        "s_mov_b64 exec, -1\n\t"
        "s_waitcnt lgkmcnt(0)\n\t"
        "s_branch Le_%=\n\t"
        "Lx0_%=:\n\t"
        "s_cmp_lt_i32 %[scn], 1\n\t"
        "s_cbranch_scc1 Ld0_%=\n\t"
        "s_sub_u32 %[scn], %[scn], 1\n\t"
        "s_add_u32 m0, %[sld], 0\n\t"
        "s_add_u32 %[sb], %[sxo], m0\n\t"
        "s_sub_u32 %[sa], %[sb], 8192\n\t"
        "s_cmp_gt_i32 %[sth], 0\n\t"
        "s_cselect_b32 %[scr], %[sa], %[sb]\n\t"
        "buffer_load_dwordx4 v107, %[ssrd], %[scr] offen offset:0 sc1 lds\n\t"
        "s_cmp_gt_i32 %[sth], 1\n\t"
        "s_cselect_b32 %[scr], %[sa], %[sb]\n\t"
        "buffer_load_dwordx4 v107, %[ssrd], %[scr] offen offset:1024 sc1 lds\n\t"
        "s_cmp_gt_i32 %[sth], 2\n\t"
        "s_cselect_b32 %[scr], %[sa], %[sb]\n\t"
        "buffer_load_dwordx4 v107, %[ssrd], %[scr] offen offset:2048 sc1 lds\n\t"
        "s_cmp_gt_i32 %[sth], 3\n\t"
        "s_cselect_b32 %[scr], %[sa], %[sb]\n\t"
        "buffer_load_dwordx4 v107, %[ssrd], %[scr] offen offset:3072 sc1 lds\n\t"
        "s_add_u32 m0, %[sld], 4096\n\t"
        "s_add_u32 %[sb], %[sxo], m0\n\t"
        "s_sub_u32 %[sa], %[sb], 8192\n\t"
        "s_cmp_gt_i32 %[sth], 4\n\t"
        "s_cselect_b32 %[scr], %[sa], %[sb]\n\t"
        "buffer_load_dwordx4 v107, %[ssrd], %[scr] offen offset:0 sc1 lds\n\t"
        "s_cmp_gt_i32 %[sth], 5\n\t"
        "s_cselect_b32 %[scr], %[sa], %[sb]\n\t"
        "buffer_load_dwordx4 v107, %[ssrd], %[scr] offen offset:1024 sc1 lds\n\t"
        "s_cmp_gt_i32 %[sth], 6\n\t"
        "s_cselect_b32 %[scr], %[sa], %[sb]\n\t"
        "buffer_load_dwordx4 v107, %[ssrd], %[scr] offen offset:2048 sc1 lds\n\t"
        "s_cmp_gt_i32 %[sth], 7\n\t"
        "s_cselect_b32 %[scr], %[sa], %[sb]\n\t"
        "buffer_load_dwordx4 v107, %[ssrd], %[scr] offen offset:3072 sc1 lds\n\t"
        "s_waitcnt vmcnt(0)\n\t"
        "ds_read_b128 v[24:27], %[g1] offset:0\n\t"
        "ds_read_b128 v[28:31], %[g1] offset:1024\n\t"
        "ds_read_b128 v[32:35], %[g1] offset:2048\n\t"
        "ds_read_b128 v[36:39], %[g1] offset:3072\n\t"
        "ds_read_b128 v[40:43], %[g1] offset:4096\n\t"
        "ds_read_b128 v[44:47], %[g1] offset:5120\n\t"
        "ds_read_b128 v[88:91], %[g1] offset:6144\n\t"
        "ds_read_b128 v[92:95], %[g1] offset:7168\n\t"
        "s_branch Lh0_%=\n\t"
        "Lx1_%=:\n\t"
        "s_cmp_lt_i32 %[scn], 1\n\t"
        "s_cbranch_scc1 Ld1_%=\n\t"
        "s_sub_u32 %[scn], %[scn], 1\n\t"
        "s_add_u32 m0, %[sld], 16384\n\t"
        "s_add_u32 %[sb], %[sxo], m0\n\t"
        "s_sub_u32 %[sa], %[sb], 8192\n\t"
        "s_cmp_gt_i32 %[sth], 16\n\t"
        "s_cselect_b32 %[scr], %[sa], %[sb]\n\t"
        "buffer_load_dwordx4 v107, %[ssrd], %[scr] offen offset:0 sc1 lds\n\t"
        "s_cmp_gt_i32 %[sth], 17\n\t"
        "s_cselect_b32 %[scr], %[sa], %[sb]\n\t"
        "buffer_load_dwordx4 v107, %[ssrd], %[scr] offen offset:1024 sc1 lds\n\t"
        "s_cmp_gt_i32 %[sth], 18\n\t"
        "s_cselect_b32 %[scr], %[sa], %[sb]\n\t"
        "buffer_load_dwordx4 v107, %[ssrd], %[scr] offen offset:2048 sc1 lds\n\t"
        "s_cmp_gt_i32 %[sth], 19\n\t"
        "s_cselect_b32 %[scr], %[sa], %[sb]\n\t"
        "buffer_load_dwordx4 v107, %[ssrd], %[scr] offen offset:3072 sc1 lds\n\t"
        "s_add_u32 m0, %[sld], 20480\n\t"
        "s_add_u32 %[sb], %[sxo], m0\n\t"
        "s_sub_u32 %[sa], %[sb], 8192\n\t"
        "s_cmp_gt_i32 %[sth], 20\n\t"
        "s_cselect_b32 %[scr], %[sa], %[sb]\n\t"
        "buffer_load_dwordx4 v107, %[ssrd], %[scr] offen offset:0 sc1 lds\n\t"
        "s_cmp_gt_i32 %[sth], 21\n\t"
        "s_cselect_b32 %[scr], %[sa], %[sb]\n\t"
        "buffer_load_dwordx4 v107, %[ssrd], %[scr] offen offset:1024 sc1 lds\n\t"
        "s_cmp_gt_i32 %[sth], 22\n\t"
        "s_cselect_b32 %[scr], %[sa], %[sb]\n\t"
        "buffer_load_dwordx4 v107, %[ssrd], %[scr] offen offset:2048 sc1 lds\n\t"
        "s_cmp_gt_i32 %[sth], 23\n\t"
        "s_cselect_b32 %[scr], %[sa], %[sb]\n\t"
        "buffer_load_dwordx4 v107, %[ssrd], %[scr] offen offset:3072 sc1 lds\n\t"
        "s_waitcnt vmcnt(0)\n\t"
        "ds_read_b128 v[24:27], %[g1] offset:16384\n\t"
        "ds_read_b128 v[28:31], %[g1] offset:17408\n\t"
        "ds_read_b128 v[32:35], %[g1] offset:18432\n\t"
        "ds_read_b128 v[36:39], %[g1] offset:19456\n\t"
        "ds_read_b128 v[40:43], %[g1] offset:20480\n\t"
        "ds_read_b128 v[44:47], %[g1] offset:21504\n\t"
        "ds_read_b128 v[88:91], %[g1] offset:22528\n\t"
        "ds_read_b128 v[92:95], %[g1] offset:23552\n\t"
        "s_branch Lh1_%=\n\t"
        "Lx2_%=:\n\t"
        "s_cmp_lt_i32 %[scn], 1\n\t"
        "s_cbranch_scc1 Ld2_%=\n\t"
        "s_sub_u32 %[scn], %[scn], 1\n\t"
        "s_add_u32 m0, %[sld], 32768\n\t"
        "s_add_u32 %[sb], %[sxo], m0\n\t"
        "s_sub_u32 %[sa], %[sb], 8192\n\t"
        "s_cmp_gt_i32 %[sth], 32\n\t"
        "s_cselect_b32 %[scr], %[sa], %[sb]\n\t"
        "buffer_load_dwordx4 v107, %[ssrd], %[scr] offen offset:0 sc1 lds\n\t"
        "s_cmp_gt_i32 %[sth], 33\n\t"
        "s_cselect_b32 %[scr], %[sa], %[sb]\n\t"
        "buffer_load_dwordx4 v107, %[ssrd], %[scr] offen offset:1024 sc1 lds\n\t"
        "s_cmp_gt_i32 %[sth], 34\n\t"
        "s_cselect_b32 %[scr], %[sa], %[sb]\n\t"
        "buffer_load_dwordx4 v107, %[ssrd], %[scr] offen offset:2048 sc1 lds\n\t"
        "s_cmp_gt_i32 %[sth], 35\n\t"
        "s_cselect_b32 %[scr], %[sa], %[sb]\n\t"
        "buffer_load_dwordx4 v107, %[ssrd], %[scr] offen offset:3072 sc1 lds\n\t"
        "s_add_u32 m0, %[sld], 36864\n\t"
        "s_add_u32 %[sb], %[sxo], m0\n\t"
        "s_sub_u32 %[sa], %[sb], 8192\n\t"
        "s_cmp_gt_i32 %[sth], 36\n\t"
        "s_cselect_b32 %[scr], %[sa], %[sb]\n\t"
        "buffer_load_dwordx4 v107, %[ssrd], %[scr] offen offset:0 sc1 lds\n\t"
        "s_cmp_gt_i32 %[sth], 37\n\t"
        "s_cselect_b32 %[scr], %[sa], %[sb]\n\t"
        "buffer_load_dwordx4 v107, %[ssrd], %[scr] offen offset:1024 sc1 lds\n\t"
        "s_cmp_gt_i32 %[sth], 38\n\t"
        "s_cselect_b32 %[scr], %[sa], %[sb]\n\t"
        "buffer_load_dwordx4 v107, %[ssrd], %[scr] offen offset:2048 sc1 lds\n\t"
        "s_cmp_gt_i32 %[sth], 39\n\t"
        "s_cselect_b32 %[scr], %[sa], %[sb]\n\t"
        "buffer_load_dwordx4 v107, %[ssrd], %[scr] offen offset:3072 sc1 lds\n\t"
        "s_waitcnt vmcnt(0)\n\t"
        "ds_read_b128 v[24:27], %[g1] offset:32768\n\t"
        "ds_read_b128 v[28:31], %[g1] offset:33792\n\t"
        "ds_read_b128 v[32:35], %[g1] offset:34816\n\t"
        "ds_read_b128 v[36:39], %[g1] offset:35840\n\t"
        "ds_read_b128 v[40:43], %[g1] offset:36864\n\t"
        "ds_read_b128 v[44:47], %[g1] offset:37888\n\t"
        "ds_read_b128 v[88:91], %[g1] offset:38912\n\t"
        "ds_read_b128 v[92:95], %[g1] offset:39936\n\t"
        "s_branch Lh2_%=\n\t"
        "Lx3_%=:\n\t"
        "s_cmp_lt_i32 %[scn], 1\n\t"
        "s_cbranch_scc1 Ld3_%=\n\t"
        "s_sub_u32 %[scn], %[scn], 1\n\t"
        "s_add_u32 m0, %[sld], 49152\n\t"
        "s_add_u32 %[sb], %[sxo], m0\n\t"
        "s_sub_u32 %[sa], %[sb], 8192\n\t"
        "s_cmp_gt_i32 %[sth], 48\n\t"
        "s_cselect_b32 %[scr], %[sa], %[sb]\n\t"
        "buffer_load_dwordx4 v107, %[ssrd], %[scr] offen offset:0 sc1 lds\n\t"
        "s_cmp_gt_i32 %[sth], 49\n\t"
        "s_cselect_b32 %[scr], %[sa], %[sb]\n\t"
        "buffer_load_dwordx4 v107, %[ssrd], %[scr] offen offset:1024 sc1 lds\n\t"
        "s_cmp_gt_i32 %[sth], 50\n\t"
        "s_cselect_b32 %[scr], %[sa], %[sb]\n\t"
        "buffer_load_dwordx4 v107, %[ssrd], %[scr] offen offset:2048 sc1 lds\n\t"
        "s_cmp_gt_i32 %[sth], 51\n\t"
        "s_cselect_b32 %[scr], %[sa], %[sb]\n\t"
        "buffer_load_dwordx4 v107, %[ssrd], %[scr] offen offset:3072 sc1 lds\n\t"
        "s_add_u32 m0, %[sld], 53248\n\t"
        "s_add_u32 %[sb], %[sxo], m0\n\t"
        "s_sub_u32 %[sa], %[sb], 8192\n\t"
        "s_cmp_gt_i32 %[sth], 52\n\t"
        "s_cselect_b32 %[scr], %[sa], %[sb]\n\t"
        "buffer_load_dwordx4 v107, %[ssrd], %[scr] offen offset:0 sc1 lds\n\t"
        "s_cmp_gt_i32 %[sth], 53\n\t"
        "s_cselect_b32 %[scr], %[sa], %[sb]\n\t"
        "buffer_load_dwordx4 v107, %[ssrd], %[scr] offen offset:1024 sc1 lds\n\t"
        "s_cmp_gt_i32 %[sth], 54\n\t"
        "s_cselect_b32 %[scr], %[sa], %[sb]\n\t"
        "buffer_load_dwordx4 v107, %[ssrd], %[scr] offen offset:2048 sc1 lds\n\t"
        "s_cmp_gt_i32 %[sth], 55\n\t"
        "s_cselect_b32 %[scr], %[sa], %[sb]\n\t"
        "buffer_load_dwordx4 v107, %[ssrd], %[scr] offen offset:3072 sc1 lds\n\t"
        "s_waitcnt vmcnt(0)\n\t"
        "ds_read_b128 v[24:27], %[g1] offset:49152\n\t"
        "ds_read_b128 v[28:31], %[g1] offset:50176\n\t"
        "ds_read_b128 v[32:35], %[g1] offset:51200\n\t"
        "ds_read_b128 v[36:39], %[g1] offset:52224\n\t"
        "ds_read_b128 v[40:43], %[g1] offset:53248\n\t"
        "ds_read_b128 v[44:47], %[g1] offset:54272\n\t"
        "ds_read_b128 v[88:91], %[g1] offset:55296\n\t"
        "ds_read_b128 v[92:95], %[g1] offset:56320\n\t"
        "s_branch Lh3_%=\n\t"
        "Lx4_%=:\n\t"
        "s_cmp_lt_i32 %[scn], 1\n\t"
        "s_cbranch_scc1 Ld4_%=\n\t"
        "s_sub_u32 %[scn], %[scn], 1\n\t"
        "v_cmp_lt_i32 vcc, 0, v96\n\t"
        "s_add_u32 m0, %[slh], 0\n\t"
        "s_add_u32 %[scr], %[sxq], 0\n\t"
        "v_cndmask_b32 v98, v97, %[g0], vcc\n\t"
        "buffer_load_dwordx4 v98, %[ssrd], %[scr] offen sc1 lds\n\t"
        "v_cmp_lt_i32 vcc, 4, v96\n\t"
        "s_add_u32 m0, %[slh], 1024\n\t"
        "s_add_u32 %[scr], %[sxq], 4096\n\t"
        "v_cndmask_b32 v99, v97, %[g0], vcc\n\t"
        "buffer_load_dwordx4 v99, %[ssrd], %[scr] offen sc1 lds\n\t"
        "v_cmp_lt_i32 vcc, 8, v96\n\t"
        "s_add_u32 m0, %[slh], 2048\n\t"
        "s_add_u32 %[scr], %[sxp], 8192\n\t"
        "v_cndmask_b32 v98, v97, %[g0], vcc\n\t"
        "buffer_load_dwordx4 v98, %[ssrd], %[scr] offen sc1 lds\n\t"
        "v_cmp_lt_i32 vcc, 12, v96\n\t"
        "s_add_u32 m0, %[slh], 3072\n\t"
        "s_add_u32 %[scr], %[sxp], 12288\n\t"
        "v_cndmask_b32 v99, v97, %[g0], vcc\n\t"
        "buffer_load_dwordx4 v99, %[ssrd], %[scr] offen sc1 lds\n\t"
        "v_cmp_lt_i32 vcc, 16, v96\n\t"
        "s_add_u32 m0, %[slh], 4096\n\t"
        "s_add_u32 %[scr], %[sxp], 16384\n\t"
        "v_cndmask_b32 v98, v97, %[g0], vcc\n\t"
        "buffer_load_dwordx4 v98, %[ssrd], %[scr] offen sc1 lds\n\t"
        "s_waitcnt vmcnt(0)\n\t"
        "v_add_u32 %[g1], %[slh], v107\n\t"
        "ds_read_b128 v[24:27], %[g1] offset:0\n\t"
        "ds_read_b128 v[28:31], %[g1] offset:1024\n\t"
        "ds_read_b128 v[32:35], %[g1] offset:2048\n\t"
        "ds_read_b128 v[36:39], %[g1] offset:3072\n\t"
        "ds_read_b128 v[40:43], %[g1] offset:4096\n\t"
        "s_branch Lh4_%=\n\t"
        "Lx5_%=:\n\t"
        "s_cmp_lt_i32 %[scn], 1\n\t"
        "s_cbranch_scc1 Ld5_%=\n\t"
        "s_sub_u32 %[scn], %[scn], 1\n\t"
        "v_cmp_lt_i32 vcc, 20, v96\n\t"
        "s_add_u32 m0, %[slh], 5120\n\t"
        "s_add_u32 %[scr], %[sxp], 20480\n\t"
        "v_cndmask_b32 v99, v97, %[g0], vcc\n\t"
        "buffer_load_dwordx4 v99, %[ssrd], %[scr] offen sc1 lds\n\t"
        "v_cmp_lt_i32 vcc, 24, v96\n\t"
        "s_add_u32 m0, %[slh], 6144\n\t"
        "s_add_u32 %[scr], %[sxp], 24576\n\t"
        "v_cndmask_b32 v98, v97, %[g0], vcc\n\t"
        "buffer_load_dwordx4 v98, %[ssrd], %[scr] offen sc1 lds\n\t"
        "v_cmp_lt_i32 vcc, 28, v96\n\t"
        "s_add_u32 m0, %[slh], 7168\n\t"
        "s_add_u32 %[scr], %[sxp], 28672\n\t"
        "v_cndmask_b32 v99, v97, %[g0], vcc\n\t"
        "buffer_load_dwordx4 v99, %[ssrd], %[scr] offen sc1 lds\n\t"
        "v_cmp_lt_i32 vcc, 32, v96\n\t"
        "s_add_u32 m0, %[slh], 8192\n\t"
        "s_add_u32 %[scr], %[sxp], 32768\n\t"
        "v_cndmask_b32 v98, v97, %[g0], vcc\n\t"
        "buffer_load_dwordx4 v98, %[ssrd], %[scr] offen sc1 lds\n\t"
        "s_waitcnt vmcnt(0)\n\t"
        "ds_read_b128 v[24:27], %[g1] offset:5120\n\t"
        "ds_read_b128 v[28:31], %[g1] offset:6144\n\t"
        "ds_read_b128 v[32:35], %[g1] offset:7168\n\t"
        "ds_read_b128 v[36:39], %[g1] offset:8192\n\t"
        "s_branch Lh5_%=\n\t"
        "Le_%=:\n\t"
        : [scr] "=&s"(cr), [sa] "=&s"(sa), [sb] "=&s"(sb), [scn] "+s"(cn), [g0] "=&v"(g0), [g1] "=&v"(g1), [g2] "=&v"(g2)
        : [ssrd] "s"(srd), [sxo] "s"(xo), [sxp] "s"(xp), [sxq] "s"(xq), [sth] "s"(th), [shh] "s"(hh), [sld] "s"(ld), [slh] "s"(lh), [og0] "n"(BC_G0)
        : "memory", "scc", "vcc", "v24", "v25", "v26", "v27", "v28", "v29", "v30", "v31", "v32", "v33", "v34", "v35", "v36", "v37", "v38", "v39", "v40", "v41", "v42", "v43", "v44", "v45", "v46", "v47");
}

DEV void gu_set() {
    asm volatile(
        "v_lshrrev_b32 v110, 5, v107\n\t"
        "v_bfe_u32 v111, v107, 6, 3\n\t"
        "v_lshrrev_b32 v96, 1, v107\n\t"
        "v_and_or_b32 v110, v110, 16, v111\n\t"
        "v_and_b32 v96, 24, v96\n\t"
        "v_mad_u32_u24 v110, v110, 48, v96\n\t"
        "v_add_u32 v110, 98048, v110\n\t"
        "v_add_u32 v111, 27632, v110\n\t"
        "v_cndmask_b32 v111, v110, v111, %[sl2]\n\t"
        :: [sl2] "s"(0xCCCCCCCCCCCCCCCCull) : "memory");
}
DEV void gu_pre() {
    asm volatile(
        "ds_read_b64_tr_b16 v[48:49], v110 offset:0\n\t"
        "ds_read_b64_tr_b16 v[50:51], v110 offset:384\n\t"
        "ds_read_b64_tr_b16 v[52:53], v110 offset:1536\n\t"
        "ds_read_b64_tr_b16 v[54:55], v110 offset:1920\n\t"
        "ds_read_b64_tr_b16 v[56:57], v110 offset:27648\n\t"
        "ds_read_b64_tr_b16 v[58:59], v110 offset:28032\n\t"
        "ds_read_b64_tr_b16 v[60:61], v110 offset:29184\n\t"
        "ds_read_b64_tr_b16 v[62:63], v110 offset:29568\n\t"
        "ds_read_b64_tr_b16 v[64:65], v111 offset:32\n\t"
        "ds_read_b64_tr_b16 v[66:67], v111 offset:416\n\t"
        "ds_read_b64_tr_b16 v[68:69], v111 offset:1568\n\t"
        "ds_read_b64_tr_b16 v[70:71], v111 offset:1952\n\t"
        "ds_read_b64_tr_b16 v[72:73], v110 offset:24576\n\t"
        "ds_read_b64_tr_b16 v[74:75], v110 offset:24960\n\t"
        "ds_read_b64_tr_b16 v[76:77], v110 offset:26112\n\t"
        "ds_read_b64_tr_b16 v[78:79], v110 offset:26496\n\t"
        "ds_read_b64_tr_b16 v[80:81], v110 offset:52224\n\t"
        "ds_read_b64_tr_b16 v[82:83], v110 offset:52608\n\t"
        "ds_read_b64_tr_b16 v[84:85], v110 offset:53760\n\t"
        "ds_read_b64_tr_b16 v[86:87], v110 offset:54144\n\t"
        "ds_read_b64_tr_b16 v[88:89], v111 offset:24608\n\t"
        "ds_read_b64_tr_b16 v[90:91], v111 offset:24992\n\t"
        "ds_read_b64_tr_b16 v[92:93], v111 offset:26144\n\t"
        "ds_read_b64_tr_b16 v[94:95], v111 offset:26528\n\t"
        "ds_read_b64_tr_b16 v[220:221], v110 offset:3072\n\t"
        "ds_read_b64_tr_b16 v[222:223], v110 offset:3456\n\t"
        "ds_read_b64_tr_b16 v[224:225], v110 offset:4608\n\t"
        "ds_read_b64_tr_b16 v[226:227], v110 offset:4992\n\t"
        "ds_read_b64_tr_b16 v[228:229], v110 offset:30720\n\t"
        "ds_read_b64_tr_b16 v[230:231], v110 offset:31104\n\t"
        "ds_read_b64_tr_b16 v[232:233], v110 offset:32256\n\t"
        "ds_read_b64_tr_b16 v[234:235], v110 offset:32640\n\t"
        "ds_read_b64_tr_b16 v[236:237], v111 offset:3104\n\t"
        "ds_read_b64_tr_b16 v[238:239], v111 offset:3488\n\t"
        "ds_read_b64_tr_b16 v[240:241], v111 offset:4640\n\t"
        "ds_read_b64_tr_b16 v[242:243], v111 offset:5024\n\t"
        ::: "memory");
}

DEV void gu_tr(u32 o) {
    asm volatile(
        "v_lshrrev_b32 v32, 5, v107\n\t"
        "v_bfe_u32 v33, v107, 6, 3\n\t"
        "v_lshrrev_b32 v34, 1, v107\n\t"
        "v_and_or_b32 v32, v32, 16, v33\n\t"
        "v_and_b32 v34, 24, v34\n\t"
        "v_mad_u32_u24 v32, v32, 48, v34\n\t"
        "v_add_u32 v32, %[so], v32\n\t"
        "v_add_u32 v33, 27632, v32\n\t"
        "v_cndmask_b32 v33, v32, v33, %[sl2]\n\t"
        "v_add_u32 v34, %[so], v107\n\t"
        "ds_read_b64_tr_b16 v[48:49], v32 offset:0\n\t"
        "ds_read_b64_tr_b16 v[50:51], v32 offset:384\n\t"
        "ds_read_b64_tr_b16 v[52:53], v32 offset:1536\n\t"
        "ds_read_b64_tr_b16 v[54:55], v32 offset:1920\n\t"
        "ds_read_b64_tr_b16 v[56:57], v32 offset:27648\n\t"
        "ds_read_b64_tr_b16 v[58:59], v32 offset:28032\n\t"
        "ds_read_b64_tr_b16 v[60:61], v32 offset:29184\n\t"
        "ds_read_b64_tr_b16 v[62:63], v32 offset:29568\n\t"
        "ds_read_b64_tr_b16 v[64:65], v33 offset:32\n\t"
        "ds_read_b64_tr_b16 v[66:67], v33 offset:416\n\t"
        "ds_read_b64_tr_b16 v[68:69], v33 offset:1568\n\t"
        "ds_read_b64_tr_b16 v[70:71], v33 offset:1952\n\t"
        "ds_read_b64_tr_b16 v[72:73], v32 offset:3072\n\t"
        "ds_read_b64_tr_b16 v[74:75], v32 offset:3456\n\t"
        "ds_read_b64_tr_b16 v[76:77], v32 offset:4608\n\t"
        "ds_read_b64_tr_b16 v[78:79], v32 offset:4992\n\t"
        "ds_read_b64_tr_b16 v[80:81], v32 offset:30720\n\t"
        "ds_read_b64_tr_b16 v[82:83], v32 offset:31104\n\t"
        "ds_read_b64_tr_b16 v[84:85], v32 offset:32256\n\t"
        "ds_read_b64_tr_b16 v[86:87], v32 offset:32640\n\t"
        "ds_read_b64_tr_b16 v[24:25], v33 offset:3104\n\t"
        "ds_read_b64_tr_b16 v[26:27], v33 offset:3488\n\t"
        "ds_read_b64_tr_b16 v[28:29], v33 offset:4640\n\t"
        "ds_read_b64_tr_b16 v[30:31], v33 offset:5024\n\t"
        "s_waitcnt lgkmcnt(12)\n\t"
        "ds_write_b128 v34, v[48:51] offset:0\n\t"
        "ds_write_b128 v34, v[52:55] offset:1024\n\t"
        "ds_write_b128 v34, v[56:59] offset:27648\n\t"
        "ds_write_b128 v34, v[60:63] offset:28672\n\t"
        "ds_write_b128 v34, v[64:67] offset:2048\n\t"
        "ds_write_b128 v34, v[68:71] offset:29696\n\t"
        "s_waitcnt lgkmcnt(6)\n\t"
        "ds_write_b128 v34, v[72:75] offset:3072\n\t"
        "ds_write_b128 v34, v[76:79] offset:4096\n\t"
        "ds_write_b128 v34, v[80:83] offset:30720\n\t"
        "ds_write_b128 v34, v[84:87] offset:31744\n\t"
        "ds_write_b128 v34, v[24:27] offset:5120\n\t"
        "ds_write_b128 v34, v[28:31] offset:32768\n\t"
        :: [so] "s"(o), [sl2] "s"(0xCCCCCCCCCCCCCCCCull) : "memory", "v24", "v25", "v26", "v27", "v28", "v29", "v30", "v31", "v32", "v33", "v34");
}

DEV void ffn(u32x4 srd, u32 cr, u32 c0, u32 c1, u32 ya, u32 ye, u32 yb, u32 re, float& m, float& rs, u32& cn) {
    u32 ap[12];
    asm volatile(
        "s_nop 4\n\t"
        "Ls_%=:\n\t"
        "buffer_load_dwordx4 v[24:27], %[c0], %[ssrd], %[scr] offen sc1\n\t"
        "buffer_load_dwordx4 v[28:31], %[c1], %[ssrd], %[scr] offen sc1\n\t"
        "v_mov_b32 v32, 0\n\t"
        "v_mov_b32 v33, 0\n\t"
        "v_mov_b32 v34, 0\n\t"
        "v_mov_b32 v35, 0\n\t"
        "v_mov_b32 v36, 0\n\t"
        "v_mov_b32 v37, 0\n\t"
        "v_mov_b32 v38, 0\n\t"
        "v_mov_b32 v39, 0\n\t"
        "v_mov_b32 v40, 0\n\t"
        "v_mov_b32 v41, 0\n\t"
        "v_mov_b32 v42, 0\n\t"
        "v_mov_b32 v43, 0\n\t"
        "v_mov_b32 v96, 0x3f803f80\n\t"
        "v_mov_b32 v97, 0x3f803f80\n\t"
        "v_mov_b32 v98, 0x3f803f80\n\t"
        "v_mov_b32 v99, 0x3f803f80\n\t"
        "v_mov_b32 v252, 0x3f803f80\n\t"
        "v_mov_b32 v253, 0x3f803f80\n\t"
        "v_mov_b32 v254, 0x3f803f80\n\t"
        "v_mov_b32 v255, 0x3f803f80\n\t"
        "v_mov_b32 v44, 0x4444\n\t"
        "v_mov_b32 v45, 0xeeee\n\t"
        "v_cndmask_b32 v44, v44, v45, %[spar]\n\t"
        "v_add_u32 v45, 98048, v107\n\t"
        "s_cmp_lg_u32 %[sre], 0\n\t"
        "s_cbranch_scc1 Lr_%=\n\t"
        "s_waitcnt vmcnt(0)\n\t"
        "Lb_%=:\n\t"
        "v_mov_b32_dpp v96, v24 quad_perm:[0,1,2,3] row_mask:0xf bank_mask:0x1 bound_ctrl:1\n\t"
        "v_mov_b32_dpp v97, v25 quad_perm:[0,1,2,3] row_mask:0xf bank_mask:0x1 bound_ctrl:1\n\t"
        "v_mov_b32_dpp v98, v26 quad_perm:[0,1,2,3] row_mask:0xf bank_mask:0x1 bound_ctrl:1\n\t"
        "v_mov_b32_dpp v99, v27 quad_perm:[0,1,2,3] row_mask:0xf bank_mask:0x1 bound_ctrl:1\n\t"
        "v_max3_u32 v46, v24, v25, v26\n\t"
        "v_max3_u32 v46, v46, v27, v28\n\t"
        "v_smfmac_f32_16x16x64_bf16 v[32:35], v[96:99], v[48:55], v44\n\t"
        "v_mov_b32_dpp v252, v28 quad_perm:[0,1,2,3] row_mask:0xf bank_mask:0x1 bound_ctrl:1\n\t"
        "v_mov_b32_dpp v253, v29 quad_perm:[0,1,2,3] row_mask:0xf bank_mask:0x1 bound_ctrl:1\n\t"
        "ds_read_b128 v[48:51], v45 offset:33792\n\t"
        "ds_read_b128 v[52:55], v45 offset:34816\n\t"
        "v_smfmac_f32_16x16x64_bf16 v[36:39], v[96:99], v[56:63], v44\n\t"
        "v_mov_b32_dpp v254, v30 quad_perm:[0,1,2,3] row_mask:0xf bank_mask:0x1 bound_ctrl:1\n\t"
        "v_mov_b32_dpp v255, v31 quad_perm:[0,1,2,3] row_mask:0xf bank_mask:0x1 bound_ctrl:1\n\t"
        "ds_read_b128 v[56:59], v45 offset:8192\n\t"
        "ds_read_b128 v[60:63], v45 offset:35840\n\t"
        "v_smfmac_f32_16x16x64_bf16 v[40:43], v[96:99], v[64:71], v44\n\t"
        "v_max3_u32 v46, v46, v29, v30\n\t"
        "v_max_u32 v46, v46, v31\n\t"
        "ds_read_b128 v[64:67], v45 offset:9216\n\t"
        "ds_read_b128 v[68:71], v45 offset:10240\n\t"
        "v_smfmac_f32_16x16x64_bf16 v[32:35], v[252:255], v[72:79], v44\n\t"
        "v_mov_b32_dpp v96, v24 row_shl:2 row_mask:0xf bank_mask:0x1 bound_ctrl:1\n\t"
        "v_mov_b32_dpp v97, v25 row_shl:2 row_mask:0xf bank_mask:0x1 bound_ctrl:1\n\t"
        "ds_read_b128 v[72:75], v45 offset:36864\n\t"
        "ds_read_b128 v[76:79], v45 offset:37888\n\t"
        "v_smfmac_f32_16x16x64_bf16 v[36:39], v[252:255], v[80:87], v44\n\t"
        "v_mov_b32_dpp v98, v26 row_shl:2 row_mask:0xf bank_mask:0x1 bound_ctrl:1\n\t"
        "v_mov_b32_dpp v99, v27 row_shl:2 row_mask:0xf bank_mask:0x1 bound_ctrl:1\n\t"
        "ds_read_b128 v[80:83], v45 offset:11264\n\t"
        "ds_read_b128 v[84:87], v45 offset:38912\n\t"
        "v_smfmac_f32_16x16x64_bf16 v[40:43], v[252:255], v[88:95], v44\n\t"
        "v_cmp_eq_u32 vcc, -1, v46\n\t"
        "ds_read_b128 v[88:91], v45 offset:12288\n\t"
        "ds_read_b128 v[92:95], v45 offset:13312\n\t"
        "v_smfmac_f32_16x16x64_bf16 v[32:35], v[96:99], v[220:227], v44\n\t"
        "v_mov_b32_dpp v252, v24 row_shl:4 row_mask:0xf bank_mask:0x1 bound_ctrl:1\n\t"
        "v_mov_b32_dpp v253, v25 row_shl:4 row_mask:0xf bank_mask:0x1 bound_ctrl:1\n\t"
        "ds_read_b128 v[220:223], v45 offset:39936\n\t"
        "ds_read_b128 v[224:227], v45 offset:40960\n\t"
        "v_smfmac_f32_16x16x64_bf16 v[36:39], v[96:99], v[228:235], v44\n\t"
        "v_mov_b32_dpp v254, v26 row_shl:4 row_mask:0xf bank_mask:0x1 bound_ctrl:1\n\t"
        "ds_read_b128 v[228:231], v45 offset:14336\n\t"
        "ds_read_b128 v[232:235], v45 offset:41984\n\t"
        "s_cbranch_vccnz Ly_%=\n\t"
        "Lz_%=:\n\t"
        "v_smfmac_f32_16x16x64_bf16 v[40:43], v[96:99], v[236:243], v44\n\t"
        "v_mov_b32_dpp v255, v27 row_shl:4 row_mask:0xf bank_mask:0x1 bound_ctrl:1\n\t"
        "ds_read_b128 v[236:239], v45 offset:15360\n\t"
        "ds_read_b128 v[240:243], v45 offset:16384\n\t"
        "v_smfmac_f32_16x16x64_bf16 v[32:35], v[252:255], v[244:251], v44\n\t"
        "v_mov_b32_dpp v96, v24 row_shl:6 row_mask:0xf bank_mask:0x1 bound_ctrl:1\n\t"
        "v_mov_b32_dpp v97, v25 row_shl:6 row_mask:0xf bank_mask:0x1 bound_ctrl:1\n\t"
        "ds_read_b128 v[244:247], v45 offset:43008\n\t"
        "ds_read_b128 v[248:251], v45 offset:44032\n\t"
        "s_waitcnt lgkmcnt(15)\n\t"
        "v_smfmac_f32_16x16x64_bf16 v[36:39], v[252:255], v[48:55], v44\n\t"
        "v_mov_b32_dpp v98, v26 row_shl:6 row_mask:0xf bank_mask:0x1 bound_ctrl:1\n\t"
        "ds_read_b128 v[48:51], v45 offset:17408\n\t"
        "ds_read_b128 v[52:55], v45 offset:45056\n\t"
        "v_smfmac_f32_16x16x64_bf16 v[40:43], v[252:255], v[56:63], v44\n\t"
        "v_mov_b32_dpp v99, v27 row_shl:6 row_mask:0xf bank_mask:0x1 bound_ctrl:1\n\t"
        "ds_read_b128 v[56:59], v45 offset:18432\n\t"
        "ds_read_b128 v[60:63], v45 offset:19456\n\t"
        "s_waitcnt lgkmcnt(15)\n\t"
        "v_smfmac_f32_16x16x64_bf16 v[32:35], v[96:99], v[64:71], v44\n\t"
        "v_mov_b32_dpp v252, v24 row_shl:8 row_mask:0xf bank_mask:0x1 bound_ctrl:1\n\t"
        "v_mov_b32_dpp v253, v25 row_shl:8 row_mask:0xf bank_mask:0x1 bound_ctrl:1\n\t"
        "ds_read_b128 v[64:67], v45 offset:46080\n\t"
        "ds_read_b128 v[68:71], v45 offset:47104\n\t"
        "v_smfmac_f32_16x16x64_bf16 v[36:39], v[96:99], v[72:79], v44\n\t"
        "v_mov_b32_dpp v254, v26 row_shl:8 row_mask:0xf bank_mask:0x1 bound_ctrl:1\n\t"
        "ds_read_b128 v[72:75], v45 offset:20480\n\t"
        "ds_read_b128 v[76:79], v45 offset:48128\n\t"
        "s_waitcnt lgkmcnt(15)\n\t"
        "v_smfmac_f32_16x16x64_bf16 v[40:43], v[96:99], v[80:87], v44\n\t"
        "v_mov_b32_dpp v255, v27 row_shl:8 row_mask:0xf bank_mask:0x1 bound_ctrl:1\n\t"
        "ds_read_b128 v[80:83], v45 offset:21504\n\t"
        "ds_read_b128 v[84:87], v45 offset:22528\n\t"
        "v_smfmac_f32_16x16x64_bf16 v[32:35], v[252:255], v[88:95], v44\n\t"
        "v_mov_b32_dpp v96, v24 row_shl:10 row_mask:0xf bank_mask:0x1 bound_ctrl:1\n\t"
        "v_mov_b32_dpp v97, v25 row_shl:10 row_mask:0xf bank_mask:0x1 bound_ctrl:1\n\t"
        "ds_read_b128 v[88:91], v45 offset:49152\n\t"
        "ds_read_b128 v[92:95], v45 offset:50176\n\t"
        "s_waitcnt lgkmcnt(15)\n\t"
        "v_smfmac_f32_16x16x64_bf16 v[36:39], v[252:255], v[220:227], v44\n\t"
        "v_mov_b32_dpp v98, v26 row_shl:10 row_mask:0xf bank_mask:0x1 bound_ctrl:1\n\t"
        "ds_read_b128 v[220:223], v45 offset:23552\n\t"
        "ds_read_b128 v[224:227], v45 offset:51200\n\t"
        "v_smfmac_f32_16x16x64_bf16 v[40:43], v[252:255], v[228:235], v44\n\t"
        "v_mov_b32_dpp v99, v27 row_shl:10 row_mask:0xf bank_mask:0x1 bound_ctrl:1\n\t"
        "s_waitcnt lgkmcnt(15)\n\t"
        "s_nop 0\n\t"
        "v_smfmac_f32_16x16x64_bf16 v[32:35], v[96:99], v[236:243], v44\n\t"
        "v_mov_b32_dpp v252, v24 row_shl:12 row_mask:0xf bank_mask:0x1 bound_ctrl:1\n\t"
        "v_mov_b32_dpp v253, v25 row_shl:12 row_mask:0xf bank_mask:0x1 bound_ctrl:1\n\t"
        "s_waitcnt lgkmcnt(14)\n\t"
        "v_smfmac_f32_16x16x64_bf16 v[36:39], v[96:99], v[244:251], v44\n\t"
        "v_mov_b32_dpp v254, v26 row_shl:12 row_mask:0xf bank_mask:0x1 bound_ctrl:1\n\t"
        "s_waitcnt lgkmcnt(12)\n\t"
        "v_smfmac_f32_16x16x64_bf16 v[40:43], v[96:99], v[48:55], v44\n\t"
        "v_mov_b32_dpp v255, v27 row_shl:12 row_mask:0xf bank_mask:0x1 bound_ctrl:1\n\t"
        "s_waitcnt lgkmcnt(10)\n\t"
        "s_nop 0\n\t"
        "v_smfmac_f32_16x16x64_bf16 v[32:35], v[252:255], v[56:63], v44\n\t"
        "v_mov_b32_dpp v96, v24 row_shl:14 row_mask:0xf bank_mask:0x1 bound_ctrl:1\n\t"
        "v_mov_b32_dpp v97, v25 row_shl:14 row_mask:0xf bank_mask:0x1 bound_ctrl:1\n\t"
        "s_waitcnt lgkmcnt(8)\n\t"
        "v_smfmac_f32_16x16x64_bf16 v[36:39], v[252:255], v[64:71], v44\n\t"
        "v_mov_b32_dpp v98, v26 row_shl:14 row_mask:0xf bank_mask:0x1 bound_ctrl:1\n\t"
        "s_waitcnt lgkmcnt(6)\n\t"
        "v_smfmac_f32_16x16x64_bf16 v[40:43], v[252:255], v[72:79], v44\n\t"
        "v_mov_b32_dpp v99, v27 row_shl:14 row_mask:0xf bank_mask:0x1 bound_ctrl:1\n\t"
        "s_waitcnt lgkmcnt(0)\n\t"
        "s_nop 0\n\t"
        "v_smfmac_f32_16x16x64_bf16 v[40:43], v[96:99], v[220:227], v44\n\t"
        "v_readlane_b32 %[sa0], v28, 2\n\t"
        "v_smfmac_f32_16x16x64_bf16 v[32:35], v[96:99], v[80:87], v44\n\t"
        "v_readlane_b32 %[sa1], v29, 2\n\t"
        "v_smfmac_f32_16x16x64_bf16 v[36:39], v[96:99], v[88:95], v44\n\t"
        "s_nop 3\n\t"
        "v_add_f32 v40, v40, v41\n\t"
        "s_nop 0\n\t"
        "v_add_f32 v46, v32, v33\n\t"
        "v_mov_b32_dpp v41, v40 row_shl:8 row_mask:0xf bank_mask:0xf bound_ctrl:1\n\t"
        "v_add_f32 v47, v36, v37\n\t"
        "v_permlane16_swap_b32 v46, v40\n\t"
        "s_nop 0\n\t"
        "v_permlane16_swap_b32 v47, v41\n\t"
        "v_fma_f32 v46, -%[sa0], v40, v46\n\t"
        "v_fma_f32 v47, -%[sa0], v41, v47\n\t"
        "v_mul_f32 v46, %[sa1], v46\n\t"
        "v_mul_f32 v47, %[sa1], v47\n\t"
        "v_mul_f32 v44, 0xbfb8aa3b, v46\n\t"
        "v_exp_f32 v44, v44\n\t"
        "v_mul_f32 v46, v46, v47\n\t"
        "v_add_f32 v44, 1.0, v44\n\t"
        "v_rcp_f32 v44, v44\n\t"
        "s_mov_b32 %[sm], %[sa0]\n\t"
        "s_mov_b32 %[srs], %[sa1]\n\t"
        "v_mul_f32 v46, v46, v44\n\t"
        "v_mov_b32 v60, 0\n\t"
        "v_mov_b32 v61, 0\n\t"
        "v_mov_b32_dpp v47, v46 row_shl:1 row_mask:0xf bank_mask:0xf bound_ctrl:1\n\t"
        "v_cvt_pk_bf16_f32 v46, v46, v47\n\t"
        "v_mov_b32 v62, 0\n\t"
        "v_readlane_b32 %[sa0], v46, 0\n\t"
        "v_mov_b32 v63, 0\n\t"
        "v_readlane_b32 %[sa1], v46, 2\n\t"
        "v_mov_b32 v64, 0\n\t"
        "v_readlane_b32 %[sa2], v46, 4\n\t"
        "v_mov_b32 v65, 0\n\t"
        "v_readlane_b32 %[sa3], v46, 6\n\t"
        "v_mov_b32 v66, 0\n\t"
        "v_readlane_b32 %[sa4], v46, 8\n\t"
        "v_mov_b32 v67, 0\n\t"
        "v_readlane_b32 %[sa5], v46, 10\n\t"
        "v_mov_b32 v68, 0\n\t"
        "v_readlane_b32 %[sa6], v46, 12\n\t"
        "v_readlane_b32 %[sa7], v46, 14\n\t"
        "v_readlane_b32 %[sa8], v46, 16\n\t"
        "v_readlane_b32 %[sa9], v46, 18\n\t"
        "v_readlane_b32 %[sa10], v46, 20\n\t"
        "v_readlane_b32 %[sa11], v46, 22\n\t"
        "v_dot2c_f32_bf16 v60, %[sa0], v112\n\t"
        "v_dot2c_f32_bf16 v61, %[sa0], v116\n\t"
        "v_dot2c_f32_bf16 v62, %[sa0], v113\n\t"
        "v_dot2c_f32_bf16 v63, %[sa0], v117\n\t"
        "v_dot2c_f32_bf16 v60, %[sa1], v120\n\t"
        "v_dot2c_f32_bf16 v61, %[sa1], v124\n\t"
        "v_dot2c_f32_bf16 v62, %[sa1], v121\n\t"
        "v_dot2c_f32_bf16 v63, %[sa1], v125\n\t"
        "v_dot2c_f32_bf16 v60, %[sa2], v128\n\t"
        "v_dot2c_f32_bf16 v61, %[sa2], v132\n\t"
        "v_dot2c_f32_bf16 v62, %[sa2], v129\n\t"
        "v_dot2c_f32_bf16 v63, %[sa2], v133\n\t"
        "v_dot2c_f32_bf16 v60, %[sa3], v136\n\t"
        "v_dot2c_f32_bf16 v61, %[sa3], v140\n\t"
        "v_dot2c_f32_bf16 v62, %[sa3], v137\n\t"
        "v_dot2c_f32_bf16 v63, %[sa3], v141\n\t"
        "v_dot2c_f32_bf16 v60, %[sa4], v144\n\t"
        "v_dot2c_f32_bf16 v61, %[sa4], v148\n\t"
        "v_dot2c_f32_bf16 v62, %[sa4], v145\n\t"
        "v_dot2c_f32_bf16 v63, %[sa4], v149\n\t"
        "v_dot2c_f32_bf16 v60, %[sa5], v152\n\t"
        "v_dot2c_f32_bf16 v61, %[sa5], v156\n\t"
        "v_dot2c_f32_bf16 v62, %[sa5], v153\n\t"
        "v_dot2c_f32_bf16 v63, %[sa5], v157\n\t"
        "v_dot2c_f32_bf16 v60, %[sa6], v160\n\t"
        "v_dot2c_f32_bf16 v61, %[sa6], v164\n\t"
        "v_dot2c_f32_bf16 v62, %[sa6], v161\n\t"
        "v_dot2c_f32_bf16 v63, %[sa6], v165\n\t"
        "v_dot2c_f32_bf16 v60, %[sa7], v168\n\t"
        "v_dot2c_f32_bf16 v61, %[sa7], v172\n\t"
        "v_dot2c_f32_bf16 v62, %[sa7], v169\n\t"
        "v_dot2c_f32_bf16 v63, %[sa7], v173\n\t"
        "v_dot2c_f32_bf16 v60, %[sa8], v176\n\t"
        "v_dot2c_f32_bf16 v61, %[sa8], v180\n\t"
        "v_dot2c_f32_bf16 v62, %[sa8], v177\n\t"
        "v_dot2c_f32_bf16 v63, %[sa8], v181\n\t"
        "v_dot2c_f32_bf16 v60, %[sa9], v184\n\t"
        "v_dot2c_f32_bf16 v61, %[sa9], v188\n\t"
        "v_dot2c_f32_bf16 v62, %[sa9], v185\n\t"
        "v_dot2c_f32_bf16 v63, %[sa9], v189\n\t"
        "v_dot2c_f32_bf16 v60, %[sa10], v192\n\t"
        "v_dot2c_f32_bf16 v61, %[sa10], v196\n\t"
        "v_dot2c_f32_bf16 v62, %[sa10], v193\n\t"
        "v_dot2c_f32_bf16 v63, %[sa10], v197\n\t"
        "v_dot2c_f32_bf16 v60, %[sa11], v200\n\t"
        "v_dot2c_f32_bf16 v61, %[sa11], v204\n\t"
        "v_dot2c_f32_bf16 v62, %[sa11], v201\n\t"
        "v_dot2c_f32_bf16 v63, %[sa11], v205\n\t"
        "v_dot2c_f32_bf16 v64, %[sa0], v114\n\t"
        "v_dot2c_f32_bf16 v65, %[sa0], v118\n\t"
        "v_dot2c_f32_bf16 v66, %[sa0], v115\n\t"
        "v_dot2c_f32_bf16 v67, %[sa0], v119\n\t"
        "v_dot2c_f32_bf16 v68, %[sa0], v208\n\t"
        "v_min_u32 v60, -2, v60\n\t"
        "v_min_u32 v61, -2, v61\n\t"
        "v_min_u32 v62, -2, v62\n\t"
        "v_min_u32 v63, -2, v63\n\t"
        "buffer_store_dwordx4 v[60:63], %[ya], %[ssrd], %[syb] offen sc1\n\t"
        "v_dot2c_f32_bf16 v64, %[sa1], v122\n\t"
        "v_dot2c_f32_bf16 v65, %[sa1], v126\n\t"
        "v_dot2c_f32_bf16 v66, %[sa1], v123\n\t"
        "v_dot2c_f32_bf16 v67, %[sa1], v127\n\t"
        "v_dot2c_f32_bf16 v68, %[sa1], v209\n\t"
        "v_dot2c_f32_bf16 v64, %[sa2], v130\n\t"
        "v_dot2c_f32_bf16 v65, %[sa2], v134\n\t"
        "v_dot2c_f32_bf16 v66, %[sa2], v131\n\t"
        "v_dot2c_f32_bf16 v67, %[sa2], v135\n\t"
        "v_dot2c_f32_bf16 v68, %[sa2], v210\n\t"
        "v_dot2c_f32_bf16 v64, %[sa3], v138\n\t"
        "v_dot2c_f32_bf16 v65, %[sa3], v142\n\t"
        "v_dot2c_f32_bf16 v66, %[sa3], v139\n\t"
        "v_dot2c_f32_bf16 v67, %[sa3], v143\n\t"
        "v_dot2c_f32_bf16 v68, %[sa3], v211\n\t"
        "v_dot2c_f32_bf16 v64, %[sa4], v146\n\t"
        "v_dot2c_f32_bf16 v65, %[sa4], v150\n\t"
        "v_dot2c_f32_bf16 v66, %[sa4], v147\n\t"
        "v_dot2c_f32_bf16 v67, %[sa4], v151\n\t"
        "v_dot2c_f32_bf16 v68, %[sa4], v212\n\t"
        "v_dot2c_f32_bf16 v64, %[sa5], v154\n\t"
        "v_dot2c_f32_bf16 v65, %[sa5], v158\n\t"
        "v_dot2c_f32_bf16 v66, %[sa5], v155\n\t"
        "v_dot2c_f32_bf16 v67, %[sa5], v159\n\t"
        "v_dot2c_f32_bf16 v68, %[sa5], v213\n\t"
        "v_dot2c_f32_bf16 v64, %[sa6], v162\n\t"
        "v_dot2c_f32_bf16 v65, %[sa6], v166\n\t"
        "v_dot2c_f32_bf16 v66, %[sa6], v163\n\t"
        "v_dot2c_f32_bf16 v67, %[sa6], v167\n\t"
        "v_dot2c_f32_bf16 v68, %[sa6], v214\n\t"
        "v_dot2c_f32_bf16 v64, %[sa7], v170\n\t"
        "v_dot2c_f32_bf16 v65, %[sa7], v174\n\t"
        "v_dot2c_f32_bf16 v66, %[sa7], v171\n\t"
        "v_dot2c_f32_bf16 v67, %[sa7], v175\n\t"
        "v_dot2c_f32_bf16 v68, %[sa7], v215\n\t"
        "v_dot2c_f32_bf16 v64, %[sa8], v178\n\t"
        "v_dot2c_f32_bf16 v65, %[sa8], v182\n\t"
        "v_dot2c_f32_bf16 v66, %[sa8], v179\n\t"
        "v_dot2c_f32_bf16 v67, %[sa8], v183\n\t"
        "v_dot2c_f32_bf16 v68, %[sa8], v216\n\t"
        "v_dot2c_f32_bf16 v64, %[sa9], v186\n\t"
        "v_dot2c_f32_bf16 v65, %[sa9], v190\n\t"
        "v_dot2c_f32_bf16 v66, %[sa9], v187\n\t"
        "v_dot2c_f32_bf16 v67, %[sa9], v191\n\t"
        "v_dot2c_f32_bf16 v68, %[sa9], v217\n\t"
        "v_dot2c_f32_bf16 v64, %[sa10], v194\n\t"
        "v_dot2c_f32_bf16 v65, %[sa10], v198\n\t"
        "v_dot2c_f32_bf16 v66, %[sa10], v195\n\t"
        "v_dot2c_f32_bf16 v67, %[sa10], v199\n\t"
        "v_dot2c_f32_bf16 v68, %[sa10], v218\n\t"
        "v_dot2c_f32_bf16 v64, %[sa11], v202\n\t"
        "v_dot2c_f32_bf16 v65, %[sa11], v206\n\t"
        "v_dot2c_f32_bf16 v66, %[sa11], v203\n\t"
        "v_dot2c_f32_bf16 v67, %[sa11], v207\n\t"
        "v_dot2c_f32_bf16 v68, %[sa11], v219\n\t"
        "v_min_u32 v64, -2, v64\n\t"
        "v_min_u32 v65, -2, v65\n\t"
        "v_min_u32 v66, -2, v66\n\t"
        "v_min_u32 v67, -2, v67\n\t"
        "v_min_u32 v68, -2, v68\n\t"
        "buffer_store_dwordx4 v[64:67], %[ya], %[ssrd], %[syc] offen sc1\n\t"
        "buffer_store_dword v68, %[ye], %[ssrd], %[syb] offen sc1\n\t"
        "v_add_u32 v45, 157472, %[c0]\n\t"
        "ds_write_b128 v45, v[24:27]\n\t"
        "s_mov_b32 exec_lo, 0x30003\n\t"
        "s_mov_b32 exec_hi, 0x30003\n\t"
        "ds_write_b128 v45, v[28:31] offset:1024\n\t"
        "v_mov_b32 v44, 153984\n\t"
        "v_mov_b32 v46, 1\n\t"
        "s_mov_b64 exec, 1\n\t"
        "ds_add_u32 v44, v46 offset:108\n\t"
        "s_mov_b64 exec, -1\n\t"
        "s_branch Le_%=\n\t"
        "Ly_%=:\n\t"
        "s_cmp_lt_i32 %[scn], 1\n\t"
        "s_cbranch_scc1 Lz_%=\n\t"
        "s_sub_u32 %[scn], %[scn], 16\n\t"
        "s_mov_b32 %[sre], 1\n\t"
        "s_nop 7\n\t"
        "s_nop 7\n\t"
        "s_waitcnt lgkmcnt(0)\n\t"
        "s_branch Ls_%=\n\t"
        "Lr_%=:\n\t"
        "ds_read_b64_tr_b16 v[64:65], v111 offset:32\n\t"
        "ds_read_b64_tr_b16 v[66:67], v111 offset:416\n\t"
        "ds_read_b64_tr_b16 v[68:69], v111 offset:1568\n\t"
        "ds_read_b64_tr_b16 v[70:71], v111 offset:1952\n\t"
        "ds_read_b64_tr_b16 v[72:73], v110 offset:24576\n\t"
        "ds_read_b64_tr_b16 v[74:75], v110 offset:24960\n\t"
        "ds_read_b64_tr_b16 v[76:77], v110 offset:26112\n\t"
        "ds_read_b64_tr_b16 v[78:79], v110 offset:26496\n\t"
        "ds_read_b64_tr_b16 v[80:81], v110 offset:52224\n\t"
        "ds_read_b64_tr_b16 v[82:83], v110 offset:52608\n\t"
        "ds_read_b64_tr_b16 v[84:85], v110 offset:53760\n\t"
        "ds_read_b64_tr_b16 v[86:87], v110 offset:54144\n\t"
        "ds_read_b64_tr_b16 v[88:89], v111 offset:24608\n\t"
        "ds_read_b64_tr_b16 v[90:91], v111 offset:24992\n\t"
        "ds_read_b64_tr_b16 v[92:93], v111 offset:26144\n\t"
        "ds_read_b64_tr_b16 v[94:95], v111 offset:26528\n\t"
        "ds_read_b64_tr_b16 v[220:221], v110 offset:3072\n\t"
        "ds_read_b64_tr_b16 v[222:223], v110 offset:3456\n\t"
        "ds_read_b64_tr_b16 v[224:225], v110 offset:4608\n\t"
        "ds_read_b64_tr_b16 v[226:227], v110 offset:4992\n\t"
        "ds_read_b64_tr_b16 v[228:229], v110 offset:30720\n\t"
        "ds_read_b64_tr_b16 v[230:231], v110 offset:31104\n\t"
        "ds_read_b64_tr_b16 v[232:233], v110 offset:32256\n\t"
        "ds_read_b64_tr_b16 v[234:235], v110 offset:32640\n\t"
        "ds_read_b64_tr_b16 v[236:237], v111 offset:3104\n\t"
        "ds_read_b64_tr_b16 v[238:239], v111 offset:3488\n\t"
        "ds_read_b64_tr_b16 v[240:241], v111 offset:4640\n\t"
        "ds_read_b64_tr_b16 v[242:243], v111 offset:5024\n\t"
        "ds_read_b128 v[244:247], v45 offset:6144\n\t"
        "ds_read_b128 v[248:251], v45 offset:7168\n\t"
        "s_waitcnt vmcnt(0)\n\t"
        "ds_read_b64_tr_b16 v[48:49], v110 offset:0\n\t"
        "ds_read_b64_tr_b16 v[50:51], v110 offset:384\n\t"
        "ds_read_b64_tr_b16 v[52:53], v110 offset:1536\n\t"
        "ds_read_b64_tr_b16 v[54:55], v110 offset:1920\n\t"
        "ds_read_b64_tr_b16 v[56:57], v110 offset:27648\n\t"
        "ds_read_b64_tr_b16 v[58:59], v110 offset:28032\n\t"
        "ds_read_b64_tr_b16 v[60:61], v110 offset:29184\n\t"
        "ds_read_b64_tr_b16 v[62:63], v110 offset:29568\n\t"
        "s_waitcnt lgkmcnt(0)\n\t"
        "s_branch Lb_%=\n\t"
        "Le_%=:\n\t"
        : [sa0] "=&s"(ap[0]), [sa1] "=&s"(ap[1]), [sa2] "=&s"(ap[2]), [sa3] "=&s"(ap[3]), [sa4] "=&s"(ap[4]), [sa5] "=&s"(ap[5]), [sa6] "=&s"(ap[6]), [sa7] "=&s"(ap[7]), [sa8] "=&s"(ap[8]), [sa9] "=&s"(ap[9]), [sa10] "=&s"(ap[10]), [sa11] "=&s"(ap[11]), [sm] "=&s"(m), [srs] "=&s"(rs), [sre] "+s"(re), [scn] "+s"(cn)
        : [ssrd] "s"(srd), [scr] "s"(cr), [c0] "v"(c0), [c1] "v"(c1), [ya] "v"(ya), [ye] "v"(ye),
          [syb] "s"(yb), [syc] "s"(yb + 262144), [spar] "s"(0xAAAAAAAAAAAAAAAAull)
        : "memory", "scc", "vcc", "v24", "v25", "v26", "v27", "v28", "v29", "v30", "v31", "v32", "v33", "v34", "v35", "v36", "v37", "v38", "v39", "v40", "v41", "v42", "v43", "v44", "v45", "v46", "v47");
}

DEV void poll_stats(u32x4 srd, u32 va, u32 so, float (&o)[3], u32& cn) {
    u32 t;
    asm volatile(
        "s_nop 4\n\t"
        "buffer_load_dwordx4 v[24:27], %[va], %[ssrd], %[so] offen offset:0 sc1\n\t"
        "buffer_load_dwordx4 v[28:31], %[va], %[ssrd], %[so] offen offset:1024 sc1\n\t"
        "buffer_load_dwordx4 v[32:35], %[va], %[ssrd], %[so] offen offset:256 sc1\n\t"
        "buffer_load_dwordx4 v[36:39], %[va], %[ssrd], %[so] offen offset:1280 sc1\n\t"
        "buffer_load_dwordx4 v[40:43], %[va], %[ssrd], %[so] offen offset:512 sc1\n\t"
        "buffer_load_dwordx4 v[44:47], %[va], %[ssrd], %[so] offen offset:1536 sc1\n\t"
        "buffer_load_dwordx4 v[48:51], %[va], %[ssrd], %[so] offen offset:768 sc1\n\t"
        "buffer_load_dwordx4 v[68:71], %[va], %[ssrd], %[so] offen offset:1792 sc1\n\t"
        "Lp_%=:\n\t"
        "s_waitcnt vmcnt(0)\n\t"
        "v_max3_u32 v27, v24, v25, v26\n\t"
        "v_max3_u32 v31, v28, v29, v30\n\t"
        "v_max3_u32 v35, v32, v33, v34\n\t"
        "v_max3_u32 v39, v36, v37, v38\n\t"
        "v_max3_u32 v43, v40, v41, v42\n\t"
        "v_max3_u32 v47, v44, v45, v46\n\t"
        "v_max3_u32 v51, v48, v49, v50\n\t"
        "v_max3_u32 v71, v68, v69, v70\n\t"
        "v_add_f32 v24, v24, v28\n\t"
        "v_add_f32 v32, v32, v36\n\t"
        "v_add_f32 v40, v40, v44\n\t"
        "v_add_f32 v48, v48, v68\n\t"
        "v_add_f32 v25, v25, v29\n\t"
        "v_add_f32 v33, v33, v37\n\t"
        "v_add_f32 v41, v41, v45\n\t"
        "v_add_f32 v49, v49, v69\n\t"
        "v_add_f32 v26, v26, v30\n\t"
        "v_add_f32 v34, v34, v38\n\t"
        "v_add_f32 v42, v42, v46\n\t"
        "v_add_f32 v50, v50, v70\n\t"
        "v_max3_u32 %[t], v27, v31, v35\n\t"
        "v_max3_u32 v39, v39, v43, v47\n\t"
        "v_max3_u32 %[t], %[t], v51, v71\n\t"
        "v_max_u32 %[t], %[t], v39\n\t"
        "v_cmp_eq_u32 vcc, -1, %[t]\n\t"
        "v_add_f32 v24, v24, v32\n\t"
        "v_add_f32 v40, v40, v48\n\t"
        "v_add_f32 v25, v25, v33\n\t"
        "v_add_f32 v41, v41, v49\n\t"
        "v_add_f32 v26, v26, v34\n\t"
        "v_add_f32 v42, v42, v50\n\t"
        "s_cbranch_vccz Lf0_%=\n\t"
        "buffer_load_dwordx4 v[24:27], %[va], %[ssrd], %[so] offen offset:0 sc1\n\t"
        "buffer_load_dwordx4 v[28:31], %[va], %[ssrd], %[so] offen offset:1024 sc1\n\t"
        "buffer_load_dwordx4 v[32:35], %[va], %[ssrd], %[so] offen offset:256 sc1\n\t"
        "buffer_load_dwordx4 v[36:39], %[va], %[ssrd], %[so] offen offset:1280 sc1\n\t"
        "buffer_load_dwordx4 v[40:43], %[va], %[ssrd], %[so] offen offset:512 sc1\n\t"
        "buffer_load_dwordx4 v[44:47], %[va], %[ssrd], %[so] offen offset:1536 sc1\n\t"
        "buffer_load_dwordx4 v[48:51], %[va], %[ssrd], %[so] offen offset:768 sc1\n\t"
        "buffer_load_dwordx4 v[68:71], %[va], %[ssrd], %[so] offen offset:1792 sc1\n\t"
        "s_sub_u32 %[scn], %[scn], 1\n\t"
        "s_cmp_lg_u32 %[scn], 0\n\t"
        "s_cbranch_scc1 Lp_%=\n\t"
        "s_waitcnt vmcnt(0)\n\t"
        "Lf0_%=:\n\t"
        "v_add_f32 %[o0], v24, v40\n\t"
        "v_add_f32 %[o1], v25, v41\n\t"
        "v_add_f32 %[o2], v26, v42\n\t"
        "Le_%=:\n\t"
        : [o0] "=&v"(o[0]), [o1] "=&v"(o[1]), [o2] "=&v"(o[2]), [t] "=&v"(t), [scn] "+s"(cn)
        : [ssrd] "s"(srd), [va] "v"(va), [so] "s"(so)
        : "memory", "scc", "vcc", "v24", "v25", "v26", "v27", "v28", "v29", "v30", "v31", "v32", "v33", "v34", "v35", "v36", "v37", "v38", "v39", "v40", "v41", "v42", "v43", "v44", "v45", "v46", "v47");
}
DEV void poll_stats1(u32x4 srd, u32 so, float (&o)[3], u32& cn) {
    u32 t;
    asm volatile(
        "s_nop 4\n\t"
        "buffer_load_dwordx4 v[24:27], v107, %[ssrd], %[so] offen sc1\n\t"
        "buffer_load_dwordx4 v[28:31], v107, %[ssrd], %[so] offen offset:1024 sc1\n\t"
        "Lp_%=:\n\t"
        "s_waitcnt vmcnt(0)\n\t"
        "v_max3_u32 %[t], v24, v25, v26\n\t"
        "v_max3_u32 %[t], %[t], v27, v28\n\t"
        "v_max3_u32 %[t], %[t], v29, v30\n\t"
        "v_max_u32 %[t], %[t], v31\n\t"
        "v_cmp_eq_u32 vcc, -1, %[t]\n\t"
        "s_cbranch_vccz Lf0_%=\n\t"
        "buffer_load_dwordx4 v[24:27], v107, %[ssrd], %[so] offen sc1\n\t"
        "buffer_load_dwordx4 v[28:31], v107, %[ssrd], %[so] offen offset:1024 sc1\n\t"
        "s_sub_u32 %[scn], %[scn], 1\n\t"
        "s_cmp_lg_u32 %[scn], 0\n\t"
        "s_cbranch_scc1 Lp_%=\n\t"
        "s_waitcnt vmcnt(0)\n\t"
        "Lf0_%=:\n\t"
        "v_add_f32 %[o0], v24, v28\n\t"
        "v_add_f32 %[o1], v25, v29\n\t"
        "v_add_f32 %[o2], v26, v30\n\t"
        "Le_%=:\n\t"
        : [o0] "=&v"(o[0]), [o1] "=&v"(o[1]), [o2] "=&v"(o[2]), [t] "=&v"(t), [scn] "+s"(cn)
        : [ssrd] "s"(srd), [so] "s"(so)
        : "memory", "scc", "vcc", "v24", "v25", "v26", "v27", "v28", "v29", "v30", "v31");
}
DEV void poll_keys2(u32x4 srd, u32 so, u32 tag, u32& o, u32& cn) {
    u32 t;
    asm volatile(
        "s_nop 4\n\t"
        "buffer_load_dwordx4 v[48:51], v107, %[ssrd], %[so] offen sc1\n\t"
        "Lp_%=:\n\t"
        "s_waitcnt vmcnt(0)\n\t"
        "v_xor_b32 %[t], %[stag], v49\n\t"
        "v_xor_b32 %[o0], %[stag], v51\n\t"
        "v_or_b32 %[t], %[t], %[o0]\n\t"
        "v_cmp_ne_u32 vcc, 0, %[t]\n\t"
        "s_cbranch_vccz Lf0_%=\n\t"
        "buffer_load_dwordx4 v[48:51], v107, %[ssrd], %[so] offen sc1\n\t"
        "s_sub_u32 %[scn], %[scn], 1\n\t"
        "s_cmp_lg_u32 %[scn], 0\n\t"
        "s_cbranch_scc1 Lp_%=\n\t"
        "s_waitcnt vmcnt(0)\n\t"
        "Lf0_%=:\n\t"
        "v_max_u32 %[o0], v48, v50\n\t"
        "Le_%=:\n\t"
        : [o0] "=&v"(o), [t] "=&v"(t), [scn] "+s"(cn)
        : [ssrd] "s"(srd), [so] "s"(so), [stag] "s"(tag)
        : "memory", "scc", "vcc");
}
DEV void poll_keys(u32x4 srd, u32 so, u32 tag, u64 lm, u32& o, u32& cn) {
    u32 t;
    asm volatile(
        "s_nop 4\n\t"
        "buffer_load_dwordx4 v[96:99], v107, %[ssrd], %[so] offen sc1\n\t"
        "v_add_u32 %[t], 98048, v107\n\t"
        "ds_read_b128 v[244:247], %[t] offset:6144\n\t"
        "ds_read_b128 v[248:251], %[t] offset:7168\n\t"
        "s_waitcnt lgkmcnt(0)\n\t"
        "Lp_%=:\n\t"
        "s_waitcnt vmcnt(0)\n\t"
        "v_xor_b32 %[t], %[stag], v97\n\t"
        "v_xor_b32 %[o0], %[stag], v99\n\t"
        "v_or_b32 %[t], %[t], %[o0]\n\t"
        "v_max_u32 %[o0], v96, v98\n\t"
        "v_cndmask_b32 %[t], %[t], 0, %[slm]\n\t"
        "v_cmp_ne_u32 vcc, 0, %[t]\n\t"
        "s_cbranch_vccz Lf0_%=\n\t"
        "buffer_load_dwordx4 v[96:99], v107, %[ssrd], %[so] offen sc1\n\t"
        "s_sub_u32 %[scn], %[scn], 1\n\t"
        "s_cmp_lg_u32 %[scn], 0\n\t"
        "s_cbranch_scc1 Lp_%=\n\t"
        "s_waitcnt vmcnt(0)\n\t"
        "Lf0_%=:\n\t"
        "v_cndmask_b32 %[o0], %[o0], 0, %[slm]\n\t"
        "Le_%=:\n\t"
        : [o0] "=&v"(o), [t] "=&v"(t), [scn] "+s"(cn)
        : [ssrd] "s"(srd), [so] "s"(so), [stag] "s"(tag), [slm] "s"(lm)
        : "memory", "scc", "vcc");
}
DEV void poll_flags(u32x4 srd, u32 fo, u32 tag, u32& cn) {
    u32 t, d0;
    asm volatile(
        "s_nop 4\n\t"
        "s_sleep 5\n\t"
        "buffer_load_dword v48, %[fo], %[ssrd], 0 offen sc1\n\t"
        "Lp_%=:\n\t"
        "s_waitcnt vmcnt(0)\n\t"
        "v_cmp_ne_u32 vcc, %[stag], v48\n\t"
        "s_cbranch_vccz Lf0_%=\n\t"
        "buffer_load_dword v48, %[fo], %[ssrd], 0 offen sc1\n\t"
        "s_sub_u32 %[scn], %[scn], 1\n\t"
        "s_cmp_lg_u32 %[scn], 0\n\t"
        "s_cbranch_scc1 Lp_%=\n\t"
        "s_waitcnt vmcnt(0)\n\t"
        "Lf0_%=:\n\t"
        "s_nop 0\n\t"
        "Le_%=:\n\t"
        : [d0] "=&s"(d0), [t] "=&v"(t), [scn] "+s"(cn)
        : [ssrd] "s"(srd), [fo] "v"(fo), [stag] "s"(tag)
        : "memory", "scc", "vcc");
}
DEV void poll_flagh(u32x4 srd, u32 fo, u32 tag, u32& cn) {
    u32 t, d0;
    asm volatile(
        "s_nop 4\n\t"
        "buffer_load_dword v48, %[fo], %[ssrd], 0 offen sc1\n\t"
        "Lp_%=:\n\t"
        "s_waitcnt vmcnt(0)\n\t"
        "v_cmp_ne_u32 vcc, %[stag], v48\n\t"
        "s_cbranch_vccz Lf0_%=\n\t"
        "buffer_load_dword v48, %[fo], %[ssrd], 0 offen sc1\n\t"
        "s_sub_u32 %[scn], %[scn], 1\n\t"
        "s_cmp_lg_u32 %[scn], 0\n\t"
        "s_cbranch_scc1 Lp_%=\n\t"
        "s_waitcnt vmcnt(0)\n\t"
        "Lf0_%=:\n\t"
        "s_nop 0\n\t"
        "Le_%=:\n\t"
        : [d0] "=&s"(d0), [t] "=&v"(t), [scn] "+s"(cn)
        : [ssrd] "s"(srd), [fo] "v"(fo), [stag] "s"(tag)
        : "memory", "scc", "vcc");
}
DEV void reduce(u32x4 srd, u32x4 out, u32 va, u32 so, int xa, float m, float rs, float sg, u32 xo, u32 oo, int fa, u32& ab, u32& cn) {
    u32 t, n;
    asm volatile(
        "ds_read_b32 v78, %[xa]\n\t"
        "v_mov_b32 v79, %[sm]\n\t"
        "v_mov_b32 v76, %[sg]\n\t"
        "v_mov_b32 v77, %[sg]\n\t"
        "v_mul_f32 v79, %[srs], -v79\n\t"
        "s_cmp_eq_u32 %[sab], 2\n\t"
        "s_cselect_b32 %[sn], 2, 1\n\t"
        "Lw_%=:\n\t"
        "ds_read_b32 %[t], %[fa]\n\t"
        "s_sleep 1\n\t"
        "s_sub_u32 %[sn], %[sn], 1\n\t"
        "s_waitcnt lgkmcnt(0)\n\t"
        "v_cmp_eq_u32 vcc, %[sab], %[t]\n\t"
        "s_cbranch_vccnz Le_%=\n\t"
        "s_cbranch_scc0 Lw_%=\n\t"
        "Lp_%=:\n\t"
        "buffer_load_dwordx4 v[48:51], %[va], %[ssrd], %[so0] offen sc1\n\t"
        "buffer_load_dwordx4 v[52:55], %[va], %[ssrd], %[so1] offen sc1\n\t"
        "buffer_load_dwordx4 v[56:59], %[va], %[ssrd], %[so2] offen sc1\n\t"
        "buffer_load_dwordx4 v[60:63], %[va], %[ssrd], %[so3] offen sc1\n\t"
        "v_lshlrev_b32 v72, 16, v78\n\t"
        "v_and_b32 v73, 0xffff0000, v78\n\t"
        "v_fma_f32 v74, v72, %[srs], v79\n\t"
        "v_fma_f32 v75, v73, %[srs], v79\n\t"
        "v_cvt_pk_bf16_f32 v75, v74, v75\n\t"
        "v_lshlrev_b32 v74, 16, v75\n\t"
        "v_and_b32 v75, 0xffff0000, v75\n\t"
        "s_cmp_eq_u32 %[sab], 2\n\t"
        "s_cselect_b32 %[sn], 3, 0\n\t"
        "Lx_%=:\n\t"
        "ds_read_b32 %[t], %[fa]\n\t"
        "s_sleep 1\n\t"
        "s_sub_u32 %[sn], %[sn], 1\n\t"
        "s_waitcnt lgkmcnt(0)\n\t"
        "v_cmp_eq_u32 vcc, %[sab], %[t]\n\t"
        "s_cbranch_vccnz Le_%=\n\t"
        "s_cbranch_scc0 Lx_%=\n\t"
        "s_waitcnt vmcnt(0)\n\t"
        "ds_read_b32 %[t], %[fa]\n\t"
        ".long 0xd3b24040, 0x18026930 ; v_pk_add_f32 v(64:65), v(48:49), v(52:53)\n\t"
        ".long 0xd3b24042, 0x18026d32 ; v_pk_add_f32 v(66:67), v(50:51), v(54:55)\n\t"
        ".long 0xd3b24044, 0x18027938 ; v_pk_add_f32 v(68:69), v(56:57), v(60:61)\n\t"
        ".long 0xd3b24046, 0x18027d3a ; v_pk_add_f32 v(70:71), v(58:59), v(62:63)\n\t"
        ".long 0xd3b24040, 0x18028940 ; v_pk_add_f32 v(64:65), v(64:65), v(68:69)\n\t"
        ".long 0xd3b24042, 0x18028d42 ; v_pk_add_f32 v(66:67), v(66:67), v(70:71)\n\t"
        "s_nop 1\n\t"
        "v_permlane32_swap_b32 v64, v66\n\t"
        "v_permlane32_swap_b32 v65, v67\n\t"
        ".long 0xd3b24040, 0x18028540 ; v_pk_add_f32 v(64:65), v(64:65), v(66:67)\n\t"
        "v_cmp_u_f32 vcc, v64, v65\n\t"
        "s_nop 0\n\t"
        "v_add_f32_dpp v64, v64, v64 row_ror:8 row_mask:0xf bank_mask:0xf\n\t"
        "v_add_f32_dpp v65, v65, v65 row_ror:8 row_mask:0xf bank_mask:0xf\n\t"
        "s_cbranch_vccnz Ln_%=\n\t"
        "Lf_%=:\n\t"
        "v_add_f32_dpp v64, v64, v64 row_ror:4 row_mask:0xf bank_mask:0xf\n\t"
        "v_add_f32_dpp v65, v65, v65 row_ror:4 row_mask:0xf bank_mask:0xf\n\t"
        "s_nop 0\n\t"
        "v_add_f32_dpp v64, v64, v64 row_ror:2 row_mask:0xf bank_mask:0xf\n\t"
        "v_add_f32_dpp v65, v65, v65 row_ror:2 row_mask:0xf bank_mask:0xf\n\t"
        "s_waitcnt lgkmcnt(0)\n\t"
        "v_add_f32_dpp v64, v64, v64 row_ror:1 row_mask:0xf bank_mask:0xf\n\t"
        "v_add_f32_dpp v65, v65, v65 row_ror:1 row_mask:0xf bank_mask:0xf\n\t"
        "v_readfirstlane_b32 %[sn], %[t]\n\t"
        "v_add_f32_dpp v64, v64, v64 row_bcast:15 row_mask:0xa bank_mask:0xf\n\t"
        "v_add_f32_dpp v65, v65, v65 row_bcast:15 row_mask:0xa bank_mask:0xf\n\t"
        "v_cvt_pk_bf16_f32 v66, v64, v65\n\t"
        "v_lshlrev_b32 v64, 16, v66\n\t"
        "v_and_b32 v65, 0xffff0000, v66\n\t"
        ".long 0xd3b14040, 0x18029940 ; v_pk_mul_f32 v(64:65), v(64:65), v(76:77)\n\t"
        "v_cvt_pk_bf16_f32 v66, v64, v65\n\t"
        "v_lshlrev_b32 v64, 16, v66\n\t"
        "v_and_b32 v65, 0xffff0000, v66\n\t"
        ".long 0xd3b24040, 0x18029540 ; v_pk_add_f32 v(64:65), v(64:65), v(74:75)\n\t"
        "s_or_b32 %[sn], %[sn], %[sab]\n\t"
        "v_cvt_pk_bf16_f32 v66, v64, v65\n\t"
        "v_lshlrev_b32 v64, 16, v66\n\t"
        "v_and_b32 v65, 0xffff0000, v66\n\t"
        ".long 0xd3b24040, 0x18029140 ; v_pk_add_f32 v(64:65), v(64:65), v(72:73)\n\t"
        "s_bitcmp1_b32 %[sn], 0\n\t"
        "v_cvt_pk_bf16_f32 v66, v64, v65\n\t"
        "s_cbranch_scc0 Lg_%=\n\t"
        "Lh_%=:\n\t"
        "buffer_store_dword v66, %[oo], %[sout], 0 offen\n\t"
        "v_min_u32 v67, -2, v66\n\t"
        "buffer_store_dword v67, %[xo], %[ssrd], 0 offen sc1\n\t"
        "s_mov_b32 %[sab], 0\n\t"
        "s_branch Le_%=\n\t"
        "Ln_%=:\n\t"
        "v_max3_u32 v48, v48, v49, v50\n\t"
        "v_max3_u32 v48, v48, v51, v52\n\t"
        "v_max3_u32 v48, v48, v53, v54\n\t"
        "v_max3_u32 v48, v48, v55, v56\n\t"
        "v_max3_u32 v48, v48, v57, v58\n\t"
        "v_max3_u32 v48, v48, v59, v60\n\t"
        "v_max3_u32 v48, v48, v61, v62\n\t"
        "v_max_u32 v48, v48, v63\n\t"
        "v_cmp_eq_u32 vcc, -1, v48\n\t"
        "s_cbranch_vccz Lf_%=\n\t"
        "s_cmp_lt_i32 %[scn], 1\n\t"
        "s_cbranch_scc1 Lf_%=\n\t"
        "s_sub_u32 %[scn], %[scn], 16\n\t"
        "s_branch Lp_%=\n\t"
        "Lg_%=:\n\t"
        "ds_read_b32 %[t], %[fa]\n\t"
        "s_cmp_lt_i32 %[scn], 1\n\t"
        "s_cbranch_scc1 Lh_%=\n\t"
        "s_sub_u32 %[scn], %[scn], 1\n\t"
        "s_waitcnt lgkmcnt(0)\n\t"
        "v_readfirstlane_b32 %[sn], %[t]\n\t"
        "s_cmp_eq_u32 %[sn], 1\n\t"
        "s_cbranch_scc1 Lh_%=\n\t"
        "s_cmp_eq_u32 %[sn], 0\n\t"
        "s_cbranch_scc1 Lg_%=\n\t"
        "Le_%=:\n\t"
        "s_waitcnt lgkmcnt(0)\n\t"
        : [t] "=&v"(t), [sn] "=&s"(n), [sab] "+s"(ab), [scn] "+s"(cn)
        : [ssrd] "s"(srd), [sout] "s"(out), [va] "v"(va), [xa] "v"(xa), [sm] "s"(m), [srs] "s"(rs), [sg] "v"(sg), [xo] "v"(xo), [oo] "v"(oo), [fa] "v"(fa), [so0] "s"(so + 0), [so1] "s"(so + 4096), [so2] "s"(so + 8192), [so3] "s"(so + 12288)
        : "memory", "scc", "vcc");
}
DEV void poll_row(u32x4 srd, u32 v1, u32 v2, u32 (&o)[6], u32& cn) {
    u32 t;
    asm volatile(
        "s_nop 4\n\t"
        "buffer_load_dwordx2 v[48:49], %[v1], %[ssrd], 0 offen sc1\n\t"
        "buffer_load_dwordx2 v[50:51], %[v1], %[ssrd], 0 offen offset:2048 sc1\n\t"
        "buffer_load_dwordx2 v[52:53], %[v2], %[ssrd], 0 offen sc1\n\t"
        "Lp_%=:\n\t"
        "s_waitcnt vmcnt(0)\n\t"
        "v_max3_u32 %[t], v48, v49, v50\n\t"
        "v_max3_u32 %[t], %[t], v51, v52\n\t"
        "v_max_u32 %[t], %[t], v53\n\t"
        "v_cmp_eq_u32 vcc, -1, %[t]\n\t"
        "s_cbranch_vccz Lf0_%=\n\t"
        "buffer_load_dwordx2 v[48:49], %[v1], %[ssrd], 0 offen sc1\n\t"
        "buffer_load_dwordx2 v[50:51], %[v1], %[ssrd], 0 offen offset:2048 sc1\n\t"
        "buffer_load_dwordx2 v[52:53], %[v2], %[ssrd], 0 offen sc1\n\t"
        "s_sub_u32 %[scn], %[scn], 1\n\t"
        "s_cmp_lg_u32 %[scn], 0\n\t"
        "s_cbranch_scc1 Lp_%=\n\t"
        "s_waitcnt vmcnt(0)\n\t"
        "Lf0_%=:\n\t"
        "v_mov_b32 %[o0], v48\n\t"
        "v_mov_b32 %[o1], v49\n\t"
        "v_mov_b32 %[o2], v50\n\t"
        "v_mov_b32 %[o3], v51\n\t"
        "v_mov_b32 %[o4], v52\n\t"
        "v_mov_b32 %[o5], v53\n\t"
        "Le_%=:\n\t"
        : [o0] "=&v"(o[0]), [o1] "=&v"(o[1]), [o2] "=&v"(o[2]), [o3] "=&v"(o[3]), [o4] "=&v"(o[4]), [o5] "=&v"(o[5]), [t] "=&v"(t), [scn] "+s"(cn)
        : [ssrd] "s"(srd), [v1] "v"(v1), [v2] "v"(v2)
        : "memory", "scc", "vcc");
}

DEV int ffn_slice(int b, int& nu) {
    const int j = b >> 3, t = j - 20, t3 = (t * 43) >> 7, r = t - 3 * t3;
    nu = (j >= 20 && r < 2) ? 24 : 16;
    return (b & 7) * 576 + (j < 20 ? j * 16 : 320 + t3 * 64 + r * 24);
}
DEV void gu_dma(const u16* wg, const u16* wu, int lane, int b, int i0, int i1) {
    int nu;
    const int f0 = ffn_slice(b, nu);
    u32 gv[3];
#pragma unroll
    for (int m = 0; m < 3; ++m) {
        const int q = 64 * m + lane, sg = (q * 171) >> 9, part = q - 3 * sg;
        const int kin = 32 * ((sg >> 1) & 1) + 8 * (sg >> 4) + (sg & 5) + 2 * ((sg >> 3) & 1);
        gv[m] = (u32)kin * 9216u + f0 * 2u + ((part == 2 && nu == 16) ? 16u : part * 16u);
    }
#pragma nounroll
    for (int i = i0; i < i1; ++i) {
        int gi = i + 2; if (i0 == 0) { gi += (0xC840 >> (((b >> 3) & 3) * 4)) & 15; if (gi >= 17) gi -= 15; } else if (gi >= 18) gi -= 18;
        const u32x4 sd = mk_srd(gi < 9 ? wg : wu, 5308416);
        asm volatile("" : "+s"(gi));
        int g9 = gi - 9 * ((gi * 57) >> 9);
        asm volatile("" : "+s"(g9));
        const u32 so = g9 * (64 * 9216), ld = L_G + gi * 3072;
#pragma unroll
        for (int m = 0; m < 3; ++m) dma16g(sd, gv[m], so, ld + m * 1024);
    }
}

DEV void xt_upd(u32 (&XT)[10], float rm, float rrs) {
#pragma unroll
    for (int i = 0; i < 10; ++i) {
        const float a = lo16(XT[i]), c = hi16(XT[i]);
        const u32 h = pk2((a - rm) * rrs, (c - rm) * rrs);
        XT[i] = pk2(a + lo16(h), c + hi16(h));
    }
}

DEV void gu_pf(const u16* wg, const u16* wu, int lane, int b, int i) {
    const int j = b >> 3;
    const u32x4 sg = mk_srd(j < 16 ? wg : wu, 5308416);
    const int q = i * 54 + lane, r = (q * 57) >> 9;
    const u32 o = lane < 54 ? (u32)((j & 15) * 36 + r) * 9216u + (b & 7) * 1152u + (q - 9 * r) * 128u : (u32)OOB;
    asm volatile("s_nop 4\n\tbuffer_load_dword v98, %0, %1, 0 offen" :: "v"(o), "s"(sg) : "memory");
}

DEV int k_pos(int p, int cb) { return p >= 9 * cb + 8 ? p : (p >= 9 * cb ? p - 9 * cb : p + 8); }
DEV u32 w_voff(int wp, int r16, int p, int cb) {
    int ch = 16 * wp + r16; if (ch > 107) ch = 107;
    const int sec = (ch * 57) >> 11, c = ch - 36 * sec;
    return (u32)(8 * p) * 3456u + (sec == 2 ? 0u : (u32)(sec + 1) * 1152u) + cb * 144u + c * 4u;
}

__global__ void NOPK __attribute__((amdgpu_num_vgpr(24))) __launch_bounds__(512, 2)
mega(const u16* __restrict__ x0, const u16* __restrict__ wqkv, const u16* __restrict__ wo, const u16* __restrict__ wr,
     const u16* __restrict__ wg, const u16* __restrict__ wu, const u16* __restrict__ wd, u16* __restrict__ out, char* __restrict__ ws) {
    const int w_ = __builtin_amdgcn_readfirstlane(threadIdx.x >> 6);
    const int b_ = blockIdx.x;
    {
        u64 pc; u32 off;
        asm volatile("s_getpc_b64 %0\n"
                     "Lcode0:\n\t"
                     "v_lshlrev_b32 %1, 7, %2\n\t"
                     "v_and_b32 v99, 3, %2\n\t"
                     "v_cmp_gt_u32 vcc, Lcode1-Lcode0, %1\n\t"
                     "s_mov_b64 exec, vcc\n\t"
                     "v_cmp_eq_u32 vcc, %3, v99\n\t"
                     "s_and_b64 exec, exec, vcc\n\t"
                     "s_cbranch_execz Lcode2\n\t"
                     "global_load_dword v99, %1, %0\n"
                     "Lcode2:\n\t"
                     "s_mov_b64 exec, -1"
                     : "=&s"(pc), "=&v"(off) : "v"(threadIdx.x), "s"((b_ >> 3) & 3) : "vcc", "scc", "memory");
    }
    const __amdgpu_buffer_rsrc_t rs = __builtin_amdgcn_make_buffer_rsrc((void*)ws, 0, WS_BYTES, 0x00020000);
    u32 tag_, tmo = 0;
    {
        IDS
        int u = rg * 4 + (w & 3);
        const bool uok = u < 126;
        if (u > 125) u = 125;
        const int su = (u * 147) >> 10, wp = u - 7 * su;
        u32 wv = w_voff(wp, r16, g, cb), so = (u32)su * 110592u;
        sts32(L_BC + lane * 4, 0u);
        asm volatile("" : "+v"(wv), "+s"(so));
        asm volatile("" :: "s"(x0), "s"(wqkv), "s"(wo), "s"(ws));
        wld8p(mk_srd(wqkv, 1990656), wv, so, w);
        bar();
        { const char* ep_ = ws; asm volatile("" : "+s"(ep_)); tag_ = *(const __attribute__((address_space(4))) u32*)(ep_ + OFF_EPOCH) + 1u; }
        const int kp = k_pos(4 * su + g, cb);
        wunit_st(mk_srd(ws, WS_BYTES), (uok && !(wp == 6 && r16 >= 12)) ? OFF_WI + ((cb * 7 + wp) * 18 + (kp >> 2)) * 2048 + ((kp & 3) * (wp == 6 ? 12 : 16) + r16) * 16 : OOB, w);
        x_dma(mk_srd(x0 - 9216, 2359296 + 18432), (u32)((rg * 64 + (lane >> 3)) * 1152) + (u32)(((lane ^ (lane >> 4)) & 7) << 4), rg == 0 ? OOB : 0,
              ((XCI >> (4 * w)) & 15) + cb, (XCN >> (4 * w)) & 15);
        if (w < 4) {
            const int fa = lane < WREP ? OFF_FLAG + ((lane * 8 + cb) * 32 + rg) * FST : OOB;
            if (w > 0) { VMWAIT(10); } else { VMWAIT(0); }
            BST32(tag_, lds_inc_rtn(L_BC + BC_FC) == 3u ? fa : OOB, 0);
            __builtin_amdgcn_s_setprio(0);
        }
#define PFI(w) ((w) >= 4 ? (w) - 4 : (w) + 2)
#define PFON(w) ((w) > 0 && (w) < 7 && PFI(w) < PFA)
        if (PFON(w)) gu_pf(wg, wu, lane, b, PFI(w));
        if (w == 6) {
#pragma unroll
            for (int i = 0; i < 3; ++i) {
                const int q = lane + 64 * i;
                if (q < 144) sts128((q >> 4) * XCH + (q & 15) * 16, (u32x4){ONES2, ONES2, ONES2, ONES2});
            }
        }
        lc_set(lane_consts(w_, b_));
    }
    MidS a;
    {
        IDS
        const int rt4 = w & 3, kh = w >> 2, hp = 2 * cb + kh;
        const u32 obase = OFF_OT + (rg * 4 + rt4) * 18432;
        const u32 wl = OFF_WOI + cb * 82944;
        const u32 lo = w < 4 ? 16384 + w * 6144 : (w < 7 ? 64512 + (w - 4) * 6144 : 57344), eo = kh * 41472 + rt4 * 4096;
        a.n7 = rt4 == 3 ? 1024 : 0;
        a.ob = obase + kh * 1024; a.st = obase + hp * 1024; a.tt = obase + 16384 + hp * 128;
        a.c0 = (rg == 0 && rt4 == 0) ? 0xff61b1e6u : 0u;
        a.l1d = kh ? (u32)-(L_WOE + 9 * 4608) : 5 * 4608; a.tvd = cb * 2048;
        a.wi = wl + lo; a.ld = lo >= 64512 ? lo - 64512 : L_WOE + lo; a.wx = wl + eo; a.ldx = L_WOE + eo;
        a.msk = (u32)(kh - 1); a.scr = rt4 == 2 ? L_SCR3 : L_SCR + (rt4 == 3 ? 2 : rt4) * 4608; a.pk = L_PK(rt4);
        a.stat = OFF_STATS + (rg * 4 + rt4) * 2048 + cb * 256;
        a.x1 = OFF_X1G + rg * 73728 + 9 * cb * 1024 + rt4 * 256;
        asm volatile("" : "+s"(a.ob), "+s"(a.st), "+s"(a.tt), "+s"(a.c0), "+s"(a.l1d), "+s"(a.tvd), "+s"(a.wi), "+s"(a.ld), "+s"(a.wx), "+s"(a.ldx), "+s"(a.msk),
                          "+s"(a.scr), "+s"(a.stat), "+s"(a.x1), "+s"(a.pk), "+s"(a.n7));
    }
    asm volatile("" : "+s"(tag_));
#define VA_MASK (CSL(w) < 6 ? ~0ull : 0x0000FFFFFFFFFFFFull)
#define V0_MASK (CSL(w) < 4 ? ~0ull : (CSL(w) == 4 ? 0xFFFFFFFFull : 0ull))
    a.lb = L_WOE + (w_ >> 2) * 9 * 4608;
    a.skip = 0;
    a.wc = L_BC + BC_WC + (w_ >> 2) * 4;
    u64 lm0 = 0;
#pragma nounroll
    for (int layer = 0; layer < 2; ++layer) {
    u32 tag = tag_; asm volatile("" : "+s"(tag));
    if (layer == 0) {
    if (w_ < 7) {
        IDS
        const u32x4 sws = mk_srd(ws, WS_BYTES);
        const u32 fo = w > 0 ? OOB : OFF_GFLAG + (lane < 18 ? lane * 8 + cb : (((lane & 8) && rg > 0) ? rg - 1 : rg) * 8 + (lane & 7)) * 16;
        int xt[3];
        {
            const int row = 16 + 16 * (w & 3) + r16;
#pragma unroll
            for (int p = 0; p < 3; ++p) {
                const int pc = 9 * cb + 4 * p + (p < 2 ? g : 0);
                xt[p] = (pc >> 3) * XCH + row * 128 + (((pc ^ (row >> 1)) & 7) << 4) + (p < 2 ? 0 : (g & 1) * 8);
            }
            asm volatile("" : "+v"(xt[0]), "+v"(xt[1]), "+v"(xt[2]));
        }
        if (w >= 1) {
            if (PFON(w)) { VMWAIT(1); } else { VMWAIT(0); }
            lds_inc(L_BC + BC_XC, lane);
        }
        if (w >= 4) {
            LWAIT0(L_BC + BC_XC, 6, 22);
            float xs[2], xq[2];
            xstats((w - 4) * 2048 + r16 * 128 + (((g ^ (r16 >> 1)) & 7) << 4), xs, xq);
#pragma unroll
            for (int t = 0; t < 2; ++t) {
                const int row = 16 * (w - 4 + 3 * t) + r16;
                float m = xs[t] * (1.f / 576.f);
                float rsd = ln_rs(fmaxf(xq[t] * (1.f / 576.f) - m * m, 0.f));
                m = ln_m(m);
                if (row < 2) { m = 0.f; rsd = 0.f; }
                if (g == (r16 >> 2) && row < 80) sts64(L_MR + row * 8, (u32x2){__float_as_uint(m), __float_as_uint(rsd)});
            }
            lds_inc(L_BC + BC_MC, lane);
        }
        if (w == 0) {
            u32 cn = SPIN_MAX;
            poll_flags(sws, OFF_FLAG + ((rg & (WREP - 1)) * 256 + cb * 32 + (lane & 31)) * FST, tag, cn);
            sts32v(L_BC + BC_GC, 1u);
            if (cn == 0) TMO(3);
        } else {
            LWAIT0(L_BC + BC_GC, 1, 23);
        }
        LWAIT0(L_BC + BC_XC, 6, 22);
        {
            const u32 ib = OFF_WI + (cb * 7 + CSL(w)) * 36864;
            u32 cn = SPIN_MAX;
            gemm0(sws, r16 * 128 + (((g ^ (r16 >> 1)) & 7) << 4), (CSL(w) == 6 ? g * 12 + (r16 < 12 ? r16 : 11) : lane) * 16, ib, xt, 2048 + 2048 * (w & 3), 3u << cb, g >= cb ? XCH : 0, g + 4 >= cb ? XCH : 0, fo, w > 0 ? 0u : tag, L_CS + w * 128, VA_MASK, V0_MASK, cn);
            if ((int)cn < 1) TMO(25);
        }
    } else {
        IDS
        __builtin_amdgcn_s_setprio(3);
        {
            u32 U[3][8];
            const int ks = rg < 18 ? rg : 0;
            const int kh = ks >= 9, ki = ks - 9 * kh;
            const u32 rb = rg < 18 ? (u32)(ki < 8 ? 36 * (2 * (cb ^ ki) + kh) + 4 * g : 36 * (8 * kh + 2 * g) + 16) * 1152u + cb * 144u : (u32)OOB;
#pragma unroll
            for (int p = 0; p < 3; ++p) old8(U[p], mk_srd(wo, 663552), rb + p * 64u + (p < 2 ? r16 * 4u : ((r16 & 7) >> 1) * 4u), (ki < 8 ? 20 : 36) * 1152);
            u32 sw_ = SENT; asm volatile("" : "+v"(sw_));
            const u32x4 sv = {sw_, sw_, sw_, sw_};
            const int ob = OFF_OT + rg * 4 * 18432;
            LWAIT(L_BC + BC_FC, 4, 43);
#pragma unroll
            for (int i = 0; i < 8; ++i) BST128(sv, lane * 16 + (i & 1) * 1024, ob + (i >> 1) * 18432 + cb * 2048);
            BST128(sv, g * 18432 + r16 * 16, ob + 16384 + cb * 256);
            BST128(sv, g * 2048 + r16 * 16, OFF_STATS + rg * 4 * 2048 + cb * 256);
            const int wb = rg < 18 ? OFF_WOI + cb * 82944 + ks * 4608 : OOB;
#pragma unroll
            for (int p = 0; p < 3; ++p) {
                vmwait8n(U[p], 10);
                u32x4 f0, f1;
                wfrag(U[p], f0, f1);
                if (p < 2) { BST128(f0, wb + p * 2048 + lane * 16, 0); BST128(f1, wb + p * 2048 + 1024 + lane * 16, 0); }
                else {
                    const u32x4 f = (r16 & 1) ? f1 : f0;
                    BST64(((u32x2){r16 < 8 ? f.x : f.y, r16 < 8 ? f.z : f.w}), wb + 4096 + ((g >> 1) * 16 + r16) * 16 + (g & 1) * 8, 0);
                }
            }
        }
        u32 wq[6];
        {
            const u32x4 sr = mk_srd(wr, 1152);
#pragma unroll
            for (int i = 0; i < 5; ++i) bld32(wq[i], sr, lane * 4u, 0u, 256 * i);
            bld32(wq[5], sr, lane < 32 ? cb * 144u + lane * 4u : ((lane < 40 && !(lane & 2)) ? cb * 144u + 128u + ((lane >> 2) & 1) * 8u + (lane & 1) * 4u : (u32)OOB), 0u, 0);
        }
        { int nu; const int f0 = ffn_slice(b, nu); dload(mk_srd(wd, (f0 + nu) * 1152), lane * 2u, f0 * 1152u); }
        asm volatile("s_waitcnt vmcnt(%6)" : "+v"(wq[0]), "+v"(wq[1]), "+v"(wq[2]), "+v"(wq[3]), "+v"(wq[4]), "+v"(wq[5]) : "n"(48) : "memory");
        if (lane == 0) BST32(tag, OFF_GFLAG + (rg * 8 + cb) * 16, 0);
        {
            float sacc = 0.f;
#pragma unroll
            for (int q = 0; q < 5; ++q) sacc = dot2(wq[q], ONES2, sacc);
            sacc = gsum(rsum(sacc));
            if (lane < 48) sts32(L_WR + lane * 4, wq[5]);
            if (lane < 36) sts32(L_WR + 192 + lane * 4, ONES2);
            if (lane == 0) sts32(L_BC + BC_WRS, __float_as_uint(sacc));
        }
        asm volatile("s_nop 4\n\tbuffer_load_dword v98, %0, %1, 0 offen sc1" :: "v"(lane < 2 ? OFF_KEY + b * 256 + lane * 128 : (lane == 2 ? OFF_XFLAG + b * 128 : OOB)), "s"(mk_srd(ws, WS_BYTES)) : "memory");
        LWAIT(L_BC + BC_GC, 1, 31);
        __builtin_amdgcn_s_sleep(30);
        __builtin_amdgcn_s_setprio(3);
#pragma unroll
        for (int i = PFA; i < 6; ++i) gu_pf(wg, wu, lane, b, i);
        vmwaitn(6 - PFA);
        dpermx(); dperm(); gu_set();
        VMWAIT(0);
        __builtin_amdgcn_s_setprio(3);
        gu_dma(wg, wu, lane, b, 0, 15);
        {
            u32 sw_ = SENT; asm volatile("" : "+v"(sw_));
            const u32x4 sv = {sw_, sw_, sw_, sw_};
            const int l16 = lane * 16;
#pragma unroll
            for (int l = 0; l < 2; ++l) {
                BST128(sv, (r16 < 9 ? g * 1280 + cb * 144 + r16 * 16 : ((r16 == 9 && cb == 0) ? g * 1280 + 1152 : OOB)), OFF_CROW + (l * 128 + rg * 4) * 1280);
            }
            if (b < 144) {
#pragma unroll
                for (int i = 0; i < 4; ++i) BST128(sv, l16 + i * 1024, OFF_YP + b * 4096);
            }
            if (b >= 251) BST128(sv, l16, OFF_X2R + (b - 251) * 1024);
#pragma unroll
            for (int i = 0; i < 9; ++i) BST128(sv, l16 + (i & 3) * 1024, OFF_X1G + rg * 73728 + cb * 9216 + (i >> 2) * 4096);
            BST128(sv, g * 2048 + r16 * 16, OFF_STATS + STATS_B + rg * 4 * 2048 + cb * 256);
        }
        __builtin_amdgcn_s_setprio(0);
    }
    }
#define XFLAG_OFF (OFF_XFLAG + ((((lane & 8) && rg > 0) ? rg - 1 : rg) * 8 + (lane & 7)) * XFS)
#define WO_PRE() do { const u32x4 sws = mk_srd(ws, WS_BYTES); dma16x4_sc1(sws, lane * 16u, a.wx, a.ldx); if (2 > 1) { dma16x4_sc1(sws, lane * 16u, a.wi, a.ld); \
    dma16x2_sc1(sws, lane * 16u, a.wi + 4096, a.ld + 4096); } a.skip = 3; } while (0)
        bool xf;
        {
            IDS
            u32 cn = LSPIN_MAX / 4;
            u32 fo = (layer == 0 && (w & 6) == 4) ? XFLAG_OFF : OOB;
            a.srd = mk_srd(ws, WS_BYTES); a.layer = layer;
            att(a);
            if (layer == 0 && w == 7) BST32(tag, lane == 0 ? OFF_XFLAG + (rg * 8 + cb) * XFS : OOB, 0);
            mid(a, fo, cn);
            xf = __builtin_amdgcn_ballot_w64(fo != tag) != 0ull;
            a.stat += STATS_B;
            if (cn == 0) TMO(4);
        }
        if (layer == 0 && w_ == 7) { IDS gu_dma(wg, wu, lane, b, 15, 18); }
        if (layer == 0) {
            IDS
#pragma nounroll
            for (u32 m = (0x21004000u >> (4 * w)) & 7; m; m &= m - 1) { gu_tr(L_G + 6144 + __builtin_ctz(m) * 6144); lds_inc(L_BC + BC_PP, lane); }
        }
        if (w_ == (layer == 0 ? 6 : 3)) {
            IDS
            const int tile = rg * 4 + g;
            __builtin_amdgcn_s_setprio(3);
            float wrsum = __uint_as_float(lds32(L_BC + BC_WRS));
            const int cr = OFF_CROW + (layer * 128 + tile) * 1280;
            int ka = r16 < 4 ? OFF_KEY + (layer * 32 + cb * 4 + r16) * 1024 + tile * 8 : OOB;
            int pa_ = L_PK(g) + (r16 < 9 ? r16 : 0) * 256 + 240, ca_ = r16 < 9 ? cr + cb * 144 + r16 * 16 : OOB, sa_ = cb == 0 ? cr + 1152 : OOB;
            u32 idx = 2047 - (rg * 64 + lane), tv = tag, sv = g * 2048 + r16 * 16;
            asm volatile("" : "+v"(wrsum), "+v"(ka), "+v"(pa_), "+v"(ca_), "+v"(sa_), "+v"(idx), "+v"(tv), "+v"(sv));
            float t[3];
            u32 cn = SPIN_MAX;
            LWAIT0(L_BC + BC_PK, 4 * (layer + 1), 27);
            if (layer == 0) { gemm1p(); __builtin_amdgcn_s_sleep(3); }
            if (layer == 1) __builtin_amdgcn_s_sleep(5);
            poll_stats(mk_srd(ws, WS_BYTES), sv, OFF_STATS + layer * STATS_B + rg * 8192, t, cn);
            const float rm0 = t[0] * (1.f / 576.f);
            const float var = fmaxf(t[1] * (1.f / 576.f) - rm0 * rm0, 0.f);
            const float rm = ln_m(rm0), rrs = ln_rs(var);
            const float score = rrs * (t[2] - rm * wrsum);
            const u32 sb = pk2(0.f, score);
            const u32 key = ((sb ^ ((u32)((int)sb >> 31) | 0x80000000u)) & 0xffff0000u) | idx;
            const u32 best = rmax(key);
            BST64(((u32x2){best, tv}), ka, 0);
            CFENCE();
            {
                const u32x4 v = lds128(pa_ - (int)(best & 15u) * 16);
                sts64(L_RS + lane * 8, (u32x2){__float_as_uint(rm), __float_as_uint(rrs)});
                sts32(L_BC + BC_TB + g * 4, best);
                CFENCE();
                sts32v(L_BC + BC_KC, layer + 1);
                BST128(((u32x4){san(v.x), san(v.y), san(v.z), san(v.w)}), ca_, 0);
                BST128(((u32x4){san(__float_as_uint(rm)), san(__float_as_uint(rrs)), san(__float_as_uint(rm)), san(__float_as_uint(rrs))}), key == best ? sa_ : OOB, 0);
            }
            if (cn == 0) TMO(5);
            if (layer == 0) {
                sts64(L_MR + (16 + lane) * 8, ln_next(rm0, var, rm, rrs));
                lds_inc(L_BC + BC_MC, lane);
            }
            __builtin_amdgcn_s_setprio(0);
        }
        if (layer == 0 && w_ == 3) {
            IDS
            float t[3];
            u32 cn = SPIN_MAX;
            LWAIT0(L_BC + BC_PK, 4, 27);
            gemm1p();
            poll_stats1(mk_srd(ws, WS_BYTES), OFF_STATS + (rg > 0 ? rg * 4 - 1 : 3) * 2048, t, cn);
            if (cn == 0) TMO(15);
            const float hm = gsum(t[0]) * (1.f / 576.f);
            const float hv = fmaxf(gsum(t[1]) * (1.f / 576.f) - hm * hm, 0.f);
            if (r16 > 1) sts64(L_MR + r16 * 8, ln_next(hm, hv, ln_m(hm), ln_rs(hv)));
            lds_inc(L_BC + BC_MC, lane);
        }
        if (layer == 0 && (w_ == 4 || w_ == 5)) {
            IDS
            u32 cn = SPIN_MAX;
            const int h = w - 4;
            if (xf) poll_flagh(mk_srd(ws, WS_BYTES), XFLAG_OFF, tag, cn);
            if (cn == 0) TMO(6);
            u32 own = OFF_X1G + rg * 73728, xp = own - (rg > 0 ? 73728 : 0) + h * 36864 - 8192, xq = xp + (h ? 0 : cb * 9216 + 8192);
            int th = 9 * cb - 8 * h, hh = 9 * cb + 8 - 36 * h;
            asm volatile("" : "+s"(own), "+s"(xp), "+s"(xq), "+s"(th), "+s"(hh));
            LWAIT0(L_BC + BC_PK, 4, 27);
            __builtin_amdgcn_s_setprio(3);
            gemm1p();
            cn = SPIN_MAX;
            xgather(mk_srd(ws, WS_BYTES), own, xp, xq, th, hh, 8192 + h * 8192, L_XH + h * 9216, cn);
            if ((int)cn < 1) TMO(39);
            __builtin_amdgcn_s_setprio(0);
        }
        if (w_ < 4) {
            if (layer == 1) {
                IDS
                const int grow = rg * 64 + w * 16 + r16;
                LWAIT(L_BC + BC_KC, 2, 24);
                const u32 best = lds32(L_BC + BC_TB + w * 4);
                const bool isb = (best & 0x7ffu) == (u32)(2047 - grow);
                const u32x2 mrs = lds64(L_RS + (16 * w + r16) * 8);
                u32 XT[10];
                xt_get(XT); xt_upd(XT, __uint_as_float(mrs.x), __uint_as_float(mrs.y));
                const __amdgpu_buffer_rsrc_t ro = __builtin_amdgcn_make_buffer_rsrc((void*)out, 0, 2359296, 0x00020000);
                const int ob = grow * 1152 + cb * 144;
                const u32x4 v0 = {XT[0], XT[1], XT[2], XT[3]}, v1 = {XT[4], XT[5], XT[6], XT[7]};
                const u32x2 v2 = {XT[8], XT[9]};
                if (!isb) {
                    __builtin_amdgcn_raw_buffer_store_b128(v0, ro, ob + g * 16, 0, 2);
                    __builtin_amdgcn_raw_buffer_store_b128(v1, ro, ob + 64 + g * 16, 0, 2);
                    if (g < 2) __builtin_amdgcn_raw_buffer_store_b64(v2, ro, ob + 128 + g * 8, 0, 2);
                }
                LWAIT(L_BC + BC_FIN, 1, 13);
                if (isb && best != lds32v(L_BC + BC_BEST1)) {
                    __builtin_amdgcn_raw_buffer_store_b128(v0, ro, ob + g * 16, 0, 0);
                    __builtin_amdgcn_raw_buffer_store_b128(v1, ro, ob + 64 + g * 16, 0, 0);
                    if (g < 2) __builtin_amdgcn_raw_buffer_store_b64(v2, ro, ob + 128 + g * 8, 0, 0);
                }
            }
        } else if (w_ == 4 && layer == 1) {
            IDS
            __builtin_amdgcn_s_setprio(3);
            u32 k, cn = SPIN_MAX;
            LWAIT(L_BC + BC_SP, 1, 17);
            poll_keys2(mk_srd(ws, WS_BYTES), OFF_KEY + (32 + (b & 31)) * 1024, tag, k, cn);
            if (cn == 0) TMO(9);
            k = rmax(k);
            const u32 best = umax2(umax2(__builtin_amdgcn_readlane(k, 0), __builtin_amdgcn_readlane(k, 16)), umax2(__builtin_amdgcn_readlane(k, 32), __builtin_amdgcn_readlane(k, 48)));
            sts32v(L_BC + BC_BEST1, best);
            sts32v(L_BC + BC_FIN, best == lds32v(L_BC + BC_SP) ? 1u : 2u);
        }
        if (w_ < 7) {
            if (layer == 0) {
                u32 cn = LSPIN_MAX, best, lc;
                {
                    IDS
                    int fb = L_BC;
                    lc = 2061 - 64 * rg;
                    asm volatile("" : "+v"(fb), "+s"(lc));
                    if (w < 4 && w != 3) { LWAIT0(L_BC + BC_PK, 4, 27); gemm1p(); }
                    LWAIT0(L_BC + BC_G0, 2, 16);
                    gemm1a(g * 1024 + r16 * 16, fb, L_CS + w * 128, VA_MASK, V0_MASK, w == 3 ? 5 : 4, best, cn);
                }
                if (w_ < 4 || best == 0u || lc - (best & 0x7ffu) < 78u) {
                IDS
                if (w < 4) __builtin_amdgcn_s_setprio(0);
                if (best == 0u) {
                    LWAIT(L_BC + BC_BEST, 1, 28);
                    best = __builtin_amdgcn_readfirstlane(lds32v(L_BC + BC_BEST));
                }
                if (w == 0) {
                    const u64 lm = LATE_MASK(2047 - (int)(best & 0x7ffu));
                    if (b == 255) BST32(tag, lane == 0 ? OFF_EPOCH : OOB, 0);
                    u32 sw_ = SENT; asm volatile("" : "+v"(sw_));
                    const u32x4 sv = {sw_, sw_, sw_, sw_};
#pragma nounroll
                    for (int t = 0; t < 3; ++t) {
                        const bool on = t == 2 ? b >= 112 : (b < 144 && ((lm >> (2 * (rg ^ (16 * t)))) & 1) == (u64)t);
                        const int o = t == 2 ? OFF_YP2 + (b - 112) * 4096 : OFF_YP + YP_B + (b ^ (4 * t)) * 4096;
                        if (on) {
#pragma unroll
                            for (int i = 0; i < 4; ++i) BST128(sv, lane * 16 + i * 1024, o);
                        }
                    }
                }
                if (w < 3) {
                    LWAIT(L_BC + BC_C1, 7, 48);
                    WO_PRE();
                }
                if (lc - (best & 0x7ffu) < 78u)
                    gemm1b(L_BC, L_XR + cb * 144 + g * 16, L_XR + cb * 144 + 128 + (g & 1) * 8, w + 1, L_CS + w * 128, VA_MASK, V0_MASK, cn);
                rs_get(L_RS + (16 * (w & 3) + r16) * 8);
                }
                if ((int)cn < 1) TMO(16);
            }
        } else {
            IDS
            if (layer == 1) __builtin_amdgcn_s_setprio(3);
            int ridx;
#pragma nounroll
            for (int rd = layer; ; ++rd) {
                IDS
                u32 c0 = r16 * 64 + g * 16, c1 = r16 < 2 ? c0 + 1024 : 1152;
                u32 ya = (lane >> 2) * 16384u + (lane & 3) * 16u, ye = (32 + g) * 16384u + r16 * 4u;
                asm volatile("" : "+v"(c0), "+v"(c1), "+v"(ya), "+v"(ye));
                const u32x4 sws = mk_srd(ws, WS_BYTES);
                u32 yp = rd == 2 ? OFF_YP2 : OFF_YP + rd * YP_B;
                asm volatile("" : "+s"(yp));
                u32 best;
                if (rd == 2) best = __builtin_amdgcn_readfirstlane(lds32v(L_BC + BC_BEST1));
                else {
                    u32 k, cn = SPIN_MAX;
                    const bool late = (lm0 >> (2 * rg)) & 1;
                    u32 ko = OFF_KEY + (layer * 32 + (b & 31)) * 1024;
                    asm volatile("" : "+s"(ko));
                    VMWAIT(0);
                    __builtin_amdgcn_s_setprio(3);
                    gu_pre();
                    if (!late) {
                        LWAIT(L_BC + BC_KC, layer + 1, 29);
                        if (layer == 0) __builtin_amdgcn_s_sleep(5);
                        if (layer == 1) __builtin_amdgcn_s_sleep(9);
                    }
                    if (layer == 0) LWAIT0(L_BC + BC_PP, 3, 33);
                    poll_keys(sws, ko, tag, lm0, k, cn);
                    if (cn == 0) TMO(7);
                    k = rmax(k);
                    best = umax2(umax2(__builtin_amdgcn_readlane(k, 0), __builtin_amdgcn_readlane(k, 16)), umax2(__builtin_amdgcn_readlane(k, 32), __builtin_amdgcn_readlane(k, 48)));
                }
                ridx = 2047 - (int)(best & 0x7ffu);
                sts32v(L_BC + BC_BEST + rd * 8, best);
                float fm, frs;
                {
                    u32 cn = LSPIN_MAX;
                    ffn(sws, OFF_CROW + (layer * 128 + (ridx >> 4)) * 1280, c0, c1, ya, ye, yp + b * 64, rd >> 1, fm, frs, cn);
                    if ((int)cn < 1) TMO(14);
                }
                u32 ab = 3 - (rd & 1);
                if (b < 144) {
                    const int q = (b & 7) * 18 + (b >> 3), c0 = q < 128 ? 8 * (q & 63) + 4 * (q >> 6) : 4 * q;
                    const u32 sbits = best >> 16;
                    const u32 sraw = (sbits & 0x8000u) ? (sbits & 0x7fffu) : (~sbits & 0xffffu);
                    const float sc = __uint_as_float(sraw << 16);
                    const float sg = rbf(__builtin_amdgcn_rcpf(1.f + __builtin_amdgcn_exp2f(-sc * 1.4426950408889634f)));
                    const u32 so = (lane & 31) == 16 ? 2 * (g & 2) : OOB;
                    u32 cn = LSPIN_MAX;
                    reduce(mk_srd(ws, WS_BYTES), mk_srd(out, 2359296), lane * 64u, yp + (q >> 2) * 16384 + (q & 3) * 16, L_XR + 2 * c0 + 2 * (g & 2), fm, frs, sg,
                           so + (layer == 0 ? OFF_X2R + 32 * q : OOB), so + (layer == 1 ? ridx * 1152 + 2 * c0 : OOB), L_BC + BC_FIN, ab, cn);
                    if ((int)cn < 1) TMO(8);
                } else if (rd == 1) {
                    LWAIT0(L_BC + BC_FIN, 1, 18);
                    ab = __builtin_amdgcn_readfirstlane(lds32v(L_BC + BC_FIN));
                }
                if (ab != 2u) break;
            }
            lm0 = LATE_MASK(ridx);
            const int prow = ridx - (rg * 64 - 16);
            const bool affected = (lm0 >> (2 * rg)) & 1;
            if (layer == 0 && affected) {
                LWAIT0(L_BC + BC_G0 + 16, 2, 41);
                if (prow >= 16) sts32v(L_RS + (prow - 16) * 8 + 4, 0u);
                u32 o[6], cn = SPIN_MAX;
                int h0 = L_XH + k_pos(lane, cb) * 256, h1 = L_XH + k_pos(64 + (r16 >> 1), cb) * 256 + (r16 & 1) * 8;
                u32 gw = 1u | (u32)prow << 8;
                asm volatile("" : "+v"(h0), "+v"(h1), "+v"(gw));
                poll_row(mk_srd(ws, WS_BYTES), OFF_X2R + lane * 32, OFF_X2R + 4096 + r16 * 32, o, cn);
                if (cn == 0) TMO(2);
                u32x4 c0 = {o[0], o[1], o[2], o[3]}; u32x2 c1 = {o[4], o[5]};
                sts128(h0, c0);
                sts64(h1, c1);
                CFENCE();
                sts32v(L_BC + BC_GO, gw);
                CFENCE();
                sts128(L_XR + lane * 16, c0);
                sts64(L_XR + 1024 + r16 * 8, c1);
                if (lane >= 16) c1 = (u32x2){0u, 0u};
                float psx = 0.f, psq = 0.f;
#pragma unroll
                for (int i = 0; i < 4; ++i) {
                    psx = dot2(c0[i], ONES2, psx); psq = dot2(c0[i], c0[i], psq);
                    if (i < 2) { psx = dot2(c1[i], ONES2, psx); psq = dot2(c1[i], c1[i], psq); }
                }
                psx = gsum(rsum(psx)); psq = gsum(rsum(psq));
                CFENCE();
                const float pm0 = psx * (1.f / 576.f);
                const float prs = ln_rs(fmaxf(psq * (1.f / 576.f) - pm0 * pm0, 0.f)), pm = ln_m(pm0);
                sts32v(L_MR, __float_as_uint(pm)); sts32v(L_MR + 4, __float_as_uint(prs)); sts32v(L_BC + BC_PST, 255u);
            }
            __builtin_amdgcn_s_setprio(0);
        }
    }
    if (tmo != 0u) BST32(tmo, OFF_TMO, 0);
    asm volatile("Lcode1:");
}

extern "C" void kernel_launch(void* const* d_in, const int* in_sizes, int n_in, void* d_out, int out_size, void* d_ws, size_t ws_size,
                              hipStream_t stream) {
    (void)in_sizes; (void)n_in; (void)out_size; (void)ws_size;
    hipFuncSetAttribute((const void*)mega, hipFuncAttributeMaxDynamicSharedMemorySize, LDS_BYTES);
    mega<<<256, 512, LDS_BYTES, stream>>>((const u16*)d_in[0], (const u16*)d_in[1], (const u16*)d_in[2], (const u16*)d_in[4],
                                          (const u16*)d_in[5], (const u16*)d_in[6], (const u16*)d_in[7], (u16*)d_out, (char*)d_ws);
}
```
